```python
import torch

def _softmax(x, axis=-1):
    return torch.softmax(x, dim=axis)
def _gelu(x):
    return torch.nn.functional.gelu(x, approximate="tanh")
def _silu(x):
    return torch.nn.functional.silu(x)
def _sigmoid(x):
    return torch.sigmoid(x)
def _layernorm(x, axis=-1, eps=1e-5):
    m = x.mean(dim=axis, keepdim=True)
    v = x.var(dim=axis, keepdim=True, unbiased=False)
    return (x - m) / torch.sqrt(v + eps)
def _scatter_add(tgt, idx, src, axis=0):
    return tgt.clone().index_add_(axis, idx, src)
def _segment_sum(src, seg, n):
    out = torch.zeros((n,) + src.shape[1:], dtype=src.dtype)
    return out.index_add_(0, seg, src)
def _pad(x, pw):
    flat = tuple(v for pair in reversed(pw) for v in pair)
    return torch.nn.functional.pad(x, flat)

kernel_name = 'interleave_layers__gqa_block_cached__glu_gated_glu__decode'

def setup_inputs():

    _w = (lambda a: (lambda t: t.to(torch.bfloat16) if t.is_floating_point() else t)(torch.as_tensor(a)))
    def _A_setup():
        B, Sq, D = 1, 1, 512
        Hq, Hkv, Sk, Dh = 32, 32, 16384, 128
        assert Hq % Hkv == 0, (Hq, Hkv)
        return {
            "x": _w((lambda *s: torch.randn(*s, dtype=torch.bfloat16) * 0.05)(B, Sq, D)),
            "k_cache": _w((lambda *s: torch.randn(*s, dtype=torch.bfloat16) * 1.6)(B, Hkv, Sk, Dh)),
            "v_cache": _w((lambda *s: torch.randn(*s, dtype=torch.bfloat16) * 1.6)(B, Hkv, Sk, Dh)),
            "wq": _w((lambda *s: torch.randn(*s, dtype=torch.bfloat16) * 1.6)(D, Hq * Dh) * (D ** -0.5)),
            "wo": _w((lambda *s: torch.randn(*s, dtype=torch.bfloat16) * 1.6)(Hq * Dh, D) * ((Hq * Dh) ** -0.5)),
        }
    _A_in = _A_setup()

    def _B_setup():
        B, S, D, F = 1, 1, 512, 1376
        return {
            "x": _w((lambda *s: torch.randn(*s, dtype=torch.bfloat16) * 0.05)(B, S, D)),
            "w_gate": _w((lambda *s: torch.randn(*s, dtype=torch.bfloat16) * 0.05)(D, F)),
            "w_up": _w((lambda *s: torch.randn(*s, dtype=torch.bfloat16) * 0.05)(D, F)),
            "w_down": _w((lambda *s: torch.randn(*s, dtype=torch.bfloat16) * 0.05)(F, D)),
        }
    _B_in = _B_setup()

    return {
        "A_x": _A_in["x"],
        "A_k_cache": _A_in["k_cache"],
        "A_v_cache": _A_in["v_cache"],
        "A_wq": _A_in["wq"],
        "A_wo": _A_in["wo"],
        "B_x": _B_in["x"],
        "B_w_gate": _B_in["w_gate"],
        "B_w_up": _B_in["w_up"],
        "B_w_down": _B_in["w_down"],
    }

def reference(A_x, A_k_cache, A_v_cache, A_wq, A_wo, B_x, B_w_gate, B_w_up, B_w_down):
    def _A_ref(x, k_cache, v_cache, wq, wo):
        B, Sq, D = x.shape
        Hq, Dh = 32, 128
        Hkv = k_cache.shape[1]
        rep = Hq // Hkv
        q = (lambda x, *d: x.permute(*d))((x @ wq).reshape(B, Sq, Hq, Dh), 0, 2, 1, 3)
        k = (lambda x, n, axis: torch.repeat_interleave(x, n, dim=axis))(k_cache, rep, axis=1)
        v = (lambda x, n, axis: torch.repeat_interleave(x, n, dim=axis))(v_cache, rep, axis=1)
        scores = torch.einsum("bhqd,bhkd->bhqk", q, k) * (Dh ** -0.5)
        p = _softmax(scores, axis=-1)
        o = torch.einsum("bhqk,bhkd->bhqd", p, v)
        return (lambda x, *d: x.permute(*d))(o, 0, 2, 1, 3).reshape(B, Sq, Hq * Dh) @ wo

    def _B_ref(x, w_gate, w_up, w_down):
        g = torch.einsum("bsd,df->bsf", x, w_gate)
        u = torch.einsum("bsd,df->bsf", x, w_up)
        h = _sigmoid(g) * u
        return torch.einsum("bsf,fd->bsd", h, w_down)

    x = A_x
    x = x + _A_ref(_layernorm(x, axis=-1), A_k_cache, A_v_cache, A_wq, A_wo)
    x = x + _B_ref(_layernorm(x, axis=-1), B_w_gate, B_w_up, B_w_down)
    x = x + _A_ref(_layernorm(x, axis=-1), A_k_cache, A_v_cache, A_wq, A_wo)
    x = x + _B_ref(_layernorm(x, axis=-1), B_w_gate, B_w_up, B_w_down)
    return x
```

```cpp
#include <hip/hip_runtime.h>
#include <hip/hip_bf16.h>
#include <stdint.h>

typedef unsigned u32x4 __attribute__((ext_vector_type(4)));
typedef __bf16 bf16x2 __attribute__((ext_vector_type(2)));
constexpr int D = 512, H = 32, SK = 16384, DH = 128, F = 1376;
constexpr int NSPLIT = 16;
constexpr int KPS = SK / NSPLIT;

__device__ __forceinline__ float bf2f(unsigned short b) { return __builtin_bit_cast(float, (unsigned)b << 16); }
__device__ __forceinline__ float rbf(float x) { return bf2f(__builtin_bit_cast(unsigned short, (__bf16)x)); }
__device__ __forceinline__ float dot2(unsigned k, unsigned q, float acc) {
  return __builtin_amdgcn_fdot2_f32_bf16(__builtin_bit_cast(bf16x2, k), __builtin_bit_cast(bf16x2, q), acc, false);
}
__device__ __forceinline__ float wave_sum(float v) {
  for (int o = 32; o > 0; o >>= 1) v += __shfl_xor(v, o);
  return v;
}
__device__ void ln512(const float* x, float* xn, float* red) {
  int t = threadIdx.x;
  float a = x[t], b = x[t + 256];
  float s = wave_sum(a + b);
  if ((t & 63) == 0) red[t >> 6] = s;
  __syncthreads();
  float m = (red[0] + red[1] + red[2] + red[3]) * (1.f / 512.f);
  __syncthreads();
  float da = a - m, db = b - m;
  float v = wave_sum(da * da + db * db);
  if ((t & 63) == 0) red[t >> 6] = v;
  __syncthreads();
  float var = (red[0] + red[1] + red[2] + red[3]) * (1.f / 512.f);
  float mb = rbf(m), vb = rbf(var);
  float den = rbf(sqrtf(rbf(vb + 1e-5f)));
  xn[t] = rbf(rbf(a - mb) / den);
  xn[t + 256] = rbf(rbf(b - mb) / den);
  __syncthreads();
}

__global__ void k_init(const unsigned short* x, float* xs) { int t = threadIdx.x; xs[t] = bf2f(x[t]); }

__global__ void __launch_bounds__(256) k_qproj(const float* xs, const unsigned short* wq, unsigned short* q) {
  __shared__ float xn[512]; __shared__ float red[4]; __shared__ float part[4][64];
  ln512(xs, xn, red);
  int t = threadIdx.x, c = blockIdx.x * 64 + (t & 63), g = t >> 6;
  float acc = 0.f;
  for (int k = g * 128; k < g * 128 + 128; ++k) acc += xn[k] * bf2f(wq[(size_t)k * 4096 + c]);
  part[g][t & 63] = acc;
  __syncthreads();
  if (t < 64) { float s = part[0][t] + part[1][t] + part[2][t] + part[3][t]; q[c] = __builtin_bit_cast(unsigned short, (__bf16)s); }
}

__global__ void __launch_bounds__(256) k_attn(const unsigned short* q, const unsigned short* K, const unsigned short* V, float* pml, float* po) {
  __shared__ float mrg[4][132];
  int s = blockIdx.x, h = blockIdx.y, t = threadIdx.x, lane = t & 63, w = t >> 6, r = lane >> 4, c = lane & 15;
  u32x4 qv = *(const u32x4*)(q + h * 128 + 8 * c);
  const unsigned short* kb = K + ((size_t)h * SK + (size_t)s * KPS) * DH;
  const unsigned short* vb = V + ((size_t)h * SK + (size_t)s * KPS) * DH;
  float m = -INFINITY, l = 0.f, acc[8];
  for (int i = 0; i < 8; ++i) acc[i] = 0.f;
  const float SCALE = 0.088388347648318440f;
  for (int k0 = w * 16; k0 < KPS; k0 += 64) {
    u32x4 kk[4], vv[4];
#pragma unroll
    for (int i = 0; i < 4; ++i) {
      kk[i] = __builtin_nontemporal_load((const u32x4*)(kb + (size_t)(k0 + 4 * i + r) * DH + 8 * c));
      vv[i] = __builtin_nontemporal_load((const u32x4*)(vb + (size_t)(k0 + 4 * i + r) * DH + 8 * c));
    }
    float sc[4];
#pragma unroll
    for (int i = 0; i < 4; ++i) {
      float d = 0.f;
#pragma unroll
      for (int j = 0; j < 4; ++j) d = dot2(kk[i][j], qv[j], d);
      d += __shfl_xor(d, 1); d += __shfl_xor(d, 2); d += __shfl_xor(d, 4); d += __shfl_xor(d, 8);
      sc[i] = rbf(rbf(d) * rbf(SCALE));
    }
    float pm = fmaxf(fmaxf(sc[0], sc[1]), fmaxf(sc[2], sc[3]));
    float mn = fmaxf(m, pm);
    float al = __expf(m - mn);
    l *= al;
#pragma unroll
    for (int j = 0; j < 8; ++j) acc[j] *= al;
#pragma unroll
    for (int i = 0; i < 4; ++i) {
      float p = __expf(sc[i] - mn);
      l += p;
#pragma unroll
      for (int j = 0; j < 4; ++j) {
        acc[2 * j] += p * __builtin_bit_cast(float, vv[i][j] << 16);
        acc[2 * j + 1] += p * __builtin_bit_cast(float, vv[i][j] & 0xffff0000u);
      }
    }
    m = mn;
  }
  float M = fmaxf(m, __shfl_xor(m, 16)); M = fmaxf(M, __shfl_xor(M, 32));
  float f = __expf(m - M);
  l *= f; l += __shfl_xor(l, 16); l += __shfl_xor(l, 32);
  for (int j = 0; j < 8; ++j) { float a = acc[j] * f; a += __shfl_xor(a, 16); a += __shfl_xor(a, 32); acc[j] = a; }
  if (r == 0) { for (int j = 0; j < 8; ++j) mrg[w][4 + 8 * c + j] = acc[j]; if (c == 0) { mrg[w][0] = M; mrg[w][1] = l; } }
  __syncthreads();
  if (t < 128) {
    float MM = fmaxf(fmaxf(mrg[0][0], mrg[1][0]), fmaxf(mrg[2][0], mrg[3][0]));
    float L = 0.f, o = 0.f;
    for (int ww = 0; ww < 4; ++ww) { float wt = __expf(mrg[ww][0] - MM); L += wt * mrg[ww][1]; o += wt * mrg[ww][4 + t]; }
    po[((size_t)h * NSPLIT + s) * 128 + t] = o;
    if (t == 0) { pml[(h * NSPLIT + s) * 2] = MM; pml[(h * NSPLIT + s) * 2 + 1] = L; }
  }
}

__global__ void __launch_bounds__(256) k_wo(const float* pml, const float* po, const unsigned short* wo, float* part) {
  __shared__ float o[128];
  int h = blockIdx.x, t = threadIdx.x;
  if (t < 128) {
    float M = -INFINITY;
    for (int s = 0; s < NSPLIT; ++s) M = fmaxf(M, pml[(h * NSPLIT + s) * 2]);
    float L = 0.f, a = 0.f;
    for (int s = 0; s < NSPLIT; ++s) { float wt = __expf(pml[(h * NSPLIT + s) * 2] - M); L += wt * pml[(h * NSPLIT + s) * 2 + 1]; a += wt * po[((size_t)h * NSPLIT + s) * 128 + t]; }
    o[t] = rbf(a / L);
  }
  __syncthreads();
  float a0 = 0.f, a1 = 0.f;
  for (int k = 0; k < 128; ++k) {
    unsigned w2 = *(const unsigned*)(wo + (size_t)(h * 128 + k) * 512 + 2 * t);
    a0 += o[k] * __builtin_bit_cast(float, w2 << 16);
    a1 += o[k] * __builtin_bit_cast(float, w2 & 0xffff0000u);
  }
  part[h * 512 + 2 * t] = a0; part[h * 512 + 2 * t + 1] = a1;
}

__global__ void __launch_bounds__(256) k_glu1(const float* xs, const float* part, float* xs_out, const unsigned short* wg, const unsigned short* wu, float* hbuf) {
  __shared__ float x1[512]; __shared__ float xn[512]; __shared__ float red[4]; __shared__ float pg[8][32], pu[8][32];
  int t = threadIdx.x;
  for (int c = t; c < 512; c += 256) { float a = 0.f; for (int h = 0; h < 32; ++h) a += part[h * 512 + c]; x1[c] = rbf(xs[c] + rbf(a)); if (blockIdx.x == 0) xs_out[c] = x1[c]; }
  __syncthreads();
  ln512(x1, xn, red);
  int c = blockIdx.x * 32 + (t & 31), g = t >> 5;
  float ag = 0.f, au = 0.f;
  for (int k = g * 64; k < g * 64 + 64; ++k) { ag += xn[k] * bf2f(wg[(size_t)k * F + c]); au += xn[k] * bf2f(wu[(size_t)k * F + c]); }
  pg[g][t & 31] = ag; pu[g][t & 31] = au;
  __syncthreads();
  if (t < 32) {
    float gg = 0.f, uu = 0.f;
    for (int i = 0; i < 8; ++i) { gg += pg[i][t]; uu += pu[i][t]; }
    gg = rbf(gg); uu = rbf(uu);
    float sg = rbf(1.f / (1.f + __expf(-gg)));
    hbuf[c] = rbf(sg * uu);
  }
}
__global__ void __launch_bounds__(256) k_glu2(const float* xs, const float* hbuf, const unsigned short* wd, float* xs_out, unsigned short* out) {
  __shared__ float p[8][32];
  int t = threadIdx.x, c = blockIdx.x * 32 + (t & 31), g = t >> 5;
  float a = 0.f;
  for (int k = g * 172; k < g * 172 + 172; ++k) a += hbuf[k] * bf2f(wd[(size_t)k * 512 + c]);
  p[g][t & 31] = a;
  __syncthreads();
  if (t < 32) { float s = 0.f; for (int i = 0; i < 8; ++i) s += p[i][t]; float r = rbf(xs[c] + rbf(s)); xs_out[c] = r; if (out) out[c] = __builtin_bit_cast(unsigned short, (__bf16)r); }
}

extern "C" void kernel_launch(void* const* d_in, const int* in_sizes, int n_in, void* d_out, int out_size, void* d_ws, size_t ws_size, hipStream_t stream) {
  const unsigned short* x = (const unsigned short*)d_in[0];
  const unsigned short* K = (const unsigned short*)d_in[1];
  const unsigned short* V = (const unsigned short*)d_in[2];
  const unsigned short* wq = (const unsigned short*)d_in[3];
  const unsigned short* wo = (const unsigned short*)d_in[4];
  const unsigned short* wg = (const unsigned short*)d_in[6];
  const unsigned short* wu = (const unsigned short*)d_in[7];
  const unsigned short* wd = (const unsigned short*)d_in[8];
  char* ws = (char*)d_ws;
  float* xa = (float*)(ws);
  float* xb = (float*)(ws + 4096);
  unsigned short* q = (unsigned short*)(ws + 8192);
  float* pml = (float*)(ws + 32768);
  float* po = (float*)(ws + 65536);
  float* part = (float*)(ws + 1048576);
  float* hbuf = (float*)(ws + 2097152);
  k_init<<<1, 512, 0, stream>>>(x, xa);
  for (int it = 0; it < 2; ++it) {
    k_qproj<<<64, 256, 0, stream>>>(xa, wq, q);
    k_attn<<<dim3(NSPLIT, H), 256, 0, stream>>>(q, K, V, pml, po);
    k_wo<<<32, 256, 0, stream>>>(pml, po, wo, part);
    k_glu1<<<43, 256, 0, stream>>>(xa, part, xb, wg, wu, hbuf);
    k_glu2<<<16, 256, 0, stream>>>(xb, hbuf, wd, xa, it == 1 ? (unsigned short*)d_out : nullptr);
  }
}
```

```cpp
#define NT1 1
#define NT2 1
#define REV2 0
#include <hip/hip_runtime.h>
#include <hip/hip_bf16.h>
#include <stdint.h>

typedef unsigned u32x4 __attribute__((ext_vector_type(4)));
typedef __bf16 bf16x2 __attribute__((ext_vector_type(2)));
constexpr int D = 512, H = 32, SK = 16384, DH = 128, F = 1376;
constexpr int NSPLIT = 16;
constexpr int KPS = SK / NSPLIT;

__device__ __forceinline__ float bf2f(unsigned short b) { return __builtin_bit_cast(float, (unsigned)b << 16); }
__device__ __forceinline__ float rbf(float x) { return x; }
__device__ __forceinline__ float dot2(unsigned k, unsigned q, float acc) {
  return __builtin_amdgcn_fdot2_f32_bf16(__builtin_bit_cast(bf16x2, k), __builtin_bit_cast(bf16x2, q), acc, false);
}
__device__ __forceinline__ float wave_sum(float v) {
  for (int o = 32; o > 0; o >>= 1) v += __shfl_xor(v, o);
  return v;
}
__device__ void ln512(const float* x, float* xn, float* red) {
  int t = threadIdx.x;
  float a = x[t], b = x[t + 256];
  float s = wave_sum(a + b);
  if ((t & 63) == 0) red[t >> 6] = s;
  __syncthreads();
  float m = (red[0] + red[1] + red[2] + red[3]) * (1.f / 512.f);
  __syncthreads();
  float da = a - m, db = b - m;
  float v = wave_sum(da * da + db * db);
  if ((t & 63) == 0) red[t >> 6] = v;
  __syncthreads();
  float var = (red[0] + red[1] + red[2] + red[3]) * (1.f / 512.f);
  float mb = rbf(m), vb = rbf(var);
  float den = rbf(sqrtf(rbf(vb + 1e-5f)));
  xn[t] = rbf(rbf(a - mb) / den);
  xn[t + 256] = rbf(rbf(b - mb) / den);
  __syncthreads();
}

__global__ void k_init(const unsigned short* x, float* xs) { int t = threadIdx.x; xs[t] = bf2f(x[t]); }

__global__ void __launch_bounds__(256) k_qproj(const float* xs, const unsigned short* wq, unsigned short* q) {
  __shared__ float xn[512]; __shared__ float red[4]; __shared__ float part[4][64];
  ln512(xs, xn, red);
  int t = threadIdx.x, c = blockIdx.x * 64 + (t & 63), g = t >> 6;
  float acc = 0.f;
  for (int k = g * 128; k < g * 128 + 128; ++k) acc += xn[k] * bf2f(wq[(size_t)k * 4096 + c]);
  part[g][t & 63] = acc;
  __syncthreads();
  if (t < 64) { float s = part[0][t] + part[1][t] + part[2][t] + part[3][t]; q[c] = __builtin_bit_cast(unsigned short, (__bf16)s); }
}

template <int NT, int REV> __global__ void __launch_bounds__(256) k_attn(const unsigned short* q, const unsigned short* K, const unsigned short* V, float* pml, float* po) {
  __shared__ float mrg[4][132];
  int s = blockIdx.x, h = blockIdx.y, t = threadIdx.x, lane = t & 63, w = t >> 6, r = lane >> 4, c = lane & 15;
  u32x4 qv = *(const u32x4*)(q + h * 128 + 8 * c);
  const unsigned short* kb = K + ((size_t)h * SK + (size_t)s * KPS) * DH;
  const unsigned short* vb = V + ((size_t)h * SK + (size_t)s * KPS) * DH;
  float m = -INFINITY, l = 0.f, acc[8];
  for (int i = 0; i < 8; ++i) acc[i] = 0.f;
  const float SCALE = 0.088388347648318440f;
  for (int k0 = w * 16; k0 < KPS; k0 += 64) {
    u32x4 kk[4], vv[4];
#pragma unroll
    for (int i = 0; i < 4; ++i) {
      const int kq = REV ? (KPS - 64 - (k0 - w * 16) + w * 16) : k0;
      const u32x4* kp = (const u32x4*)(kb + (size_t)(kq + 4 * i + r) * DH + 8 * c);
      const u32x4* vp = (const u32x4*)(vb + (size_t)(kq + 4 * i + r) * DH + 8 * c);
      kk[i] = NT ? __builtin_nontemporal_load(kp) : *kp;
      vv[i] = NT ? __builtin_nontemporal_load(vp) : *vp;
    }
    float sc[4];
#pragma unroll
    for (int i = 0; i < 4; ++i) {
      float d = 0.f;
#pragma unroll
      for (int j = 0; j < 4; ++j) d = dot2(kk[i][j], qv[j], d);
      d += __shfl_xor(d, 1); d += __shfl_xor(d, 2); d += __shfl_xor(d, 4); d += __shfl_xor(d, 8);
      sc[i] = rbf(rbf(d) * rbf(SCALE));
    }
    float pm = fmaxf(fmaxf(sc[0], sc[1]), fmaxf(sc[2], sc[3]));
    float mn = fmaxf(m, pm);
    float al = __expf(m - mn);
    l *= al;
#pragma unroll
    for (int j = 0; j < 8; ++j) acc[j] *= al;
#pragma unroll
    for (int i = 0; i < 4; ++i) {
      float p = __expf(sc[i] - mn);
      l += p;
#pragma unroll
      for (int j = 0; j < 4; ++j) {
        acc[2 * j] += p * __builtin_bit_cast(float, vv[i][j] << 16);
        acc[2 * j + 1] += p * __builtin_bit_cast(float, vv[i][j] & 0xffff0000u);
      }
    }
    m = mn;
  }
  float M = fmaxf(m, __shfl_xor(m, 16)); M = fmaxf(M, __shfl_xor(M, 32));
  float f = __expf(m - M);
  l *= f; l += __shfl_xor(l, 16); l += __shfl_xor(l, 32);
  for (int j = 0; j < 8; ++j) { float a = acc[j] * f; a += __shfl_xor(a, 16); a += __shfl_xor(a, 32); acc[j] = a; }
  if (r == 0) { for (int j = 0; j < 8; ++j) mrg[w][4 + 8 * c + j] = acc[j]; if (c == 0) { mrg[w][0] = M; mrg[w][1] = l; } }
  __syncthreads();
  if (t < 128) {
    float MM = fmaxf(fmaxf(mrg[0][0], mrg[1][0]), fmaxf(mrg[2][0], mrg[3][0]));
    float L = 0.f, o = 0.f;
    for (int ww = 0; ww < 4; ++ww) { float wt = __expf(mrg[ww][0] - MM); L += wt * mrg[ww][1]; o += wt * mrg[ww][4 + t]; }
    po[((size_t)h * NSPLIT + s) * 128 + t] = o;
    if (t == 0) { pml[(h * NSPLIT + s) * 2] = MM; pml[(h * NSPLIT + s) * 2 + 1] = L; }
  }
}

__global__ void __launch_bounds__(256) k_wo(const float* pml, const float* po, const unsigned short* wo, float* part) {
  __shared__ float o[128];
  int h = blockIdx.x, t = threadIdx.x;
  if (t < 128) {
    float M = -INFINITY;
    for (int s = 0; s < NSPLIT; ++s) M = fmaxf(M, pml[(h * NSPLIT + s) * 2]);
    float L = 0.f, a = 0.f;
    for (int s = 0; s < NSPLIT; ++s) { float wt = __expf(pml[(h * NSPLIT + s) * 2] - M); L += wt * pml[(h * NSPLIT + s) * 2 + 1]; a += wt * po[((size_t)h * NSPLIT + s) * 128 + t]; }
    o[t] = rbf(a / L);
  }
  __syncthreads();
  float a0 = 0.f, a1 = 0.f;
  for (int k = 0; k < 128; ++k) {
    unsigned w2 = *(const unsigned*)(wo + (size_t)(h * 128 + k) * 512 + 2 * t);
    a0 += o[k] * __builtin_bit_cast(float, w2 << 16);
    a1 += o[k] * __builtin_bit_cast(float, w2 & 0xffff0000u);
  }
  part[h * 512 + 2 * t] = a0; part[h * 512 + 2 * t + 1] = a1;
}

__global__ void __launch_bounds__(256) k_glu1(const float* xs, const float* part, float* xs_out, const unsigned short* wg, const unsigned short* wu, float* hbuf) {
  __shared__ float x1[512]; __shared__ float xn[512]; __shared__ float red[4]; __shared__ float pg[8][32], pu[8][32];
  int t = threadIdx.x;
  for (int c = t; c < 512; c += 256) { float a = 0.f; for (int h = 0; h < 32; ++h) a += part[h * 512 + c]; x1[c] = rbf(xs[c] + rbf(a)); if (blockIdx.x == 0) xs_out[c] = x1[c]; }
  __syncthreads();
  ln512(x1, xn, red);
  int c = blockIdx.x * 32 + (t & 31), g = t >> 5;
  float ag = 0.f, au = 0.f;
  for (int k = g * 64; k < g * 64 + 64; ++k) { ag += xn[k] * bf2f(wg[(size_t)k * F + c]); au += xn[k] * bf2f(wu[(size_t)k * F + c]); }
  pg[g][t & 31] = ag; pu[g][t & 31] = au;
  __syncthreads();
  if (t < 32) {
    float gg = 0.f, uu = 0.f;
    for (int i = 0; i < 8; ++i) { gg += pg[i][t]; uu += pu[i][t]; }
    gg = rbf(gg); uu = rbf(uu);
    float sg = rbf(1.f / (1.f + __expf(-gg)));
    hbuf[c] = rbf(sg * uu);
  }
}
__global__ void __launch_bounds__(256) k_glu2(const float* xs, const float* hbuf, const unsigned short* wd, float* xs_out, unsigned short* out) {
  __shared__ float p[8][32];
  int t = threadIdx.x, c = blockIdx.x * 32 + (t & 31), g = t >> 5;
  float a = 0.f;
  for (int k = g * 172; k < g * 172 + 172; ++k) a += hbuf[k] * bf2f(wd[(size_t)k * 512 + c]);
  p[g][t & 31] = a;
  __syncthreads();
  if (t < 32) { float s = 0.f; for (int i = 0; i < 8; ++i) s += p[i][t]; float r = rbf(xs[c] + rbf(s)); xs_out[c] = r; if (out) out[c] = __builtin_bit_cast(unsigned short, (__bf16)r); }
}

extern "C" void kernel_launch(void* const* d_in, const int* in_sizes, int n_in, void* d_out, int out_size, void* d_ws, size_t ws_size, hipStream_t stream) {
  const unsigned short* x = (const unsigned short*)d_in[0];
  const unsigned short* K = (const unsigned short*)d_in[1];
  const unsigned short* V = (const unsigned short*)d_in[2];
  const unsigned short* wq = (const unsigned short*)d_in[3];
  const unsigned short* wo = (const unsigned short*)d_in[4];
  const unsigned short* wg = (const unsigned short*)d_in[6];
  const unsigned short* wu = (const unsigned short*)d_in[7];
  const unsigned short* wd = (const unsigned short*)d_in[8];
  char* ws = (char*)d_ws;
  float* xa = (float*)(ws);
  float* xb = (float*)(ws + 4096);
  unsigned short* q = (unsigned short*)(ws + 8192);
  float* pml = (float*)(ws + 32768);
  float* po = (float*)(ws + 65536);
  float* part = (float*)(ws + 1048576);
  float* hbuf = (float*)(ws + 2097152);
  k_init<<<1, 512, 0, stream>>>(x, xa);
  for (int it = 0; it < 2; ++it) {
    k_qproj<<<64, 256, 0, stream>>>(xa, wq, q);
    if (it == 0) k_attn<NT1, 0><<<dim3(NSPLIT, H), 256, 0, stream>>>(q, K, V, pml, po);
    else k_attn<NT2, REV2><<<dim3(NSPLIT, H), 256, 0, stream>>>(q, K, V, pml, po);
    k_wo<<<32, 256, 0, stream>>>(pml, po, wo, part);
    k_glu1<<<43, 256, 0, stream>>>(xa, part, xb, wg, wu, hbuf);
    k_glu2<<<16, 256, 0, stream>>>(xb, hbuf, wd, xa, it == 1 ? (unsigned short*)d_out : nullptr);
  }
}
```

```cpp
#define NT1 1
#define NT2 1
#define REV2 0
#include <hip/hip_runtime.h>
#include <hip/hip_bf16.h>
#include <stdint.h>

typedef unsigned u32x4 __attribute__((ext_vector_type(4)));
typedef __bf16 bf16x2 __attribute__((ext_vector_type(2)));
constexpr int D = 512, H = 32, SK = 16384, DH = 128, F = 1376;
constexpr int NSPLIT = 16;
constexpr int KPS = SK / NSPLIT;

__device__ __forceinline__ float bf2f(unsigned short b) { return __builtin_bit_cast(float, (unsigned)b << 16); }
__device__ __forceinline__ float rbf(float x) { return bf2f(__builtin_bit_cast(unsigned short, (__bf16)x)); }
__device__ __forceinline__ float dot2(unsigned k, unsigned q, float acc) {
  return __builtin_amdgcn_fdot2_f32_bf16(__builtin_bit_cast(bf16x2, k), __builtin_bit_cast(bf16x2, q), acc, false);
}
__device__ __forceinline__ float wave_sum(float v) {
  for (int o = 32; o > 0; o >>= 1) v += __shfl_xor(v, o);
  return v;
}
__device__ void ln512(const float* x, float* xn, float* red) {
  int t = threadIdx.x;
  float a = x[t], b = x[t + 256];
  float s = wave_sum(a + b);
  if ((t & 63) == 0) red[t >> 6] = s;
  __syncthreads();
  float m = (red[0] + red[1] + red[2] + red[3]) * (1.f / 512.f);
  __syncthreads();
  float da = a - m, db = b - m;
  float v = wave_sum(da * da + db * db);
  if ((t & 63) == 0) red[t >> 6] = v;
  __syncthreads();
  float var = (red[0] + red[1] + red[2] + red[3]) * (1.f / 512.f);
  float mb = rbf(m), vb = rbf(var);
  float den = rbf(sqrtf(rbf(vb + 1e-5f)));
  xn[t] = rbf(rbf(a - mb) / den);
  xn[t + 256] = rbf(rbf(b - mb) / den);
  __syncthreads();
}

__global__ void k_init(const unsigned short* x, float* xs) { int t = threadIdx.x; xs[t] = bf2f(x[t]); }

__global__ void __launch_bounds__(256) k_qproj(const float* xs, const unsigned short* wq, unsigned short* q) {
  __shared__ float xn[512]; __shared__ float red[4]; __shared__ float part[4][64];
  ln512(xs, xn, red);
  int t = threadIdx.x, c = blockIdx.x * 64 + (t & 63), g = t >> 6;
  float acc = 0.f;
  for (int k = g * 128; k < g * 128 + 128; ++k) acc += xn[k] * bf2f(wq[(size_t)k * 4096 + c]);
  part[g][t & 63] = acc;
  __syncthreads();
  if (t < 64) { float s = part[0][t] + part[1][t] + part[2][t] + part[3][t]; q[c] = __builtin_bit_cast(unsigned short, (__bf16)s); }
}

template <int NT, int REV> __global__ void __launch_bounds__(256) k_attn(const unsigned short* q, const unsigned short* K, const unsigned short* V, float* pml, float* po) {
  __shared__ float mrg[4][132];
  int s = blockIdx.x, h = blockIdx.y, t = threadIdx.x, lane = t & 63, w = t >> 6, r = lane >> 4, c = lane & 15;
  u32x4 qv = *(const u32x4*)(q + h * 128 + 8 * c);
  const unsigned short* kb = K + ((size_t)h * SK + (size_t)s * KPS) * DH;
  const unsigned short* vb = V + ((size_t)h * SK + (size_t)s * KPS) * DH;
  float m = -INFINITY, l = 0.f, acc[8];
  for (int i = 0; i < 8; ++i) acc[i] = 0.f;
  const float SCALE = 0.088388347648318440f;
  for (int k0 = w * 16; k0 < KPS; k0 += 64) {
    u32x4 kk[4], vv[4];
#pragma unroll
    for (int i = 0; i < 4; ++i) {
      const int kq = REV ? (KPS - 64 - (k0 - w * 16) + w * 16) : k0;
      const u32x4* kp = (const u32x4*)(kb + (size_t)(kq + 4 * i + r) * DH + 8 * c);
      const u32x4* vp = (const u32x4*)(vb + (size_t)(kq + 4 * i + r) * DH + 8 * c);
      kk[i] = NT ? __builtin_nontemporal_load(kp) : *kp;
      vv[i] = NT ? __builtin_nontemporal_load(vp) : *vp;
    }
    float sc[4];
#pragma unroll
    for (int i = 0; i < 4; ++i) {
      float d = 0.f;
#pragma unroll
      for (int j = 0; j < 4; ++j) d = dot2(kk[i][j], qv[j], d);
      d += __shfl_xor(d, 1); d += __shfl_xor(d, 2); d += __shfl_xor(d, 4); d += __shfl_xor(d, 8);
      sc[i] = rbf(rbf(d) * rbf(SCALE));
    }
    float pm = fmaxf(fmaxf(sc[0], sc[1]), fmaxf(sc[2], sc[3]));
    float mn = fmaxf(m, pm);
    float al = __expf(m - mn);
    l *= al;
#pragma unroll
    for (int j = 0; j < 8; ++j) acc[j] *= al;
#pragma unroll
    for (int i = 0; i < 4; ++i) {
      float p = __expf(sc[i] - mn);
      l += p;
#pragma unroll
      for (int j = 0; j < 4; ++j) {
        acc[2 * j] += p * __builtin_bit_cast(float, vv[i][j] << 16);
        acc[2 * j + 1] += p * __builtin_bit_cast(float, vv[i][j] & 0xffff0000u);
      }
    }
    m = mn;
  }
  float M = fmaxf(m, __shfl_xor(m, 16)); M = fmaxf(M, __shfl_xor(M, 32));
  float f = __expf(m - M);
  l *= f; l += __shfl_xor(l, 16); l += __shfl_xor(l, 32);
  for (int j = 0; j < 8; ++j) { float a = acc[j] * f; a += __shfl_xor(a, 16); a += __shfl_xor(a, 32); acc[j] = a; }
  if (r == 0) { for (int j = 0; j < 8; ++j) mrg[w][4 + 8 * c + j] = acc[j]; if (c == 0) { mrg[w][0] = M; mrg[w][1] = l; } }
  __syncthreads();
  if (t < 128) {
    float MM = fmaxf(fmaxf(mrg[0][0], mrg[1][0]), fmaxf(mrg[2][0], mrg[3][0]));
    float L = 0.f, o = 0.f;
    for (int ww = 0; ww < 4; ++ww) { float wt = __expf(mrg[ww][0] - MM); L += wt * mrg[ww][1]; o += wt * mrg[ww][4 + t]; }
    po[((size_t)h * NSPLIT + s) * 128 + t] = o;
    if (t == 0) { pml[(h * NSPLIT + s) * 2] = MM; pml[(h * NSPLIT + s) * 2 + 1] = L; }
  }
}

__global__ void __launch_bounds__(256) k_wo(const float* pml, const float* po, const unsigned short* wo, float* part) {
  __shared__ float o[128];
  int h = blockIdx.x, t = threadIdx.x;
  if (t < 128) {
    float M = -INFINITY;
    for (int s = 0; s < NSPLIT; ++s) M = fmaxf(M, pml[(h * NSPLIT + s) * 2]);
    float L = 0.f, a = 0.f;
    for (int s = 0; s < NSPLIT; ++s) { float wt = __expf(pml[(h * NSPLIT + s) * 2] - M); L += wt * pml[(h * NSPLIT + s) * 2 + 1]; a += wt * po[((size_t)h * NSPLIT + s) * 128 + t]; }
    o[t] = rbf(a / L);
  }
  __syncthreads();
  float a0 = 0.f, a1 = 0.f;
  for (int k = 0; k < 128; ++k) {
    unsigned w2 = *(const unsigned*)(wo + (size_t)(h * 128 + k) * 512 + 2 * t);
    a0 += o[k] * __builtin_bit_cast(float, w2 << 16);
    a1 += o[k] * __builtin_bit_cast(float, w2 & 0xffff0000u);
  }
  part[h * 512 + 2 * t] = a0; part[h * 512 + 2 * t + 1] = a1;
}

__global__ void __launch_bounds__(256) k_glu1(const float* xs, const float* part, float* xs_out, const unsigned short* wg, const unsigned short* wu, float* hbuf) {
  __shared__ float x1[512]; __shared__ float xn[512]; __shared__ float red[4]; __shared__ float pg[8][32], pu[8][32];
  int t = threadIdx.x;
  for (int c = t; c < 512; c += 256) { float a = 0.f; for (int h = 0; h < 32; ++h) a += part[h * 512 + c]; x1[c] = rbf(xs[c] + rbf(a)); if (blockIdx.x == 0) xs_out[c] = x1[c]; }
  __syncthreads();
  ln512(x1, xn, red);
  int c = blockIdx.x * 32 + (t & 31), g = t >> 5;
  float ag = 0.f, au = 0.f;
  for (int k = g * 64; k < g * 64 + 64; ++k) { ag += xn[k] * bf2f(wg[(size_t)k * F + c]); au += xn[k] * bf2f(wu[(size_t)k * F + c]); }
  pg[g][t & 31] = ag; pu[g][t & 31] = au;
  __syncthreads();
  if (t < 32) {
    float gg = 0.f, uu = 0.f;
    for (int i = 0; i < 8; ++i) { gg += pg[i][t]; uu += pu[i][t]; }
    gg = rbf(gg); uu = rbf(uu);
    float sg = rbf(1.f / (1.f + __expf(-gg)));
    hbuf[c] = rbf(sg * uu);
  }
}
__global__ void __launch_bounds__(256) k_glu2(const float* xs, const float* hbuf, const unsigned short* wd, float* xs_out, unsigned short* out) {
  __shared__ float p[8][32];
  int t = threadIdx.x, c = blockIdx.x * 32 + (t & 31), g = t >> 5;
  float a = 0.f;
  for (int k = g * 172; k < g * 172 + 172; ++k) a += hbuf[k] * bf2f(wd[(size_t)k * 512 + c]);
  p[g][t & 31] = a;
  __syncthreads();
  if (t < 32) { float s = 0.f; for (int i = 0; i < 8; ++i) s += p[i][t]; float r = rbf(xs[c] + rbf(s)); xs_out[c] = r; if (out) out[c] = __builtin_bit_cast(unsigned short, (__bf16)r); }
}


template <int W, int DEPTH, int AUX>
__global__ void __launch_bounds__(W * 64, 1) k_probe(const char* bufK, const char* bufV, int nk, int nv, unsigned* sink) {
  int lane = threadIdx.x & 63, w = __builtin_amdgcn_readfirstlane(threadIdx.x >> 6);
  int wg = blockIdx.x;
  u32x4 acc = {0, 0, 0, 0};
  for (int which = 0; which < 2; ++which) {
    const char* base = (which ? bufV : bufK) + (size_t)wg * (1 << 19);
    int n1 = which ? nv : nk;
    if (n1 == 0) continue;
    __amdgpu_buffer_rsrc_t rs = __builtin_amdgcn_make_buffer_rsrc((void*)base, 0, 1 << 19, 0x00020000);
    unsigned voff = lane * 16 + w * 1024;
    u32x4 r[DEPTH];
    int n = n1 / W;
    unsigned so = 0;
#pragma unroll
    for (int i = 0; i < DEPTH; ++i) { r[i] = __builtin_amdgcn_raw_buffer_load_b128(rs, voff, so, AUX); so += W * 1024; }
    for (int j = DEPTH; j < n; j += DEPTH) {
#pragma unroll
      for (int i = 0; i < DEPTH; ++i) { acc ^= r[i]; r[i] = __builtin_amdgcn_raw_buffer_load_b128(rs, voff, so, AUX); so += W * 1024; }
    }
#pragma unroll
    for (int i = 0; i < DEPTH; ++i) acc ^= r[i];
  }
  if (acc[0] == 0x12345678 && acc[1] == 0x9abcdef0) sink[0] = acc[2];
}

extern "C" void kernel_launch(void* const* d_in, const int* in_sizes, int n_in, void* d_out, int out_size, void* d_ws, size_t ws_size, hipStream_t stream) {
  const unsigned short* x = (const unsigned short*)d_in[0];
  const unsigned short* K = (const unsigned short*)d_in[1];
  const unsigned short* V = (const unsigned short*)d_in[2];
  const unsigned short* wq = (const unsigned short*)d_in[3];
  const unsigned short* wo = (const unsigned short*)d_in[4];
  const unsigned short* wg = (const unsigned short*)d_in[6];
  const unsigned short* wu = (const unsigned short*)d_in[7];
  const unsigned short* wd = (const unsigned short*)d_in[8];
  char* ws = (char*)d_ws;
  float* xa = (float*)(ws);
  float* xb = (float*)(ws + 4096);
  unsigned short* q = (unsigned short*)(ws + 8192);
  float* pml = (float*)(ws + 32768);
  float* po = (float*)(ws + 65536);
  float* part = (float*)(ws + 1048576);
  float* hbuf = (float*)(ws + 2097152);
  k_probe<4,16,2><<<256,256,0,stream>>>((const char*)K,(const char*)V,512,0,(unsigned*)(ws+(8<<20)));
  k_init<<<1, 512, 0, stream>>>(x, xa);
  for (int it = 0; it < 2; ++it) {
    k_qproj<<<64, 256, 0, stream>>>(xa, wq, q);
    if (it == 0) k_attn<NT1, 0><<<dim3(NSPLIT, H), 256, 0, stream>>>(q, K, V, pml, po);
    else k_attn<NT2, REV2><<<dim3(NSPLIT, H), 256, 0, stream>>>(q, K, V, pml, po);
    k_wo<<<32, 256, 0, stream>>>(pml, po, wo, part);
    k_glu1<<<43, 256, 0, stream>>>(xa, part, xb, wg, wu, hbuf);
    k_glu2<<<16, 256, 0, stream>>>(xb, hbuf, wd, xa, it == 1 ? (unsigned short*)d_out : nullptr);
  }
}
```

```cpp
#include <hip/hip_runtime.h>
#include <stdint.h>

__global__ void __launch_bounds__(256, 1) fused_block_kernel(const void* x, const void* K, const void* V, const void* wq, const void* wo,
                                                             const void* wg, const void* wu, const void* wd, void* out, void* ws) {
  __shared__ __attribute__((aligned(16))) char lds[163840];
  const int wid = __builtin_amdgcn_readfirstlane(threadIdx.x >> 6);
  const unsigned ldsaddr = (unsigned)(uintptr_t)(__attribute__((address_space(3))) char*)lds;
  asm volatile(
"s_load_dwordx16 s[8:23], %0, 0\n"
"s_load_dwordx4 s[24:27], %0, 64\n"
"s_lshr_b32 s48, %1, 6\n"
"s_lshl_b32 s48, s48, 3\n"
"s_and_b32 s49, %1, 7\n"
"s_add_u32 s48, s48, s49\n"
"s_bfe_u32 s49, %1, 0x30003\n"
"s_mov_b32 s50, %2\n"
"v_mbcnt_lo_u32_b32 v0, -1, 0\n"
"v_mbcnt_hi_u32_b32 v0, -1, v0\n"
"v_lshlrev_b32 v2, 4, v0\n"
"LSTART:\n"
"s_getpc_b64 s[98:99]\n"
"s_mov_b32 s40, s98\n"
"s_and_b32 s41, s99, 0xffff\n"
"s_mov_b32 s42, LEND-LSTART\n"
"s_mov_b32 s43, 0x20000\n"
"s_lshr_b32 s56, %1, 3\n"
"s_lshl_b32 s56, s56, 2\n"
"s_add_u32 s56, s56, s50\n"
"s_lshl_b32 s56, s56, 10\n"
"buffer_load_dwordx4 a[252:255], v2, s[40:43], s56 offen\n"
"s_mov_b32 s54, 0x3fffff\n"
"s_mov_b32 s60, 0x11111111\n"
"s_mov_b32 s61, 0x11111111\n"
"s_mov_b32 s62, 0x22222222\n"
"s_mov_b32 s63, 0x22222222\n"
"s_mov_b32 s64, 0x44444444\n"
"s_mov_b32 s65, 0x44444444\n"
"s_mov_b32 s66, 0x88888888\n"
"s_mov_b32 s67, 0x88888888\n"
"s_mov_b32 s68, 0x05040100\n"
"s_mov_b32 s69, 0x07060302\n"
"s_waitcnt lgkmcnt(0)\n"
"s_memrealtime s[96:97]\n"
"s_and_b32 s99, s48, 1\n"
"s_lshl_b32 s99, s99, 13\n"
"s_lshl_b32 s56, s48, 22\n"
"s_add_u32 s28, s10, s56\n"
"s_addc_u32 s29, s11, 0\n"
"s_and_b32 s29, s29, 0xffff\n"
"s_mov_b32 s30, 0x400000\n"
"s_mov_b32 s31, 0x20000\n"
"s_add_u32 s32, s12, s56\n"
"s_addc_u32 s33, s13, 0\n"
"s_and_b32 s33, s33, 0xffff\n"
"s_mov_b32 s34, 0x400000\n"
"s_mov_b32 s35, 0x20000\n"
"s_mov_b32 s36, s26\n"
"s_and_b32 s37, s27, 0xffff\n"
"s_mov_b32 s38, 0x200000\n"
"s_mov_b32 s39, 0x20000\n"
"s_mul_i32 s51, s48, 2\n"
"s_add_u32 s51, s51, 7\n"
"s_and_b32 s51, s51, 7\n"
"s_waitcnt lgkmcnt(0)\n"
"s_mov_b32 s98, s96\n"
"s_cmp_eq_u32 s49, s51\n"
"s_cbranch_scc1 LCHAIN\n"
"v_bfe_u32 v56, v0, 2, 2\n"
"v_lshlrev_b32 v1, 8, v56\n"
"v_lshrrev_b32 v56, 4, v0\n"
"v_lshl_or_b32 v1, v56, 6, v1\n"
"v_and_b32 v56, 3, v0\n"
"v_lshl_or_b32 v1, v56, 4, v1\n"
"s_mul_i32 s56, s50, 39936\n"
"s_mov_b32 s57, s56\n"
"v_add_u32 v3, s56, v2\n"
"s_sub_u32 s56, s49, s51\n"
"s_sub_u32 s56, s56, 1\n"
"s_and_b32 s56, s56, 7\n"
"s_lshl_b32 s56, s56, 2\n"
"s_add_u32 s56, s56, s50\n"
"s_mul_i32 s53, s56, 1024\n"
"s_mul_i32 s56, s48, 65536\n"
"s_add_u32 s53, s53, s56\n"
"buffer_load_dword v38, off, s[36:39], 0 sc1\n"
"s_add_u32 s52, s53, 0x0\n"
"s_and_b32 s52, s52, s54\n"
"buffer_load_dwordx4 v[64:67], v1, s[28:31], s52 offen offset:0 nt\n"
"s_add_u32 s52, s53, 0x7000\n"
"s_and_b32 s52, s52, s54\n"
"buffer_load_dwordx4 v[68:71], v1, s[28:31], s52 offen offset:0 nt\n"
"s_add_u32 s52, s53, 0xe000\n"
"s_and_b32 s52, s52, s54\n"
"buffer_load_dwordx4 v[72:75], v1, s[28:31], s52 offen offset:0 nt\n"
"s_add_u32 s52, s53, 0x15000\n"
"s_and_b32 s52, s52, s54\n"
"buffer_load_dwordx4 v[76:79], v1, s[28:31], s52 offen offset:0 nt\n"
"v_mov_b32 v4, 0\n"
"v_mov_b32 v5, 0\n"
"v_mov_b32 v6, 0\n"
"v_mov_b32 v7, 0\n"
"v_mov_b32 v8, 0\n"
"v_mov_b32 v9, 0\n"
"v_mov_b32 v10, 0\n"
"v_mov_b32 v11, 0\n"
"v_mov_b32 v12, 0\n"
"v_mov_b32 v13, 0\n"
"v_mov_b32 v14, 0\n"
"v_mov_b32 v15, 0\n"
"v_mov_b32 v16, 0\n"
"v_mov_b32 v17, 0\n"
"v_mov_b32 v18, 0\n"
"v_mov_b32 v19, 0\n"
"v_mov_b32 v44, 0xff800000\n"
"v_mov_b32 v45, 0xff800000\n"
"v_mov_b32 v46, 0xff800000\n"
"v_mov_b32 v47, 0xff800000\n"
"v_mov_b32 v48, 0xff800000\n"
"v_mov_b32 v49, 0xff800000\n"
"v_mov_b32 v50, 0xff800000\n"
"v_mov_b32 v51, 0xff800000\n"
"v_mov_b32 v52, 0xff800000\n"
"v_lshrrev_b32 v32, 4, v0\n"
"v_lshlrev_b32 v32, 7, v32\n"
"v_and_b32 v58, 3, v0\n"
"v_lshl_or_b32 v32, v58, 5, v32\n"
"s_waitcnt vmcnt(4)\n"
"v_readfirstlane_b32 s59, v38\n"
"s_mul_i32 s59, s59, 1664525\n"
"s_add_u32 s59, s59, 1013904223\n"
"s_or_b32 s56, %1, s50\n"
"v_add_u32 v33, s56, v0\n"
"v_cmp_eq_u32 vcc, 0, v33\n"
"v_mov_b32 v33, 0x7ff00000\n"
"s_nop 0\n"
"v_cndmask_b32 v33, v33, v0, vcc\n"
"v_mov_b32 v34, s98\n"
"v_mov_b32 v35, s59\n"
"buffer_store_dwordx2 v[34:35], v33, s[36:39], 0 offen offset:512 sc1\n"
"s_add_u32 s52, s53, 0x1c000\n"
"s_and_b32 s52, s52, s54\n"
"buffer_load_dwordx4 v[80:83], v1, s[28:31], s52 offen offset:0 nt\n"
"s_add_u32 s52, s53, 0x23000\n"
"s_and_b32 s52, s52, s54\n"
"buffer_load_dwordx4 v[84:87], v1, s[28:31], s52 offen offset:0 nt\n"
"s_add_u32 s52, s53, 0x2a000\n"
"s_and_b32 s52, s52, s54\n"
"buffer_load_dwordx4 v[88:91], v1, s[28:31], s52 offen offset:0 nt\n"
"Lpace_1:\n"
"s_memrealtime s[96:97]\n"
"s_waitcnt lgkmcnt(0)\n"
"s_sub_u32 s96, s96, s98\n"
"s_cmp_ge_u32 s96, 38\n"
"s_cbranch_scc1 Lpgo_2\n"
"s_sleep 2\n"
"s_branch Lpace_1\n"
"Lpgo_2:\n"
"s_add_u32 s52, s53, 0x31000\n"
"s_and_b32 s52, s52, s54\n"
"buffer_load_dwordx4 v[92:95], v1, s[28:31], s52 offen offset:0 nt\n"
"s_add_u32 s52, s53, 0x38000\n"
"s_and_b32 s52, s52, s54\n"
"buffer_load_dwordx4 v[96:99], v1, s[28:31], s52 offen offset:0 nt\n"
"s_add_u32 s52, s53, 0x3f000\n"
"s_and_b32 s52, s52, s54\n"
"buffer_load_dwordx4 v[100:103], v1, s[28:31], s52 offen offset:0 nt\n"
"s_add_u32 s52, s53, 0x46000\n"
"s_and_b32 s52, s52, s54\n"
"buffer_load_dwordx4 v[104:107], v1, s[28:31], s52 offen offset:0 nt\n"
"Lpace_3:\n"
"s_memrealtime s[96:97]\n"
"s_waitcnt lgkmcnt(0)\n"
"s_sub_u32 s96, s96, s98\n"
"s_cmp_ge_u32 s96, 95\n"
"s_cbranch_scc1 Lpgo_4\n"
"s_sleep 2\n"
"s_branch Lpace_3\n"
"Lpgo_4:\n"
"s_add_u32 s52, s53, 0x4d000\n"
"s_and_b32 s52, s52, s54\n"
"buffer_load_dwordx4 v[108:111], v1, s[28:31], s52 offen offset:0 nt\n"
"s_add_u32 s52, s53, 0x54000\n"
"s_and_b32 s52, s52, s54\n"
"buffer_load_dwordx4 v[112:115], v1, s[28:31], s52 offen offset:0 nt\n"
"s_add_u32 s52, s53, 0x5b000\n"
"s_and_b32 s52, s52, s54\n"
"buffer_load_dwordx4 v[116:119], v1, s[28:31], s52 offen offset:0 nt\n"
"s_add_u32 s52, s53, 0x62000\n"
"s_and_b32 s52, s52, s54\n"
"buffer_load_dwordx4 v[120:123], v1, s[28:31], s52 offen offset:0 nt\n"
"Lpace_5:\n"
"s_memrealtime s[96:97]\n"
"s_waitcnt lgkmcnt(0)\n"
"s_sub_u32 s96, s96, s98\n"
"s_cmp_ge_u32 s96, 152\n"
"s_cbranch_scc1 Lpgo_6\n"
"s_sleep 2\n"
"s_branch Lpace_5\n"
"Lpgo_6:\n"
"s_add_u32 s52, s53, 0x69000\n"
"s_and_b32 s52, s52, s54\n"
"buffer_load_dwordx4 v[136:139], v1, s[28:31], s52 offen offset:0 nt\n"
"s_add_u32 s52, s53, 0x70000\n"
"s_and_b32 s52, s52, s54\n"
"buffer_load_dwordx4 v[140:143], v1, s[28:31], s52 offen offset:0 nt\n"
"s_add_u32 s52, s53, 0x77000\n"
"s_and_b32 s52, s52, s54\n"
"buffer_load_dwordx4 v[144:147], v1, s[28:31], s52 offen offset:0 nt\n"
"s_add_u32 s52, s53, 0x7e000\n"
"s_and_b32 s52, s52, s54\n"
"buffer_load_dwordx4 v[124:127], v1, s[28:31], s52 offen offset:0 nt\n"
"Lpace_7:\n"
"s_memrealtime s[96:97]\n"
"s_waitcnt lgkmcnt(0)\n"
"s_sub_u32 s96, s96, s98\n"
"s_cmp_ge_u32 s96, 209\n"
"s_cbranch_scc1 Lpgo_8\n"
"s_sleep 2\n"
"s_branch Lpace_7\n"
"Lpgo_8:\n"
"s_add_u32 s52, s53, 0x85000\n"
"s_and_b32 s52, s52, s54\n"
"buffer_load_dwordx4 v[128:131], v1, s[28:31], s52 offen offset:0 nt\n"
"buffer_load_dwordx2 v[36:37], off, s[36:39], 0 offset:512 sc1\n"
"s_add_u32 s52, s53, 0x8c000\n"
"s_and_b32 s52, s52, s54\n"
"buffer_load_dwordx4 v[132:135], v1, s[28:31], s52 offen offset:0 nt\n"
"s_add_u32 s52, s53, 0x93000\n"
"s_and_b32 s52, s52, s54\n"
"buffer_load_dwordx4 v[148:151], v1, s[28:31], s52 offen offset:0 nt\n"
"s_add_u32 s52, s53, 0x9a000\n"
"s_and_b32 s52, s52, s54\n"
"buffer_load_dwordx4 v[152:155], v1, s[28:31], s52 offen offset:0 nt\n"
"Lpace_9:\n"
"s_memrealtime s[96:97]\n"
"s_waitcnt lgkmcnt(0)\n"
"s_sub_u32 s96, s96, s98\n"
"s_cmp_ge_u32 s96, 265\n"
"s_cbranch_scc1 Lpgo_10\n"
"s_sleep 2\n"
"s_branch Lpace_9\n"
"Lpgo_10:\n"
"s_add_u32 s52, s53, 0xa1000\n"
"s_and_b32 s52, s52, s54\n"
"buffer_load_dwordx4 v[156:159], v1, s[28:31], s52 offen offset:0 nt\n"
"v_and_b32 v39, 7, v0\n"
"v_lshlrev_b32 v33, 19, v39\n"
"buffer_load_dword v39, v33, s[32:35], 0 offen nt\n"
"buffer_load_dword v39, v33, s[28:31], 0 offen nt\n"
"s_add_u32 s52, s53, 0xa8000\n"
"s_and_b32 s52, s52, s54\n"
"buffer_load_dwordx4 v[160:163], v1, s[28:31], s52 offen offset:0 nt\n"
"s_add_u32 s52, s53, 0xaf000\n"
"s_and_b32 s52, s52, s54\n"
"buffer_load_dwordx4 v[164:167], v1, s[28:31], s52 offen offset:0 nt\n"
"s_add_u32 s52, s53, 0xb6000\n"
"s_and_b32 s52, s52, s54\n"
"buffer_load_dwordx4 v[168:171], v1, s[28:31], s52 offen offset:0 nt\n"
"Lpace_11:\n"
"s_memrealtime s[96:97]\n"
"s_waitcnt lgkmcnt(0)\n"
"s_sub_u32 s96, s96, s98\n"
"s_cmp_ge_u32 s96, 322\n"
"s_cbranch_scc1 Lpgo_12\n"
"s_sleep 2\n"
"s_branch Lpace_11\n"
"Lpgo_12:\n"
"s_add_u32 s52, s53, 0xbd000\n"
"s_and_b32 s52, s52, s54\n"
"buffer_load_dwordx4 v[184:187], v1, s[28:31], s52 offen offset:0 nt\n"
"s_add_u32 s52, s53, 0xc4000\n"
"s_and_b32 s52, s52, s54\n"
"buffer_load_dwordx4 v[188:191], v1, s[28:31], s52 offen offset:0 nt\n"
"s_add_u32 s52, s53, 0xcb000\n"
"s_and_b32 s52, s52, s54\n"
"buffer_load_dwordx4 v[192:195], v1, s[28:31], s52 offen offset:0 nt\n"
"s_add_u32 s52, s53, 0xd2000\n"
"s_and_b32 s52, s52, s54\n"
"buffer_load_dwordx4 v[208:211], v1, s[28:31], s52 offen offset:0 nt\n"
"Lpace_13:\n"
"s_memrealtime s[96:97]\n"
"s_waitcnt lgkmcnt(0)\n"
"s_sub_u32 s96, s96, s98\n"
"s_cmp_ge_u32 s96, 379\n"
"s_cbranch_scc1 Lpgo_14\n"
"s_sleep 2\n"
"s_branch Lpace_13\n"
"Lpgo_14:\n"
"s_add_u32 s52, s53, 0xd9000\n"
"s_and_b32 s52, s52, s54\n"
"buffer_load_dwordx4 v[212:215], v1, s[28:31], s52 offen offset:0 nt\n"
"s_waitcnt vmcnt(14)\n"
"v_readfirstlane_b32 s96, v36\n"
"v_readfirstlane_b32 s97, v37\n"
"s_cmp_eq_u32 s97, s59\n"
"s_cselect_b32 s96, s96, s98\n"
"s_sub_u32 s97, s98, s96\n"
"s_cmp_lt_u32 s97, 2000\n"
"s_cselect_b32 s98, s96, s98\n"
"s_add_u32 s52, s53, 0xe0000\n"
"s_and_b32 s52, s52, s54\n"
"buffer_load_dwordx4 v[216:219], v1, s[28:31], s52 offen offset:0 nt\n"
"s_add_u32 s52, s53, 0xe7000\n"
"s_and_b32 s52, s52, s54\n"
"buffer_load_dwordx4 v[232:235], v1, s[28:31], s52 offen offset:0 nt\n"
"s_add_u32 s52, s53, 0xee000\n"
"s_and_b32 s52, s52, s54\n"
"buffer_load_dwordx4 v[236:239], v1, s[28:31], s52 offen offset:0 nt\n"
"Lpace_15:\n"
"s_memrealtime s[96:97]\n"
"s_waitcnt lgkmcnt(0)\n"
"s_sub_u32 s96, s96, s98\n"
"s_cmp_ge_u32 s96, 436\n"
"s_cbranch_scc1 Lpgo_16\n"
"s_sleep 2\n"
"s_branch Lpace_15\n"
"Lpgo_16:\n"
"s_add_u32 s52, s53, 0xf5000\n"
"s_and_b32 s52, s52, s54\n"
"buffer_load_dwordx4 v[240:243], v1, s[28:31], s52 offen offset:0 nt\n"
"s_mov_b32 s8, 0x87878787\n"
"s_mov_b32 s9, 0x6040200\n"
"s_mov_b32 s10, 0x7050301\n"
"s_mov_b32 s11, 0x5010400\n"
"s_mov_b32 s12, 0x7030602\n"
"s_mov_b32 s19, 0x0\n"
"s_mov_b32 s21, 0x0\n"
"s_mov_b32 s51, 0x0\n"
"s_mov_b32 s46, -0x1\n"
"s_mov_b32 s47, -0x1\n"
"v_pk_lshlrev_b16 v63, 1, v64 op_sel_hi:[0,1]\n"
"v_pk_lshlrev_b16 v62, 1, v65 op_sel_hi:[0,1]\n"
"v_pk_max_u16 v63, v63, v62\n"
"v_pk_lshlrev_b16 v62, 1, v66 op_sel_hi:[0,1]\n"
"v_pk_max_u16 v63, v63, v62\n"
"v_pk_lshlrev_b16 v62, 1, v67 op_sel_hi:[0,1]\n"
"v_pk_max_u16 v63, v63, v62\n"
"v_pk_lshlrev_b16 v62, 1, v68 op_sel_hi:[0,1]\n"
"v_pk_max_u16 v63, v63, v62\n"
"v_pk_lshlrev_b16 v62, 1, v69 op_sel_hi:[0,1]\n"
"v_pk_max_u16 v63, v63, v62\n"
"v_pk_lshlrev_b16 v62, 1, v70 op_sel_hi:[0,1]\n"
"v_pk_max_u16 v63, v63, v62\n"
"v_pk_lshlrev_b16 v62, 1, v71 op_sel_hi:[0,1]\n"
"v_pk_max_u16 v63, v63, v62\n"
"v_pk_lshlrev_b16 v62, 1, v72 op_sel_hi:[0,1]\n"
"v_pk_max_u16 v63, v63, v62\n"
"v_pk_lshlrev_b16 v62, 1, v73 op_sel_hi:[0,1]\n"
"v_pk_max_u16 v63, v63, v62\n"
"v_pk_lshlrev_b16 v62, 1, v74 op_sel_hi:[0,1]\n"
"v_pk_max_u16 v63, v63, v62\n"
"v_pk_lshlrev_b16 v62, 1, v75 op_sel_hi:[0,1]\n"
"v_pk_max_u16 v63, v63, v62\n"
"v_pk_lshlrev_b16 v62, 1, v76 op_sel_hi:[0,1]\n"
"v_pk_max_u16 v63, v63, v62\n"
"v_pk_lshlrev_b16 v62, 1, v77 op_sel_hi:[0,1]\n"
"v_pk_max_u16 v63, v63, v62\n"
"v_pk_lshlrev_b16 v62, 1, v78 op_sel_hi:[0,1]\n"
"v_pk_max_u16 v63, v63, v62\n"
"v_pk_lshlrev_b16 v62, 1, v79 op_sel_hi:[0,1]\n"
"v_pk_max_u16 v63, v63, v62\n"
"v_pk_lshlrev_b16 v62, 1, v80 op_sel_hi:[0,1]\n"
"v_pk_max_u16 v63, v63, v62\n"
"v_pk_lshlrev_b16 v62, 1, v81 op_sel_hi:[0,1]\n"
"v_pk_max_u16 v63, v63, v62\n"
"v_pk_lshlrev_b16 v62, 1, v82 op_sel_hi:[0,1]\n"
"v_pk_max_u16 v63, v63, v62\n"
"v_pk_lshlrev_b16 v62, 1, v83 op_sel_hi:[0,1]\n"
"v_pk_max_u16 v63, v63, v62\n"
"v_pk_lshlrev_b16 v62, 1, v84 op_sel_hi:[0,1]\n"
"v_pk_max_u16 v63, v63, v62\n"
"v_pk_lshlrev_b16 v62, 1, v85 op_sel_hi:[0,1]\n"
"v_pk_max_u16 v63, v63, v62\n"
"v_pk_lshlrev_b16 v62, 1, v86 op_sel_hi:[0,1]\n"
"v_pk_max_u16 v63, v63, v62\n"
"v_pk_lshlrev_b16 v62, 1, v87 op_sel_hi:[0,1]\n"
"v_pk_max_u16 v63, v63, v62\n"
"v_pk_lshlrev_b16 v62, 1, v88 op_sel_hi:[0,1]\n"
"v_pk_max_u16 v63, v63, v62\n"
"v_pk_lshlrev_b16 v62, 1, v89 op_sel_hi:[0,1]\n"
"v_pk_max_u16 v63, v63, v62\n"
"v_pk_lshlrev_b16 v62, 1, v90 op_sel_hi:[0,1]\n"
"v_pk_max_u16 v63, v63, v62\n"
"v_pk_lshlrev_b16 v62, 1, v91 op_sel_hi:[0,1]\n"
"v_pk_max_u16 v63, v63, v62\n"
"v_pk_lshlrev_b16 v62, 1, v92 op_sel_hi:[0,1]\n"
"v_pk_max_u16 v63, v63, v62\n"
"v_pk_lshlrev_b16 v62, 1, v93 op_sel_hi:[0,1]\n"
"v_pk_max_u16 v63, v63, v62\n"
"v_pk_lshlrev_b16 v62, 1, v94 op_sel_hi:[0,1]\n"
"v_pk_max_u16 v63, v63, v62\n"
"v_pk_lshlrev_b16 v62, 1, v95 op_sel_hi:[0,1]\n"
"v_pk_max_u16 v63, v63, v62\n"
"v_lshrrev_b32 v62, 16, v63\n"
"v_and_b32 v63, 0xffff, v63\n"
"v_max_u32 v63, v63, v62\n"
"s_nop 1\n"
"v_max_u32_dpp v63, v63, v63 quad_perm:[1,0,3,2] row_mask:0xf bank_mask:0xf\n"
"s_nop 1\n"
"v_max_u32_dpp v63, v63, v63 quad_perm:[2,3,0,1] row_mask:0xf bank_mask:0xf\n"
"s_nop 1\n"
"v_max_u32_dpp v63, v63, v63 row_half_mirror row_mask:0xf bank_mask:0xf\n"
"s_nop 1\n"
"v_max_u32_dpp v63, v63, v63 row_mirror row_mask:0xf bank_mask:0xf\n"
"s_nop 1\n"
"v_max_u32_dpp v63, v63, v63 row_bcast:15 row_mask:0xa bank_mask:0xf\n"
"s_nop 1\n"
"v_max_u32_dpp v63, v63, v63 row_bcast:31 row_mask:0xc bank_mask:0xf\n"
"s_nop 0\n"
"v_readlane_b32 s72, v63, 63\n"
"s_nop 3\n"
"s_lshr_b32 s72, s72, 8\n"
"s_sub_i32 s72, s72, 15\n"
"s_max_i32 s72, s72, 0\n"
"s_min_i32 s72, s72, 126\n"
"s_mul_i32 s14, s72, 0x00800080\n"
"s_lshl_b32 s16, s14, 1\n"
"s_add_u32 s16, s16, 0x00ff00ff\n"
"s_lshl_b32 s72, s72, 23\n"
"s_add_u32 s17, s72, 0x3f800000\n"
"v_pk_sub_u16 v61, v64, s14 clamp\n"
"v_pk_sub_u16 v62, v65, s14 clamp\n"
"v_perm_b32 v58, v62, v61, s9\n"
"v_perm_b32 v61, v62, v61, s10\n"
"v_pk_sub_u16 v62, v66, s14 clamp\n"
"v_pk_sub_u16 v63, v67, s14 clamp\n"
"v_perm_b32 v59, v63, v62, s9\n"
"v_perm_b32 v62, v63, v62, s10\n"
"v_alignbit_b32 v62, v62, v62, 28\n"
"v_bfi_b32 v60, s8, v61, v62\n"
"v_bfi_b32 v61, s8, v62, v61\n"
"v_pk_mad_u16 v62, v64, 2, -1 op_sel_hi:[1,0,0]\n"
"v_pk_mad_u16 v63, v65, 2, -1 op_sel_hi:[1,0,0]\n"
"v_pk_min_u16 v62, v62, v63\n"
"v_pk_mad_u16 v63, v66, 2, -1 op_sel_hi:[1,0,0]\n"
"v_pk_min_u16 v62, v62, v63\n"
"v_pk_mad_u16 v63, v67, 2, -1 op_sel_hi:[1,0,0]\n"
"v_pk_min_u16 v62, v62, v63\n"
"v_pk_sub_u16 v62, s16, v62 clamp\n"
"v_or_b32 v61, v61, v62\n"
"v_cmp_ne_u32 vcc, 0, v61\n"
"s_cbranch_vccnz Lx_17\n"
"Lb_18:\n"
"v_accvgpr_write_b32 a0, v58\n"
"v_accvgpr_write_b32 a1, v59\n"
"v_accvgpr_write_b32 a2, v60\n"
"s_add_u32 s52, s53, 0xfc000\n"
"s_and_b32 s52, s52, s54\n"
"buffer_load_dwordx4 v[64:67], v1, s[28:31], s52 offen offset:0 nt\n"
"s_add_u32 s52, s53, 0x103000\n"
"s_and_b32 s52, s52, s54\n"
"buffer_load_dwordx4 v[172:175], v1, s[28:31], s52 offen offset:0 nt\n"
"v_pk_sub_u16 v61, v68, s14 clamp\n"
"v_pk_sub_u16 v62, v69, s14 clamp\n"
"v_perm_b32 v58, v62, v61, s9\n"
"v_perm_b32 v61, v62, v61, s10\n"
"v_pk_sub_u16 v62, v70, s14 clamp\n"
"v_pk_sub_u16 v63, v71, s14 clamp\n"
"v_perm_b32 v59, v63, v62, s9\n"
"v_perm_b32 v62, v63, v62, s10\n"
"v_alignbit_b32 v62, v62, v62, 28\n"
"v_bfi_b32 v60, s8, v61, v62\n"
"v_bfi_b32 v61, s8, v62, v61\n"
"v_pk_mad_u16 v62, v68, 2, -1 op_sel_hi:[1,0,0]\n"
"v_pk_mad_u16 v63, v69, 2, -1 op_sel_hi:[1,0,0]\n"
"v_pk_min_u16 v62, v62, v63\n"
"v_pk_mad_u16 v63, v70, 2, -1 op_sel_hi:[1,0,0]\n"
"v_pk_min_u16 v62, v62, v63\n"
"v_pk_mad_u16 v63, v71, 2, -1 op_sel_hi:[1,0,0]\n"
"v_pk_min_u16 v62, v62, v63\n"
"v_pk_sub_u16 v62, s16, v62 clamp\n"
"v_or_b32 v61, v61, v62\n"
"v_cmp_ne_u32 vcc, 0, v61\n"
"s_cbranch_vccnz Lx_19\n"
"Lb_20:\n"
"v_accvgpr_write_b32 a3, v58\n"
"v_accvgpr_write_b32 a4, v59\n"
"v_accvgpr_write_b32 a5, v60\n"
"v_pk_sub_u16 v61, v72, s14 clamp\n"
"v_pk_sub_u16 v62, v73, s14 clamp\n"
"v_perm_b32 v58, v62, v61, s9\n"
"v_perm_b32 v61, v62, v61, s10\n"
"v_pk_sub_u16 v62, v74, s14 clamp\n"
"v_pk_sub_u16 v63, v75, s14 clamp\n"
"v_perm_b32 v59, v63, v62, s9\n"
"v_perm_b32 v62, v63, v62, s10\n"
"v_alignbit_b32 v62, v62, v62, 28\n"
"v_bfi_b32 v60, s8, v61, v62\n"
"v_bfi_b32 v61, s8, v62, v61\n"
"v_pk_mad_u16 v62, v72, 2, -1 op_sel_hi:[1,0,0]\n"
"v_pk_mad_u16 v63, v73, 2, -1 op_sel_hi:[1,0,0]\n"
"v_pk_min_u16 v62, v62, v63\n"
"v_pk_mad_u16 v63, v74, 2, -1 op_sel_hi:[1,0,0]\n"
"v_pk_min_u16 v62, v62, v63\n"
"v_pk_mad_u16 v63, v75, 2, -1 op_sel_hi:[1,0,0]\n"
"v_pk_min_u16 v62, v62, v63\n"
"v_pk_sub_u16 v62, s16, v62 clamp\n"
"v_or_b32 v61, v61, v62\n"
"v_cmp_ne_u32 vcc, 0, v61\n"
"s_cbranch_vccnz Lx_21\n"
"Lb_22:\n"
"v_accvgpr_write_b32 a6, v58\n"
"v_accvgpr_write_b32 a7, v59\n"
"v_accvgpr_write_b32 a8, v60\n"
"s_add_u32 s52, s53, 0x10a000\n"
"s_and_b32 s52, s52, s54\n"
"buffer_load_dwordx4 v[68:71], v1, s[28:31], s52 offen offset:0 nt\n"
"v_pk_sub_u16 v61, v76, s14 clamp\n"
"v_pk_sub_u16 v62, v77, s14 clamp\n"
"v_perm_b32 v58, v62, v61, s9\n"
"v_perm_b32 v61, v62, v61, s10\n"
"v_pk_sub_u16 v62, v78, s14 clamp\n"
"v_pk_sub_u16 v63, v79, s14 clamp\n"
"v_perm_b32 v59, v63, v62, s9\n"
"v_perm_b32 v62, v63, v62, s10\n"
"v_alignbit_b32 v62, v62, v62, 28\n"
"v_bfi_b32 v60, s8, v61, v62\n"
"v_bfi_b32 v61, s8, v62, v61\n"
"v_pk_mad_u16 v62, v76, 2, -1 op_sel_hi:[1,0,0]\n"
"v_pk_mad_u16 v63, v77, 2, -1 op_sel_hi:[1,0,0]\n"
"v_pk_min_u16 v62, v62, v63\n"
"v_pk_mad_u16 v63, v78, 2, -1 op_sel_hi:[1,0,0]\n"
"v_pk_min_u16 v62, v62, v63\n"
"v_pk_mad_u16 v63, v79, 2, -1 op_sel_hi:[1,0,0]\n"
"v_pk_min_u16 v62, v62, v63\n"
"v_pk_sub_u16 v62, s16, v62 clamp\n"
"v_or_b32 v61, v61, v62\n"
"v_cmp_ne_u32 vcc, 0, v61\n"
"s_cbranch_vccnz Lx_23\n"
"Lb_24:\n"
"v_accvgpr_write_b32 a9, v58\n"
"v_accvgpr_write_b32 a10, v59\n"
"v_accvgpr_write_b32 a11, v60\n"
"Lpace_25:\n"
"s_memrealtime s[96:97]\n"
"s_waitcnt lgkmcnt(0)\n"
"s_sub_u32 s96, s96, s98\n"
"s_cmp_ge_u32 s96, 493\n"
"s_cbranch_scc1 Lpgo_26\n"
"s_sleep 2\n"
"s_branch Lpace_25\n"
"Lpgo_26:\n"
"s_add_u32 s52, s53, 0x111000\n"
"s_and_b32 s52, s52, s54\n"
"buffer_load_dwordx4 v[72:75], v1, s[28:31], s52 offen offset:0 nt\n"
"v_pk_sub_u16 v61, v80, s14 clamp\n"
"v_pk_sub_u16 v62, v81, s14 clamp\n"
"v_perm_b32 v58, v62, v61, s9\n"
"v_perm_b32 v61, v62, v61, s10\n"
"v_pk_sub_u16 v62, v82, s14 clamp\n"
"v_pk_sub_u16 v63, v83, s14 clamp\n"
"v_perm_b32 v59, v63, v62, s9\n"
"v_perm_b32 v62, v63, v62, s10\n"
"v_alignbit_b32 v62, v62, v62, 28\n"
"v_bfi_b32 v60, s8, v61, v62\n"
"v_bfi_b32 v61, s8, v62, v61\n"
"v_pk_mad_u16 v62, v80, 2, -1 op_sel_hi:[1,0,0]\n"
"v_pk_mad_u16 v63, v81, 2, -1 op_sel_hi:[1,0,0]\n"
"v_pk_min_u16 v62, v62, v63\n"
"v_pk_mad_u16 v63, v82, 2, -1 op_sel_hi:[1,0,0]\n"
"v_pk_min_u16 v62, v62, v63\n"
"v_pk_mad_u16 v63, v83, 2, -1 op_sel_hi:[1,0,0]\n"
"v_pk_min_u16 v62, v62, v63\n"
"v_pk_sub_u16 v62, s16, v62 clamp\n"
"v_or_b32 v61, v61, v62\n"
"v_cmp_ne_u32 vcc, 0, v61\n"
"s_cbranch_vccnz Lx_27\n"
"Lb_28:\n"
"v_accvgpr_write_b32 a12, v58\n"
"v_accvgpr_write_b32 a13, v59\n"
"v_accvgpr_write_b32 a14, v60\n"
"s_add_u32 s52, s53, 0x118000\n"
"s_and_b32 s52, s52, s54\n"
"buffer_load_dwordx4 v[76:79], v1, s[28:31], s52 offen offset:0 nt\n"
"v_pk_sub_u16 v61, v84, s14 clamp\n"
"v_pk_sub_u16 v62, v85, s14 clamp\n"
"v_perm_b32 v58, v62, v61, s9\n"
"v_perm_b32 v61, v62, v61, s10\n"
"v_pk_sub_u16 v62, v86, s14 clamp\n"
"v_pk_sub_u16 v63, v87, s14 clamp\n"
"v_perm_b32 v59, v63, v62, s9\n"
"v_perm_b32 v62, v63, v62, s10\n"
"v_alignbit_b32 v62, v62, v62, 28\n"
"v_bfi_b32 v60, s8, v61, v62\n"
"v_bfi_b32 v61, s8, v62, v61\n"
"v_pk_mad_u16 v62, v84, 2, -1 op_sel_hi:[1,0,0]\n"
"v_pk_mad_u16 v63, v85, 2, -1 op_sel_hi:[1,0,0]\n"
"v_pk_min_u16 v62, v62, v63\n"
"v_pk_mad_u16 v63, v86, 2, -1 op_sel_hi:[1,0,0]\n"
"v_pk_min_u16 v62, v62, v63\n"
"v_pk_mad_u16 v63, v87, 2, -1 op_sel_hi:[1,0,0]\n"
"v_pk_min_u16 v62, v62, v63\n"
"v_pk_sub_u16 v62, s16, v62 clamp\n"
"v_or_b32 v61, v61, v62\n"
"v_cmp_ne_u32 vcc, 0, v61\n"
"s_cbranch_vccnz Lx_29\n"
"Lb_30:\n"
"v_accvgpr_write_b32 a15, v58\n"
"v_accvgpr_write_b32 a16, v59\n"
"v_accvgpr_write_b32 a17, v60\n"
"s_add_u32 s52, s53, 0x11f000\n"
"s_and_b32 s52, s52, s54\n"
"buffer_load_dwordx4 v[80:83], v1, s[28:31], s52 offen offset:0 nt\n"
"v_pk_sub_u16 v61, v88, s14 clamp\n"
"v_pk_sub_u16 v62, v89, s14 clamp\n"
"v_perm_b32 v58, v62, v61, s9\n"
"v_perm_b32 v61, v62, v61, s10\n"
"v_pk_sub_u16 v62, v90, s14 clamp\n"
"v_pk_sub_u16 v63, v91, s14 clamp\n"
"v_perm_b32 v59, v63, v62, s9\n"
"v_perm_b32 v62, v63, v62, s10\n"
"v_alignbit_b32 v62, v62, v62, 28\n"
"v_bfi_b32 v60, s8, v61, v62\n"
"v_bfi_b32 v61, s8, v62, v61\n"
"v_pk_mad_u16 v62, v88, 2, -1 op_sel_hi:[1,0,0]\n"
"v_pk_mad_u16 v63, v89, 2, -1 op_sel_hi:[1,0,0]\n"
"v_pk_min_u16 v62, v62, v63\n"
"v_pk_mad_u16 v63, v90, 2, -1 op_sel_hi:[1,0,0]\n"
"v_pk_min_u16 v62, v62, v63\n"
"v_pk_mad_u16 v63, v91, 2, -1 op_sel_hi:[1,0,0]\n"
"v_pk_min_u16 v62, v62, v63\n"
"v_pk_sub_u16 v62, s16, v62 clamp\n"
"v_or_b32 v61, v61, v62\n"
"v_cmp_ne_u32 vcc, 0, v61\n"
"s_cbranch_vccnz Lx_31\n"
"Lb_32:\n"
"v_accvgpr_write_b32 a18, v58\n"
"v_accvgpr_write_b32 a19, v59\n"
"v_accvgpr_write_b32 a20, v60\n"
"s_add_u32 s52, s53, 0x126000\n"
"s_and_b32 s52, s52, s54\n"
"buffer_load_dwordx4 v[84:87], v1, s[28:31], s52 offen offset:0 nt\n"
"v_pk_sub_u16 v61, v92, s14 clamp\n"
"v_pk_sub_u16 v62, v93, s14 clamp\n"
"v_perm_b32 v58, v62, v61, s9\n"
"v_perm_b32 v61, v62, v61, s10\n"
"v_pk_sub_u16 v62, v94, s14 clamp\n"
"v_pk_sub_u16 v63, v95, s14 clamp\n"
"v_perm_b32 v59, v63, v62, s9\n"
"v_perm_b32 v62, v63, v62, s10\n"
"v_alignbit_b32 v62, v62, v62, 28\n"
"v_bfi_b32 v60, s8, v61, v62\n"
"v_bfi_b32 v61, s8, v62, v61\n"
"v_pk_mad_u16 v62, v92, 2, -1 op_sel_hi:[1,0,0]\n"
"v_pk_mad_u16 v63, v93, 2, -1 op_sel_hi:[1,0,0]\n"
"v_pk_min_u16 v62, v62, v63\n"
"v_pk_mad_u16 v63, v94, 2, -1 op_sel_hi:[1,0,0]\n"
"v_pk_min_u16 v62, v62, v63\n"
"v_pk_mad_u16 v63, v95, 2, -1 op_sel_hi:[1,0,0]\n"
"v_pk_min_u16 v62, v62, v63\n"
"v_pk_sub_u16 v62, s16, v62 clamp\n"
"v_or_b32 v61, v61, v62\n"
"v_cmp_ne_u32 vcc, 0, v61\n"
"s_cbranch_vccnz Lx_33\n"
"Lb_34:\n"
"v_accvgpr_write_b32 a21, v58\n"
"v_accvgpr_write_b32 a22, v59\n"
"v_accvgpr_write_b32 a23, v60\n"
"Lpace_35:\n"
"s_memrealtime s[96:97]\n"
"s_waitcnt lgkmcnt(0)\n"
"s_sub_u32 s96, s96, s98\n"
"s_cmp_ge_u32 s96, 549\n"
"s_cbranch_scc1 Lpgo_36\n"
"s_sleep 2\n"
"s_branch Lpace_35\n"
"Lpgo_36:\n"
"s_add_u32 s52, s53, 0x12d000\n"
"s_and_b32 s52, s52, s54\n"
"buffer_load_dwordx4 v[88:91], v1, s[28:31], s52 offen offset:0 nt\n"
"v_pk_sub_u16 v61, v96, s14 clamp\n"
"v_pk_sub_u16 v62, v97, s14 clamp\n"
"v_perm_b32 v58, v62, v61, s9\n"
"v_perm_b32 v61, v62, v61, s10\n"
"v_pk_sub_u16 v62, v98, s14 clamp\n"
"v_pk_sub_u16 v63, v99, s14 clamp\n"
"v_perm_b32 v59, v63, v62, s9\n"
"v_perm_b32 v62, v63, v62, s10\n"
"v_alignbit_b32 v62, v62, v62, 28\n"
"v_bfi_b32 v60, s8, v61, v62\n"
"v_bfi_b32 v61, s8, v62, v61\n"
"v_pk_mad_u16 v62, v96, 2, -1 op_sel_hi:[1,0,0]\n"
"v_pk_mad_u16 v63, v97, 2, -1 op_sel_hi:[1,0,0]\n"
"v_pk_min_u16 v62, v62, v63\n"
"v_pk_mad_u16 v63, v98, 2, -1 op_sel_hi:[1,0,0]\n"
"v_pk_min_u16 v62, v62, v63\n"
"v_pk_mad_u16 v63, v99, 2, -1 op_sel_hi:[1,0,0]\n"
"v_pk_min_u16 v62, v62, v63\n"
"v_pk_sub_u16 v62, s16, v62 clamp\n"
"v_or_b32 v61, v61, v62\n"
"v_cmp_ne_u32 vcc, 0, v61\n"
"s_cbranch_vccnz Lx_37\n"
"Lb_38:\n"
"v_accvgpr_write_b32 a24, v58\n"
"v_accvgpr_write_b32 a25, v59\n"
"v_accvgpr_write_b32 a26, v60\n"
"s_add_u32 s52, s53, 0x134000\n"
"s_and_b32 s52, s52, s54\n"
"buffer_load_dwordx4 v[92:95], v1, s[28:31], s52 offen offset:0 nt\n"
"v_pk_sub_u16 v61, v100, s14 clamp\n"
"v_pk_sub_u16 v62, v101, s14 clamp\n"
"v_perm_b32 v58, v62, v61, s9\n"
"v_perm_b32 v61, v62, v61, s10\n"
"v_pk_sub_u16 v62, v102, s14 clamp\n"
"v_pk_sub_u16 v63, v103, s14 clamp\n"
"v_perm_b32 v59, v63, v62, s9\n"
"v_perm_b32 v62, v63, v62, s10\n"
"v_alignbit_b32 v62, v62, v62, 28\n"
"v_bfi_b32 v60, s8, v61, v62\n"
"v_bfi_b32 v61, s8, v62, v61\n"
"v_pk_mad_u16 v62, v100, 2, -1 op_sel_hi:[1,0,0]\n"
"v_pk_mad_u16 v63, v101, 2, -1 op_sel_hi:[1,0,0]\n"
"v_pk_min_u16 v62, v62, v63\n"
"v_pk_mad_u16 v63, v102, 2, -1 op_sel_hi:[1,0,0]\n"
"v_pk_min_u16 v62, v62, v63\n"
"v_pk_mad_u16 v63, v103, 2, -1 op_sel_hi:[1,0,0]\n"
"v_pk_min_u16 v62, v62, v63\n"
"v_pk_sub_u16 v62, s16, v62 clamp\n"
"v_or_b32 v61, v61, v62\n"
"v_cmp_ne_u32 vcc, 0, v61\n"
"s_cbranch_vccnz Lx_39\n"
"Lb_40:\n"
"v_accvgpr_write_b32 a27, v58\n"
"v_accvgpr_write_b32 a28, v59\n"
"v_accvgpr_write_b32 a29, v60\n"
"s_add_u32 s52, s53, 0x13b000\n"
"s_and_b32 s52, s52, s54\n"
"buffer_load_dwordx4 v[96:99], v1, s[28:31], s52 offen offset:0 nt\n"
"v_pk_sub_u16 v61, v104, s14 clamp\n"
"v_pk_sub_u16 v62, v105, s14 clamp\n"
"v_perm_b32 v58, v62, v61, s9\n"
"v_perm_b32 v61, v62, v61, s10\n"
"v_pk_sub_u16 v62, v106, s14 clamp\n"
"v_pk_sub_u16 v63, v107, s14 clamp\n"
"v_perm_b32 v59, v63, v62, s9\n"
"v_perm_b32 v62, v63, v62, s10\n"
"v_alignbit_b32 v62, v62, v62, 28\n"
"v_bfi_b32 v60, s8, v61, v62\n"
"v_bfi_b32 v61, s8, v62, v61\n"
"v_pk_mad_u16 v62, v104, 2, -1 op_sel_hi:[1,0,0]\n"
"v_pk_mad_u16 v63, v105, 2, -1 op_sel_hi:[1,0,0]\n"
"v_pk_min_u16 v62, v62, v63\n"
"v_pk_mad_u16 v63, v106, 2, -1 op_sel_hi:[1,0,0]\n"
"v_pk_min_u16 v62, v62, v63\n"
"v_pk_mad_u16 v63, v107, 2, -1 op_sel_hi:[1,0,0]\n"
"v_pk_min_u16 v62, v62, v63\n"
"v_pk_sub_u16 v62, s16, v62 clamp\n"
"v_or_b32 v61, v61, v62\n"
"v_cmp_ne_u32 vcc, 0, v61\n"
"s_cbranch_vccnz Lx_41\n"
"Lb_42:\n"
"v_accvgpr_write_b32 a30, v58\n"
"v_accvgpr_write_b32 a31, v59\n"
"v_accvgpr_write_b32 a32, v60\n"
"s_add_u32 s52, s53, 0x142000\n"
"s_and_b32 s52, s52, s54\n"
"buffer_load_dwordx4 v[100:103], v1, s[28:31], s52 offen offset:0 nt\n"
"v_pk_sub_u16 v61, v108, s14 clamp\n"
"v_pk_sub_u16 v62, v109, s14 clamp\n"
"v_perm_b32 v58, v62, v61, s9\n"
"v_perm_b32 v61, v62, v61, s10\n"
"v_pk_sub_u16 v62, v110, s14 clamp\n"
"v_pk_sub_u16 v63, v111, s14 clamp\n"
"v_perm_b32 v59, v63, v62, s9\n"
"v_perm_b32 v62, v63, v62, s10\n"
"v_alignbit_b32 v62, v62, v62, 28\n"
"v_bfi_b32 v60, s8, v61, v62\n"
"v_bfi_b32 v61, s8, v62, v61\n"
"v_pk_mad_u16 v62, v108, 2, -1 op_sel_hi:[1,0,0]\n"
"v_pk_mad_u16 v63, v109, 2, -1 op_sel_hi:[1,0,0]\n"
"v_pk_min_u16 v62, v62, v63\n"
"v_pk_mad_u16 v63, v110, 2, -1 op_sel_hi:[1,0,0]\n"
"v_pk_min_u16 v62, v62, v63\n"
"v_pk_mad_u16 v63, v111, 2, -1 op_sel_hi:[1,0,0]\n"
"v_pk_min_u16 v62, v62, v63\n"
"v_pk_sub_u16 v62, s16, v62 clamp\n"
"v_or_b32 v61, v61, v62\n"
"v_cmp_ne_u32 vcc, 0, v61\n"
"s_cbranch_vccnz Lx_43\n"
"Lb_44:\n"
"v_accvgpr_write_b32 a33, v58\n"
"v_accvgpr_write_b32 a34, v59\n"
"v_accvgpr_write_b32 a35, v60\n"
"Lpace_45:\n"
"s_memrealtime s[96:97]\n"
"s_waitcnt lgkmcnt(0)\n"
"s_sub_u32 s96, s96, s98\n"
"s_cmp_ge_u32 s96, 606\n"
"s_cbranch_scc1 Lpgo_46\n"
"s_sleep 2\n"
"s_branch Lpace_45\n"
"Lpgo_46:\n"
"s_add_u32 s52, s53, 0x149000\n"
"s_and_b32 s52, s52, s54\n"
"buffer_load_dwordx4 v[104:107], v1, s[28:31], s52 offen offset:0 nt\n"
"v_pk_sub_u16 v61, v112, s14 clamp\n"
"v_pk_sub_u16 v62, v113, s14 clamp\n"
"v_perm_b32 v58, v62, v61, s9\n"
"v_perm_b32 v61, v62, v61, s10\n"
"v_pk_sub_u16 v62, v114, s14 clamp\n"
"v_pk_sub_u16 v63, v115, s14 clamp\n"
"v_perm_b32 v59, v63, v62, s9\n"
"v_perm_b32 v62, v63, v62, s10\n"
"v_alignbit_b32 v62, v62, v62, 28\n"
"v_bfi_b32 v60, s8, v61, v62\n"
"v_bfi_b32 v61, s8, v62, v61\n"
"v_pk_mad_u16 v62, v112, 2, -1 op_sel_hi:[1,0,0]\n"
"v_pk_mad_u16 v63, v113, 2, -1 op_sel_hi:[1,0,0]\n"
"v_pk_min_u16 v62, v62, v63\n"
"v_pk_mad_u16 v63, v114, 2, -1 op_sel_hi:[1,0,0]\n"
"v_pk_min_u16 v62, v62, v63\n"
"v_pk_mad_u16 v63, v115, 2, -1 op_sel_hi:[1,0,0]\n"
"v_pk_min_u16 v62, v62, v63\n"
"v_pk_sub_u16 v62, s16, v62 clamp\n"
"v_or_b32 v61, v61, v62\n"
"v_cmp_ne_u32 vcc, 0, v61\n"
"s_cbranch_vccnz Lx_47\n"
"Lb_48:\n"
"v_accvgpr_write_b32 a36, v58\n"
"v_accvgpr_write_b32 a37, v59\n"
"v_accvgpr_write_b32 a38, v60\n"
"s_add_u32 s52, s53, 0x150000\n"
"s_and_b32 s52, s52, s54\n"
"buffer_load_dwordx4 v[108:111], v1, s[28:31], s52 offen offset:0 nt\n"
"v_pk_sub_u16 v61, v116, s14 clamp\n"
"v_pk_sub_u16 v62, v117, s14 clamp\n"
"v_perm_b32 v58, v62, v61, s9\n"
"v_perm_b32 v61, v62, v61, s10\n"
"v_pk_sub_u16 v62, v118, s14 clamp\n"
"v_pk_sub_u16 v63, v119, s14 clamp\n"
"v_perm_b32 v59, v63, v62, s9\n"
"v_perm_b32 v62, v63, v62, s10\n"
"v_alignbit_b32 v62, v62, v62, 28\n"
"v_bfi_b32 v60, s8, v61, v62\n"
"v_bfi_b32 v61, s8, v62, v61\n"
"v_pk_mad_u16 v62, v116, 2, -1 op_sel_hi:[1,0,0]\n"
"v_pk_mad_u16 v63, v117, 2, -1 op_sel_hi:[1,0,0]\n"
"v_pk_min_u16 v62, v62, v63\n"
"v_pk_mad_u16 v63, v118, 2, -1 op_sel_hi:[1,0,0]\n"
"v_pk_min_u16 v62, v62, v63\n"
"v_pk_mad_u16 v63, v119, 2, -1 op_sel_hi:[1,0,0]\n"
"v_pk_min_u16 v62, v62, v63\n"
"v_pk_sub_u16 v62, s16, v62 clamp\n"
"v_or_b32 v61, v61, v62\n"
"v_cmp_ne_u32 vcc, 0, v61\n"
"s_cbranch_vccnz Lx_49\n"
"Lb_50:\n"
"v_accvgpr_write_b32 a39, v58\n"
"v_accvgpr_write_b32 a40, v59\n"
"v_accvgpr_write_b32 a41, v60\n"
"s_add_u32 s52, s53, 0x157000\n"
"s_and_b32 s52, s52, s54\n"
"buffer_load_dwordx4 v[112:115], v1, s[28:31], s52 offen offset:0 nt\n"
"v_pk_sub_u16 v61, v120, s14 clamp\n"
"v_pk_sub_u16 v62, v121, s14 clamp\n"
"v_perm_b32 v58, v62, v61, s9\n"
"v_perm_b32 v61, v62, v61, s10\n"
"v_pk_sub_u16 v62, v122, s14 clamp\n"
"v_pk_sub_u16 v63, v123, s14 clamp\n"
"v_perm_b32 v59, v63, v62, s9\n"
"v_perm_b32 v62, v63, v62, s10\n"
"v_alignbit_b32 v62, v62, v62, 28\n"
"v_bfi_b32 v60, s8, v61, v62\n"
"v_bfi_b32 v61, s8, v62, v61\n"
"v_pk_mad_u16 v62, v120, 2, -1 op_sel_hi:[1,0,0]\n"
"v_pk_mad_u16 v63, v121, 2, -1 op_sel_hi:[1,0,0]\n"
"v_pk_min_u16 v62, v62, v63\n"
"v_pk_mad_u16 v63, v122, 2, -1 op_sel_hi:[1,0,0]\n"
"v_pk_min_u16 v62, v62, v63\n"
"v_pk_mad_u16 v63, v123, 2, -1 op_sel_hi:[1,0,0]\n"
"v_pk_min_u16 v62, v62, v63\n"
"v_pk_sub_u16 v62, s16, v62 clamp\n"
"v_or_b32 v61, v61, v62\n"
"v_cmp_ne_u32 vcc, 0, v61\n"
"s_cbranch_vccnz Lx_51\n"
"Lb_52:\n"
"v_accvgpr_write_b32 a42, v58\n"
"v_accvgpr_write_b32 a43, v59\n"
"v_accvgpr_write_b32 a44, v60\n"
"s_add_u32 s52, s53, 0x15e000\n"
"s_and_b32 s52, s52, s54\n"
"buffer_load_dwordx4 v[116:119], v1, s[28:31], s52 offen offset:0 nt\n"
"v_pk_sub_u16 v61, v136, s14 clamp\n"
"v_pk_sub_u16 v62, v137, s14 clamp\n"
"v_perm_b32 v58, v62, v61, s9\n"
"v_perm_b32 v61, v62, v61, s10\n"
"v_pk_sub_u16 v62, v138, s14 clamp\n"
"v_pk_sub_u16 v63, v139, s14 clamp\n"
"v_perm_b32 v59, v63, v62, s9\n"
"v_perm_b32 v62, v63, v62, s10\n"
"v_alignbit_b32 v62, v62, v62, 28\n"
"v_bfi_b32 v60, s8, v61, v62\n"
"v_bfi_b32 v61, s8, v62, v61\n"
"v_pk_mad_u16 v62, v136, 2, -1 op_sel_hi:[1,0,0]\n"
"v_pk_mad_u16 v63, v137, 2, -1 op_sel_hi:[1,0,0]\n"
"v_pk_min_u16 v62, v62, v63\n"
"v_pk_mad_u16 v63, v138, 2, -1 op_sel_hi:[1,0,0]\n"
"v_pk_min_u16 v62, v62, v63\n"
"v_pk_mad_u16 v63, v139, 2, -1 op_sel_hi:[1,0,0]\n"
"v_pk_min_u16 v62, v62, v63\n"
"v_pk_sub_u16 v62, s16, v62 clamp\n"
"v_or_b32 v61, v61, v62\n"
"v_cmp_ne_u32 vcc, 0, v61\n"
"s_cbranch_vccnz Lx_53\n"
"Lb_54:\n"
"v_accvgpr_write_b32 a45, v58\n"
"v_accvgpr_write_b32 a46, v59\n"
"v_accvgpr_write_b32 a47, v60\n"
"Lpace_55:\n"
"s_memrealtime s[96:97]\n"
"s_waitcnt lgkmcnt(0)\n"
"s_sub_u32 s96, s96, s98\n"
"s_cmp_ge_u32 s96, 663\n"
"s_cbranch_scc1 Lpgo_56\n"
"s_sleep 2\n"
"s_branch Lpace_55\n"
"Lpgo_56:\n"
"s_add_u32 s52, s53, 0x165000\n"
"s_and_b32 s52, s52, s54\n"
"buffer_load_dwordx4 v[120:123], v1, s[28:31], s52 offen offset:0 nt\n"
"v_pk_sub_u16 v61, v140, s14 clamp\n"
"v_pk_sub_u16 v62, v141, s14 clamp\n"
"v_perm_b32 v58, v62, v61, s9\n"
"v_perm_b32 v61, v62, v61, s10\n"
"v_pk_sub_u16 v62, v142, s14 clamp\n"
"v_pk_sub_u16 v63, v143, s14 clamp\n"
"v_perm_b32 v59, v63, v62, s9\n"
"v_perm_b32 v62, v63, v62, s10\n"
"v_alignbit_b32 v62, v62, v62, 28\n"
"v_bfi_b32 v60, s8, v61, v62\n"
"v_bfi_b32 v61, s8, v62, v61\n"
"v_pk_mad_u16 v62, v140, 2, -1 op_sel_hi:[1,0,0]\n"
"v_pk_mad_u16 v63, v141, 2, -1 op_sel_hi:[1,0,0]\n"
"v_pk_min_u16 v62, v62, v63\n"
"v_pk_mad_u16 v63, v142, 2, -1 op_sel_hi:[1,0,0]\n"
"v_pk_min_u16 v62, v62, v63\n"
"v_pk_mad_u16 v63, v143, 2, -1 op_sel_hi:[1,0,0]\n"
"v_pk_min_u16 v62, v62, v63\n"
"v_pk_sub_u16 v62, s16, v62 clamp\n"
"v_or_b32 v61, v61, v62\n"
"v_cmp_ne_u32 vcc, 0, v61\n"
"s_cbranch_vccnz Lx_57\n"
"Lb_58:\n"
"v_accvgpr_write_b32 a48, v58\n"
"v_accvgpr_write_b32 a49, v59\n"
"v_accvgpr_write_b32 a50, v60\n"
"s_add_u32 s52, s53, 0x16c000\n"
"s_and_b32 s52, s52, s54\n"
"buffer_load_dwordx4 v[136:139], v1, s[28:31], s52 offen offset:0 nt\n"
"v_pk_sub_u16 v61, v144, s14 clamp\n"
"v_pk_sub_u16 v62, v145, s14 clamp\n"
"v_perm_b32 v58, v62, v61, s9\n"
"v_perm_b32 v61, v62, v61, s10\n"
"v_pk_sub_u16 v62, v146, s14 clamp\n"
"v_pk_sub_u16 v63, v147, s14 clamp\n"
"v_perm_b32 v59, v63, v62, s9\n"
"v_perm_b32 v62, v63, v62, s10\n"
"v_alignbit_b32 v62, v62, v62, 28\n"
"v_bfi_b32 v60, s8, v61, v62\n"
"v_bfi_b32 v61, s8, v62, v61\n"
"v_pk_mad_u16 v62, v144, 2, -1 op_sel_hi:[1,0,0]\n"
"v_pk_mad_u16 v63, v145, 2, -1 op_sel_hi:[1,0,0]\n"
"v_pk_min_u16 v62, v62, v63\n"
"v_pk_mad_u16 v63, v146, 2, -1 op_sel_hi:[1,0,0]\n"
"v_pk_min_u16 v62, v62, v63\n"
"v_pk_mad_u16 v63, v147, 2, -1 op_sel_hi:[1,0,0]\n"
"v_pk_min_u16 v62, v62, v63\n"
"v_pk_sub_u16 v62, s16, v62 clamp\n"
"v_or_b32 v61, v61, v62\n"
"v_cmp_ne_u32 vcc, 0, v61\n"
"s_cbranch_vccnz Lx_59\n"
"Lb_60:\n"
"v_accvgpr_write_b32 a51, v58\n"
"v_accvgpr_write_b32 a52, v59\n"
"v_accvgpr_write_b32 a53, v60\n"
"s_add_u32 s52, s53, 0x173000\n"
"s_and_b32 s52, s52, s54\n"
"buffer_load_dwordx4 v[140:143], v1, s[28:31], s52 offen offset:0 nt\n"
"v_pk_sub_u16 v61, v124, s14 clamp\n"
"v_pk_sub_u16 v62, v125, s14 clamp\n"
"v_perm_b32 v58, v62, v61, s9\n"
"v_perm_b32 v61, v62, v61, s10\n"
"v_pk_sub_u16 v62, v126, s14 clamp\n"
"v_pk_sub_u16 v63, v127, s14 clamp\n"
"v_perm_b32 v59, v63, v62, s9\n"
"v_perm_b32 v62, v63, v62, s10\n"
"v_alignbit_b32 v62, v62, v62, 28\n"
"v_bfi_b32 v60, s8, v61, v62\n"
"v_bfi_b32 v61, s8, v62, v61\n"
"v_pk_mad_u16 v62, v124, 2, -1 op_sel_hi:[1,0,0]\n"
"v_pk_mad_u16 v63, v125, 2, -1 op_sel_hi:[1,0,0]\n"
"v_pk_min_u16 v62, v62, v63\n"
"v_pk_mad_u16 v63, v126, 2, -1 op_sel_hi:[1,0,0]\n"
"v_pk_min_u16 v62, v62, v63\n"
"v_pk_mad_u16 v63, v127, 2, -1 op_sel_hi:[1,0,0]\n"
"v_pk_min_u16 v62, v62, v63\n"
"v_pk_sub_u16 v62, s16, v62 clamp\n"
"v_or_b32 v61, v61, v62\n"
"v_cmp_ne_u32 vcc, 0, v61\n"
"s_cbranch_vccnz Lx_61\n"
"Lb_62:\n"
"v_accvgpr_write_b32 a54, v58\n"
"v_accvgpr_write_b32 a55, v59\n"
"v_accvgpr_write_b32 a56, v60\n"
"s_add_u32 s52, s53, 0x17a000\n"
"s_and_b32 s52, s52, s54\n"
"buffer_load_dwordx4 v[144:147], v1, s[28:31], s52 offen offset:0 nt\n"
"v_pk_sub_u16 v61, v128, s14 clamp\n"
"v_pk_sub_u16 v62, v129, s14 clamp\n"
"v_perm_b32 v58, v62, v61, s9\n"
"v_perm_b32 v61, v62, v61, s10\n"
"v_pk_sub_u16 v62, v130, s14 clamp\n"
"v_pk_sub_u16 v63, v131, s14 clamp\n"
"v_perm_b32 v59, v63, v62, s9\n"
"v_perm_b32 v62, v63, v62, s10\n"
"v_alignbit_b32 v62, v62, v62, 28\n"
"v_bfi_b32 v60, s8, v61, v62\n"
"v_bfi_b32 v61, s8, v62, v61\n"
"v_pk_mad_u16 v62, v128, 2, -1 op_sel_hi:[1,0,0]\n"
"v_pk_mad_u16 v63, v129, 2, -1 op_sel_hi:[1,0,0]\n"
"v_pk_min_u16 v62, v62, v63\n"
"v_pk_mad_u16 v63, v130, 2, -1 op_sel_hi:[1,0,0]\n"
"v_pk_min_u16 v62, v62, v63\n"
"v_pk_mad_u16 v63, v131, 2, -1 op_sel_hi:[1,0,0]\n"
"v_pk_min_u16 v62, v62, v63\n"
"v_pk_sub_u16 v62, s16, v62 clamp\n"
"v_or_b32 v61, v61, v62\n"
"v_cmp_ne_u32 vcc, 0, v61\n"
"s_cbranch_vccnz Lx_63\n"
"Lb_64:\n"
"v_accvgpr_write_b32 a57, v58\n"
"v_accvgpr_write_b32 a58, v59\n"
"v_accvgpr_write_b32 a59, v60\n"
"Lpace_65:\n"
"s_memrealtime s[96:97]\n"
"s_waitcnt lgkmcnt(0)\n"
"s_sub_u32 s96, s96, s98\n"
"s_cmp_ge_u32 s96, 720\n"
"s_cbranch_scc1 Lpgo_66\n"
"s_sleep 2\n"
"s_branch Lpace_65\n"
"Lpgo_66:\n"
"s_add_u32 s52, s53, 0x181000\n"
"s_and_b32 s52, s52, s54\n"
"buffer_load_dwordx4 v[124:127], v1, s[28:31], s52 offen offset:0 nt\n"
"s_waitcnt vmcnt(37)\n"
"v_pk_sub_u16 v61, v132, s14 clamp\n"
"v_pk_sub_u16 v62, v133, s14 clamp\n"
"v_perm_b32 v58, v62, v61, s9\n"
"v_perm_b32 v61, v62, v61, s10\n"
"v_pk_sub_u16 v62, v134, s14 clamp\n"
"v_pk_sub_u16 v63, v135, s14 clamp\n"
"v_perm_b32 v59, v63, v62, s9\n"
"v_perm_b32 v62, v63, v62, s10\n"
"v_alignbit_b32 v62, v62, v62, 28\n"
"v_bfi_b32 v60, s8, v61, v62\n"
"v_bfi_b32 v61, s8, v62, v61\n"
"v_pk_mad_u16 v62, v132, 2, -1 op_sel_hi:[1,0,0]\n"
"v_pk_mad_u16 v63, v133, 2, -1 op_sel_hi:[1,0,0]\n"
"v_pk_min_u16 v62, v62, v63\n"
"v_pk_mad_u16 v63, v134, 2, -1 op_sel_hi:[1,0,0]\n"
"v_pk_min_u16 v62, v62, v63\n"
"v_pk_mad_u16 v63, v135, 2, -1 op_sel_hi:[1,0,0]\n"
"v_pk_min_u16 v62, v62, v63\n"
"v_pk_sub_u16 v62, s16, v62 clamp\n"
"v_or_b32 v61, v61, v62\n"
"v_cmp_ne_u32 vcc, 0, v61\n"
"s_cbranch_vccnz Lx_67\n"
"Lb_68:\n"
"v_accvgpr_write_b32 a60, v58\n"
"v_accvgpr_write_b32 a61, v59\n"
"v_accvgpr_write_b32 a62, v60\n"
"s_add_u32 s52, s53, 0x188000\n"
"s_and_b32 s52, s52, s54\n"
"buffer_load_dwordx4 v[128:131], v1, s[28:31], s52 offen offset:0 nt\n"
"s_waitcnt vmcnt(37)\n"
"v_pk_sub_u16 v61, v148, s14 clamp\n"
"v_pk_sub_u16 v62, v149, s14 clamp\n"
"v_perm_b32 v58, v62, v61, s9\n"
"v_perm_b32 v61, v62, v61, s10\n"
"v_pk_sub_u16 v62, v150, s14 clamp\n"
"v_pk_sub_u16 v63, v151, s14 clamp\n"
"v_perm_b32 v59, v63, v62, s9\n"
"v_perm_b32 v62, v63, v62, s10\n"
"v_alignbit_b32 v62, v62, v62, 28\n"
"v_bfi_b32 v60, s8, v61, v62\n"
"v_bfi_b32 v61, s8, v62, v61\n"
"v_pk_mad_u16 v62, v148, 2, -1 op_sel_hi:[1,0,0]\n"
"v_pk_mad_u16 v63, v149, 2, -1 op_sel_hi:[1,0,0]\n"
"v_pk_min_u16 v62, v62, v63\n"
"v_pk_mad_u16 v63, v150, 2, -1 op_sel_hi:[1,0,0]\n"
"v_pk_min_u16 v62, v62, v63\n"
"v_pk_mad_u16 v63, v151, 2, -1 op_sel_hi:[1,0,0]\n"
"v_pk_min_u16 v62, v62, v63\n"
"v_pk_sub_u16 v62, s16, v62 clamp\n"
"v_or_b32 v61, v61, v62\n"
"v_cmp_ne_u32 vcc, 0, v61\n"
"s_cbranch_vccnz Lx_69\n"
"Lb_70:\n"
"v_accvgpr_write_b32 a63, v58\n"
"v_accvgpr_write_b32 a64, v59\n"
"v_accvgpr_write_b32 a65, v60\n"
"s_add_u32 s52, s53, 0x18f000\n"
"s_and_b32 s52, s52, s54\n"
"buffer_load_dwordx4 v[132:135], v1, s[28:31], s52 offen offset:0 nt\n"
"s_waitcnt vmcnt(37)\n"
"v_pk_sub_u16 v61, v152, s14 clamp\n"
"v_pk_sub_u16 v62, v153, s14 clamp\n"
"v_perm_b32 v58, v62, v61, s9\n"
"v_perm_b32 v61, v62, v61, s10\n"
"v_pk_sub_u16 v62, v154, s14 clamp\n"
"v_pk_sub_u16 v63, v155, s14 clamp\n"
"v_perm_b32 v59, v63, v62, s9\n"
"v_perm_b32 v62, v63, v62, s10\n"
"v_alignbit_b32 v62, v62, v62, 28\n"
"v_bfi_b32 v60, s8, v61, v62\n"
"v_bfi_b32 v61, s8, v62, v61\n"
"v_pk_mad_u16 v62, v152, 2, -1 op_sel_hi:[1,0,0]\n"
"v_pk_mad_u16 v63, v153, 2, -1 op_sel_hi:[1,0,0]\n"
"v_pk_min_u16 v62, v62, v63\n"
"v_pk_mad_u16 v63, v154, 2, -1 op_sel_hi:[1,0,0]\n"
"v_pk_min_u16 v62, v62, v63\n"
"v_pk_mad_u16 v63, v155, 2, -1 op_sel_hi:[1,0,0]\n"
"v_pk_min_u16 v62, v62, v63\n"
"v_pk_sub_u16 v62, s16, v62 clamp\n"
"v_or_b32 v61, v61, v62\n"
"v_cmp_ne_u32 vcc, 0, v61\n"
"s_cbranch_vccnz Lx_71\n"
"Lb_72:\n"
"v_accvgpr_write_b32 a66, v58\n"
"v_accvgpr_write_b32 a67, v59\n"
"v_accvgpr_write_b32 a68, v60\n"
"s_add_u32 s52, s53, 0x196000\n"
"s_and_b32 s52, s52, s54\n"
"buffer_load_dwordx4 v[148:151], v1, s[28:31], s52 offen offset:0 nt\n"
"s_waitcnt vmcnt(37)\n"
"v_pk_sub_u16 v61, v156, s14 clamp\n"
"v_pk_sub_u16 v62, v157, s14 clamp\n"
"v_perm_b32 v58, v62, v61, s9\n"
"v_perm_b32 v61, v62, v61, s10\n"
"v_pk_sub_u16 v62, v158, s14 clamp\n"
"v_pk_sub_u16 v63, v159, s14 clamp\n"
"v_perm_b32 v59, v63, v62, s9\n"
"v_perm_b32 v62, v63, v62, s10\n"
"v_alignbit_b32 v62, v62, v62, 28\n"
"v_bfi_b32 v60, s8, v61, v62\n"
"v_bfi_b32 v61, s8, v62, v61\n"
"v_pk_mad_u16 v62, v156, 2, -1 op_sel_hi:[1,0,0]\n"
"v_pk_mad_u16 v63, v157, 2, -1 op_sel_hi:[1,0,0]\n"
"v_pk_min_u16 v62, v62, v63\n"
"v_pk_mad_u16 v63, v158, 2, -1 op_sel_hi:[1,0,0]\n"
"v_pk_min_u16 v62, v62, v63\n"
"v_pk_mad_u16 v63, v159, 2, -1 op_sel_hi:[1,0,0]\n"
"v_pk_min_u16 v62, v62, v63\n"
"v_pk_sub_u16 v62, s16, v62 clamp\n"
"v_or_b32 v61, v61, v62\n"
"v_cmp_ne_u32 vcc, 0, v61\n"
"s_cbranch_vccnz Lx_73\n"
"Lb_74:\n"
"v_accvgpr_write_b32 a69, v58\n"
"v_accvgpr_write_b32 a70, v59\n"
"v_accvgpr_write_b32 a71, v60\n"
"Lpace_75:\n"
"s_memrealtime s[96:97]\n"
"s_waitcnt lgkmcnt(0)\n"
"s_sub_u32 s96, s96, s98\n"
"s_cmp_ge_u32 s96, 777\n"
"s_cbranch_scc1 Lpgo_76\n"
"s_sleep 2\n"
"s_branch Lpace_75\n"
"Lpgo_76:\n"
"s_add_u32 s52, s53, 0x19d000\n"
"s_and_b32 s52, s52, s54\n"
"buffer_load_dwordx4 v[152:155], v1, s[28:31], s52 offen offset:0 nt\n"
"s_waitcnt vmcnt(35)\n"
"v_pk_sub_u16 v61, v160, s14 clamp\n"
"v_pk_sub_u16 v62, v161, s14 clamp\n"
"v_perm_b32 v58, v62, v61, s9\n"
"v_perm_b32 v61, v62, v61, s10\n"
"v_pk_sub_u16 v62, v162, s14 clamp\n"
"v_pk_sub_u16 v63, v163, s14 clamp\n"
"v_perm_b32 v59, v63, v62, s9\n"
"v_perm_b32 v62, v63, v62, s10\n"
"v_alignbit_b32 v62, v62, v62, 28\n"
"v_bfi_b32 v60, s8, v61, v62\n"
"v_bfi_b32 v61, s8, v62, v61\n"
"v_pk_mad_u16 v62, v160, 2, -1 op_sel_hi:[1,0,0]\n"
"v_pk_mad_u16 v63, v161, 2, -1 op_sel_hi:[1,0,0]\n"
"v_pk_min_u16 v62, v62, v63\n"
"v_pk_mad_u16 v63, v162, 2, -1 op_sel_hi:[1,0,0]\n"
"v_pk_min_u16 v62, v62, v63\n"
"v_pk_mad_u16 v63, v163, 2, -1 op_sel_hi:[1,0,0]\n"
"v_pk_min_u16 v62, v62, v63\n"
"v_pk_sub_u16 v62, s16, v62 clamp\n"
"v_or_b32 v61, v61, v62\n"
"v_cmp_ne_u32 vcc, 0, v61\n"
"s_cbranch_vccnz Lx_77\n"
"Lb_78:\n"
"v_accvgpr_write_b32 a72, v58\n"
"v_accvgpr_write_b32 a73, v59\n"
"v_accvgpr_write_b32 a74, v60\n"
"s_add_u32 s52, s53, 0x1a4000\n"
"s_and_b32 s52, s52, s54\n"
"buffer_load_dwordx4 v[156:159], v1, s[28:31], s52 offen offset:0 nt\n"
"s_waitcnt vmcnt(35)\n"
"v_pk_sub_u16 v61, v164, s14 clamp\n"
"v_pk_sub_u16 v62, v165, s14 clamp\n"
"v_perm_b32 v58, v62, v61, s9\n"
"v_perm_b32 v61, v62, v61, s10\n"
"v_pk_sub_u16 v62, v166, s14 clamp\n"
"v_pk_sub_u16 v63, v167, s14 clamp\n"
"v_perm_b32 v59, v63, v62, s9\n"
"v_perm_b32 v62, v63, v62, s10\n"
"v_alignbit_b32 v62, v62, v62, 28\n"
"v_bfi_b32 v60, s8, v61, v62\n"
"v_bfi_b32 v61, s8, v62, v61\n"
"v_pk_mad_u16 v62, v164, 2, -1 op_sel_hi:[1,0,0]\n"
"v_pk_mad_u16 v63, v165, 2, -1 op_sel_hi:[1,0,0]\n"
"v_pk_min_u16 v62, v62, v63\n"
"v_pk_mad_u16 v63, v166, 2, -1 op_sel_hi:[1,0,0]\n"
"v_pk_min_u16 v62, v62, v63\n"
"v_pk_mad_u16 v63, v167, 2, -1 op_sel_hi:[1,0,0]\n"
"v_pk_min_u16 v62, v62, v63\n"
"v_pk_sub_u16 v62, s16, v62 clamp\n"
"v_or_b32 v61, v61, v62\n"
"v_cmp_ne_u32 vcc, 0, v61\n"
"s_cbranch_vccnz Lx_79\n"
"Lb_80:\n"
"v_accvgpr_write_b32 a75, v58\n"
"v_accvgpr_write_b32 a76, v59\n"
"v_accvgpr_write_b32 a77, v60\n"
"s_add_u32 s52, s53, 0x1ab000\n"
"s_and_b32 s52, s52, s54\n"
"buffer_load_dwordx4 v[160:163], v1, s[28:31], s52 offen offset:0 nt\n"
"s_waitcnt vmcnt(35)\n"
"v_pk_sub_u16 v61, v168, s14 clamp\n"
"v_pk_sub_u16 v62, v169, s14 clamp\n"
"v_perm_b32 v58, v62, v61, s9\n"
"v_perm_b32 v61, v62, v61, s10\n"
"v_pk_sub_u16 v62, v170, s14 clamp\n"
"v_pk_sub_u16 v63, v171, s14 clamp\n"
"v_perm_b32 v59, v63, v62, s9\n"
"v_perm_b32 v62, v63, v62, s10\n"
"v_alignbit_b32 v62, v62, v62, 28\n"
"v_bfi_b32 v60, s8, v61, v62\n"
"v_bfi_b32 v61, s8, v62, v61\n"
"v_pk_mad_u16 v62, v168, 2, -1 op_sel_hi:[1,0,0]\n"
"v_pk_mad_u16 v63, v169, 2, -1 op_sel_hi:[1,0,0]\n"
"v_pk_min_u16 v62, v62, v63\n"
"v_pk_mad_u16 v63, v170, 2, -1 op_sel_hi:[1,0,0]\n"
"v_pk_min_u16 v62, v62, v63\n"
"v_pk_mad_u16 v63, v171, 2, -1 op_sel_hi:[1,0,0]\n"
"v_pk_min_u16 v62, v62, v63\n"
"v_pk_sub_u16 v62, s16, v62 clamp\n"
"v_or_b32 v61, v61, v62\n"
"v_cmp_ne_u32 vcc, 0, v61\n"
"s_cbranch_vccnz Lx_81\n"
"Lb_82:\n"
"v_accvgpr_write_b32 a78, v58\n"
"v_accvgpr_write_b32 a79, v59\n"
"v_accvgpr_write_b32 a80, v60\n"
"s_add_u32 s52, s53, 0x1b2000\n"
"s_and_b32 s52, s52, s54\n"
"buffer_load_dwordx4 v[164:167], v1, s[28:31], s52 offen offset:0 nt\n"
"s_waitcnt vmcnt(35)\n"
"v_pk_sub_u16 v61, v184, s14 clamp\n"
"v_pk_sub_u16 v62, v185, s14 clamp\n"
"v_perm_b32 v58, v62, v61, s9\n"
"v_perm_b32 v61, v62, v61, s10\n"
"v_pk_sub_u16 v62, v186, s14 clamp\n"
"v_pk_sub_u16 v63, v187, s14 clamp\n"
"v_perm_b32 v59, v63, v62, s9\n"
"v_perm_b32 v62, v63, v62, s10\n"
"v_alignbit_b32 v62, v62, v62, 28\n"
"v_bfi_b32 v60, s8, v61, v62\n"
"v_bfi_b32 v61, s8, v62, v61\n"
"v_pk_mad_u16 v62, v184, 2, -1 op_sel_hi:[1,0,0]\n"
"v_pk_mad_u16 v63, v185, 2, -1 op_sel_hi:[1,0,0]\n"
"v_pk_min_u16 v62, v62, v63\n"
"v_pk_mad_u16 v63, v186, 2, -1 op_sel_hi:[1,0,0]\n"
"v_pk_min_u16 v62, v62, v63\n"
"v_pk_mad_u16 v63, v187, 2, -1 op_sel_hi:[1,0,0]\n"
"v_pk_min_u16 v62, v62, v63\n"
"v_pk_sub_u16 v62, s16, v62 clamp\n"
"v_or_b32 v61, v61, v62\n"
"v_cmp_ne_u32 vcc, 0, v61\n"
"s_cbranch_vccnz Lx_83\n"
"Lb_84:\n"
"v_accvgpr_write_b32 a81, v58\n"
"v_accvgpr_write_b32 a82, v59\n"
"v_accvgpr_write_b32 a83, v60\n"
"Lpace_85:\n"
"s_memrealtime s[96:97]\n"
"s_waitcnt lgkmcnt(0)\n"
"s_sub_u32 s96, s96, s98\n"
"s_cmp_ge_u32 s96, 833\n"
"s_cbranch_scc1 Lpgo_86\n"
"s_sleep 2\n"
"s_branch Lpace_85\n"
"Lpgo_86:\n"
"s_add_u32 s52, s53, 0x1b9000\n"
"s_and_b32 s52, s52, s54\n"
"buffer_load_dwordx4 v[168:171], v1, s[28:31], s52 offen offset:0 nt\n"
"s_waitcnt vmcnt(35)\n"
"v_pk_sub_u16 v61, v188, s14 clamp\n"
"v_pk_sub_u16 v62, v189, s14 clamp\n"
"v_perm_b32 v58, v62, v61, s9\n"
"v_perm_b32 v61, v62, v61, s10\n"
"v_pk_sub_u16 v62, v190, s14 clamp\n"
"v_pk_sub_u16 v63, v191, s14 clamp\n"
"v_perm_b32 v59, v63, v62, s9\n"
"v_perm_b32 v62, v63, v62, s10\n"
"v_alignbit_b32 v62, v62, v62, 28\n"
"v_bfi_b32 v60, s8, v61, v62\n"
"v_bfi_b32 v61, s8, v62, v61\n"
"v_pk_mad_u16 v62, v188, 2, -1 op_sel_hi:[1,0,0]\n"
"v_pk_mad_u16 v63, v189, 2, -1 op_sel_hi:[1,0,0]\n"
"v_pk_min_u16 v62, v62, v63\n"
"v_pk_mad_u16 v63, v190, 2, -1 op_sel_hi:[1,0,0]\n"
"v_pk_min_u16 v62, v62, v63\n"
"v_pk_mad_u16 v63, v191, 2, -1 op_sel_hi:[1,0,0]\n"
"v_pk_min_u16 v62, v62, v63\n"
"v_pk_sub_u16 v62, s16, v62 clamp\n"
"v_or_b32 v61, v61, v62\n"
"v_cmp_ne_u32 vcc, 0, v61\n"
"s_cbranch_vccnz Lx_87\n"
"Lb_88:\n"
"v_accvgpr_write_b32 a84, v58\n"
"v_accvgpr_write_b32 a85, v59\n"
"v_accvgpr_write_b32 a86, v60\n"
"s_add_u32 s52, s53, 0x1c0000\n"
"s_and_b32 s52, s52, s54\n"
"buffer_load_dwordx4 v[184:187], v1, s[28:31], s52 offen offset:0 nt\n"
"s_waitcnt vmcnt(35)\n"
"v_pk_sub_u16 v61, v192, s14 clamp\n"
"v_pk_sub_u16 v62, v193, s14 clamp\n"
"v_perm_b32 v58, v62, v61, s9\n"
"v_perm_b32 v61, v62, v61, s10\n"
"v_pk_sub_u16 v62, v194, s14 clamp\n"
"v_pk_sub_u16 v63, v195, s14 clamp\n"
"v_perm_b32 v59, v63, v62, s9\n"
"v_perm_b32 v62, v63, v62, s10\n"
"v_alignbit_b32 v62, v62, v62, 28\n"
"v_bfi_b32 v60, s8, v61, v62\n"
"v_bfi_b32 v61, s8, v62, v61\n"
"v_pk_mad_u16 v62, v192, 2, -1 op_sel_hi:[1,0,0]\n"
"v_pk_mad_u16 v63, v193, 2, -1 op_sel_hi:[1,0,0]\n"
"v_pk_min_u16 v62, v62, v63\n"
"v_pk_mad_u16 v63, v194, 2, -1 op_sel_hi:[1,0,0]\n"
"v_pk_min_u16 v62, v62, v63\n"
"v_pk_mad_u16 v63, v195, 2, -1 op_sel_hi:[1,0,0]\n"
"v_pk_min_u16 v62, v62, v63\n"
"v_pk_sub_u16 v62, s16, v62 clamp\n"
"v_or_b32 v61, v61, v62\n"
"v_cmp_ne_u32 vcc, 0, v61\n"
"s_cbranch_vccnz Lx_89\n"
"Lb_90:\n"
"v_accvgpr_write_b32 a87, v58\n"
"v_accvgpr_write_b32 a88, v59\n"
"v_accvgpr_write_b32 a89, v60\n"
"s_add_u32 s52, s53, 0x1c7000\n"
"s_and_b32 s52, s52, s54\n"
"buffer_load_dwordx4 v[188:191], v1, s[28:31], s52 offen offset:0 nt\n"
"s_waitcnt vmcnt(35)\n"
"v_pk_sub_u16 v61, v208, s14 clamp\n"
"v_pk_sub_u16 v62, v209, s14 clamp\n"
"v_perm_b32 v58, v62, v61, s9\n"
"v_perm_b32 v61, v62, v61, s10\n"
"v_pk_sub_u16 v62, v210, s14 clamp\n"
"v_pk_sub_u16 v63, v211, s14 clamp\n"
"v_perm_b32 v59, v63, v62, s9\n"
"v_perm_b32 v62, v63, v62, s10\n"
"v_alignbit_b32 v62, v62, v62, 28\n"
"v_bfi_b32 v60, s8, v61, v62\n"
"v_bfi_b32 v61, s8, v62, v61\n"
"v_pk_mad_u16 v62, v208, 2, -1 op_sel_hi:[1,0,0]\n"
"v_pk_mad_u16 v63, v209, 2, -1 op_sel_hi:[1,0,0]\n"
"v_pk_min_u16 v62, v62, v63\n"
"v_pk_mad_u16 v63, v210, 2, -1 op_sel_hi:[1,0,0]\n"
"v_pk_min_u16 v62, v62, v63\n"
"v_pk_mad_u16 v63, v211, 2, -1 op_sel_hi:[1,0,0]\n"
"v_pk_min_u16 v62, v62, v63\n"
"v_pk_sub_u16 v62, s16, v62 clamp\n"
"v_or_b32 v61, v61, v62\n"
"v_cmp_ne_u32 vcc, 0, v61\n"
"s_cbranch_vccnz Lx_91\n"
"Lb_92:\n"
"v_accvgpr_write_b32 a90, v58\n"
"v_accvgpr_write_b32 a91, v59\n"
"v_accvgpr_write_b32 a92, v60\n"
"s_add_u32 s52, s53, 0x1ce000\n"
"s_and_b32 s52, s52, s54\n"
"buffer_load_dwordx4 v[192:195], v1, s[28:31], s52 offen offset:0 nt\n"
"s_waitcnt vmcnt(35)\n"
"v_pk_sub_u16 v61, v212, s14 clamp\n"
"v_pk_sub_u16 v62, v213, s14 clamp\n"
"v_perm_b32 v58, v62, v61, s9\n"
"v_perm_b32 v61, v62, v61, s10\n"
"v_pk_sub_u16 v62, v214, s14 clamp\n"
"v_pk_sub_u16 v63, v215, s14 clamp\n"
"v_perm_b32 v59, v63, v62, s9\n"
"v_perm_b32 v62, v63, v62, s10\n"
"v_alignbit_b32 v62, v62, v62, 28\n"
"v_bfi_b32 v60, s8, v61, v62\n"
"v_bfi_b32 v61, s8, v62, v61\n"
"v_pk_mad_u16 v62, v212, 2, -1 op_sel_hi:[1,0,0]\n"
"v_pk_mad_u16 v63, v213, 2, -1 op_sel_hi:[1,0,0]\n"
"v_pk_min_u16 v62, v62, v63\n"
"v_pk_mad_u16 v63, v214, 2, -1 op_sel_hi:[1,0,0]\n"
"v_pk_min_u16 v62, v62, v63\n"
"v_pk_mad_u16 v63, v215, 2, -1 op_sel_hi:[1,0,0]\n"
"v_pk_min_u16 v62, v62, v63\n"
"v_pk_sub_u16 v62, s16, v62 clamp\n"
"v_or_b32 v61, v61, v62\n"
"v_cmp_ne_u32 vcc, 0, v61\n"
"s_cbranch_vccnz Lx_93\n"
"Lb_94:\n"
"v_accvgpr_write_b32 a93, v58\n"
"v_accvgpr_write_b32 a94, v59\n"
"v_accvgpr_write_b32 a95, v60\n"
"Lpace_95:\n"
"s_memrealtime s[96:97]\n"
"s_waitcnt lgkmcnt(0)\n"
"s_sub_u32 s96, s96, s98\n"
"s_cmp_ge_u32 s96, 890\n"
"s_cbranch_scc1 Lpgo_96\n"
"s_sleep 2\n"
"s_branch Lpace_95\n"
"Lpgo_96:\n"
"s_add_u32 s52, s53, 0x1d5000\n"
"s_and_b32 s52, s52, s54\n"
"buffer_load_dwordx4 v[208:211], v1, s[28:31], s52 offen offset:0 nt\n"
"s_waitcnt vmcnt(35)\n"
"v_pk_sub_u16 v61, v216, s14 clamp\n"
"v_pk_sub_u16 v62, v217, s14 clamp\n"
"v_perm_b32 v58, v62, v61, s9\n"
"v_perm_b32 v61, v62, v61, s10\n"
"v_pk_sub_u16 v62, v218, s14 clamp\n"
"v_pk_sub_u16 v63, v219, s14 clamp\n"
"v_perm_b32 v59, v63, v62, s9\n"
"v_perm_b32 v62, v63, v62, s10\n"
"v_alignbit_b32 v62, v62, v62, 28\n"
"v_bfi_b32 v60, s8, v61, v62\n"
"v_bfi_b32 v61, s8, v62, v61\n"
"v_pk_mad_u16 v62, v216, 2, -1 op_sel_hi:[1,0,0]\n"
"v_pk_mad_u16 v63, v217, 2, -1 op_sel_hi:[1,0,0]\n"
"v_pk_min_u16 v62, v62, v63\n"
"v_pk_mad_u16 v63, v218, 2, -1 op_sel_hi:[1,0,0]\n"
"v_pk_min_u16 v62, v62, v63\n"
"v_pk_mad_u16 v63, v219, 2, -1 op_sel_hi:[1,0,0]\n"
"v_pk_min_u16 v62, v62, v63\n"
"v_pk_sub_u16 v62, s16, v62 clamp\n"
"v_or_b32 v61, v61, v62\n"
"v_cmp_ne_u32 vcc, 0, v61\n"
"s_cbranch_vccnz Lx_97\n"
"Lb_98:\n"
"v_accvgpr_write_b32 a96, v58\n"
"v_accvgpr_write_b32 a97, v59\n"
"v_accvgpr_write_b32 a98, v60\n"
"s_add_u32 s52, s53, 0x1dc000\n"
"s_and_b32 s52, s52, s54\n"
"buffer_load_dwordx4 v[212:215], v1, s[28:31], s52 offen offset:0 nt\n"
"s_waitcnt vmcnt(35)\n"
"v_pk_sub_u16 v61, v232, s14 clamp\n"
"v_pk_sub_u16 v62, v233, s14 clamp\n"
"v_perm_b32 v58, v62, v61, s9\n"
"v_perm_b32 v61, v62, v61, s10\n"
"v_pk_sub_u16 v62, v234, s14 clamp\n"
"v_pk_sub_u16 v63, v235, s14 clamp\n"
"v_perm_b32 v59, v63, v62, s9\n"
"v_perm_b32 v62, v63, v62, s10\n"
"v_alignbit_b32 v62, v62, v62, 28\n"
"v_bfi_b32 v60, s8, v61, v62\n"
"v_bfi_b32 v61, s8, v62, v61\n"
"v_pk_mad_u16 v62, v232, 2, -1 op_sel_hi:[1,0,0]\n"
"v_pk_mad_u16 v63, v233, 2, -1 op_sel_hi:[1,0,0]\n"
"v_pk_min_u16 v62, v62, v63\n"
"v_pk_mad_u16 v63, v234, 2, -1 op_sel_hi:[1,0,0]\n"
"v_pk_min_u16 v62, v62, v63\n"
"v_pk_mad_u16 v63, v235, 2, -1 op_sel_hi:[1,0,0]\n"
"v_pk_min_u16 v62, v62, v63\n"
"v_pk_sub_u16 v62, s16, v62 clamp\n"
"v_or_b32 v61, v61, v62\n"
"v_cmp_ne_u32 vcc, 0, v61\n"
"s_cbranch_vccnz Lx_99\n"
"Lb_100:\n"
"v_accvgpr_write_b32 a99, v58\n"
"v_accvgpr_write_b32 a100, v59\n"
"v_accvgpr_write_b32 a101, v60\n"
"s_add_u32 s52, s53, 0x1e3000\n"
"s_and_b32 s52, s52, s54\n"
"buffer_load_dwordx4 v[216:219], v1, s[28:31], s52 offen offset:0 nt\n"
"s_waitcnt vmcnt(35)\n"
"v_pk_sub_u16 v61, v236, s14 clamp\n"
"v_pk_sub_u16 v62, v237, s14 clamp\n"
"v_perm_b32 v58, v62, v61, s9\n"
"v_perm_b32 v61, v62, v61, s10\n"
"v_pk_sub_u16 v62, v238, s14 clamp\n"
"v_pk_sub_u16 v63, v239, s14 clamp\n"
"v_perm_b32 v59, v63, v62, s9\n"
"v_perm_b32 v62, v63, v62, s10\n"
"v_alignbit_b32 v62, v62, v62, 28\n"
"v_bfi_b32 v60, s8, v61, v62\n"
"v_bfi_b32 v61, s8, v62, v61\n"
"v_pk_mad_u16 v62, v236, 2, -1 op_sel_hi:[1,0,0]\n"
"v_pk_mad_u16 v63, v237, 2, -1 op_sel_hi:[1,0,0]\n"
"v_pk_min_u16 v62, v62, v63\n"
"v_pk_mad_u16 v63, v238, 2, -1 op_sel_hi:[1,0,0]\n"
"v_pk_min_u16 v62, v62, v63\n"
"v_pk_mad_u16 v63, v239, 2, -1 op_sel_hi:[1,0,0]\n"
"v_pk_min_u16 v62, v62, v63\n"
"v_pk_sub_u16 v62, s16, v62 clamp\n"
"v_or_b32 v61, v61, v62\n"
"v_cmp_ne_u32 vcc, 0, v61\n"
"s_cbranch_vccnz Lx_101\n"
"Lb_102:\n"
"v_accvgpr_write_b32 a102, v58\n"
"v_accvgpr_write_b32 a103, v59\n"
"v_accvgpr_write_b32 a104, v60\n"
"s_add_u32 s52, s53, 0x1ea000\n"
"s_and_b32 s52, s52, s54\n"
"buffer_load_dwordx4 v[232:235], v1, s[28:31], s52 offen offset:0 nt\n"
"s_waitcnt vmcnt(35)\n"
"v_pk_sub_u16 v61, v240, s14 clamp\n"
"v_pk_sub_u16 v62, v241, s14 clamp\n"
"v_perm_b32 v58, v62, v61, s9\n"
"v_perm_b32 v61, v62, v61, s10\n"
"v_pk_sub_u16 v62, v242, s14 clamp\n"
"v_pk_sub_u16 v63, v243, s14 clamp\n"
"v_perm_b32 v59, v63, v62, s9\n"
"v_perm_b32 v62, v63, v62, s10\n"
"v_alignbit_b32 v62, v62, v62, 28\n"
"v_bfi_b32 v60, s8, v61, v62\n"
"v_bfi_b32 v61, s8, v62, v61\n"
"v_pk_mad_u16 v62, v240, 2, -1 op_sel_hi:[1,0,0]\n"
"v_pk_mad_u16 v63, v241, 2, -1 op_sel_hi:[1,0,0]\n"
"v_pk_min_u16 v62, v62, v63\n"
"v_pk_mad_u16 v63, v242, 2, -1 op_sel_hi:[1,0,0]\n"
"v_pk_min_u16 v62, v62, v63\n"
"v_pk_mad_u16 v63, v243, 2, -1 op_sel_hi:[1,0,0]\n"
"v_pk_min_u16 v62, v62, v63\n"
"v_pk_sub_u16 v62, s16, v62 clamp\n"
"v_or_b32 v61, v61, v62\n"
"v_cmp_ne_u32 vcc, 0, v61\n"
"s_cbranch_vccnz Lx_103\n"
"Lb_104:\n"
"v_accvgpr_write_b32 a105, v58\n"
"v_accvgpr_write_b32 a106, v59\n"
"v_accvgpr_write_b32 a107, v60\n"
"Lpace_105:\n"
"s_memrealtime s[96:97]\n"
"s_waitcnt lgkmcnt(0)\n"
"s_sub_u32 s96, s96, s98\n"
"s_cmp_ge_u32 s96, 947\n"
"s_cbranch_scc1 Lpgo_106\n"
"s_sleep 2\n"
"s_branch Lpace_105\n"
"Lpgo_106:\n"
"s_add_u32 s52, s53, 0x1f1000\n"
"s_and_b32 s52, s52, s54\n"
"buffer_load_dwordx4 v[236:239], v1, s[28:31], s52 offen offset:0 nt\n"
"s_waitcnt vmcnt(35)\n"
"v_pk_sub_u16 v61, v64, s14 clamp\n"
"v_pk_sub_u16 v62, v65, s14 clamp\n"
"v_perm_b32 v58, v62, v61, s9\n"
"v_perm_b32 v61, v62, v61, s10\n"
"v_pk_sub_u16 v62, v66, s14 clamp\n"
"v_pk_sub_u16 v63, v67, s14 clamp\n"
"v_perm_b32 v59, v63, v62, s9\n"
"v_perm_b32 v62, v63, v62, s10\n"
"v_alignbit_b32 v62, v62, v62, 28\n"
"v_bfi_b32 v60, s8, v61, v62\n"
"v_bfi_b32 v61, s8, v62, v61\n"
"v_pk_mad_u16 v62, v64, 2, -1 op_sel_hi:[1,0,0]\n"
"v_pk_mad_u16 v63, v65, 2, -1 op_sel_hi:[1,0,0]\n"
"v_pk_min_u16 v62, v62, v63\n"
"v_pk_mad_u16 v63, v66, 2, -1 op_sel_hi:[1,0,0]\n"
"v_pk_min_u16 v62, v62, v63\n"
"v_pk_mad_u16 v63, v67, 2, -1 op_sel_hi:[1,0,0]\n"
"v_pk_min_u16 v62, v62, v63\n"
"v_pk_sub_u16 v62, s16, v62 clamp\n"
"v_or_b32 v61, v61, v62\n"
"v_cmp_ne_u32 vcc, 0, v61\n"
"s_cbranch_vccnz Lx_107\n"
"Lb_108:\n"
"v_accvgpr_write_b32 a108, v58\n"
"v_accvgpr_write_b32 a109, v59\n"
"v_accvgpr_write_b32 a110, v60\n"
"s_add_u32 s52, s53, 0x1f8000\n"
"s_and_b32 s52, s52, s54\n"
"buffer_load_dwordx4 v[240:243], v1, s[28:31], s52 offen offset:0 nt\n"
"s_waitcnt vmcnt(35)\n"
"v_pk_sub_u16 v61, v172, s14 clamp\n"
"v_pk_sub_u16 v62, v173, s14 clamp\n"
"v_perm_b32 v58, v62, v61, s9\n"
"v_perm_b32 v61, v62, v61, s10\n"
"v_pk_sub_u16 v62, v174, s14 clamp\n"
"v_pk_sub_u16 v63, v175, s14 clamp\n"
"v_perm_b32 v59, v63, v62, s9\n"
"v_perm_b32 v62, v63, v62, s10\n"
"v_alignbit_b32 v62, v62, v62, 28\n"
"v_bfi_b32 v60, s8, v61, v62\n"
"v_bfi_b32 v61, s8, v62, v61\n"
"v_pk_mad_u16 v62, v172, 2, -1 op_sel_hi:[1,0,0]\n"
"v_pk_mad_u16 v63, v173, 2, -1 op_sel_hi:[1,0,0]\n"
"v_pk_min_u16 v62, v62, v63\n"
"v_pk_mad_u16 v63, v174, 2, -1 op_sel_hi:[1,0,0]\n"
"v_pk_min_u16 v62, v62, v63\n"
"v_pk_mad_u16 v63, v175, 2, -1 op_sel_hi:[1,0,0]\n"
"v_pk_min_u16 v62, v62, v63\n"
"v_pk_sub_u16 v62, s16, v62 clamp\n"
"v_or_b32 v61, v61, v62\n"
"v_cmp_ne_u32 vcc, 0, v61\n"
"s_cbranch_vccnz Lx_109\n"
"Lb_110:\n"
"v_accvgpr_write_b32 a111, v58\n"
"v_accvgpr_write_b32 a112, v59\n"
"v_accvgpr_write_b32 a113, v60\n"
"s_add_u32 s52, s53, 0x1ff000\n"
"s_and_b32 s52, s52, s54\n"
"buffer_load_dwordx4 v[172:175], v1, s[28:31], s52 offen offset:0 nt\n"
"s_waitcnt vmcnt(35)\n"
"v_pk_sub_u16 v61, v68, s14 clamp\n"
"v_pk_sub_u16 v62, v69, s14 clamp\n"
"v_perm_b32 v58, v62, v61, s9\n"
"v_perm_b32 v61, v62, v61, s10\n"
"v_pk_sub_u16 v62, v70, s14 clamp\n"
"v_pk_sub_u16 v63, v71, s14 clamp\n"
"v_perm_b32 v59, v63, v62, s9\n"
"v_perm_b32 v62, v63, v62, s10\n"
"v_alignbit_b32 v62, v62, v62, 28\n"
"v_bfi_b32 v60, s8, v61, v62\n"
"v_bfi_b32 v61, s8, v62, v61\n"
"v_pk_mad_u16 v62, v68, 2, -1 op_sel_hi:[1,0,0]\n"
"v_pk_mad_u16 v63, v69, 2, -1 op_sel_hi:[1,0,0]\n"
"v_pk_min_u16 v62, v62, v63\n"
"v_pk_mad_u16 v63, v70, 2, -1 op_sel_hi:[1,0,0]\n"
"v_pk_min_u16 v62, v62, v63\n"
"v_pk_mad_u16 v63, v71, 2, -1 op_sel_hi:[1,0,0]\n"
"v_pk_min_u16 v62, v62, v63\n"
"v_pk_sub_u16 v62, s16, v62 clamp\n"
"v_or_b32 v61, v61, v62\n"
"v_cmp_ne_u32 vcc, 0, v61\n"
"s_cbranch_vccnz Lx_111\n"
"Lb_112:\n"
"v_accvgpr_write_b32 a114, v58\n"
"v_accvgpr_write_b32 a115, v59\n"
"v_accvgpr_write_b32 a116, v60\n"
"s_add_u32 s52, s53, 0x206000\n"
"s_and_b32 s52, s52, s54\n"
"buffer_load_dwordx4 v[176:179], v1, s[28:31], s52 offen offset:0 nt\n"
"s_waitcnt vmcnt(35)\n"
"v_pk_sub_u16 v61, v72, s14 clamp\n"
"v_pk_sub_u16 v62, v73, s14 clamp\n"
"v_perm_b32 v58, v62, v61, s9\n"
"v_perm_b32 v61, v62, v61, s10\n"
"v_pk_sub_u16 v62, v74, s14 clamp\n"
"v_pk_sub_u16 v63, v75, s14 clamp\n"
"v_perm_b32 v59, v63, v62, s9\n"
"v_perm_b32 v62, v63, v62, s10\n"
"v_alignbit_b32 v62, v62, v62, 28\n"
"v_bfi_b32 v60, s8, v61, v62\n"
"v_bfi_b32 v61, s8, v62, v61\n"
"v_pk_mad_u16 v62, v72, 2, -1 op_sel_hi:[1,0,0]\n"
"v_pk_mad_u16 v63, v73, 2, -1 op_sel_hi:[1,0,0]\n"
"v_pk_min_u16 v62, v62, v63\n"
"v_pk_mad_u16 v63, v74, 2, -1 op_sel_hi:[1,0,0]\n"
"v_pk_min_u16 v62, v62, v63\n"
"v_pk_mad_u16 v63, v75, 2, -1 op_sel_hi:[1,0,0]\n"
"v_pk_min_u16 v62, v62, v63\n"
"v_pk_sub_u16 v62, s16, v62 clamp\n"
"v_or_b32 v61, v61, v62\n"
"v_cmp_ne_u32 vcc, 0, v61\n"
"s_cbranch_vccnz Lx_113\n"
"Lb_114:\n"
"v_accvgpr_write_b32 a117, v58\n"
"v_accvgpr_write_b32 a118, v59\n"
"v_accvgpr_write_b32 a119, v60\n"
"Lpace_115:\n"
"s_memrealtime s[96:97]\n"
"s_waitcnt lgkmcnt(0)\n"
"s_sub_u32 s96, s96, s98\n"
"s_cmp_ge_u32 s96, 1004\n"
"s_cbranch_scc1 Lpgo_116\n"
"s_sleep 2\n"
"s_branch Lpace_115\n"
"Lpgo_116:\n"
"s_add_u32 s52, s53, 0x20d000\n"
"s_and_b32 s52, s52, s54\n"
"buffer_load_dwordx4 v[180:183], v1, s[28:31], s52 offen offset:0 nt\n"
"s_waitcnt vmcnt(35)\n"
"v_pk_sub_u16 v61, v76, s14 clamp\n"
"v_pk_sub_u16 v62, v77, s14 clamp\n"
"v_perm_b32 v58, v62, v61, s9\n"
"v_perm_b32 v61, v62, v61, s10\n"
"v_pk_sub_u16 v62, v78, s14 clamp\n"
"v_pk_sub_u16 v63, v79, s14 clamp\n"
"v_perm_b32 v59, v63, v62, s9\n"
"v_perm_b32 v62, v63, v62, s10\n"
"v_alignbit_b32 v62, v62, v62, 28\n"
"v_bfi_b32 v60, s8, v61, v62\n"
"v_bfi_b32 v61, s8, v62, v61\n"
"v_pk_mad_u16 v62, v76, 2, -1 op_sel_hi:[1,0,0]\n"
"v_pk_mad_u16 v63, v77, 2, -1 op_sel_hi:[1,0,0]\n"
"v_pk_min_u16 v62, v62, v63\n"
"v_pk_mad_u16 v63, v78, 2, -1 op_sel_hi:[1,0,0]\n"
"v_pk_min_u16 v62, v62, v63\n"
"v_pk_mad_u16 v63, v79, 2, -1 op_sel_hi:[1,0,0]\n"
"v_pk_min_u16 v62, v62, v63\n"
"v_pk_sub_u16 v62, s16, v62 clamp\n"
"v_or_b32 v61, v61, v62\n"
"v_cmp_ne_u32 vcc, 0, v61\n"
"s_cbranch_vccnz Lx_117\n"
"Lb_118:\n"
"v_accvgpr_write_b32 a120, v58\n"
"v_accvgpr_write_b32 a121, v59\n"
"v_accvgpr_write_b32 a122, v60\n"
"s_add_u32 s52, s53, 0x214000\n"
"s_and_b32 s52, s52, s54\n"
"buffer_load_dwordx4 v[196:199], v1, s[28:31], s52 offen offset:0 nt\n"
"s_waitcnt vmcnt(35)\n"
"v_pk_sub_u16 v61, v80, s14 clamp\n"
"v_pk_sub_u16 v62, v81, s14 clamp\n"
"v_perm_b32 v58, v62, v61, s9\n"
"v_perm_b32 v61, v62, v61, s10\n"
"v_pk_sub_u16 v62, v82, s14 clamp\n"
"v_pk_sub_u16 v63, v83, s14 clamp\n"
"v_perm_b32 v59, v63, v62, s9\n"
"v_perm_b32 v62, v63, v62, s10\n"
"v_alignbit_b32 v62, v62, v62, 28\n"
"v_bfi_b32 v60, s8, v61, v62\n"
"v_bfi_b32 v61, s8, v62, v61\n"
"v_pk_mad_u16 v62, v80, 2, -1 op_sel_hi:[1,0,0]\n"
"v_pk_mad_u16 v63, v81, 2, -1 op_sel_hi:[1,0,0]\n"
"v_pk_min_u16 v62, v62, v63\n"
"v_pk_mad_u16 v63, v82, 2, -1 op_sel_hi:[1,0,0]\n"
"v_pk_min_u16 v62, v62, v63\n"
"v_pk_mad_u16 v63, v83, 2, -1 op_sel_hi:[1,0,0]\n"
"v_pk_min_u16 v62, v62, v63\n"
"v_pk_sub_u16 v62, s16, v62 clamp\n"
"v_or_b32 v61, v61, v62\n"
"v_cmp_ne_u32 vcc, 0, v61\n"
"s_cbranch_vccnz Lx_119\n"
"Lb_120:\n"
"v_accvgpr_write_b32 a123, v58\n"
"v_accvgpr_write_b32 a124, v59\n"
"v_accvgpr_write_b32 a125, v60\n"
"s_add_u32 s52, s53, 0x21b000\n"
"s_and_b32 s52, s52, s54\n"
"buffer_load_dwordx4 v[200:203], v1, s[28:31], s52 offen offset:0 nt\n"
"s_waitcnt vmcnt(35)\n"
"v_pk_sub_u16 v61, v84, s14 clamp\n"
"v_pk_sub_u16 v62, v85, s14 clamp\n"
"v_perm_b32 v58, v62, v61, s9\n"
"v_perm_b32 v61, v62, v61, s10\n"
"v_pk_sub_u16 v62, v86, s14 clamp\n"
"v_pk_sub_u16 v63, v87, s14 clamp\n"
"v_perm_b32 v59, v63, v62, s9\n"
"v_perm_b32 v62, v63, v62, s10\n"
"v_alignbit_b32 v62, v62, v62, 28\n"
"v_bfi_b32 v60, s8, v61, v62\n"
"v_bfi_b32 v61, s8, v62, v61\n"
"v_pk_mad_u16 v62, v84, 2, -1 op_sel_hi:[1,0,0]\n"
"v_pk_mad_u16 v63, v85, 2, -1 op_sel_hi:[1,0,0]\n"
"v_pk_min_u16 v62, v62, v63\n"
"v_pk_mad_u16 v63, v86, 2, -1 op_sel_hi:[1,0,0]\n"
"v_pk_min_u16 v62, v62, v63\n"
"v_pk_mad_u16 v63, v87, 2, -1 op_sel_hi:[1,0,0]\n"
"v_pk_min_u16 v62, v62, v63\n"
"v_pk_sub_u16 v62, s16, v62 clamp\n"
"v_or_b32 v61, v61, v62\n"
"v_cmp_ne_u32 vcc, 0, v61\n"
"s_cbranch_vccnz Lx_121\n"
"Lb_122:\n"
"v_accvgpr_write_b32 a126, v58\n"
"v_accvgpr_write_b32 a127, v59\n"
"v_accvgpr_write_b32 a128, v60\n"
"s_add_u32 s52, s53, 0x222000\n"
"s_and_b32 s52, s52, s54\n"
"buffer_load_dwordx4 v[204:207], v1, s[28:31], s52 offen offset:0 nt\n"
"s_waitcnt vmcnt(35)\n"
"v_pk_sub_u16 v61, v88, s14 clamp\n"
"v_pk_sub_u16 v62, v89, s14 clamp\n"
"v_perm_b32 v58, v62, v61, s9\n"
"v_perm_b32 v61, v62, v61, s10\n"
"v_pk_sub_u16 v62, v90, s14 clamp\n"
"v_pk_sub_u16 v63, v91, s14 clamp\n"
"v_perm_b32 v59, v63, v62, s9\n"
"v_perm_b32 v62, v63, v62, s10\n"
"v_alignbit_b32 v62, v62, v62, 28\n"
"v_bfi_b32 v60, s8, v61, v62\n"
"v_bfi_b32 v61, s8, v62, v61\n"
"v_pk_mad_u16 v62, v88, 2, -1 op_sel_hi:[1,0,0]\n"
"v_pk_mad_u16 v63, v89, 2, -1 op_sel_hi:[1,0,0]\n"
"v_pk_min_u16 v62, v62, v63\n"
"v_pk_mad_u16 v63, v90, 2, -1 op_sel_hi:[1,0,0]\n"
"v_pk_min_u16 v62, v62, v63\n"
"v_pk_mad_u16 v63, v91, 2, -1 op_sel_hi:[1,0,0]\n"
"v_pk_min_u16 v62, v62, v63\n"
"v_pk_sub_u16 v62, s16, v62 clamp\n"
"v_or_b32 v61, v61, v62\n"
"v_cmp_ne_u32 vcc, 0, v61\n"
"s_cbranch_vccnz Lx_123\n"
"Lb_124:\n"
"v_accvgpr_write_b32 a129, v58\n"
"v_accvgpr_write_b32 a130, v59\n"
"v_accvgpr_write_b32 a131, v60\n"
"Lpace_125:\n"
"s_memrealtime s[96:97]\n"
"s_waitcnt lgkmcnt(0)\n"
"s_sub_u32 s96, s96, s98\n"
"s_cmp_ge_u32 s96, 1061\n"
"s_cbranch_scc1 Lpgo_126\n"
"s_sleep 2\n"
"s_branch Lpace_125\n"
"Lpgo_126:\n"
"s_add_u32 s52, s53, 0x229000\n"
"s_and_b32 s52, s52, s54\n"
"buffer_load_dwordx4 v[88:91], v1, s[28:31], s52 offen offset:0 nt\n"
"s_waitcnt vmcnt(35)\n"
"v_pk_sub_u16 v61, v92, s14 clamp\n"
"v_pk_sub_u16 v62, v93, s14 clamp\n"
"v_perm_b32 v58, v62, v61, s9\n"
"v_perm_b32 v61, v62, v61, s10\n"
"v_pk_sub_u16 v62, v94, s14 clamp\n"
"v_pk_sub_u16 v63, v95, s14 clamp\n"
"v_perm_b32 v59, v63, v62, s9\n"
"v_perm_b32 v62, v63, v62, s10\n"
"v_alignbit_b32 v62, v62, v62, 28\n"
"v_bfi_b32 v60, s8, v61, v62\n"
"v_bfi_b32 v61, s8, v62, v61\n"
"v_pk_mad_u16 v62, v92, 2, -1 op_sel_hi:[1,0,0]\n"
"v_pk_mad_u16 v63, v93, 2, -1 op_sel_hi:[1,0,0]\n"
"v_pk_min_u16 v62, v62, v63\n"
"v_pk_mad_u16 v63, v94, 2, -1 op_sel_hi:[1,0,0]\n"
"v_pk_min_u16 v62, v62, v63\n"
"v_pk_mad_u16 v63, v95, 2, -1 op_sel_hi:[1,0,0]\n"
"v_pk_min_u16 v62, v62, v63\n"
"v_pk_sub_u16 v62, s16, v62 clamp\n"
"v_or_b32 v61, v61, v62\n"
"v_cmp_ne_u32 vcc, 0, v61\n"
"s_cbranch_vccnz Lx_127\n"
"Lb_128:\n"
"v_accvgpr_write_b32 a132, v58\n"
"v_accvgpr_write_b32 a133, v59\n"
"v_accvgpr_write_b32 a134, v60\n"
"s_add_u32 s52, s53, 0x230000\n"
"s_and_b32 s52, s52, s54\n"
"buffer_load_dwordx4 v[92:95], v1, s[28:31], s52 offen offset:0 nt\n"
"s_waitcnt vmcnt(35)\n"
"v_pk_sub_u16 v61, v96, s14 clamp\n"
"v_pk_sub_u16 v62, v97, s14 clamp\n"
"v_perm_b32 v58, v62, v61, s9\n"
"v_perm_b32 v61, v62, v61, s10\n"
"v_pk_sub_u16 v62, v98, s14 clamp\n"
"v_pk_sub_u16 v63, v99, s14 clamp\n"
"v_perm_b32 v59, v63, v62, s9\n"
"v_perm_b32 v62, v63, v62, s10\n"
"v_alignbit_b32 v62, v62, v62, 28\n"
"v_bfi_b32 v60, s8, v61, v62\n"
"v_bfi_b32 v61, s8, v62, v61\n"
"v_pk_mad_u16 v62, v96, 2, -1 op_sel_hi:[1,0,0]\n"
"v_pk_mad_u16 v63, v97, 2, -1 op_sel_hi:[1,0,0]\n"
"v_pk_min_u16 v62, v62, v63\n"
"v_pk_mad_u16 v63, v98, 2, -1 op_sel_hi:[1,0,0]\n"
"v_pk_min_u16 v62, v62, v63\n"
"v_pk_mad_u16 v63, v99, 2, -1 op_sel_hi:[1,0,0]\n"
"v_pk_min_u16 v62, v62, v63\n"
"v_pk_sub_u16 v62, s16, v62 clamp\n"
"v_or_b32 v61, v61, v62\n"
"v_cmp_ne_u32 vcc, 0, v61\n"
"s_cbranch_vccnz Lx_129\n"
"Lb_130:\n"
"v_accvgpr_write_b32 a135, v58\n"
"v_accvgpr_write_b32 a136, v59\n"
"v_accvgpr_write_b32 a137, v60\n"
"s_add_u32 s52, s53, 0x237000\n"
"s_and_b32 s52, s52, s54\n"
"buffer_load_dwordx4 v[96:99], v1, s[28:31], s52 offen offset:0 nt\n"
"s_waitcnt vmcnt(35)\n"
"v_pk_sub_u16 v61, v100, s14 clamp\n"
"v_pk_sub_u16 v62, v101, s14 clamp\n"
"v_perm_b32 v58, v62, v61, s9\n"
"v_perm_b32 v61, v62, v61, s10\n"
"v_pk_sub_u16 v62, v102, s14 clamp\n"
"v_pk_sub_u16 v63, v103, s14 clamp\n"
"v_perm_b32 v59, v63, v62, s9\n"
"v_perm_b32 v62, v63, v62, s10\n"
"v_alignbit_b32 v62, v62, v62, 28\n"
"v_bfi_b32 v60, s8, v61, v62\n"
"v_bfi_b32 v61, s8, v62, v61\n"
"v_pk_mad_u16 v62, v100, 2, -1 op_sel_hi:[1,0,0]\n"
"v_pk_mad_u16 v63, v101, 2, -1 op_sel_hi:[1,0,0]\n"
"v_pk_min_u16 v62, v62, v63\n"
"v_pk_mad_u16 v63, v102, 2, -1 op_sel_hi:[1,0,0]\n"
"v_pk_min_u16 v62, v62, v63\n"
"v_pk_mad_u16 v63, v103, 2, -1 op_sel_hi:[1,0,0]\n"
"v_pk_min_u16 v62, v62, v63\n"
"v_pk_sub_u16 v62, s16, v62 clamp\n"
"v_or_b32 v61, v61, v62\n"
"v_cmp_ne_u32 vcc, 0, v61\n"
"s_cbranch_vccnz Lx_131\n"
"Lb_132:\n"
"v_accvgpr_write_b32 a138, v58\n"
"v_accvgpr_write_b32 a139, v59\n"
"v_accvgpr_write_b32 a140, v60\n"
"s_add_u32 s52, s53, 0x23e000\n"
"s_and_b32 s52, s52, s54\n"
"buffer_load_dwordx4 v[100:103], v1, s[28:31], s52 offen offset:0 nt\n"
"s_waitcnt vmcnt(35)\n"
"v_pk_sub_u16 v61, v104, s14 clamp\n"
"v_pk_sub_u16 v62, v105, s14 clamp\n"
"v_perm_b32 v58, v62, v61, s9\n"
"v_perm_b32 v61, v62, v61, s10\n"
"v_pk_sub_u16 v62, v106, s14 clamp\n"
"v_pk_sub_u16 v63, v107, s14 clamp\n"
"v_perm_b32 v59, v63, v62, s9\n"
"v_perm_b32 v62, v63, v62, s10\n"
"v_alignbit_b32 v62, v62, v62, 28\n"
"v_bfi_b32 v60, s8, v61, v62\n"
"v_bfi_b32 v61, s8, v62, v61\n"
"v_pk_mad_u16 v62, v104, 2, -1 op_sel_hi:[1,0,0]\n"
"v_pk_mad_u16 v63, v105, 2, -1 op_sel_hi:[1,0,0]\n"
"v_pk_min_u16 v62, v62, v63\n"
"v_pk_mad_u16 v63, v106, 2, -1 op_sel_hi:[1,0,0]\n"
"v_pk_min_u16 v62, v62, v63\n"
"v_pk_mad_u16 v63, v107, 2, -1 op_sel_hi:[1,0,0]\n"
"v_pk_min_u16 v62, v62, v63\n"
"v_pk_sub_u16 v62, s16, v62 clamp\n"
"v_or_b32 v61, v61, v62\n"
"v_cmp_ne_u32 vcc, 0, v61\n"
"s_cbranch_vccnz Lx_133\n"
"Lb_134:\n"
"v_accvgpr_write_b32 a141, v58\n"
"v_accvgpr_write_b32 a142, v59\n"
"v_accvgpr_write_b32 a143, v60\n"
"Lpace_135:\n"
"s_memrealtime s[96:97]\n"
"s_waitcnt lgkmcnt(0)\n"
"s_sub_u32 s96, s96, s98\n"
"s_cmp_ge_u32 s96, 1117\n"
"s_cbranch_scc1 Lpgo_136\n"
"s_sleep 2\n"
"s_branch Lpace_135\n"
"Lpgo_136:\n"
"s_add_u32 s52, s53, 0x245000\n"
"s_and_b32 s52, s52, s54\n"
"buffer_load_dwordx4 v[104:107], v1, s[28:31], s52 offen offset:0 nt\n"
"s_waitcnt vmcnt(35)\n"
"v_pk_sub_u16 v61, v108, s14 clamp\n"
"v_pk_sub_u16 v62, v109, s14 clamp\n"
"v_perm_b32 v58, v62, v61, s9\n"
"v_perm_b32 v61, v62, v61, s10\n"
"v_pk_sub_u16 v62, v110, s14 clamp\n"
"v_pk_sub_u16 v63, v111, s14 clamp\n"
"v_perm_b32 v59, v63, v62, s9\n"
"v_perm_b32 v62, v63, v62, s10\n"
"v_alignbit_b32 v62, v62, v62, 28\n"
"v_bfi_b32 v60, s8, v61, v62\n"
"v_bfi_b32 v61, s8, v62, v61\n"
"v_pk_mad_u16 v62, v108, 2, -1 op_sel_hi:[1,0,0]\n"
"v_pk_mad_u16 v63, v109, 2, -1 op_sel_hi:[1,0,0]\n"
"v_pk_min_u16 v62, v62, v63\n"
"v_pk_mad_u16 v63, v110, 2, -1 op_sel_hi:[1,0,0]\n"
"v_pk_min_u16 v62, v62, v63\n"
"v_pk_mad_u16 v63, v111, 2, -1 op_sel_hi:[1,0,0]\n"
"v_pk_min_u16 v62, v62, v63\n"
"v_pk_sub_u16 v62, s16, v62 clamp\n"
"v_or_b32 v61, v61, v62\n"
"v_cmp_ne_u32 vcc, 0, v61\n"
"s_cbranch_vccnz Lx_137\n"
"Lb_138:\n"
"v_accvgpr_write_b32 a144, v58\n"
"v_accvgpr_write_b32 a145, v59\n"
"v_accvgpr_write_b32 a146, v60\n"
"s_add_u32 s52, s53, 0x24c000\n"
"s_and_b32 s52, s52, s54\n"
"buffer_load_dwordx4 v[108:111], v1, s[28:31], s52 offen offset:0 nt\n"
"s_waitcnt vmcnt(35)\n"
"v_pk_sub_u16 v61, v112, s14 clamp\n"
"v_pk_sub_u16 v62, v113, s14 clamp\n"
"v_perm_b32 v58, v62, v61, s9\n"
"v_perm_b32 v61, v62, v61, s10\n"
"v_pk_sub_u16 v62, v114, s14 clamp\n"
"v_pk_sub_u16 v63, v115, s14 clamp\n"
"v_perm_b32 v59, v63, v62, s9\n"
"v_perm_b32 v62, v63, v62, s10\n"
"v_alignbit_b32 v62, v62, v62, 28\n"
"v_bfi_b32 v60, s8, v61, v62\n"
"v_bfi_b32 v61, s8, v62, v61\n"
"v_pk_mad_u16 v62, v112, 2, -1 op_sel_hi:[1,0,0]\n"
"v_pk_mad_u16 v63, v113, 2, -1 op_sel_hi:[1,0,0]\n"
"v_pk_min_u16 v62, v62, v63\n"
"v_pk_mad_u16 v63, v114, 2, -1 op_sel_hi:[1,0,0]\n"
"v_pk_min_u16 v62, v62, v63\n"
"v_pk_mad_u16 v63, v115, 2, -1 op_sel_hi:[1,0,0]\n"
"v_pk_min_u16 v62, v62, v63\n"
"v_pk_sub_u16 v62, s16, v62 clamp\n"
"v_or_b32 v61, v61, v62\n"
"v_cmp_ne_u32 vcc, 0, v61\n"
"s_cbranch_vccnz Lx_139\n"
"Lb_140:\n"
"v_accvgpr_write_b32 a147, v58\n"
"v_accvgpr_write_b32 a148, v59\n"
"v_accvgpr_write_b32 a149, v60\n"
"s_add_u32 s52, s53, 0x253000\n"
"s_and_b32 s52, s52, s54\n"
"buffer_load_dwordx4 v[112:115], v1, s[28:31], s52 offen offset:0 nt\n"
"s_waitcnt vmcnt(35)\n"
"v_pk_sub_u16 v61, v116, s14 clamp\n"
"v_pk_sub_u16 v62, v117, s14 clamp\n"
"v_perm_b32 v58, v62, v61, s9\n"
"v_perm_b32 v61, v62, v61, s10\n"
"v_pk_sub_u16 v62, v118, s14 clamp\n"
"v_pk_sub_u16 v63, v119, s14 clamp\n"
"v_perm_b32 v59, v63, v62, s9\n"
"v_perm_b32 v62, v63, v62, s10\n"
"v_alignbit_b32 v62, v62, v62, 28\n"
"v_bfi_b32 v60, s8, v61, v62\n"
"v_bfi_b32 v61, s8, v62, v61\n"
"v_pk_mad_u16 v62, v116, 2, -1 op_sel_hi:[1,0,0]\n"
"v_pk_mad_u16 v63, v117, 2, -1 op_sel_hi:[1,0,0]\n"
"v_pk_min_u16 v62, v62, v63\n"
"v_pk_mad_u16 v63, v118, 2, -1 op_sel_hi:[1,0,0]\n"
"v_pk_min_u16 v62, v62, v63\n"
"v_pk_mad_u16 v63, v119, 2, -1 op_sel_hi:[1,0,0]\n"
"v_pk_min_u16 v62, v62, v63\n"
"v_pk_sub_u16 v62, s16, v62 clamp\n"
"v_or_b32 v61, v61, v62\n"
"v_cmp_ne_u32 vcc, 0, v61\n"
"s_cbranch_vccnz Lx_141\n"
"Lb_142:\n"
"v_accvgpr_write_b32 a150, v58\n"
"v_accvgpr_write_b32 a151, v59\n"
"v_accvgpr_write_b32 a152, v60\n"
"s_add_u32 s52, s53, 0x25a000\n"
"s_and_b32 s52, s52, s54\n"
"buffer_load_dwordx4 v[116:119], v1, s[28:31], s52 offen offset:0 nt\n"
"s_waitcnt vmcnt(35)\n"
"v_pk_sub_u16 v61, v120, s14 clamp\n"
"v_pk_sub_u16 v62, v121, s14 clamp\n"
"v_perm_b32 v58, v62, v61, s9\n"
"v_perm_b32 v61, v62, v61, s10\n"
"v_pk_sub_u16 v62, v122, s14 clamp\n"
"v_pk_sub_u16 v63, v123, s14 clamp\n"
"v_perm_b32 v59, v63, v62, s9\n"
"v_perm_b32 v62, v63, v62, s10\n"
"v_alignbit_b32 v62, v62, v62, 28\n"
"v_bfi_b32 v60, s8, v61, v62\n"
"v_bfi_b32 v61, s8, v62, v61\n"
"v_pk_mad_u16 v62, v120, 2, -1 op_sel_hi:[1,0,0]\n"
"v_pk_mad_u16 v63, v121, 2, -1 op_sel_hi:[1,0,0]\n"
"v_pk_min_u16 v62, v62, v63\n"
"v_pk_mad_u16 v63, v122, 2, -1 op_sel_hi:[1,0,0]\n"
"v_pk_min_u16 v62, v62, v63\n"
"v_pk_mad_u16 v63, v123, 2, -1 op_sel_hi:[1,0,0]\n"
"v_pk_min_u16 v62, v62, v63\n"
"v_pk_sub_u16 v62, s16, v62 clamp\n"
"v_or_b32 v61, v61, v62\n"
"v_cmp_ne_u32 vcc, 0, v61\n"
"s_cbranch_vccnz Lx_143\n"
"Lb_144:\n"
"v_accvgpr_write_b32 a153, v58\n"
"v_accvgpr_write_b32 a154, v59\n"
"v_accvgpr_write_b32 a155, v60\n"
"Lpace_145:\n"
"s_memrealtime s[96:97]\n"
"s_waitcnt lgkmcnt(0)\n"
"s_sub_u32 s96, s96, s98\n"
"s_cmp_ge_u32 s96, 1174\n"
"s_cbranch_scc1 Lpgo_146\n"
"s_sleep 2\n"
"s_branch Lpace_145\n"
"Lpgo_146:\n"
"s_add_u32 s52, s53, 0x261000\n"
"s_and_b32 s52, s52, s54\n"
"buffer_load_dwordx4 v[120:123], v1, s[28:31], s52 offen offset:0 nt\n"
"s_waitcnt vmcnt(35)\n"
"v_pk_sub_u16 v61, v136, s14 clamp\n"
"v_pk_sub_u16 v62, v137, s14 clamp\n"
"v_perm_b32 v58, v62, v61, s9\n"
"v_perm_b32 v61, v62, v61, s10\n"
"v_pk_sub_u16 v62, v138, s14 clamp\n"
"v_pk_sub_u16 v63, v139, s14 clamp\n"
"v_perm_b32 v59, v63, v62, s9\n"
"v_perm_b32 v62, v63, v62, s10\n"
"v_alignbit_b32 v62, v62, v62, 28\n"
"v_bfi_b32 v60, s8, v61, v62\n"
"v_bfi_b32 v61, s8, v62, v61\n"
"v_pk_mad_u16 v62, v136, 2, -1 op_sel_hi:[1,0,0]\n"
"v_pk_mad_u16 v63, v137, 2, -1 op_sel_hi:[1,0,0]\n"
"v_pk_min_u16 v62, v62, v63\n"
"v_pk_mad_u16 v63, v138, 2, -1 op_sel_hi:[1,0,0]\n"
"v_pk_min_u16 v62, v62, v63\n"
"v_pk_mad_u16 v63, v139, 2, -1 op_sel_hi:[1,0,0]\n"
"v_pk_min_u16 v62, v62, v63\n"
"v_pk_sub_u16 v62, s16, v62 clamp\n"
"v_or_b32 v61, v61, v62\n"
"v_cmp_ne_u32 vcc, 0, v61\n"
"s_cbranch_vccnz Lx_147\n"
"Lb_148:\n"
"v_accvgpr_write_b32 a156, v58\n"
"v_accvgpr_write_b32 a157, v59\n"
"v_accvgpr_write_b32 a158, v60\n"
"s_add_u32 s52, s53, 0x268000\n"
"s_and_b32 s52, s52, s54\n"
"buffer_load_dwordx4 v[136:139], v1, s[28:31], s52 offen offset:0 nt\n"
"s_waitcnt vmcnt(35)\n"
"v_pk_sub_u16 v61, v140, s14 clamp\n"
"v_pk_sub_u16 v62, v141, s14 clamp\n"
"v_perm_b32 v58, v62, v61, s9\n"
"v_perm_b32 v61, v62, v61, s10\n"
"v_pk_sub_u16 v62, v142, s14 clamp\n"
"v_pk_sub_u16 v63, v143, s14 clamp\n"
"v_perm_b32 v59, v63, v62, s9\n"
"v_perm_b32 v62, v63, v62, s10\n"
"v_alignbit_b32 v62, v62, v62, 28\n"
"v_bfi_b32 v60, s8, v61, v62\n"
"v_bfi_b32 v61, s8, v62, v61\n"
"v_pk_mad_u16 v62, v140, 2, -1 op_sel_hi:[1,0,0]\n"
"v_pk_mad_u16 v63, v141, 2, -1 op_sel_hi:[1,0,0]\n"
"v_pk_min_u16 v62, v62, v63\n"
"v_pk_mad_u16 v63, v142, 2, -1 op_sel_hi:[1,0,0]\n"
"v_pk_min_u16 v62, v62, v63\n"
"v_pk_mad_u16 v63, v143, 2, -1 op_sel_hi:[1,0,0]\n"
"v_pk_min_u16 v62, v62, v63\n"
"v_pk_sub_u16 v62, s16, v62 clamp\n"
"v_or_b32 v61, v61, v62\n"
"v_cmp_ne_u32 vcc, 0, v61\n"
"s_cbranch_vccnz Lx_149\n"
"Lb_150:\n"
"v_accvgpr_write_b32 a159, v58\n"
"v_accvgpr_write_b32 a160, v59\n"
"v_accvgpr_write_b32 a161, v60\n"
"s_add_u32 s52, s53, 0x26f000\n"
"s_and_b32 s52, s52, s54\n"
"buffer_load_dwordx4 v[140:143], v1, s[28:31], s52 offen offset:0 nt\n"
"s_waitcnt vmcnt(35)\n"
"v_pk_sub_u16 v61, v144, s14 clamp\n"
"v_pk_sub_u16 v62, v145, s14 clamp\n"
"v_perm_b32 v58, v62, v61, s9\n"
"v_perm_b32 v61, v62, v61, s10\n"
"v_pk_sub_u16 v62, v146, s14 clamp\n"
"v_pk_sub_u16 v63, v147, s14 clamp\n"
"v_perm_b32 v59, v63, v62, s9\n"
"v_perm_b32 v62, v63, v62, s10\n"
"v_alignbit_b32 v62, v62, v62, 28\n"
"v_bfi_b32 v60, s8, v61, v62\n"
"v_bfi_b32 v61, s8, v62, v61\n"
"v_pk_mad_u16 v62, v144, 2, -1 op_sel_hi:[1,0,0]\n"
"v_pk_mad_u16 v63, v145, 2, -1 op_sel_hi:[1,0,0]\n"
"v_pk_min_u16 v62, v62, v63\n"
"v_pk_mad_u16 v63, v146, 2, -1 op_sel_hi:[1,0,0]\n"
"v_pk_min_u16 v62, v62, v63\n"
"v_pk_mad_u16 v63, v147, 2, -1 op_sel_hi:[1,0,0]\n"
"v_pk_min_u16 v62, v62, v63\n"
"v_pk_sub_u16 v62, s16, v62 clamp\n"
"v_or_b32 v61, v61, v62\n"
"v_cmp_ne_u32 vcc, 0, v61\n"
"s_cbranch_vccnz Lx_151\n"
"Lb_152:\n"
"v_accvgpr_write_b32 a162, v58\n"
"v_accvgpr_write_b32 a163, v59\n"
"v_accvgpr_write_b32 a164, v60\n"
"s_add_u32 s52, s53, 0x276000\n"
"s_and_b32 s52, s52, s54\n"
"buffer_load_dwordx4 v[144:147], v1, s[28:31], s52 offen offset:0 nt\n"
"s_waitcnt vmcnt(35)\n"
"v_pk_sub_u16 v61, v124, s14 clamp\n"
"v_pk_sub_u16 v62, v125, s14 clamp\n"
"v_perm_b32 v58, v62, v61, s9\n"
"v_perm_b32 v61, v62, v61, s10\n"
"v_pk_sub_u16 v62, v126, s14 clamp\n"
"v_pk_sub_u16 v63, v127, s14 clamp\n"
"v_perm_b32 v59, v63, v62, s9\n"
"v_perm_b32 v62, v63, v62, s10\n"
"v_alignbit_b32 v62, v62, v62, 28\n"
"v_bfi_b32 v60, s8, v61, v62\n"
"v_bfi_b32 v61, s8, v62, v61\n"
"v_pk_mad_u16 v62, v124, 2, -1 op_sel_hi:[1,0,0]\n"
"v_pk_mad_u16 v63, v125, 2, -1 op_sel_hi:[1,0,0]\n"
"v_pk_min_u16 v62, v62, v63\n"
"v_pk_mad_u16 v63, v126, 2, -1 op_sel_hi:[1,0,0]\n"
"v_pk_min_u16 v62, v62, v63\n"
"v_pk_mad_u16 v63, v127, 2, -1 op_sel_hi:[1,0,0]\n"
"v_pk_min_u16 v62, v62, v63\n"
"v_pk_sub_u16 v62, s16, v62 clamp\n"
"v_or_b32 v61, v61, v62\n"
"v_cmp_ne_u32 vcc, 0, v61\n"
"s_cbranch_vccnz Lx_153\n"
"Lb_154:\n"
"v_accvgpr_write_b32 a165, v58\n"
"v_accvgpr_write_b32 a166, v59\n"
"v_accvgpr_write_b32 a167, v60\n"
"Lpace_155:\n"
"s_memrealtime s[96:97]\n"
"s_waitcnt lgkmcnt(0)\n"
"s_sub_u32 s96, s96, s98\n"
"s_cmp_ge_u32 s96, 1231\n"
"s_cbranch_scc1 Lpgo_156\n"
"s_sleep 2\n"
"s_branch Lpace_155\n"
"Lpgo_156:\n"
"s_add_u32 s52, s53, 0x27d000\n"
"s_and_b32 s52, s52, s54\n"
"buffer_load_dwordx4 v[124:127], v1, s[28:31], s52 offen offset:0 nt\n"
"s_waitcnt vmcnt(35)\n"
"v_pk_sub_u16 v61, v128, s14 clamp\n"
"v_pk_sub_u16 v62, v129, s14 clamp\n"
"v_perm_b32 v58, v62, v61, s9\n"
"v_perm_b32 v61, v62, v61, s10\n"
"v_pk_sub_u16 v62, v130, s14 clamp\n"
"v_pk_sub_u16 v63, v131, s14 clamp\n"
"v_perm_b32 v59, v63, v62, s9\n"
"v_perm_b32 v62, v63, v62, s10\n"
"v_alignbit_b32 v62, v62, v62, 28\n"
"v_bfi_b32 v60, s8, v61, v62\n"
"v_bfi_b32 v61, s8, v62, v61\n"
"v_pk_mad_u16 v62, v128, 2, -1 op_sel_hi:[1,0,0]\n"
"v_pk_mad_u16 v63, v129, 2, -1 op_sel_hi:[1,0,0]\n"
"v_pk_min_u16 v62, v62, v63\n"
"v_pk_mad_u16 v63, v130, 2, -1 op_sel_hi:[1,0,0]\n"
"v_pk_min_u16 v62, v62, v63\n"
"v_pk_mad_u16 v63, v131, 2, -1 op_sel_hi:[1,0,0]\n"
"v_pk_min_u16 v62, v62, v63\n"
"v_pk_sub_u16 v62, s16, v62 clamp\n"
"v_or_b32 v61, v61, v62\n"
"v_cmp_ne_u32 vcc, 0, v61\n"
"s_cbranch_vccnz Lx_157\n"
"Lb_158:\n"
"v_accvgpr_write_b32 a168, v58\n"
"v_accvgpr_write_b32 a169, v59\n"
"v_accvgpr_write_b32 a170, v60\n"
"s_add_u32 s52, s53, 0x284000\n"
"s_and_b32 s52, s52, s54\n"
"buffer_load_dwordx4 v[128:131], v1, s[28:31], s52 offen offset:0 nt\n"
"s_waitcnt vmcnt(35)\n"
"v_pk_sub_u16 v61, v132, s14 clamp\n"
"v_pk_sub_u16 v62, v133, s14 clamp\n"
"v_perm_b32 v58, v62, v61, s9\n"
"v_perm_b32 v61, v62, v61, s10\n"
"v_pk_sub_u16 v62, v134, s14 clamp\n"
"v_pk_sub_u16 v63, v135, s14 clamp\n"
"v_perm_b32 v59, v63, v62, s9\n"
"v_perm_b32 v62, v63, v62, s10\n"
"v_alignbit_b32 v62, v62, v62, 28\n"
"v_bfi_b32 v60, s8, v61, v62\n"
"v_bfi_b32 v61, s8, v62, v61\n"
"v_pk_mad_u16 v62, v132, 2, -1 op_sel_hi:[1,0,0]\n"
"v_pk_mad_u16 v63, v133, 2, -1 op_sel_hi:[1,0,0]\n"
"v_pk_min_u16 v62, v62, v63\n"
"v_pk_mad_u16 v63, v134, 2, -1 op_sel_hi:[1,0,0]\n"
"v_pk_min_u16 v62, v62, v63\n"
"v_pk_mad_u16 v63, v135, 2, -1 op_sel_hi:[1,0,0]\n"
"v_pk_min_u16 v62, v62, v63\n"
"v_pk_sub_u16 v62, s16, v62 clamp\n"
"v_or_b32 v61, v61, v62\n"
"v_cmp_ne_u32 vcc, 0, v61\n"
"s_cbranch_vccnz Lx_159\n"
"Lb_160:\n"
"v_accvgpr_write_b32 a171, v58\n"
"v_accvgpr_write_b32 a172, v59\n"
"v_accvgpr_write_b32 a173, v60\n"
"s_add_u32 s52, s53, 0x28b000\n"
"s_and_b32 s52, s52, s54\n"
"buffer_load_dwordx4 v[132:135], v1, s[28:31], s52 offen offset:0 nt\n"
"s_waitcnt vmcnt(35)\n"
"v_pk_sub_u16 v61, v148, s14 clamp\n"
"v_pk_sub_u16 v62, v149, s14 clamp\n"
"v_perm_b32 v58, v62, v61, s9\n"
"v_perm_b32 v61, v62, v61, s10\n"
"v_pk_sub_u16 v62, v150, s14 clamp\n"
"v_pk_sub_u16 v63, v151, s14 clamp\n"
"v_perm_b32 v59, v63, v62, s9\n"
"v_perm_b32 v62, v63, v62, s10\n"
"v_alignbit_b32 v62, v62, v62, 28\n"
"v_bfi_b32 v60, s8, v61, v62\n"
"v_bfi_b32 v61, s8, v62, v61\n"
"v_pk_mad_u16 v62, v148, 2, -1 op_sel_hi:[1,0,0]\n"
"v_pk_mad_u16 v63, v149, 2, -1 op_sel_hi:[1,0,0]\n"
"v_pk_min_u16 v62, v62, v63\n"
"v_pk_mad_u16 v63, v150, 2, -1 op_sel_hi:[1,0,0]\n"
"v_pk_min_u16 v62, v62, v63\n"
"v_pk_mad_u16 v63, v151, 2, -1 op_sel_hi:[1,0,0]\n"
"v_pk_min_u16 v62, v62, v63\n"
"v_pk_sub_u16 v62, s16, v62 clamp\n"
"v_or_b32 v61, v61, v62\n"
"v_cmp_ne_u32 vcc, 0, v61\n"
"s_cbranch_vccnz Lx_161\n"
"Lb_162:\n"
"v_accvgpr_write_b32 a174, v58\n"
"v_accvgpr_write_b32 a175, v59\n"
"v_accvgpr_write_b32 a176, v60\n"
"s_add_u32 s52, s53, 0x292000\n"
"s_and_b32 s52, s52, s54\n"
"buffer_load_dwordx4 v[148:151], v1, s[28:31], s52 offen offset:0 nt\n"
"s_waitcnt vmcnt(35)\n"
"v_pk_sub_u16 v61, v152, s14 clamp\n"
"v_pk_sub_u16 v62, v153, s14 clamp\n"
"v_perm_b32 v58, v62, v61, s9\n"
"v_perm_b32 v61, v62, v61, s10\n"
"v_pk_sub_u16 v62, v154, s14 clamp\n"
"v_pk_sub_u16 v63, v155, s14 clamp\n"
"v_perm_b32 v59, v63, v62, s9\n"
"v_perm_b32 v62, v63, v62, s10\n"
"v_alignbit_b32 v62, v62, v62, 28\n"
"v_bfi_b32 v60, s8, v61, v62\n"
"v_bfi_b32 v61, s8, v62, v61\n"
"v_pk_mad_u16 v62, v152, 2, -1 op_sel_hi:[1,0,0]\n"
"v_pk_mad_u16 v63, v153, 2, -1 op_sel_hi:[1,0,0]\n"
"v_pk_min_u16 v62, v62, v63\n"
"v_pk_mad_u16 v63, v154, 2, -1 op_sel_hi:[1,0,0]\n"
"v_pk_min_u16 v62, v62, v63\n"
"v_pk_mad_u16 v63, v155, 2, -1 op_sel_hi:[1,0,0]\n"
"v_pk_min_u16 v62, v62, v63\n"
"v_pk_sub_u16 v62, s16, v62 clamp\n"
"v_or_b32 v61, v61, v62\n"
"v_cmp_ne_u32 vcc, 0, v61\n"
"s_cbranch_vccnz Lx_163\n"
"Lb_164:\n"
"v_accvgpr_write_b32 a177, v58\n"
"v_accvgpr_write_b32 a178, v59\n"
"v_accvgpr_write_b32 a179, v60\n"
"Lpace_165:\n"
"s_memrealtime s[96:97]\n"
"s_waitcnt lgkmcnt(0)\n"
"s_sub_u32 s96, s96, s98\n"
"s_cmp_ge_u32 s96, 1288\n"
"s_cbranch_scc1 Lpgo_166\n"
"s_sleep 2\n"
"s_branch Lpace_165\n"
"Lpgo_166:\n"
"s_add_u32 s52, s53, 0x299000\n"
"s_and_b32 s52, s52, s54\n"
"buffer_load_dwordx4 v[152:155], v1, s[28:31], s52 offen offset:0 nt\n"
"v_readfirstlane_b32 s59, v38\n"
"s_mul_i32 s59, s59, 1664525\n"
"s_add_u32 s59, s59, 1013904223\n"
"s_lshr_b32 s56, s48, 4\n"
"s_lshl_b32 s56, s56, 4\n"
"s_add_u32 s56, s56, s48\n"
"s_lshl_b32 s56, s56, 9\n"
"s_add_u32 s56, s56, 0x8000\n"
"s_add_u32 s56, s56, s99\n"
"buffer_load_dwordx4 v[20:23], v32, s[36:39], s56 offen sc1\n"
"buffer_load_dwordx4 v[24:27], v32, s[36:39], s56 offen offset:16 sc1\n"
"s_waitcnt vmcnt(37)\n"
"v_pk_sub_u16 v61, v156, s14 clamp\n"
"v_pk_sub_u16 v62, v157, s14 clamp\n"
"v_perm_b32 v58, v62, v61, s9\n"
"v_perm_b32 v61, v62, v61, s10\n"
"v_pk_sub_u16 v62, v158, s14 clamp\n"
"v_pk_sub_u16 v63, v159, s14 clamp\n"
"v_perm_b32 v59, v63, v62, s9\n"
"v_perm_b32 v62, v63, v62, s10\n"
"v_alignbit_b32 v62, v62, v62, 28\n"
"v_bfi_b32 v60, s8, v61, v62\n"
"v_bfi_b32 v61, s8, v62, v61\n"
"v_pk_mad_u16 v62, v156, 2, -1 op_sel_hi:[1,0,0]\n"
"v_pk_mad_u16 v63, v157, 2, -1 op_sel_hi:[1,0,0]\n"
"v_pk_min_u16 v62, v62, v63\n"
"v_pk_mad_u16 v63, v158, 2, -1 op_sel_hi:[1,0,0]\n"
"v_pk_min_u16 v62, v62, v63\n"
"v_pk_mad_u16 v63, v159, 2, -1 op_sel_hi:[1,0,0]\n"
"v_pk_min_u16 v62, v62, v63\n"
"v_pk_sub_u16 v62, s16, v62 clamp\n"
"v_or_b32 v61, v61, v62\n"
"v_cmp_ne_u32 vcc, 0, v61\n"
"s_cbranch_vccnz Lx_167\n"
"Lb_168:\n"
"v_accvgpr_write_b32 a180, v58\n"
"v_accvgpr_write_b32 a181, v59\n"
"v_accvgpr_write_b32 a182, v60\n"
"s_add_u32 s52, s53, 0x2a0000\n"
"s_and_b32 s52, s52, s54\n"
"buffer_load_dwordx4 v[156:159], v1, s[28:31], s52 offen offset:0 nt\n"
"s_waitcnt vmcnt(37)\n"
"v_pk_sub_u16 v61, v160, s14 clamp\n"
"v_pk_sub_u16 v62, v161, s14 clamp\n"
"v_perm_b32 v58, v62, v61, s9\n"
"v_perm_b32 v61, v62, v61, s10\n"
"v_pk_sub_u16 v62, v162, s14 clamp\n"
"v_pk_sub_u16 v63, v163, s14 clamp\n"
"v_perm_b32 v59, v63, v62, s9\n"
"v_perm_b32 v62, v63, v62, s10\n"
"v_alignbit_b32 v62, v62, v62, 28\n"
"v_bfi_b32 v60, s8, v61, v62\n"
"v_bfi_b32 v61, s8, v62, v61\n"
"v_pk_mad_u16 v62, v160, 2, -1 op_sel_hi:[1,0,0]\n"
"v_pk_mad_u16 v63, v161, 2, -1 op_sel_hi:[1,0,0]\n"
"v_pk_min_u16 v62, v62, v63\n"
"v_pk_mad_u16 v63, v162, 2, -1 op_sel_hi:[1,0,0]\n"
"v_pk_min_u16 v62, v62, v63\n"
"v_pk_mad_u16 v63, v163, 2, -1 op_sel_hi:[1,0,0]\n"
"v_pk_min_u16 v62, v62, v63\n"
"v_pk_sub_u16 v62, s16, v62 clamp\n"
"v_or_b32 v61, v61, v62\n"
"v_cmp_ne_u32 vcc, 0, v61\n"
"s_cbranch_vccnz Lx_169\n"
"Lb_170:\n"
"v_accvgpr_write_b32 a183, v58\n"
"v_accvgpr_write_b32 a184, v59\n"
"v_accvgpr_write_b32 a185, v60\n"
"s_add_u32 s52, s53, 0x2a7000\n"
"s_and_b32 s52, s52, s54\n"
"buffer_load_dwordx4 v[160:163], v1, s[28:31], s52 offen offset:0 nt\n"
"s_waitcnt vmcnt(37)\n"
"v_pk_sub_u16 v61, v164, s14 clamp\n"
"v_pk_sub_u16 v62, v165, s14 clamp\n"
"v_perm_b32 v58, v62, v61, s9\n"
"v_perm_b32 v61, v62, v61, s10\n"
"v_pk_sub_u16 v62, v166, s14 clamp\n"
"v_pk_sub_u16 v63, v167, s14 clamp\n"
"v_perm_b32 v59, v63, v62, s9\n"
"v_perm_b32 v62, v63, v62, s10\n"
"v_alignbit_b32 v62, v62, v62, 28\n"
"v_bfi_b32 v60, s8, v61, v62\n"
"v_bfi_b32 v61, s8, v62, v61\n"
"v_pk_mad_u16 v62, v164, 2, -1 op_sel_hi:[1,0,0]\n"
"v_pk_mad_u16 v63, v165, 2, -1 op_sel_hi:[1,0,0]\n"
"v_pk_min_u16 v62, v62, v63\n"
"v_pk_mad_u16 v63, v166, 2, -1 op_sel_hi:[1,0,0]\n"
"v_pk_min_u16 v62, v62, v63\n"
"v_pk_mad_u16 v63, v167, 2, -1 op_sel_hi:[1,0,0]\n"
"v_pk_min_u16 v62, v62, v63\n"
"v_pk_sub_u16 v62, s16, v62 clamp\n"
"v_or_b32 v61, v61, v62\n"
"v_cmp_ne_u32 vcc, 0, v61\n"
"s_cbranch_vccnz Lx_171\n"
"Lb_172:\n"
"v_accvgpr_write_b32 a186, v58\n"
"v_accvgpr_write_b32 a187, v59\n"
"v_accvgpr_write_b32 a188, v60\n"
"s_add_u32 s52, s53, 0x2ae000\n"
"s_and_b32 s52, s52, s54\n"
"buffer_load_dwordx4 v[164:167], v1, s[28:31], s52 offen offset:0 nt\n"
"s_waitcnt vmcnt(37)\n"
"v_pk_sub_u16 v61, v168, s14 clamp\n"
"v_pk_sub_u16 v62, v169, s14 clamp\n"
"v_perm_b32 v58, v62, v61, s9\n"
"v_perm_b32 v61, v62, v61, s10\n"
"v_pk_sub_u16 v62, v170, s14 clamp\n"
"v_pk_sub_u16 v63, v171, s14 clamp\n"
"v_perm_b32 v59, v63, v62, s9\n"
"v_perm_b32 v62, v63, v62, s10\n"
"v_alignbit_b32 v62, v62, v62, 28\n"
"v_bfi_b32 v60, s8, v61, v62\n"
"v_bfi_b32 v61, s8, v62, v61\n"
"v_pk_mad_u16 v62, v168, 2, -1 op_sel_hi:[1,0,0]\n"
"v_pk_mad_u16 v63, v169, 2, -1 op_sel_hi:[1,0,0]\n"
"v_pk_min_u16 v62, v62, v63\n"
"v_pk_mad_u16 v63, v170, 2, -1 op_sel_hi:[1,0,0]\n"
"v_pk_min_u16 v62, v62, v63\n"
"v_pk_mad_u16 v63, v171, 2, -1 op_sel_hi:[1,0,0]\n"
"v_pk_min_u16 v62, v62, v63\n"
"v_pk_sub_u16 v62, s16, v62 clamp\n"
"v_or_b32 v61, v61, v62\n"
"v_cmp_ne_u32 vcc, 0, v61\n"
"s_cbranch_vccnz Lx_173\n"
"Lb_174:\n"
"v_accvgpr_write_b32 a189, v58\n"
"v_accvgpr_write_b32 a190, v59\n"
"v_accvgpr_write_b32 a191, v60\n"
"Lpace_175:\n"
"s_memrealtime s[96:97]\n"
"s_waitcnt lgkmcnt(0)\n"
"s_sub_u32 s96, s96, s98\n"
"s_cmp_ge_u32 s96, 1345\n"
"s_cbranch_scc1 Lpgo_176\n"
"s_sleep 2\n"
"s_branch Lpace_175\n"
"Lpgo_176:\n"
"s_add_u32 s52, s53, 0x2b5000\n"
"s_and_b32 s52, s52, s54\n"
"buffer_load_dwordx4 v[168:171], v1, s[28:31], s52 offen offset:0 nt\n"
"s_waitcnt vmcnt(37)\n"
"v_pk_sub_u16 v61, v184, s14 clamp\n"
"v_pk_sub_u16 v62, v185, s14 clamp\n"
"v_perm_b32 v58, v62, v61, s9\n"
"v_perm_b32 v61, v62, v61, s10\n"
"v_pk_sub_u16 v62, v186, s14 clamp\n"
"v_pk_sub_u16 v63, v187, s14 clamp\n"
"v_perm_b32 v59, v63, v62, s9\n"
"v_perm_b32 v62, v63, v62, s10\n"
"v_alignbit_b32 v62, v62, v62, 28\n"
"v_bfi_b32 v60, s8, v61, v62\n"
"v_bfi_b32 v61, s8, v62, v61\n"
"v_pk_mad_u16 v62, v184, 2, -1 op_sel_hi:[1,0,0]\n"
"v_pk_mad_u16 v63, v185, 2, -1 op_sel_hi:[1,0,0]\n"
"v_pk_min_u16 v62, v62, v63\n"
"v_pk_mad_u16 v63, v186, 2, -1 op_sel_hi:[1,0,0]\n"
"v_pk_min_u16 v62, v62, v63\n"
"v_pk_mad_u16 v63, v187, 2, -1 op_sel_hi:[1,0,0]\n"
"v_pk_min_u16 v62, v62, v63\n"
"v_pk_sub_u16 v62, s16, v62 clamp\n"
"v_or_b32 v61, v61, v62\n"
"v_cmp_ne_u32 vcc, 0, v61\n"
"s_cbranch_vccnz Lx_177\n"
"Lb_178:\n"
"v_accvgpr_write_b32 a192, v58\n"
"v_accvgpr_write_b32 a193, v59\n"
"v_accvgpr_write_b32 a194, v60\n"
"s_add_u32 s52, s53, 0x2bc000\n"
"s_and_b32 s52, s52, s54\n"
"buffer_load_dwordx4 v[184:187], v1, s[28:31], s52 offen offset:0 nt\n"
"s_waitcnt vmcnt(37)\n"
"v_pk_sub_u16 v61, v188, s14 clamp\n"
"v_pk_sub_u16 v62, v189, s14 clamp\n"
"v_perm_b32 v58, v62, v61, s9\n"
"v_perm_b32 v61, v62, v61, s10\n"
"v_pk_sub_u16 v62, v190, s14 clamp\n"
"v_pk_sub_u16 v63, v191, s14 clamp\n"
"v_perm_b32 v59, v63, v62, s9\n"
"v_perm_b32 v62, v63, v62, s10\n"
"v_alignbit_b32 v62, v62, v62, 28\n"
"v_bfi_b32 v60, s8, v61, v62\n"
"v_bfi_b32 v61, s8, v62, v61\n"
"v_pk_mad_u16 v62, v188, 2, -1 op_sel_hi:[1,0,0]\n"
"v_pk_mad_u16 v63, v189, 2, -1 op_sel_hi:[1,0,0]\n"
"v_pk_min_u16 v62, v62, v63\n"
"v_pk_mad_u16 v63, v190, 2, -1 op_sel_hi:[1,0,0]\n"
"v_pk_min_u16 v62, v62, v63\n"
"v_pk_mad_u16 v63, v191, 2, -1 op_sel_hi:[1,0,0]\n"
"v_pk_min_u16 v62, v62, v63\n"
"v_pk_sub_u16 v62, s16, v62 clamp\n"
"v_or_b32 v61, v61, v62\n"
"v_cmp_ne_u32 vcc, 0, v61\n"
"s_cbranch_vccnz Lx_179\n"
"Lb_180:\n"
"v_accvgpr_write_b32 a195, v58\n"
"v_accvgpr_write_b32 a196, v59\n"
"v_accvgpr_write_b32 a197, v60\n"
"s_add_u32 s52, s53, 0x2c3000\n"
"s_and_b32 s52, s52, s54\n"
"buffer_load_dwordx4 v[188:191], v1, s[28:31], s52 offen offset:0 nt\n"
"s_waitcnt vmcnt(37)\n"
"v_pk_sub_u16 v61, v192, s14 clamp\n"
"v_pk_sub_u16 v62, v193, s14 clamp\n"
"v_perm_b32 v58, v62, v61, s9\n"
"v_perm_b32 v61, v62, v61, s10\n"
"v_pk_sub_u16 v62, v194, s14 clamp\n"
"v_pk_sub_u16 v63, v195, s14 clamp\n"
"v_perm_b32 v59, v63, v62, s9\n"
"v_perm_b32 v62, v63, v62, s10\n"
"v_alignbit_b32 v62, v62, v62, 28\n"
"v_bfi_b32 v60, s8, v61, v62\n"
"v_bfi_b32 v61, s8, v62, v61\n"
"v_pk_mad_u16 v62, v192, 2, -1 op_sel_hi:[1,0,0]\n"
"v_pk_mad_u16 v63, v193, 2, -1 op_sel_hi:[1,0,0]\n"
"v_pk_min_u16 v62, v62, v63\n"
"v_pk_mad_u16 v63, v194, 2, -1 op_sel_hi:[1,0,0]\n"
"v_pk_min_u16 v62, v62, v63\n"
"v_pk_mad_u16 v63, v195, 2, -1 op_sel_hi:[1,0,0]\n"
"v_pk_min_u16 v62, v62, v63\n"
"v_pk_sub_u16 v62, s16, v62 clamp\n"
"v_or_b32 v61, v61, v62\n"
"v_cmp_ne_u32 vcc, 0, v61\n"
"s_cbranch_vccnz Lx_181\n"
"Lb_182:\n"
"v_accvgpr_write_b32 a198, v58\n"
"v_accvgpr_write_b32 a199, v59\n"
"v_accvgpr_write_b32 a200, v60\n"
"s_add_u32 s52, s53, 0x2ca000\n"
"s_and_b32 s52, s52, s54\n"
"buffer_load_dwordx4 v[192:195], v1, s[28:31], s52 offen offset:0 nt\n"
"s_waitcnt vmcnt(37)\n"
"v_pk_sub_u16 v61, v208, s14 clamp\n"
"v_pk_sub_u16 v62, v209, s14 clamp\n"
"v_perm_b32 v58, v62, v61, s9\n"
"v_perm_b32 v61, v62, v61, s10\n"
"v_pk_sub_u16 v62, v210, s14 clamp\n"
"v_pk_sub_u16 v63, v211, s14 clamp\n"
"v_perm_b32 v59, v63, v62, s9\n"
"v_perm_b32 v62, v63, v62, s10\n"
"v_alignbit_b32 v62, v62, v62, 28\n"
"v_bfi_b32 v60, s8, v61, v62\n"
"v_bfi_b32 v61, s8, v62, v61\n"
"v_pk_mad_u16 v62, v208, 2, -1 op_sel_hi:[1,0,0]\n"
"v_pk_mad_u16 v63, v209, 2, -1 op_sel_hi:[1,0,0]\n"
"v_pk_min_u16 v62, v62, v63\n"
"v_pk_mad_u16 v63, v210, 2, -1 op_sel_hi:[1,0,0]\n"
"v_pk_min_u16 v62, v62, v63\n"
"v_pk_mad_u16 v63, v211, 2, -1 op_sel_hi:[1,0,0]\n"
"v_pk_min_u16 v62, v62, v63\n"
"v_pk_sub_u16 v62, s16, v62 clamp\n"
"v_or_b32 v61, v61, v62\n"
"v_cmp_ne_u32 vcc, 0, v61\n"
"s_cbranch_vccnz Lx_183\n"
"Lb_184:\n"
"v_accvgpr_write_b32 a201, v58\n"
"v_accvgpr_write_b32 a202, v59\n"
"v_accvgpr_write_b32 a203, v60\n"
"Lpace_185:\n"
"s_memrealtime s[96:97]\n"
"s_waitcnt lgkmcnt(0)\n"
"s_sub_u32 s96, s96, s98\n"
"s_cmp_ge_u32 s96, 1401\n"
"s_cbranch_scc1 Lpgo_186\n"
"s_sleep 2\n"
"s_branch Lpace_185\n"
"Lpgo_186:\n"
"s_add_u32 s52, s53, 0x2d1000\n"
"s_and_b32 s52, s52, s54\n"
"buffer_load_dwordx4 v[208:211], v1, s[28:31], s52 offen offset:0 nt\n"
"s_waitcnt vmcnt(37)\n"
"v_pk_sub_u16 v61, v212, s14 clamp\n"
"v_pk_sub_u16 v62, v213, s14 clamp\n"
"v_perm_b32 v58, v62, v61, s9\n"
"v_perm_b32 v61, v62, v61, s10\n"
"v_pk_sub_u16 v62, v214, s14 clamp\n"
"v_pk_sub_u16 v63, v215, s14 clamp\n"
"v_perm_b32 v59, v63, v62, s9\n"
"v_perm_b32 v62, v63, v62, s10\n"
"v_alignbit_b32 v62, v62, v62, 28\n"
"v_bfi_b32 v60, s8, v61, v62\n"
"v_bfi_b32 v61, s8, v62, v61\n"
"v_pk_mad_u16 v62, v212, 2, -1 op_sel_hi:[1,0,0]\n"
"v_pk_mad_u16 v63, v213, 2, -1 op_sel_hi:[1,0,0]\n"
"v_pk_min_u16 v62, v62, v63\n"
"v_pk_mad_u16 v63, v214, 2, -1 op_sel_hi:[1,0,0]\n"
"v_pk_min_u16 v62, v62, v63\n"
"v_pk_mad_u16 v63, v215, 2, -1 op_sel_hi:[1,0,0]\n"
"v_pk_min_u16 v62, v62, v63\n"
"v_pk_sub_u16 v62, s16, v62 clamp\n"
"v_or_b32 v61, v61, v62\n"
"v_cmp_ne_u32 vcc, 0, v61\n"
"s_cbranch_vccnz Lx_187\n"
"Lb_188:\n"
"v_accvgpr_write_b32 a204, v58\n"
"v_accvgpr_write_b32 a205, v59\n"
"v_accvgpr_write_b32 a206, v60\n"
"s_add_u32 s52, s53, 0x2d8000\n"
"s_and_b32 s52, s52, s54\n"
"buffer_load_dwordx4 v[212:215], v1, s[28:31], s52 offen offset:0 nt\n"
"s_waitcnt vmcnt(37)\n"
"v_pk_sub_u16 v61, v216, s14 clamp\n"
"v_pk_sub_u16 v62, v217, s14 clamp\n"
"v_perm_b32 v58, v62, v61, s9\n"
"v_perm_b32 v61, v62, v61, s10\n"
"v_pk_sub_u16 v62, v218, s14 clamp\n"
"v_pk_sub_u16 v63, v219, s14 clamp\n"
"v_perm_b32 v59, v63, v62, s9\n"
"v_perm_b32 v62, v63, v62, s10\n"
"v_alignbit_b32 v62, v62, v62, 28\n"
"v_bfi_b32 v60, s8, v61, v62\n"
"v_bfi_b32 v61, s8, v62, v61\n"
"v_pk_mad_u16 v62, v216, 2, -1 op_sel_hi:[1,0,0]\n"
"v_pk_mad_u16 v63, v217, 2, -1 op_sel_hi:[1,0,0]\n"
"v_pk_min_u16 v62, v62, v63\n"
"v_pk_mad_u16 v63, v218, 2, -1 op_sel_hi:[1,0,0]\n"
"v_pk_min_u16 v62, v62, v63\n"
"v_pk_mad_u16 v63, v219, 2, -1 op_sel_hi:[1,0,0]\n"
"v_pk_min_u16 v62, v62, v63\n"
"v_pk_sub_u16 v62, s16, v62 clamp\n"
"v_or_b32 v61, v61, v62\n"
"v_cmp_ne_u32 vcc, 0, v61\n"
"s_cbranch_vccnz Lx_189\n"
"Lb_190:\n"
"v_accvgpr_write_b32 a207, v58\n"
"v_accvgpr_write_b32 a208, v59\n"
"v_accvgpr_write_b32 a209, v60\n"
"s_add_u32 s52, s53, 0x2df000\n"
"s_and_b32 s52, s52, s54\n"
"buffer_load_dwordx4 v[216:219], v1, s[28:31], s52 offen offset:0 nt\n"
"s_waitcnt vmcnt(37)\n"
"v_pk_sub_u16 v61, v232, s14 clamp\n"
"v_pk_sub_u16 v62, v233, s14 clamp\n"
"v_perm_b32 v58, v62, v61, s9\n"
"v_perm_b32 v61, v62, v61, s10\n"
"v_pk_sub_u16 v62, v234, s14 clamp\n"
"v_pk_sub_u16 v63, v235, s14 clamp\n"
"v_perm_b32 v59, v63, v62, s9\n"
"v_perm_b32 v62, v63, v62, s10\n"
"v_alignbit_b32 v62, v62, v62, 28\n"
"v_bfi_b32 v60, s8, v61, v62\n"
"v_bfi_b32 v61, s8, v62, v61\n"
"v_pk_mad_u16 v62, v232, 2, -1 op_sel_hi:[1,0,0]\n"
"v_pk_mad_u16 v63, v233, 2, -1 op_sel_hi:[1,0,0]\n"
"v_pk_min_u16 v62, v62, v63\n"
"v_pk_mad_u16 v63, v234, 2, -1 op_sel_hi:[1,0,0]\n"
"v_pk_min_u16 v62, v62, v63\n"
"v_pk_mad_u16 v63, v235, 2, -1 op_sel_hi:[1,0,0]\n"
"v_pk_min_u16 v62, v62, v63\n"
"v_pk_sub_u16 v62, s16, v62 clamp\n"
"v_or_b32 v61, v61, v62\n"
"v_cmp_ne_u32 vcc, 0, v61\n"
"s_cbranch_vccnz Lx_191\n"
"Lb_192:\n"
"v_accvgpr_write_b32 a210, v58\n"
"v_accvgpr_write_b32 a211, v59\n"
"v_accvgpr_write_b32 a212, v60\n"
"s_add_u32 s52, s53, 0x2e6000\n"
"s_and_b32 s52, s52, s54\n"
"buffer_load_dwordx4 v[232:235], v1, s[28:31], s52 offen offset:0 nt\n"
"s_waitcnt vmcnt(37)\n"
"v_pk_sub_u16 v61, v236, s14 clamp\n"
"v_pk_sub_u16 v62, v237, s14 clamp\n"
"v_perm_b32 v58, v62, v61, s9\n"
"v_perm_b32 v61, v62, v61, s10\n"
"v_pk_sub_u16 v62, v238, s14 clamp\n"
"v_pk_sub_u16 v63, v239, s14 clamp\n"
"v_perm_b32 v59, v63, v62, s9\n"
"v_perm_b32 v62, v63, v62, s10\n"
"v_alignbit_b32 v62, v62, v62, 28\n"
"v_bfi_b32 v60, s8, v61, v62\n"
"v_bfi_b32 v61, s8, v62, v61\n"
"v_pk_mad_u16 v62, v236, 2, -1 op_sel_hi:[1,0,0]\n"
"v_pk_mad_u16 v63, v237, 2, -1 op_sel_hi:[1,0,0]\n"
"v_pk_min_u16 v62, v62, v63\n"
"v_pk_mad_u16 v63, v238, 2, -1 op_sel_hi:[1,0,0]\n"
"v_pk_min_u16 v62, v62, v63\n"
"v_pk_mad_u16 v63, v239, 2, -1 op_sel_hi:[1,0,0]\n"
"v_pk_min_u16 v62, v62, v63\n"
"v_pk_sub_u16 v62, s16, v62 clamp\n"
"v_or_b32 v61, v61, v62\n"
"v_cmp_ne_u32 vcc, 0, v61\n"
"s_cbranch_vccnz Lx_193\n"
"Lb_194:\n"
"v_accvgpr_write_b32 a213, v58\n"
"v_accvgpr_write_b32 a214, v59\n"
"v_accvgpr_write_b32 a215, v60\n"
"Lpace_195:\n"
"s_memrealtime s[96:97]\n"
"s_waitcnt lgkmcnt(0)\n"
"s_sub_u32 s96, s96, s98\n"
"s_cmp_ge_u32 s96, 1458\n"
"s_cbranch_scc1 Lpgo_196\n"
"s_sleep 2\n"
"s_branch Lpace_195\n"
"Lpgo_196:\n"
"s_add_u32 s52, s53, 0x2ed000\n"
"s_and_b32 s52, s52, s54\n"
"buffer_load_dwordx4 v[236:239], v1, s[28:31], s52 offen offset:0 nt\n"
"s_waitcnt vmcnt(37)\n"
"v_pk_sub_u16 v61, v240, s14 clamp\n"
"v_pk_sub_u16 v62, v241, s14 clamp\n"
"v_perm_b32 v64, v62, v61, s9\n"
"v_perm_b32 v61, v62, v61, s10\n"
"v_pk_sub_u16 v62, v242, s14 clamp\n"
"v_pk_sub_u16 v63, v243, s14 clamp\n"
"v_perm_b32 v65, v63, v62, s9\n"
"v_perm_b32 v62, v63, v62, s10\n"
"v_alignbit_b32 v62, v62, v62, 28\n"
"v_bfi_b32 v66, s8, v61, v62\n"
"v_bfi_b32 v61, s8, v62, v61\n"
"v_pk_mad_u16 v62, v240, 2, -1 op_sel_hi:[1,0,0]\n"
"v_pk_mad_u16 v63, v241, 2, -1 op_sel_hi:[1,0,0]\n"
"v_pk_min_u16 v62, v62, v63\n"
"v_pk_mad_u16 v63, v242, 2, -1 op_sel_hi:[1,0,0]\n"
"v_pk_min_u16 v62, v62, v63\n"
"v_pk_mad_u16 v63, v243, 2, -1 op_sel_hi:[1,0,0]\n"
"v_pk_min_u16 v62, v62, v63\n"
"v_pk_sub_u16 v62, s16, v62 clamp\n"
"v_or_b32 v61, v61, v62\n"
"v_cmp_ne_u32 vcc, 0, v61\n"
"s_cbranch_vccnz Lx_197\n"
"Lb_198:\n"
"s_add_u32 s52, s53, 0x2f4000\n"
"s_and_b32 s52, s52, s54\n"
"buffer_load_dwordx4 v[240:243], v1, s[28:31], s52 offen offset:0 nt\n"
"s_waitcnt vmcnt(37)\n"
"v_pk_sub_u16 v61, v172, s14 clamp\n"
"v_pk_sub_u16 v62, v173, s14 clamp\n"
"v_perm_b32 v67, v62, v61, s9\n"
"v_perm_b32 v61, v62, v61, s10\n"
"v_pk_sub_u16 v62, v174, s14 clamp\n"
"v_pk_sub_u16 v63, v175, s14 clamp\n"
"v_perm_b32 v68, v63, v62, s9\n"
"v_perm_b32 v62, v63, v62, s10\n"
"v_alignbit_b32 v62, v62, v62, 28\n"
"v_bfi_b32 v69, s8, v61, v62\n"
"v_bfi_b32 v61, s8, v62, v61\n"
"v_pk_mad_u16 v62, v172, 2, -1 op_sel_hi:[1,0,0]\n"
"v_pk_mad_u16 v63, v173, 2, -1 op_sel_hi:[1,0,0]\n"
"v_pk_min_u16 v62, v62, v63\n"
"v_pk_mad_u16 v63, v174, 2, -1 op_sel_hi:[1,0,0]\n"
"v_pk_min_u16 v62, v62, v63\n"
"v_pk_mad_u16 v63, v175, 2, -1 op_sel_hi:[1,0,0]\n"
"v_pk_min_u16 v62, v62, v63\n"
"v_pk_sub_u16 v62, s16, v62 clamp\n"
"v_or_b32 v61, v61, v62\n"
"v_cmp_ne_u32 vcc, 0, v61\n"
"s_cbranch_vccnz Lx_199\n"
"Lb_200:\n"
"s_add_u32 s52, s53, 0x2fb000\n"
"s_and_b32 s52, s52, s54\n"
"buffer_load_dwordx4 v[172:175], v1, s[28:31], s52 offen offset:0 nt\n"
"s_waitcnt vmcnt(37)\n"
"v_pk_sub_u16 v61, v176, s14 clamp\n"
"v_pk_sub_u16 v62, v177, s14 clamp\n"
"v_perm_b32 v70, v62, v61, s9\n"
"v_perm_b32 v61, v62, v61, s10\n"
"v_pk_sub_u16 v62, v178, s14 clamp\n"
"v_pk_sub_u16 v63, v179, s14 clamp\n"
"v_perm_b32 v71, v63, v62, s9\n"
"v_perm_b32 v62, v63, v62, s10\n"
"v_alignbit_b32 v62, v62, v62, 28\n"
"v_bfi_b32 v72, s8, v61, v62\n"
"v_bfi_b32 v61, s8, v62, v61\n"
"v_pk_mad_u16 v62, v176, 2, -1 op_sel_hi:[1,0,0]\n"
"v_pk_mad_u16 v63, v177, 2, -1 op_sel_hi:[1,0,0]\n"
"v_pk_min_u16 v62, v62, v63\n"
"v_pk_mad_u16 v63, v178, 2, -1 op_sel_hi:[1,0,0]\n"
"v_pk_min_u16 v62, v62, v63\n"
"v_pk_mad_u16 v63, v179, 2, -1 op_sel_hi:[1,0,0]\n"
"v_pk_min_u16 v62, v62, v63\n"
"v_pk_sub_u16 v62, s16, v62 clamp\n"
"v_or_b32 v61, v61, v62\n"
"v_cmp_ne_u32 vcc, 0, v61\n"
"s_cbranch_vccnz Lx_201\n"
"Lb_202:\n"
"s_add_u32 s52, s53, 0x302000\n"
"s_and_b32 s52, s52, s54\n"
"buffer_load_dwordx4 v[176:179], v1, s[28:31], s52 offen offset:0 nt\n"
"s_waitcnt vmcnt(37)\n"
"v_pk_sub_u16 v61, v180, s14 clamp\n"
"v_pk_sub_u16 v62, v181, s14 clamp\n"
"v_perm_b32 v73, v62, v61, s9\n"
"v_perm_b32 v61, v62, v61, s10\n"
"v_pk_sub_u16 v62, v182, s14 clamp\n"
"v_pk_sub_u16 v63, v183, s14 clamp\n"
"v_perm_b32 v74, v63, v62, s9\n"
"v_perm_b32 v62, v63, v62, s10\n"
"v_alignbit_b32 v62, v62, v62, 28\n"
"v_bfi_b32 v75, s8, v61, v62\n"
"v_bfi_b32 v61, s8, v62, v61\n"
"v_pk_mad_u16 v62, v180, 2, -1 op_sel_hi:[1,0,0]\n"
"v_pk_mad_u16 v63, v181, 2, -1 op_sel_hi:[1,0,0]\n"
"v_pk_min_u16 v62, v62, v63\n"
"v_pk_mad_u16 v63, v182, 2, -1 op_sel_hi:[1,0,0]\n"
"v_pk_min_u16 v62, v62, v63\n"
"v_pk_mad_u16 v63, v183, 2, -1 op_sel_hi:[1,0,0]\n"
"v_pk_min_u16 v62, v62, v63\n"
"v_pk_sub_u16 v62, s16, v62 clamp\n"
"v_or_b32 v61, v61, v62\n"
"v_cmp_ne_u32 vcc, 0, v61\n"
"s_cbranch_vccnz Lx_203\n"
"Lb_204:\n"
"Lpace_205:\n"
"s_memrealtime s[96:97]\n"
"s_waitcnt lgkmcnt(0)\n"
"s_sub_u32 s96, s96, s98\n"
"s_cmp_ge_u32 s96, 1515\n"
"s_cbranch_scc1 Lpgo_206\n"
"s_sleep 2\n"
"s_branch Lpace_205\n"
"Lpgo_206:\n"
"s_add_u32 s52, s53, 0x309000\n"
"s_and_b32 s52, s52, s54\n"
"buffer_load_dwordx4 v[180:183], v1, s[28:31], s52 offen offset:0 nt\n"
"s_waitcnt vmcnt(37)\n"
"v_pk_sub_u16 v61, v196, s14 clamp\n"
"v_pk_sub_u16 v62, v197, s14 clamp\n"
"v_perm_b32 v76, v62, v61, s9\n"
"v_perm_b32 v61, v62, v61, s10\n"
"v_pk_sub_u16 v62, v198, s14 clamp\n"
"v_pk_sub_u16 v63, v199, s14 clamp\n"
"v_perm_b32 v77, v63, v62, s9\n"
"v_perm_b32 v62, v63, v62, s10\n"
"v_alignbit_b32 v62, v62, v62, 28\n"
"v_bfi_b32 v78, s8, v61, v62\n"
"v_bfi_b32 v61, s8, v62, v61\n"
"v_pk_mad_u16 v62, v196, 2, -1 op_sel_hi:[1,0,0]\n"
"v_pk_mad_u16 v63, v197, 2, -1 op_sel_hi:[1,0,0]\n"
"v_pk_min_u16 v62, v62, v63\n"
"v_pk_mad_u16 v63, v198, 2, -1 op_sel_hi:[1,0,0]\n"
"v_pk_min_u16 v62, v62, v63\n"
"v_pk_mad_u16 v63, v199, 2, -1 op_sel_hi:[1,0,0]\n"
"v_pk_min_u16 v62, v62, v63\n"
"v_pk_sub_u16 v62, s16, v62 clamp\n"
"v_or_b32 v61, v61, v62\n"
"v_cmp_ne_u32 vcc, 0, v61\n"
"s_cbranch_vccnz Lx_207\n"
"Lb_208:\n"
"s_add_u32 s52, s53, 0x310000\n"
"s_and_b32 s52, s52, s54\n"
"buffer_load_dwordx4 v[196:199], v1, s[28:31], s52 offen offset:0 nt\n"
"s_waitcnt vmcnt(37)\n"
"v_pk_sub_u16 v61, v200, s14 clamp\n"
"v_pk_sub_u16 v62, v201, s14 clamp\n"
"v_perm_b32 v79, v62, v61, s9\n"
"v_perm_b32 v61, v62, v61, s10\n"
"v_pk_sub_u16 v62, v202, s14 clamp\n"
"v_pk_sub_u16 v63, v203, s14 clamp\n"
"v_perm_b32 v80, v63, v62, s9\n"
"v_perm_b32 v62, v63, v62, s10\n"
"v_alignbit_b32 v62, v62, v62, 28\n"
"v_bfi_b32 v81, s8, v61, v62\n"
"v_bfi_b32 v61, s8, v62, v61\n"
"v_pk_mad_u16 v62, v200, 2, -1 op_sel_hi:[1,0,0]\n"
"v_pk_mad_u16 v63, v201, 2, -1 op_sel_hi:[1,0,0]\n"
"v_pk_min_u16 v62, v62, v63\n"
"v_pk_mad_u16 v63, v202, 2, -1 op_sel_hi:[1,0,0]\n"
"v_pk_min_u16 v62, v62, v63\n"
"v_pk_mad_u16 v63, v203, 2, -1 op_sel_hi:[1,0,0]\n"
"v_pk_min_u16 v62, v62, v63\n"
"v_pk_sub_u16 v62, s16, v62 clamp\n"
"v_or_b32 v61, v61, v62\n"
"v_cmp_ne_u32 vcc, 0, v61\n"
"s_cbranch_vccnz Lx_209\n"
"Lb_210:\n"
"s_add_u32 s52, s53, 0x317000\n"
"s_and_b32 s52, s52, s54\n"
"buffer_load_dwordx4 v[200:203], v1, s[28:31], s52 offen offset:0 nt\n"
"s_waitcnt vmcnt(37)\n"
"v_pk_sub_u16 v61, v204, s14 clamp\n"
"v_pk_sub_u16 v62, v205, s14 clamp\n"
"v_perm_b32 v82, v62, v61, s9\n"
"v_perm_b32 v61, v62, v61, s10\n"
"v_pk_sub_u16 v62, v206, s14 clamp\n"
"v_pk_sub_u16 v63, v207, s14 clamp\n"
"v_perm_b32 v83, v63, v62, s9\n"
"v_perm_b32 v62, v63, v62, s10\n"
"v_alignbit_b32 v62, v62, v62, 28\n"
"v_bfi_b32 v84, s8, v61, v62\n"
"v_bfi_b32 v61, s8, v62, v61\n"
"v_pk_mad_u16 v62, v204, 2, -1 op_sel_hi:[1,0,0]\n"
"v_pk_mad_u16 v63, v205, 2, -1 op_sel_hi:[1,0,0]\n"
"v_pk_min_u16 v62, v62, v63\n"
"v_pk_mad_u16 v63, v206, 2, -1 op_sel_hi:[1,0,0]\n"
"v_pk_min_u16 v62, v62, v63\n"
"v_pk_mad_u16 v63, v207, 2, -1 op_sel_hi:[1,0,0]\n"
"v_pk_min_u16 v62, v62, v63\n"
"v_pk_sub_u16 v62, s16, v62 clamp\n"
"v_or_b32 v61, v61, v62\n"
"v_cmp_ne_u32 vcc, 0, v61\n"
"s_cbranch_vccnz Lx_211\n"
"Lb_212:\n"
"s_add_u32 s52, s53, 0x31e000\n"
"s_and_b32 s52, s52, s54\n"
"buffer_load_dwordx4 v[204:207], v1, s[28:31], s52 offen offset:0 nt\n"
"s_waitcnt vmcnt(37)\n"
"v_pk_sub_u16 v61, v88, s14 clamp\n"
"v_pk_sub_u16 v62, v89, s14 clamp\n"
"v_perm_b32 v85, v62, v61, s9\n"
"v_perm_b32 v61, v62, v61, s10\n"
"v_pk_sub_u16 v62, v90, s14 clamp\n"
"v_pk_sub_u16 v63, v91, s14 clamp\n"
"v_perm_b32 v86, v63, v62, s9\n"
"v_perm_b32 v62, v63, v62, s10\n"
"v_alignbit_b32 v62, v62, v62, 28\n"
"v_bfi_b32 v87, s8, v61, v62\n"
"v_bfi_b32 v61, s8, v62, v61\n"
"v_pk_mad_u16 v62, v88, 2, -1 op_sel_hi:[1,0,0]\n"
"v_pk_mad_u16 v63, v89, 2, -1 op_sel_hi:[1,0,0]\n"
"v_pk_min_u16 v62, v62, v63\n"
"v_pk_mad_u16 v63, v90, 2, -1 op_sel_hi:[1,0,0]\n"
"v_pk_min_u16 v62, v62, v63\n"
"v_pk_mad_u16 v63, v91, 2, -1 op_sel_hi:[1,0,0]\n"
"v_pk_min_u16 v62, v62, v63\n"
"v_pk_sub_u16 v62, s16, v62 clamp\n"
"v_or_b32 v61, v61, v62\n"
"v_cmp_ne_u32 vcc, 0, v61\n"
"s_cbranch_vccnz Lx_213\n"
"Lb_214:\n"
"Lpace_215:\n"
"s_memrealtime s[96:97]\n"
"s_waitcnt lgkmcnt(0)\n"
"s_sub_u32 s96, s96, s98\n"
"s_cmp_ge_u32 s96, 1572\n"
"s_cbranch_scc1 Lpgo_216\n"
"s_sleep 2\n"
"s_branch Lpace_215\n"
"Lpgo_216:\n"
"s_add_u32 s52, s53, 0x325000\n"
"s_and_b32 s52, s52, s54\n"
"buffer_load_dwordx4 v[220:223], v1, s[28:31], s52 offen offset:0 nt\n"
"s_waitcnt vmcnt(20)\n"
"v_cmp_ne_u32_e64 vcc, s59, v21\n"
"v_cmp_ne_u32_e64 s[72:73], s59, v23\n"
"v_cmp_ne_u32_e64 s[74:75], s59, v25\n"
"v_cmp_ne_u32_e64 s[76:77], s59, v27\n"
"s_or_b64 vcc, vcc, s[72:73]\n"
"s_or_b64 vcc, vcc, s[74:75]\n"
"s_or_b64 vcc, vcc, s[76:77]\n"
"s_cbranch_scc0 Lqok_217\n"
"s_lshr_b32 s56, s48, 4\n"
"s_lshl_b32 s56, s56, 4\n"
"s_add_u32 s56, s56, s48\n"
"s_lshl_b32 s56, s56, 9\n"
"s_add_u32 s56, s56, 0x8000\n"
"s_add_u32 s56, s56, s99\n"
"s_mov_b32 s55, 4194304\n"
"Ltpoll_218:\n"
"buffer_load_dwordx4 v[20:23], v32, s[36:39], s56 offen sc1\n"
"buffer_load_dwordx4 v[24:27], v32, s[36:39], s56 offen offset:16 sc1\n"
"s_waitcnt vmcnt(0)\n"
"v_cmp_ne_u32_e64 vcc, s59, v21\n"
"v_cmp_ne_u32_e64 s[72:73], s59, v23\n"
"v_cmp_ne_u32_e64 s[74:75], s59, v25\n"
"v_cmp_ne_u32_e64 s[76:77], s59, v27\n"
"s_or_b64 vcc, vcc, s[72:73]\n"
"s_or_b64 vcc, vcc, s[74:75]\n"
"s_or_b64 vcc, vcc, s[76:77]\n"
"s_cbranch_scc0 Ltdone_219\n"
"s_sleep 2\n"
"s_sub_u32 s55, s55, 1\n"
"s_cbranch_scc0 Ltpoll_218\n"
"Ltdone_219:\n"
"Lqok_217:\n"
"v_lshlrev_b32 v61, 16, v20\n"
"v_and_b32 v62, 0xffff0000, v20\n"
"v_mov_b32 v40, v20\n"
"v_mul_f32 v61, s17, v61\n"
"v_mul_f32 v62, s17, v62\n"
"v_perm_b32 v20, v62, v61, s69\n"
"v_max_f32 v63, |v61|, |v62|\n"
"v_lshlrev_b32 v61, 16, v22\n"
"v_and_b32 v62, 0xffff0000, v22\n"
"v_mov_b32 v41, v22\n"
"v_mul_f32 v61, s17, v61\n"
"v_mul_f32 v62, s17, v62\n"
"v_perm_b32 v22, v62, v61, s69\n"
"v_max3_f32 v63, |v61|, |v62|, v63\n"
"v_lshlrev_b32 v61, 16, v24\n"
"v_and_b32 v62, 0xffff0000, v24\n"
"v_mov_b32 v42, v24\n"
"v_mul_f32 v61, s17, v61\n"
"v_mul_f32 v62, s17, v62\n"
"v_perm_b32 v24, v62, v61, s69\n"
"v_max3_f32 v63, |v61|, |v62|, v63\n"
"v_lshlrev_b32 v61, 16, v26\n"
"v_and_b32 v62, 0xffff0000, v26\n"
"v_mov_b32 v43, v26\n"
"v_mul_f32 v61, s17, v61\n"
"v_mul_f32 v62, s17, v62\n"
"v_perm_b32 v26, v62, v61, s69\n"
"v_max3_f32 v63, |v61|, |v62|, v63\n"
"v_cmp_eq_u32 vcc, 0x7f800000, v63\n"
"v_mov_b32_dpp v4, v20 quad_perm:[0,1,2,3] row_mask:0xf bank_mask:0x1\n"
"v_mov_b32_dpp v5, v22 quad_perm:[0,1,2,3] row_mask:0xf bank_mask:0x1\n"
"v_mov_b32_dpp v6, v24 quad_perm:[0,1,2,3] row_mask:0xf bank_mask:0x1\n"
"v_mov_b32_dpp v7, v26 quad_perm:[0,1,2,3] row_mask:0xf bank_mask:0x1\n"
"v_mov_b32_dpp v8, v20 quad_perm:[0,1,2,3] row_mask:0xf bank_mask:0x2\n"
"v_mov_b32_dpp v9, v22 quad_perm:[0,1,2,3] row_mask:0xf bank_mask:0x2\n"
"v_mov_b32_dpp v10, v24 quad_perm:[0,1,2,3] row_mask:0xf bank_mask:0x2\n"
"v_mov_b32_dpp v11, v26 quad_perm:[0,1,2,3] row_mask:0xf bank_mask:0x2\n"
"v_mov_b32_dpp v12, v20 quad_perm:[0,1,2,3] row_mask:0xf bank_mask:0x4\n"
"v_mov_b32_dpp v13, v22 quad_perm:[0,1,2,3] row_mask:0xf bank_mask:0x4\n"
"v_mov_b32_dpp v14, v24 quad_perm:[0,1,2,3] row_mask:0xf bank_mask:0x4\n"
"v_mov_b32_dpp v15, v26 quad_perm:[0,1,2,3] row_mask:0xf bank_mask:0x4\n"
"v_mov_b32_dpp v16, v20 quad_perm:[0,1,2,3] row_mask:0xf bank_mask:0x8\n"
"v_mov_b32_dpp v17, v22 quad_perm:[0,1,2,3] row_mask:0xf bank_mask:0x8\n"
"v_mov_b32_dpp v18, v24 quad_perm:[0,1,2,3] row_mask:0xf bank_mask:0x8\n"
"v_mov_b32_dpp v19, v26 quad_perm:[0,1,2,3] row_mask:0xf bank_mask:0x8\n"
"s_cbranch_vccnz LSLOW\n"
"s_add_u32 s52, s53, 0x32c000\n"
"s_and_b32 s52, s52, s54\n"
"buffer_load_dwordx4 v[224:227], v1, s[28:31], s52 offen offset:0 nt\n"
"v_pk_sub_u16 v61, v92, s14 clamp\n"
"v_pk_sub_u16 v62, v93, s14 clamp\n"
"v_perm_b32 v88, v62, v61, s9\n"
"v_perm_b32 v61, v62, v61, s10\n"
"v_pk_sub_u16 v62, v94, s14 clamp\n"
"v_pk_sub_u16 v63, v95, s14 clamp\n"
"v_perm_b32 v89, v63, v62, s9\n"
"v_perm_b32 v62, v63, v62, s10\n"
"v_alignbit_b32 v62, v62, v62, 28\n"
"v_bfi_b32 v90, s8, v61, v62\n"
"v_bfi_b32 v61, s8, v62, v61\n"
"v_pk_mad_u16 v62, v92, 2, -1 op_sel_hi:[1,0,0]\n"
"v_pk_mad_u16 v63, v93, 2, -1 op_sel_hi:[1,0,0]\n"
"v_pk_min_u16 v62, v62, v63\n"
"v_pk_mad_u16 v63, v94, 2, -1 op_sel_hi:[1,0,0]\n"
"v_pk_min_u16 v62, v62, v63\n"
"v_pk_mad_u16 v63, v95, 2, -1 op_sel_hi:[1,0,0]\n"
"v_pk_min_u16 v62, v62, v63\n"
"v_pk_sub_u16 v62, s16, v62 clamp\n"
"v_or_b32 v61, v61, v62\n"
"v_cmp_ne_u32 vcc, 0, v61\n"
"s_cbranch_vccnz Lx_220\n"
"Lb_221:\n"
"v_and_b32 v61, s8, v90\n"
"v_alignbit_b32 v62, v90, v90, 4\n"
"v_perm_b32 v24, v61, v88, s11\n"
"v_and_b32 v62, s8, v62\n"
"v_perm_b32 v25, v61, v88, s12\n"
"v_perm_b32 v26, v62, v89, s11\n"
"v_perm_b32 v27, v62, v89, s12\n"
"s_nop 1\n"
"v_mfma_f32_16x16x32_bf16 v[32:35], v[24:27], v[4:7], 0\n"
"s_add_u32 s52, s53, 0x333000\n"
"s_and_b32 s52, s52, s54\n"
"buffer_load_dwordx4 v[228:231], v1, s[28:31], s52 offen offset:0 nt\n"
"v_pk_sub_u16 v61, v96, s14 clamp\n"
"v_pk_sub_u16 v62, v97, s14 clamp\n"
"v_perm_b32 v91, v62, v61, s9\n"
"v_perm_b32 v61, v62, v61, s10\n"
"v_pk_sub_u16 v62, v98, s14 clamp\n"
"v_pk_sub_u16 v63, v99, s14 clamp\n"
"v_perm_b32 v92, v63, v62, s9\n"
"v_perm_b32 v62, v63, v62, s10\n"
"v_alignbit_b32 v62, v62, v62, 28\n"
"v_bfi_b32 v93, s8, v61, v62\n"
"v_bfi_b32 v61, s8, v62, v61\n"
"v_pk_mad_u16 v62, v96, 2, -1 op_sel_hi:[1,0,0]\n"
"v_pk_mad_u16 v63, v97, 2, -1 op_sel_hi:[1,0,0]\n"
"v_pk_min_u16 v62, v62, v63\n"
"v_pk_mad_u16 v63, v98, 2, -1 op_sel_hi:[1,0,0]\n"
"v_pk_min_u16 v62, v62, v63\n"
"v_pk_mad_u16 v63, v99, 2, -1 op_sel_hi:[1,0,0]\n"
"v_pk_min_u16 v62, v62, v63\n"
"v_pk_sub_u16 v62, s16, v62 clamp\n"
"v_or_b32 v61, v61, v62\n"
"v_cmp_ne_u32 vcc, 0, v61\n"
"s_cbranch_vccnz Lx_222\n"
"Lb_223:\n"
"v_and_b32 v61, s8, v93\n"
"v_alignbit_b32 v62, v93, v93, 4\n"
"v_perm_b32 v24, v61, v91, s11\n"
"v_and_b32 v62, s8, v62\n"
"v_perm_b32 v25, v61, v91, s12\n"
"v_perm_b32 v26, v62, v92, s11\n"
"v_perm_b32 v27, v62, v92, s12\n"
"s_nop 1\n"
"v_mfma_f32_16x16x32_bf16 v[32:35], v[24:27], v[8:11], v[32:35]\n"
"v_pk_sub_u16 v61, v100, s14 clamp\n"
"v_pk_sub_u16 v62, v101, s14 clamp\n"
"v_perm_b32 v94, v62, v61, s9\n"
"v_perm_b32 v61, v62, v61, s10\n"
"v_pk_sub_u16 v62, v102, s14 clamp\n"
"v_pk_sub_u16 v63, v103, s14 clamp\n"
"v_perm_b32 v95, v63, v62, s9\n"
"v_perm_b32 v62, v63, v62, s10\n"
"v_alignbit_b32 v62, v62, v62, 28\n"
"v_bfi_b32 v96, s8, v61, v62\n"
"v_bfi_b32 v61, s8, v62, v61\n"
"v_pk_mad_u16 v62, v100, 2, -1 op_sel_hi:[1,0,0]\n"
"v_pk_mad_u16 v63, v101, 2, -1 op_sel_hi:[1,0,0]\n"
"v_pk_min_u16 v62, v62, v63\n"
"v_pk_mad_u16 v63, v102, 2, -1 op_sel_hi:[1,0,0]\n"
"v_pk_min_u16 v62, v62, v63\n"
"v_pk_mad_u16 v63, v103, 2, -1 op_sel_hi:[1,0,0]\n"
"v_pk_min_u16 v62, v62, v63\n"
"v_pk_sub_u16 v62, s16, v62 clamp\n"
"v_or_b32 v61, v61, v62\n"
"v_cmp_ne_u32 vcc, 0, v61\n"
"s_cbranch_vccnz Lx_224\n"
"Lb_225:\n"
"v_and_b32 v61, s8, v96\n"
"v_alignbit_b32 v62, v96, v96, 4\n"
"v_perm_b32 v24, v61, v94, s11\n"
"v_and_b32 v62, s8, v62\n"
"v_perm_b32 v25, v61, v94, s12\n"
"v_perm_b32 v26, v62, v95, s11\n"
"v_perm_b32 v27, v62, v95, s12\n"
"s_nop 1\n"
"v_mfma_f32_16x16x32_bf16 v[32:35], v[24:27], v[12:15], v[32:35]\n"
"s_add_u32 s52, s53, 0x33a000\n"
"s_and_b32 s52, s52, s54\n"
"buffer_load_dwordx4 v[244:247], v1, s[28:31], s52 offen offset:0 nt\n"
"v_pk_sub_u16 v61, v104, s14 clamp\n"
"v_pk_sub_u16 v62, v105, s14 clamp\n"
"v_perm_b32 v97, v62, v61, s9\n"
"v_perm_b32 v61, v62, v61, s10\n"
"v_pk_sub_u16 v62, v106, s14 clamp\n"
"v_pk_sub_u16 v63, v107, s14 clamp\n"
"v_perm_b32 v98, v63, v62, s9\n"
"v_perm_b32 v62, v63, v62, s10\n"
"v_alignbit_b32 v62, v62, v62, 28\n"
"v_bfi_b32 v99, s8, v61, v62\n"
"v_bfi_b32 v61, s8, v62, v61\n"
"v_pk_mad_u16 v62, v104, 2, -1 op_sel_hi:[1,0,0]\n"
"v_pk_mad_u16 v63, v105, 2, -1 op_sel_hi:[1,0,0]\n"
"v_pk_min_u16 v62, v62, v63\n"
"v_pk_mad_u16 v63, v106, 2, -1 op_sel_hi:[1,0,0]\n"
"v_pk_min_u16 v62, v62, v63\n"
"v_pk_mad_u16 v63, v107, 2, -1 op_sel_hi:[1,0,0]\n"
"v_pk_min_u16 v62, v62, v63\n"
"v_pk_sub_u16 v62, s16, v62 clamp\n"
"v_or_b32 v61, v61, v62\n"
"v_cmp_ne_u32 vcc, 0, v61\n"
"s_cbranch_vccnz Lx_226\n"
"Lb_227:\n"
"v_and_b32 v61, s8, v99\n"
"v_alignbit_b32 v62, v99, v99, 4\n"
"v_perm_b32 v24, v61, v97, s11\n"
"v_and_b32 v62, s8, v62\n"
"v_perm_b32 v25, v61, v97, s12\n"
"v_perm_b32 v26, v62, v98, s11\n"
"v_perm_b32 v27, v62, v98, s12\n"
"s_nop 1\n"
"v_mfma_f32_16x16x32_bf16 v[32:35], v[24:27], v[16:19], v[32:35]\n"
"v_accvgpr_read_b32 v60, a2\n"
"v_accvgpr_read_b32 v58, a0\n"
"v_accvgpr_read_b32 v59, a1\n"
"v_and_b32 v61, s8, v60\n"
"v_alignbit_b32 v62, v60, v60, 4\n"
"v_perm_b32 v20, v61, v58, s11\n"
"v_and_b32 v62, s8, v62\n"
"v_perm_b32 v21, v61, v58, s12\n"
"v_perm_b32 v22, v62, v59, s11\n"
"v_perm_b32 v23, v62, v59, s12\n"
"v_accvgpr_read_b32 v60, a5\n"
"v_accvgpr_read_b32 v58, a3\n"
"v_mfma_f32_16x16x32_bf16 v[36:39], v[20:23], v[4:7], 0\n"
"v_accvgpr_read_b32 v59, a4\n"
"v_and_b32 v61, s8, v60\n"
"v_alignbit_b32 v62, v60, v60, 4\n"
"v_perm_b32 v28, v61, v58, s11\n"
"v_and_b32 v62, s8, v62\n"
"v_perm_b32 v29, v61, v58, s12\n"
"v_perm_b32 v30, v62, v59, s11\n"
"v_perm_b32 v31, v62, v59, s12\n"
"s_nop 1\n"
"v_mfma_f32_16x16x32_bf16 v[36:39], v[28:31], v[8:11], v[36:39]\n"
"Lpace_228:\n"
"s_memrealtime s[96:97]\n"
"s_waitcnt lgkmcnt(0)\n"
"s_sub_u32 s96, s96, s98\n"
"s_cmp_ge_u32 s96, 1629\n"
"s_cbranch_scc1 Lpgo_229\n"
"s_sleep 2\n"
"s_branch Lpace_228\n"
"Lpgo_229:\n"
"s_add_u32 s52, s53, 0x341000\n"
"s_and_b32 s52, s52, s54\n"
"buffer_load_dwordx4 v[248:251], v1, s[28:31], s52 offen offset:0 nt\n"
"v_pk_sub_u16 v61, v108, s14 clamp\n"
"v_pk_sub_u16 v62, v109, s14 clamp\n"
"v_perm_b32 v100, v62, v61, s9\n"
"v_perm_b32 v61, v62, v61, s10\n"
"v_pk_sub_u16 v62, v110, s14 clamp\n"
"v_pk_sub_u16 v63, v111, s14 clamp\n"
"v_perm_b32 v101, v63, v62, s9\n"
"v_perm_b32 v62, v63, v62, s10\n"
"v_alignbit_b32 v62, v62, v62, 28\n"
"v_bfi_b32 v102, s8, v61, v62\n"
"v_bfi_b32 v61, s8, v62, v61\n"
"v_pk_mad_u16 v62, v108, 2, -1 op_sel_hi:[1,0,0]\n"
"v_pk_mad_u16 v63, v109, 2, -1 op_sel_hi:[1,0,0]\n"
"v_pk_min_u16 v62, v62, v63\n"
"v_pk_mad_u16 v63, v110, 2, -1 op_sel_hi:[1,0,0]\n"
"v_pk_min_u16 v62, v62, v63\n"
"v_pk_mad_u16 v63, v111, 2, -1 op_sel_hi:[1,0,0]\n"
"v_pk_min_u16 v62, v62, v63\n"
"v_pk_sub_u16 v62, s16, v62 clamp\n"
"v_or_b32 v61, v61, v62\n"
"v_cmp_ne_u32 vcc, 0, v61\n"
"s_cbranch_vccnz Lx_230\n"
"Lb_231:\n"
"v_and_b32 v61, s8, v102\n"
"v_alignbit_b32 v62, v102, v102, 4\n"
"v_perm_b32 v24, v61, v100, s11\n"
"v_and_b32 v62, s8, v62\n"
"v_perm_b32 v25, v61, v100, s12\n"
"v_perm_b32 v26, v62, v101, s11\n"
"v_perm_b32 v27, v62, v101, s12\n"
"v_mov_b32_dpp v63, v32 quad_perm:[0,0,0,0] row_mask:0xf bank_mask:0xf\n"
"v_add_f32_dpp v63, v33, v63 quad_perm:[1,1,1,1] row_mask:0xf bank_mask:0xf\n"
"v_add_f32_dpp v63, v34, v63 quad_perm:[2,2,2,2] row_mask:0xf bank_mask:0xf\n"
"v_add_f32_dpp v63, v35, v63 quad_perm:[3,3,3,3] row_mask:0xf bank_mask:0xf\n"
"v_cndmask_b32_e64 v49, v49, v63, s[60:61]\n"
"v_mfma_f32_16x16x32_bf16 v[32:35], v[24:27], v[4:7], 0\n"
"v_accvgpr_read_b32 v60, a8\n"
"v_accvgpr_read_b32 v58, a6\n"
"v_accvgpr_read_b32 v59, a7\n"
"v_and_b32 v61, s8, v60\n"
"v_alignbit_b32 v62, v60, v60, 4\n"
"v_perm_b32 v20, v61, v58, s11\n"
"v_and_b32 v62, s8, v62\n"
"v_perm_b32 v21, v61, v58, s12\n"
"v_perm_b32 v22, v62, v59, s11\n"
"v_perm_b32 v23, v62, v59, s12\n"
"v_accvgpr_read_b32 v60, a11\n"
"v_accvgpr_read_b32 v58, a9\n"
"v_mfma_f32_16x16x32_bf16 v[36:39], v[20:23], v[12:15], v[36:39]\n"
"v_accvgpr_read_b32 v59, a10\n"
"v_and_b32 v61, s8, v60\n"
"v_alignbit_b32 v62, v60, v60, 4\n"
"v_perm_b32 v28, v61, v58, s11\n"
"v_and_b32 v62, s8, v62\n"
"v_perm_b32 v29, v61, v58, s12\n"
"v_perm_b32 v30, v62, v59, s11\n"
"v_perm_b32 v31, v62, v59, s12\n"
"s_nop 1\n"
"v_mfma_f32_16x16x32_bf16 v[36:39], v[28:31], v[16:19], v[36:39]\n"
"s_add_u32 s52, s53, 0x348000\n"
"s_and_b32 s52, s52, s54\n"
"buffer_load_dwordx4 v[252:255], v1, s[28:31], s52 offen offset:0 nt\n"
"v_pk_sub_u16 v61, v112, s14 clamp\n"
"v_pk_sub_u16 v62, v113, s14 clamp\n"
"v_perm_b32 v103, v62, v61, s9\n"
"v_perm_b32 v61, v62, v61, s10\n"
"v_pk_sub_u16 v62, v114, s14 clamp\n"
"v_pk_sub_u16 v63, v115, s14 clamp\n"
"v_perm_b32 v104, v63, v62, s9\n"
"v_perm_b32 v62, v63, v62, s10\n"
"v_alignbit_b32 v62, v62, v62, 28\n"
"v_bfi_b32 v105, s8, v61, v62\n"
"v_bfi_b32 v61, s8, v62, v61\n"
"v_pk_mad_u16 v62, v112, 2, -1 op_sel_hi:[1,0,0]\n"
"v_pk_mad_u16 v63, v113, 2, -1 op_sel_hi:[1,0,0]\n"
"v_pk_min_u16 v62, v62, v63\n"
"v_pk_mad_u16 v63, v114, 2, -1 op_sel_hi:[1,0,0]\n"
"v_pk_min_u16 v62, v62, v63\n"
"v_pk_mad_u16 v63, v115, 2, -1 op_sel_hi:[1,0,0]\n"
"v_pk_min_u16 v62, v62, v63\n"
"v_pk_sub_u16 v62, s16, v62 clamp\n"
"v_or_b32 v61, v61, v62\n"
"v_cmp_ne_u32 vcc, 0, v61\n"
"s_cbranch_vccnz Lx_232\n"
"Lb_233:\n"
"v_and_b32 v61, s8, v105\n"
"v_alignbit_b32 v62, v105, v105, 4\n"
"v_perm_b32 v24, v61, v103, s11\n"
"v_and_b32 v62, s8, v62\n"
"v_perm_b32 v25, v61, v103, s12\n"
"v_perm_b32 v26, v62, v104, s11\n"
"v_perm_b32 v27, v62, v104, s12\n"
"s_nop 1\n"
"v_mfma_f32_16x16x32_bf16 v[32:35], v[24:27], v[8:11], v[32:35]\n"
"v_accvgpr_read_b32 v60, a14\n"
"v_accvgpr_read_b32 v58, a12\n"
"v_accvgpr_read_b32 v59, a13\n"
"v_and_b32 v61, s8, v60\n"
"v_alignbit_b32 v62, v60, v60, 4\n"
"v_perm_b32 v20, v61, v58, s11\n"
"v_and_b32 v62, s8, v62\n"
"v_perm_b32 v21, v61, v58, s12\n"
"v_perm_b32 v22, v62, v59, s11\n"
"v_perm_b32 v23, v62, v59, s12\n"
"v_mov_b32_dpp v63, v36 quad_perm:[0,0,0,0] row_mask:0xf bank_mask:0xf\n"
"v_add_f32_dpp v63, v37, v63 quad_perm:[1,1,1,1] row_mask:0xf bank_mask:0xf\n"
"v_add_f32_dpp v63, v38, v63 quad_perm:[2,2,2,2] row_mask:0xf bank_mask:0xf\n"
"v_add_f32_dpp v63, v39, v63 quad_perm:[3,3,3,3] row_mask:0xf bank_mask:0xf\n"
"v_cndmask_b32_e64 v44, v44, v63, s[60:61]\n"
"v_accvgpr_read_b32 v60, a17\n"
"v_accvgpr_read_b32 v58, a15\n"
"v_mfma_f32_16x16x32_bf16 v[36:39], v[20:23], v[4:7], 0\n"
"v_accvgpr_read_b32 v59, a16\n"
"v_and_b32 v61, s8, v60\n"
"v_alignbit_b32 v62, v60, v60, 4\n"
"v_perm_b32 v28, v61, v58, s11\n"
"v_and_b32 v62, s8, v62\n"
"v_perm_b32 v29, v61, v58, s12\n"
"v_perm_b32 v30, v62, v59, s11\n"
"v_perm_b32 v31, v62, v59, s12\n"
"s_nop 1\n"
"v_mfma_f32_16x16x32_bf16 v[36:39], v[28:31], v[8:11], v[36:39]\n"
"s_add_u32 s52, s53, 0x34f000\n"
"s_and_b32 s52, s52, s54\n"
"buffer_load_dwordx4 v[112:115], v1, s[28:31], s52 offen offset:0 nt\n"
"v_pk_sub_u16 v61, v116, s14 clamp\n"
"v_pk_sub_u16 v62, v117, s14 clamp\n"
"v_perm_b32 v106, v62, v61, s9\n"
"v_perm_b32 v61, v62, v61, s10\n"
"v_pk_sub_u16 v62, v118, s14 clamp\n"
"v_pk_sub_u16 v63, v119, s14 clamp\n"
"v_perm_b32 v107, v63, v62, s9\n"
"v_perm_b32 v62, v63, v62, s10\n"
"v_alignbit_b32 v62, v62, v62, 28\n"
"v_bfi_b32 v108, s8, v61, v62\n"
"v_bfi_b32 v61, s8, v62, v61\n"
"v_pk_mad_u16 v62, v116, 2, -1 op_sel_hi:[1,0,0]\n"
"v_pk_mad_u16 v63, v117, 2, -1 op_sel_hi:[1,0,0]\n"
"v_pk_min_u16 v62, v62, v63\n"
"v_pk_mad_u16 v63, v118, 2, -1 op_sel_hi:[1,0,0]\n"
"v_pk_min_u16 v62, v62, v63\n"
"v_pk_mad_u16 v63, v119, 2, -1 op_sel_hi:[1,0,0]\n"
"v_pk_min_u16 v62, v62, v63\n"
"v_pk_sub_u16 v62, s16, v62 clamp\n"
"v_or_b32 v61, v61, v62\n"
"v_cmp_ne_u32 vcc, 0, v61\n"
"s_cbranch_vccnz Lx_234\n"
"Lb_235:\n"
"v_and_b32 v61, s8, v108\n"
"v_alignbit_b32 v62, v108, v108, 4\n"
"v_perm_b32 v24, v61, v106, s11\n"
"v_and_b32 v62, s8, v62\n"
"v_perm_b32 v25, v61, v106, s12\n"
"v_perm_b32 v26, v62, v107, s11\n"
"v_perm_b32 v27, v62, v107, s12\n"
"s_nop 1\n"
"v_mfma_f32_16x16x32_bf16 v[32:35], v[24:27], v[12:15], v[32:35]\n"
"v_accvgpr_read_b32 v60, a20\n"
"v_accvgpr_read_b32 v58, a18\n"
"v_accvgpr_read_b32 v59, a19\n"
"v_and_b32 v61, s8, v60\n"
"v_alignbit_b32 v62, v60, v60, 4\n"
"v_perm_b32 v20, v61, v58, s11\n"
"v_and_b32 v62, s8, v62\n"
"v_perm_b32 v21, v61, v58, s12\n"
"v_perm_b32 v22, v62, v59, s11\n"
"v_perm_b32 v23, v62, v59, s12\n"
"v_accvgpr_read_b32 v60, a23\n"
"v_accvgpr_read_b32 v58, a21\n"
"v_mfma_f32_16x16x32_bf16 v[36:39], v[20:23], v[12:15], v[36:39]\n"
"v_accvgpr_read_b32 v59, a22\n"
"v_and_b32 v61, s8, v60\n"
"v_alignbit_b32 v62, v60, v60, 4\n"
"v_perm_b32 v28, v61, v58, s11\n"
"v_and_b32 v62, s8, v62\n"
"v_perm_b32 v29, v61, v58, s12\n"
"v_perm_b32 v30, v62, v59, s11\n"
"v_perm_b32 v31, v62, v59, s12\n"
"s_nop 1\n"
"v_mfma_f32_16x16x32_bf16 v[36:39], v[28:31], v[16:19], v[36:39]\n"
"s_add_u32 s52, s53, 0x356000\n"
"s_and_b32 s52, s52, s54\n"
"buffer_load_dwordx4 v[116:119], v1, s[28:31], s52 offen offset:0 nt\n"
"v_pk_sub_u16 v61, v120, s14 clamp\n"
"v_pk_sub_u16 v62, v121, s14 clamp\n"
"v_perm_b32 v109, v62, v61, s9\n"
"v_perm_b32 v61, v62, v61, s10\n"
"v_pk_sub_u16 v62, v122, s14 clamp\n"
"v_pk_sub_u16 v63, v123, s14 clamp\n"
"v_perm_b32 v110, v63, v62, s9\n"
"v_perm_b32 v62, v63, v62, s10\n"
"v_alignbit_b32 v62, v62, v62, 28\n"
"v_bfi_b32 v111, s8, v61, v62\n"
"v_bfi_b32 v61, s8, v62, v61\n"
"v_pk_mad_u16 v62, v120, 2, -1 op_sel_hi:[1,0,0]\n"
"v_pk_mad_u16 v63, v121, 2, -1 op_sel_hi:[1,0,0]\n"
"v_pk_min_u16 v62, v62, v63\n"
"v_pk_mad_u16 v63, v122, 2, -1 op_sel_hi:[1,0,0]\n"
"v_pk_min_u16 v62, v62, v63\n"
"v_pk_mad_u16 v63, v123, 2, -1 op_sel_hi:[1,0,0]\n"
"v_pk_min_u16 v62, v62, v63\n"
"v_pk_sub_u16 v62, s16, v62 clamp\n"
"v_or_b32 v61, v61, v62\n"
"v_cmp_ne_u32 vcc, 0, v61\n"
"s_cbranch_vccnz Lx_236\n"
"Lb_237:\n"
"v_and_b32 v61, s8, v111\n"
"v_alignbit_b32 v62, v111, v111, 4\n"
"v_perm_b32 v24, v61, v109, s11\n"
"v_and_b32 v62, s8, v62\n"
"v_perm_b32 v25, v61, v109, s12\n"
"v_perm_b32 v26, v62, v110, s11\n"
"v_perm_b32 v27, v62, v110, s12\n"
"s_nop 1\n"
"v_mfma_f32_16x16x32_bf16 v[32:35], v[24:27], v[16:19], v[32:35]\n"
"v_accvgpr_read_b32 v60, a26\n"
"v_accvgpr_read_b32 v58, a24\n"
"v_accvgpr_read_b32 v59, a25\n"
"v_and_b32 v61, s8, v60\n"
"v_alignbit_b32 v62, v60, v60, 4\n"
"v_perm_b32 v20, v61, v58, s11\n"
"v_and_b32 v62, s8, v62\n"
"v_perm_b32 v21, v61, v58, s12\n"
"v_perm_b32 v22, v62, v59, s11\n"
"v_perm_b32 v23, v62, v59, s12\n"
"v_mov_b32_dpp v63, v36 quad_perm:[0,0,0,0] row_mask:0xf bank_mask:0xf\n"
"v_add_f32_dpp v63, v37, v63 quad_perm:[1,1,1,1] row_mask:0xf bank_mask:0xf\n"
"v_add_f32_dpp v63, v38, v63 quad_perm:[2,2,2,2] row_mask:0xf bank_mask:0xf\n"
"v_add_f32_dpp v63, v39, v63 quad_perm:[3,3,3,3] row_mask:0xf bank_mask:0xf\n"
"v_cndmask_b32_e64 v44, v44, v63, s[62:63]\n"
"v_accvgpr_read_b32 v60, a29\n"
"v_accvgpr_read_b32 v58, a27\n"
"v_mfma_f32_16x16x32_bf16 v[36:39], v[20:23], v[4:7], 0\n"
"v_accvgpr_read_b32 v59, a28\n"
"v_and_b32 v61, s8, v60\n"
"v_alignbit_b32 v62, v60, v60, 4\n"
"v_perm_b32 v28, v61, v58, s11\n"
"v_and_b32 v62, s8, v62\n"
"v_perm_b32 v29, v61, v58, s12\n"
"v_perm_b32 v30, v62, v59, s11\n"
"v_perm_b32 v31, v62, v59, s12\n"
"s_nop 1\n"
"v_mfma_f32_16x16x32_bf16 v[36:39], v[28:31], v[8:11], v[36:39]\n"
"Lpace_238:\n"
"s_memrealtime s[96:97]\n"
"s_waitcnt lgkmcnt(0)\n"
"s_sub_u32 s96, s96, s98\n"
"s_cmp_ge_u32 s96, 1685\n"
"s_cbranch_scc1 Lpgo_239\n"
"s_sleep 2\n"
"s_branch Lpace_238\n"
"Lpgo_239:\n"
"s_add_u32 s52, s53, 0x35d000\n"
"s_and_b32 s52, s52, s54\n"
"buffer_load_dwordx4 v[120:123], v1, s[28:31], s52 offen offset:0 nt\n"
"v_pk_sub_u16 v61, v136, s14 clamp\n"
"v_pk_sub_u16 v62, v137, s14 clamp\n"
"v_perm_b32 v58, v62, v61, s9\n"
"v_perm_b32 v61, v62, v61, s10\n"
"v_pk_sub_u16 v62, v138, s14 clamp\n"
"v_pk_sub_u16 v63, v139, s14 clamp\n"
"v_perm_b32 v59, v63, v62, s9\n"
"v_perm_b32 v62, v63, v62, s10\n"
"v_alignbit_b32 v62, v62, v62, 28\n"
"v_bfi_b32 v60, s8, v61, v62\n"
"v_bfi_b32 v61, s8, v62, v61\n"
"v_pk_mad_u16 v62, v136, 2, -1 op_sel_hi:[1,0,0]\n"
"v_pk_mad_u16 v63, v137, 2, -1 op_sel_hi:[1,0,0]\n"
"v_pk_min_u16 v62, v62, v63\n"
"v_pk_mad_u16 v63, v138, 2, -1 op_sel_hi:[1,0,0]\n"
"v_pk_min_u16 v62, v62, v63\n"
"v_pk_mad_u16 v63, v139, 2, -1 op_sel_hi:[1,0,0]\n"
"v_pk_min_u16 v62, v62, v63\n"
"v_pk_sub_u16 v62, s16, v62 clamp\n"
"v_or_b32 v61, v61, v62\n"
"v_cmp_ne_u32 vcc, 0, v61\n"
"s_cbranch_vccnz Lx_240\n"
"Lb_241:\n"
"ds_write_b32 v3, v60 offset:0\n"
"ds_write_b64 v3, v[58:59] offset:1024\n"
"v_and_b32 v61, s8, v60\n"
"v_alignbit_b32 v62, v60, v60, 4\n"
"v_perm_b32 v24, v61, v58, s11\n"
"v_and_b32 v62, s8, v62\n"
"v_perm_b32 v25, v61, v58, s12\n"
"v_perm_b32 v26, v62, v59, s11\n"
"v_perm_b32 v27, v62, v59, s12\n"
"v_mov_b32_dpp v63, v32 quad_perm:[0,0,0,0] row_mask:0xf bank_mask:0xf\n"
"v_add_f32_dpp v63, v33, v63 quad_perm:[1,1,1,1] row_mask:0xf bank_mask:0xf\n"
"v_add_f32_dpp v63, v34, v63 quad_perm:[2,2,2,2] row_mask:0xf bank_mask:0xf\n"
"v_add_f32_dpp v63, v35, v63 quad_perm:[3,3,3,3] row_mask:0xf bank_mask:0xf\n"
"v_cndmask_b32_e64 v49, v49, v63, s[62:63]\n"
"v_mfma_f32_16x16x32_bf16 v[32:35], v[24:27], v[4:7], 0\n"
"s_add_u32 s52, s53, 0x364000\n"
"s_and_b32 s52, s52, s54\n"
"buffer_load_dwordx4 v[136:139], v1, s[28:31], s52 offen offset:0 nt\n"
"v_pk_sub_u16 v61, v140, s14 clamp\n"
"v_pk_sub_u16 v62, v141, s14 clamp\n"
"v_perm_b32 v58, v62, v61, s9\n"
"v_perm_b32 v61, v62, v61, s10\n"
"v_pk_sub_u16 v62, v142, s14 clamp\n"
"v_pk_sub_u16 v63, v143, s14 clamp\n"
"v_perm_b32 v59, v63, v62, s9\n"
"v_perm_b32 v62, v63, v62, s10\n"
"v_alignbit_b32 v62, v62, v62, 28\n"
"v_bfi_b32 v60, s8, v61, v62\n"
"v_bfi_b32 v61, s8, v62, v61\n"
"v_pk_mad_u16 v62, v140, 2, -1 op_sel_hi:[1,0,0]\n"
"v_pk_mad_u16 v63, v141, 2, -1 op_sel_hi:[1,0,0]\n"
"v_pk_min_u16 v62, v62, v63\n"
"v_pk_mad_u16 v63, v142, 2, -1 op_sel_hi:[1,0,0]\n"
"v_pk_min_u16 v62, v62, v63\n"
"v_pk_mad_u16 v63, v143, 2, -1 op_sel_hi:[1,0,0]\n"
"v_pk_min_u16 v62, v62, v63\n"
"v_pk_sub_u16 v62, s16, v62 clamp\n"
"v_or_b32 v61, v61, v62\n"
"v_cmp_ne_u32 vcc, 0, v61\n"
"s_cbranch_vccnz Lx_242\n"
"Lb_243:\n"
"ds_write_b32 v3, v60 offset:4\n"
"ds_write_b64 v3, v[58:59] offset:1032\n"
"v_and_b32 v61, s8, v60\n"
"v_alignbit_b32 v62, v60, v60, 4\n"
"v_perm_b32 v24, v61, v58, s11\n"
"v_and_b32 v62, s8, v62\n"
"v_perm_b32 v25, v61, v58, s12\n"
"v_perm_b32 v26, v62, v59, s11\n"
"v_perm_b32 v27, v62, v59, s12\n"
"s_nop 1\n"
"v_mfma_f32_16x16x32_bf16 v[32:35], v[24:27], v[8:11], v[32:35]\n"
"v_accvgpr_read_b32 v60, a32\n"
"v_accvgpr_read_b32 v58, a30\n"
"v_accvgpr_read_b32 v59, a31\n"
"v_and_b32 v61, s8, v60\n"
"v_alignbit_b32 v62, v60, v60, 4\n"
"v_perm_b32 v20, v61, v58, s11\n"
"v_and_b32 v62, s8, v62\n"
"v_perm_b32 v21, v61, v58, s12\n"
"v_perm_b32 v22, v62, v59, s11\n"
"v_perm_b32 v23, v62, v59, s12\n"
"v_accvgpr_read_b32 v60, a35\n"
"v_accvgpr_read_b32 v58, a33\n"
"v_mfma_f32_16x16x32_bf16 v[36:39], v[20:23], v[12:15], v[36:39]\n"
"v_accvgpr_read_b32 v59, a34\n"
"v_and_b32 v61, s8, v60\n"
"v_alignbit_b32 v62, v60, v60, 4\n"
"v_perm_b32 v28, v61, v58, s11\n"
"v_and_b32 v62, s8, v62\n"
"v_perm_b32 v29, v61, v58, s12\n"
"v_perm_b32 v30, v62, v59, s11\n"
"v_perm_b32 v31, v62, v59, s12\n"
"s_nop 1\n"
"v_mfma_f32_16x16x32_bf16 v[36:39], v[28:31], v[16:19], v[36:39]\n"
"s_add_u32 s52, s53, 0x36b000\n"
"s_and_b32 s52, s52, s54\n"
"buffer_load_dwordx4 v[140:143], v1, s[28:31], s52 offen offset:0 nt\n"
"v_pk_sub_u16 v61, v144, s14 clamp\n"
"v_pk_sub_u16 v62, v145, s14 clamp\n"
"v_perm_b32 v58, v62, v61, s9\n"
"v_perm_b32 v61, v62, v61, s10\n"
"v_pk_sub_u16 v62, v146, s14 clamp\n"
"v_pk_sub_u16 v63, v147, s14 clamp\n"
"v_perm_b32 v59, v63, v62, s9\n"
"v_perm_b32 v62, v63, v62, s10\n"
"v_alignbit_b32 v62, v62, v62, 28\n"
"v_bfi_b32 v60, s8, v61, v62\n"
"v_bfi_b32 v61, s8, v62, v61\n"
"v_pk_mad_u16 v62, v144, 2, -1 op_sel_hi:[1,0,0]\n"
"v_pk_mad_u16 v63, v145, 2, -1 op_sel_hi:[1,0,0]\n"
"v_pk_min_u16 v62, v62, v63\n"
"v_pk_mad_u16 v63, v146, 2, -1 op_sel_hi:[1,0,0]\n"
"v_pk_min_u16 v62, v62, v63\n"
"v_pk_mad_u16 v63, v147, 2, -1 op_sel_hi:[1,0,0]\n"
"v_pk_min_u16 v62, v62, v63\n"
"v_pk_sub_u16 v62, s16, v62 clamp\n"
"v_or_b32 v61, v61, v62\n"
"v_cmp_ne_u32 vcc, 0, v61\n"
"s_cbranch_vccnz Lx_244\n"
"Lb_245:\n"
"ds_write_b32 v3, v60 offset:8\n"
"ds_write_b64 v3, v[58:59] offset:2048\n"
"v_and_b32 v61, s8, v60\n"
"v_alignbit_b32 v62, v60, v60, 4\n"
"v_perm_b32 v24, v61, v58, s11\n"
"v_and_b32 v62, s8, v62\n"
"v_perm_b32 v25, v61, v58, s12\n"
"v_perm_b32 v26, v62, v59, s11\n"
"v_perm_b32 v27, v62, v59, s12\n"
"s_nop 1\n"
"v_mfma_f32_16x16x32_bf16 v[32:35], v[24:27], v[12:15], v[32:35]\n"
"v_accvgpr_read_b32 v60, a38\n"
"v_accvgpr_read_b32 v58, a36\n"
"v_accvgpr_read_b32 v59, a37\n"
"v_and_b32 v61, s8, v60\n"
"v_alignbit_b32 v62, v60, v60, 4\n"
"v_perm_b32 v20, v61, v58, s11\n"
"v_and_b32 v62, s8, v62\n"
"v_perm_b32 v21, v61, v58, s12\n"
"v_perm_b32 v22, v62, v59, s11\n"
"v_perm_b32 v23, v62, v59, s12\n"
"v_mov_b32_dpp v63, v36 quad_perm:[0,0,0,0] row_mask:0xf bank_mask:0xf\n"
"v_add_f32_dpp v63, v37, v63 quad_perm:[1,1,1,1] row_mask:0xf bank_mask:0xf\n"
"v_add_f32_dpp v63, v38, v63 quad_perm:[2,2,2,2] row_mask:0xf bank_mask:0xf\n"
"v_add_f32_dpp v63, v39, v63 quad_perm:[3,3,3,3] row_mask:0xf bank_mask:0xf\n"
"v_cndmask_b32_e64 v44, v44, v63, s[64:65]\n"
"v_accvgpr_read_b32 v60, a41\n"
"v_accvgpr_read_b32 v58, a39\n"
"v_mfma_f32_16x16x32_bf16 v[36:39], v[20:23], v[4:7], 0\n"
"v_accvgpr_read_b32 v59, a40\n"
"v_and_b32 v61, s8, v60\n"
"v_alignbit_b32 v62, v60, v60, 4\n"
"v_perm_b32 v28, v61, v58, s11\n"
"v_and_b32 v62, s8, v62\n"
"v_perm_b32 v29, v61, v58, s12\n"
"v_perm_b32 v30, v62, v59, s11\n"
"v_perm_b32 v31, v62, v59, s12\n"
"s_nop 1\n"
"v_mfma_f32_16x16x32_bf16 v[36:39], v[28:31], v[8:11], v[36:39]\n"
"s_add_u32 s52, s53, 0x372000\n"
"s_and_b32 s52, s52, s54\n"
"buffer_load_dwordx4 v[144:147], v1, s[28:31], s52 offen offset:0 nt\n"
"v_pk_sub_u16 v61, v124, s14 clamp\n"
"v_pk_sub_u16 v62, v125, s14 clamp\n"
"v_perm_b32 v58, v62, v61, s9\n"
"v_perm_b32 v61, v62, v61, s10\n"
"v_pk_sub_u16 v62, v126, s14 clamp\n"
"v_pk_sub_u16 v63, v127, s14 clamp\n"
"v_perm_b32 v59, v63, v62, s9\n"
"v_perm_b32 v62, v63, v62, s10\n"
"v_alignbit_b32 v62, v62, v62, 28\n"
"v_bfi_b32 v60, s8, v61, v62\n"
"v_bfi_b32 v61, s8, v62, v61\n"
"v_pk_mad_u16 v62, v124, 2, -1 op_sel_hi:[1,0,0]\n"
"v_pk_mad_u16 v63, v125, 2, -1 op_sel_hi:[1,0,0]\n"
"v_pk_min_u16 v62, v62, v63\n"
"v_pk_mad_u16 v63, v126, 2, -1 op_sel_hi:[1,0,0]\n"
"v_pk_min_u16 v62, v62, v63\n"
"v_pk_mad_u16 v63, v127, 2, -1 op_sel_hi:[1,0,0]\n"
"v_pk_min_u16 v62, v62, v63\n"
"v_pk_sub_u16 v62, s16, v62 clamp\n"
"v_or_b32 v61, v61, v62\n"
"v_cmp_ne_u32 vcc, 0, v61\n"
"s_cbranch_vccnz Lx_246\n"
"Lb_247:\n"
"ds_write_b32 v3, v60 offset:12\n"
"ds_write_b64 v3, v[58:59] offset:2056\n"
"v_and_b32 v61, s8, v60\n"
"v_alignbit_b32 v62, v60, v60, 4\n"
"v_perm_b32 v24, v61, v58, s11\n"
"v_and_b32 v62, s8, v62\n"
"v_perm_b32 v25, v61, v58, s12\n"
"v_perm_b32 v26, v62, v59, s11\n"
"v_perm_b32 v27, v62, v59, s12\n"
"s_nop 1\n"
"v_mfma_f32_16x16x32_bf16 v[32:35], v[24:27], v[16:19], v[32:35]\n"
"v_accvgpr_read_b32 v60, a44\n"
"v_accvgpr_read_b32 v58, a42\n"
"v_accvgpr_read_b32 v59, a43\n"
"v_and_b32 v61, s8, v60\n"
"v_alignbit_b32 v62, v60, v60, 4\n"
"v_perm_b32 v20, v61, v58, s11\n"
"v_and_b32 v62, s8, v62\n"
"v_perm_b32 v21, v61, v58, s12\n"
"v_perm_b32 v22, v62, v59, s11\n"
"v_perm_b32 v23, v62, v59, s12\n"
"v_accvgpr_read_b32 v60, a47\n"
"v_accvgpr_read_b32 v58, a45\n"
"v_mfma_f32_16x16x32_bf16 v[36:39], v[20:23], v[12:15], v[36:39]\n"
"v_accvgpr_read_b32 v59, a46\n"
"v_and_b32 v61, s8, v60\n"
"v_alignbit_b32 v62, v60, v60, 4\n"
"v_perm_b32 v28, v61, v58, s11\n"
"v_and_b32 v62, s8, v62\n"
"v_perm_b32 v29, v61, v58, s12\n"
"v_perm_b32 v30, v62, v59, s11\n"
"v_perm_b32 v31, v62, v59, s12\n"
"s_nop 1\n"
"v_mfma_f32_16x16x32_bf16 v[36:39], v[28:31], v[16:19], v[36:39]\n"
"Lpace_248:\n"
"s_memrealtime s[96:97]\n"
"s_waitcnt lgkmcnt(0)\n"
"s_sub_u32 s96, s96, s98\n"
"s_cmp_ge_u32 s96, 1742\n"
"s_cbranch_scc1 Lpgo_249\n"
"s_sleep 2\n"
"s_branch Lpace_248\n"
"Lpgo_249:\n"
"s_add_u32 s52, s53, 0x379000\n"
"s_and_b32 s52, s52, s54\n"
"buffer_load_dwordx4 v[124:127], v1, s[28:31], s52 offen offset:0 nt\n"
"v_pk_sub_u16 v61, v128, s14 clamp\n"
"v_pk_sub_u16 v62, v129, s14 clamp\n"
"v_perm_b32 v58, v62, v61, s9\n"
"v_perm_b32 v61, v62, v61, s10\n"
"v_pk_sub_u16 v62, v130, s14 clamp\n"
"v_pk_sub_u16 v63, v131, s14 clamp\n"
"v_perm_b32 v59, v63, v62, s9\n"
"v_perm_b32 v62, v63, v62, s10\n"
"v_alignbit_b32 v62, v62, v62, 28\n"
"v_bfi_b32 v60, s8, v61, v62\n"
"v_bfi_b32 v61, s8, v62, v61\n"
"v_pk_mad_u16 v62, v128, 2, -1 op_sel_hi:[1,0,0]\n"
"v_pk_mad_u16 v63, v129, 2, -1 op_sel_hi:[1,0,0]\n"
"v_pk_min_u16 v62, v62, v63\n"
"v_pk_mad_u16 v63, v130, 2, -1 op_sel_hi:[1,0,0]\n"
"v_pk_min_u16 v62, v62, v63\n"
"v_pk_mad_u16 v63, v131, 2, -1 op_sel_hi:[1,0,0]\n"
"v_pk_min_u16 v62, v62, v63\n"
"v_pk_sub_u16 v62, s16, v62 clamp\n"
"v_or_b32 v61, v61, v62\n"
"v_cmp_ne_u32 vcc, 0, v61\n"
"s_cbranch_vccnz Lx_250\n"
"Lb_251:\n"
"ds_write_b32 v3, v60 offset:3072\n"
"ds_write_b64 v3, v[58:59] offset:4096\n"
"v_and_b32 v61, s8, v60\n"
"v_alignbit_b32 v62, v60, v60, 4\n"
"v_perm_b32 v24, v61, v58, s11\n"
"v_and_b32 v62, s8, v62\n"
"v_perm_b32 v25, v61, v58, s12\n"
"v_perm_b32 v26, v62, v59, s11\n"
"v_perm_b32 v27, v62, v59, s12\n"
"v_mov_b32_dpp v63, v32 quad_perm:[0,0,0,0] row_mask:0xf bank_mask:0xf\n"
"v_add_f32_dpp v63, v33, v63 quad_perm:[1,1,1,1] row_mask:0xf bank_mask:0xf\n"
"v_add_f32_dpp v63, v34, v63 quad_perm:[2,2,2,2] row_mask:0xf bank_mask:0xf\n"
"v_add_f32_dpp v63, v35, v63 quad_perm:[3,3,3,3] row_mask:0xf bank_mask:0xf\n"
"v_cndmask_b32_e64 v49, v49, v63, s[64:65]\n"
"v_mfma_f32_16x16x32_bf16 v[32:35], v[24:27], v[4:7], 0\n"
"v_accvgpr_read_b32 v60, a50\n"
"v_accvgpr_read_b32 v58, a48\n"
"v_accvgpr_read_b32 v59, a49\n"
"v_and_b32 v61, s8, v60\n"
"v_alignbit_b32 v62, v60, v60, 4\n"
"v_perm_b32 v20, v61, v58, s11\n"
"v_and_b32 v62, s8, v62\n"
"v_perm_b32 v21, v61, v58, s12\n"
"v_perm_b32 v22, v62, v59, s11\n"
"v_perm_b32 v23, v62, v59, s12\n"
"v_mov_b32_dpp v63, v36 quad_perm:[0,0,0,0] row_mask:0xf bank_mask:0xf\n"
"v_add_f32_dpp v63, v37, v63 quad_perm:[1,1,1,1] row_mask:0xf bank_mask:0xf\n"
"v_add_f32_dpp v63, v38, v63 quad_perm:[2,2,2,2] row_mask:0xf bank_mask:0xf\n"
"v_add_f32_dpp v63, v39, v63 quad_perm:[3,3,3,3] row_mask:0xf bank_mask:0xf\n"
"v_cndmask_b32_e64 v44, v44, v63, s[66:67]\n"
"v_accvgpr_read_b32 v60, a53\n"
"v_accvgpr_read_b32 v58, a51\n"
"v_mfma_f32_16x16x32_bf16 v[36:39], v[20:23], v[4:7], 0\n"
"v_accvgpr_read_b32 v59, a52\n"
"v_and_b32 v61, s8, v60\n"
"v_alignbit_b32 v62, v60, v60, 4\n"
"v_perm_b32 v28, v61, v58, s11\n"
"v_and_b32 v62, s8, v62\n"
"v_perm_b32 v29, v61, v58, s12\n"
"v_perm_b32 v30, v62, v59, s11\n"
"v_perm_b32 v31, v62, v59, s12\n"
"s_nop 1\n"
"v_mfma_f32_16x16x32_bf16 v[36:39], v[28:31], v[8:11], v[36:39]\n"
"s_add_u32 s52, s53, 0x380000\n"
"s_and_b32 s52, s52, s54\n"
"buffer_load_dwordx4 v[128:131], v1, s[28:31], s52 offen offset:0 nt\n"
"v_pk_sub_u16 v61, v132, s14 clamp\n"
"v_pk_sub_u16 v62, v133, s14 clamp\n"
"v_perm_b32 v58, v62, v61, s9\n"
"v_perm_b32 v61, v62, v61, s10\n"
"v_pk_sub_u16 v62, v134, s14 clamp\n"
"v_pk_sub_u16 v63, v135, s14 clamp\n"
"v_perm_b32 v59, v63, v62, s9\n"
"v_perm_b32 v62, v63, v62, s10\n"
"v_alignbit_b32 v62, v62, v62, 28\n"
"v_bfi_b32 v60, s8, v61, v62\n"
"v_bfi_b32 v61, s8, v62, v61\n"
"v_pk_mad_u16 v62, v132, 2, -1 op_sel_hi:[1,0,0]\n"
"v_pk_mad_u16 v63, v133, 2, -1 op_sel_hi:[1,0,0]\n"
"v_pk_min_u16 v62, v62, v63\n"
"v_pk_mad_u16 v63, v134, 2, -1 op_sel_hi:[1,0,0]\n"
"v_pk_min_u16 v62, v62, v63\n"
"v_pk_mad_u16 v63, v135, 2, -1 op_sel_hi:[1,0,0]\n"
"v_pk_min_u16 v62, v62, v63\n"
"v_pk_sub_u16 v62, s16, v62 clamp\n"
"v_or_b32 v61, v61, v62\n"
"v_cmp_ne_u32 vcc, 0, v61\n"
"s_cbranch_vccnz Lx_252\n"
"Lb_253:\n"
"ds_write_b32 v3, v60 offset:3076\n"
"ds_write_b64 v3, v[58:59] offset:4104\n"
"v_and_b32 v61, s8, v60\n"
"v_alignbit_b32 v62, v60, v60, 4\n"
"v_perm_b32 v24, v61, v58, s11\n"
"v_and_b32 v62, s8, v62\n"
"v_perm_b32 v25, v61, v58, s12\n"
"v_perm_b32 v26, v62, v59, s11\n"
"v_perm_b32 v27, v62, v59, s12\n"
"s_nop 1\n"
"v_mfma_f32_16x16x32_bf16 v[32:35], v[24:27], v[8:11], v[32:35]\n"
"s_add_u32 s52, s53, 0x387000\n"
"s_and_b32 s52, s52, s54\n"
"buffer_load_dwordx4 v[132:135], v1, s[28:31], s52 offen offset:0 nt\n"
"v_pk_sub_u16 v61, v148, s14 clamp\n"
"v_pk_sub_u16 v62, v149, s14 clamp\n"
"v_perm_b32 v58, v62, v61, s9\n"
"v_perm_b32 v61, v62, v61, s10\n"
"v_pk_sub_u16 v62, v150, s14 clamp\n"
"v_pk_sub_u16 v63, v151, s14 clamp\n"
"v_perm_b32 v59, v63, v62, s9\n"
"v_perm_b32 v62, v63, v62, s10\n"
"v_alignbit_b32 v62, v62, v62, 28\n"
"v_bfi_b32 v60, s8, v61, v62\n"
"v_bfi_b32 v61, s8, v62, v61\n"
"v_pk_mad_u16 v62, v148, 2, -1 op_sel_hi:[1,0,0]\n"
"v_pk_mad_u16 v63, v149, 2, -1 op_sel_hi:[1,0,0]\n"
"v_pk_min_u16 v62, v62, v63\n"
"v_pk_mad_u16 v63, v150, 2, -1 op_sel_hi:[1,0,0]\n"
"v_pk_min_u16 v62, v62, v63\n"
"v_pk_mad_u16 v63, v151, 2, -1 op_sel_hi:[1,0,0]\n"
"v_pk_min_u16 v62, v62, v63\n"
"v_pk_sub_u16 v62, s16, v62 clamp\n"
"v_or_b32 v61, v61, v62\n"
"v_cmp_ne_u32 vcc, 0, v61\n"
"s_cbranch_vccnz Lx_254\n"
"Lb_255:\n"
"ds_write_b32 v3, v60 offset:3080\n"
"ds_write_b64 v3, v[58:59] offset:5120\n"
"v_and_b32 v61, s8, v60\n"
"v_alignbit_b32 v62, v60, v60, 4\n"
"v_perm_b32 v24, v61, v58, s11\n"
"v_and_b32 v62, s8, v62\n"
"v_perm_b32 v25, v61, v58, s12\n"
"v_perm_b32 v26, v62, v59, s11\n"
"v_perm_b32 v27, v62, v59, s12\n"
"s_nop 1\n"
"v_mfma_f32_16x16x32_bf16 v[32:35], v[24:27], v[12:15], v[32:35]\n"
"v_accvgpr_read_b32 v60, a56\n"
"v_accvgpr_read_b32 v58, a54\n"
"v_accvgpr_read_b32 v59, a55\n"
"v_and_b32 v61, s8, v60\n"
"v_alignbit_b32 v62, v60, v60, 4\n"
"v_perm_b32 v20, v61, v58, s11\n"
"v_and_b32 v62, s8, v62\n"
"v_perm_b32 v21, v61, v58, s12\n"
"v_perm_b32 v22, v62, v59, s11\n"
"v_perm_b32 v23, v62, v59, s12\n"
"v_accvgpr_read_b32 v60, a59\n"
"v_accvgpr_read_b32 v58, a57\n"
"v_mfma_f32_16x16x32_bf16 v[36:39], v[20:23], v[12:15], v[36:39]\n"
"v_accvgpr_read_b32 v59, a58\n"
"v_and_b32 v61, s8, v60\n"
"v_alignbit_b32 v62, v60, v60, 4\n"
"v_perm_b32 v28, v61, v58, s11\n"
"v_and_b32 v62, s8, v62\n"
"v_perm_b32 v29, v61, v58, s12\n"
"v_perm_b32 v30, v62, v59, s11\n"
"v_perm_b32 v31, v62, v59, s12\n"
"s_nop 1\n"
"v_mfma_f32_16x16x32_bf16 v[36:39], v[28:31], v[16:19], v[36:39]\n"
"s_add_u32 s52, s53, 0x38e000\n"
"s_and_b32 s52, s52, s54\n"
"buffer_load_dwordx4 v[148:151], v1, s[28:31], s52 offen offset:0 nt\n"
"v_pk_sub_u16 v61, v152, s14 clamp\n"
"v_pk_sub_u16 v62, v153, s14 clamp\n"
"v_perm_b32 v58, v62, v61, s9\n"
"v_perm_b32 v61, v62, v61, s10\n"
"v_pk_sub_u16 v62, v154, s14 clamp\n"
"v_pk_sub_u16 v63, v155, s14 clamp\n"
"v_perm_b32 v59, v63, v62, s9\n"
"v_perm_b32 v62, v63, v62, s10\n"
"v_alignbit_b32 v62, v62, v62, 28\n"
"v_bfi_b32 v60, s8, v61, v62\n"
"v_bfi_b32 v61, s8, v62, v61\n"
"v_pk_mad_u16 v62, v152, 2, -1 op_sel_hi:[1,0,0]\n"
"v_pk_mad_u16 v63, v153, 2, -1 op_sel_hi:[1,0,0]\n"
"v_pk_min_u16 v62, v62, v63\n"
"v_pk_mad_u16 v63, v154, 2, -1 op_sel_hi:[1,0,0]\n"
"v_pk_min_u16 v62, v62, v63\n"
"v_pk_mad_u16 v63, v155, 2, -1 op_sel_hi:[1,0,0]\n"
"v_pk_min_u16 v62, v62, v63\n"
"v_pk_sub_u16 v62, s16, v62 clamp\n"
"v_or_b32 v61, v61, v62\n"
"v_cmp_ne_u32 vcc, 0, v61\n"
"s_cbranch_vccnz Lx_256\n"
"Lb_257:\n"
"ds_write_b32 v3, v60 offset:3084\n"
"ds_write_b64 v3, v[58:59] offset:5128\n"
"v_and_b32 v61, s8, v60\n"
"v_alignbit_b32 v62, v60, v60, 4\n"
"v_perm_b32 v24, v61, v58, s11\n"
"v_and_b32 v62, s8, v62\n"
"v_perm_b32 v25, v61, v58, s12\n"
"v_perm_b32 v26, v62, v59, s11\n"
"v_perm_b32 v27, v62, v59, s12\n"
"s_nop 1\n"
"v_mfma_f32_16x16x32_bf16 v[32:35], v[24:27], v[16:19], v[32:35]\n"
"v_accvgpr_read_b32 v60, a62\n"
"v_accvgpr_read_b32 v58, a60\n"
"v_accvgpr_read_b32 v59, a61\n"
"v_and_b32 v61, s8, v60\n"
"v_alignbit_b32 v62, v60, v60, 4\n"
"v_perm_b32 v20, v61, v58, s11\n"
"v_and_b32 v62, s8, v62\n"
"v_perm_b32 v21, v61, v58, s12\n"
"v_perm_b32 v22, v62, v59, s11\n"
"v_perm_b32 v23, v62, v59, s12\n"
"v_mov_b32_dpp v63, v36 quad_perm:[0,0,0,0] row_mask:0xf bank_mask:0xf\n"
"v_add_f32_dpp v63, v37, v63 quad_perm:[1,1,1,1] row_mask:0xf bank_mask:0xf\n"
"v_add_f32_dpp v63, v38, v63 quad_perm:[2,2,2,2] row_mask:0xf bank_mask:0xf\n"
"v_add_f32_dpp v63, v39, v63 quad_perm:[3,3,3,3] row_mask:0xf bank_mask:0xf\n"
"v_cndmask_b32_e64 v45, v45, v63, s[60:61]\n"
"v_accvgpr_read_b32 v60, a65\n"
"v_accvgpr_read_b32 v58, a63\n"
"v_mfma_f32_16x16x32_bf16 v[36:39], v[20:23], v[4:7], 0\n"
"v_accvgpr_read_b32 v59, a64\n"
"v_and_b32 v61, s8, v60\n"
"v_alignbit_b32 v62, v60, v60, 4\n"
"v_perm_b32 v28, v61, v58, s11\n"
"v_and_b32 v62, s8, v62\n"
"v_perm_b32 v29, v61, v58, s12\n"
"v_perm_b32 v30, v62, v59, s11\n"
"v_perm_b32 v31, v62, v59, s12\n"
"s_nop 1\n"
"v_mfma_f32_16x16x32_bf16 v[36:39], v[28:31], v[8:11], v[36:39]\n"
"Lpace_258:\n"
"s_memrealtime s[96:97]\n"
"s_waitcnt lgkmcnt(0)\n"
"s_sub_u32 s96, s96, s98\n"
"s_cmp_ge_u32 s96, 1799\n"
"s_cbranch_scc1 Lpgo_259\n"
"s_sleep 2\n"
"s_branch Lpace_258\n"
"Lpgo_259:\n"
"s_add_u32 s52, s53, 0x395000\n"
"s_and_b32 s52, s52, s54\n"
"buffer_load_dwordx4 v[152:155], v1, s[28:31], s52 offen offset:0 nt\n"
"s_waitcnt vmcnt(35)\n"
"v_pk_sub_u16 v61, v156, s14 clamp\n"
"v_pk_sub_u16 v62, v157, s14 clamp\n"
"v_perm_b32 v58, v62, v61, s9\n"
"v_perm_b32 v61, v62, v61, s10\n"
"v_pk_sub_u16 v62, v158, s14 clamp\n"
"v_pk_sub_u16 v63, v159, s14 clamp\n"
"v_perm_b32 v59, v63, v62, s9\n"
"v_perm_b32 v62, v63, v62, s10\n"
"v_alignbit_b32 v62, v62, v62, 28\n"
"v_bfi_b32 v60, s8, v61, v62\n"
"v_bfi_b32 v61, s8, v62, v61\n"
"v_pk_mad_u16 v62, v156, 2, -1 op_sel_hi:[1,0,0]\n"
"v_pk_mad_u16 v63, v157, 2, -1 op_sel_hi:[1,0,0]\n"
"v_pk_min_u16 v62, v62, v63\n"
"v_pk_mad_u16 v63, v158, 2, -1 op_sel_hi:[1,0,0]\n"
"v_pk_min_u16 v62, v62, v63\n"
"v_pk_mad_u16 v63, v159, 2, -1 op_sel_hi:[1,0,0]\n"
"v_pk_min_u16 v62, v62, v63\n"
"v_pk_sub_u16 v62, s16, v62 clamp\n"
"v_or_b32 v61, v61, v62\n"
"v_cmp_ne_u32 vcc, 0, v61\n"
"s_cbranch_vccnz Lx_260\n"
"Lb_261:\n"
"ds_write_b32 v3, v60 offset:6144\n"
"ds_write_b64 v3, v[58:59] offset:7168\n"
"v_and_b32 v61, s8, v60\n"
"v_alignbit_b32 v62, v60, v60, 4\n"
"v_perm_b32 v24, v61, v58, s11\n"
"v_and_b32 v62, s8, v62\n"
"v_perm_b32 v25, v61, v58, s12\n"
"v_perm_b32 v26, v62, v59, s11\n"
"v_perm_b32 v27, v62, v59, s12\n"
"v_mov_b32_dpp v63, v32 quad_perm:[0,0,0,0] row_mask:0xf bank_mask:0xf\n"
"v_add_f32_dpp v63, v33, v63 quad_perm:[1,1,1,1] row_mask:0xf bank_mask:0xf\n"
"v_add_f32_dpp v63, v34, v63 quad_perm:[2,2,2,2] row_mask:0xf bank_mask:0xf\n"
"v_add_f32_dpp v63, v35, v63 quad_perm:[3,3,3,3] row_mask:0xf bank_mask:0xf\n"
"v_cndmask_b32_e64 v49, v49, v63, s[66:67]\n"
"v_mfma_f32_16x16x32_bf16 v[32:35], v[24:27], v[4:7], 0\n"
"v_accvgpr_read_b32 v60, a68\n"
"v_accvgpr_read_b32 v58, a66\n"
"v_accvgpr_read_b32 v59, a67\n"
"v_and_b32 v61, s8, v60\n"
"v_alignbit_b32 v62, v60, v60, 4\n"
"v_perm_b32 v20, v61, v58, s11\n"
"v_and_b32 v62, s8, v62\n"
"v_perm_b32 v21, v61, v58, s12\n"
"v_perm_b32 v22, v62, v59, s11\n"
"v_perm_b32 v23, v62, v59, s12\n"
"v_accvgpr_read_b32 v60, a71\n"
"v_accvgpr_read_b32 v58, a69\n"
"v_mfma_f32_16x16x32_bf16 v[36:39], v[20:23], v[12:15], v[36:39]\n"
"v_accvgpr_read_b32 v59, a70\n"
"v_and_b32 v61, s8, v60\n"
"v_alignbit_b32 v62, v60, v60, 4\n"
"v_perm_b32 v28, v61, v58, s11\n"
"v_and_b32 v62, s8, v62\n"
"v_perm_b32 v29, v61, v58, s12\n"
"v_perm_b32 v30, v62, v59, s11\n"
"v_perm_b32 v31, v62, v59, s12\n"
"s_nop 1\n"
"v_mfma_f32_16x16x32_bf16 v[36:39], v[28:31], v[16:19], v[36:39]\n"
"s_add_u32 s52, s53, 0x39c000\n"
"s_and_b32 s52, s52, s54\n"
"buffer_load_dwordx4 v[156:159], v1, s[28:31], s52 offen offset:0 nt\n"
"s_waitcnt vmcnt(35)\n"
"v_pk_sub_u16 v61, v160, s14 clamp\n"
"v_pk_sub_u16 v62, v161, s14 clamp\n"
"v_perm_b32 v58, v62, v61, s9\n"
"v_perm_b32 v61, v62, v61, s10\n"
"v_pk_sub_u16 v62, v162, s14 clamp\n"
"v_pk_sub_u16 v63, v163, s14 clamp\n"
"v_perm_b32 v59, v63, v62, s9\n"
"v_perm_b32 v62, v63, v62, s10\n"
"v_alignbit_b32 v62, v62, v62, 28\n"
"v_bfi_b32 v60, s8, v61, v62\n"
"v_bfi_b32 v61, s8, v62, v61\n"
"v_pk_mad_u16 v62, v160, 2, -1 op_sel_hi:[1,0,0]\n"
"v_pk_mad_u16 v63, v161, 2, -1 op_sel_hi:[1,0,0]\n"
"v_pk_min_u16 v62, v62, v63\n"
"v_pk_mad_u16 v63, v162, 2, -1 op_sel_hi:[1,0,0]\n"
"v_pk_min_u16 v62, v62, v63\n"
"v_pk_mad_u16 v63, v163, 2, -1 op_sel_hi:[1,0,0]\n"
"v_pk_min_u16 v62, v62, v63\n"
"v_pk_sub_u16 v62, s16, v62 clamp\n"
"v_or_b32 v61, v61, v62\n"
"v_cmp_ne_u32 vcc, 0, v61\n"
"s_cbranch_vccnz Lx_262\n"
"Lb_263:\n"
"ds_write_b32 v3, v60 offset:6148\n"
"ds_write_b64 v3, v[58:59] offset:7176\n"
"v_and_b32 v61, s8, v60\n"
"v_alignbit_b32 v62, v60, v60, 4\n"
"v_perm_b32 v24, v61, v58, s11\n"
"v_and_b32 v62, s8, v62\n"
"v_perm_b32 v25, v61, v58, s12\n"
"v_perm_b32 v26, v62, v59, s11\n"
"v_perm_b32 v27, v62, v59, s12\n"
"s_nop 1\n"
"v_mfma_f32_16x16x32_bf16 v[32:35], v[24:27], v[8:11], v[32:35]\n"
"v_accvgpr_read_b32 v60, a74\n"
"v_accvgpr_read_b32 v58, a72\n"
"v_accvgpr_read_b32 v59, a73\n"
"v_and_b32 v61, s8, v60\n"
"v_alignbit_b32 v62, v60, v60, 4\n"
"v_perm_b32 v20, v61, v58, s11\n"
"v_and_b32 v62, s8, v62\n"
"v_perm_b32 v21, v61, v58, s12\n"
"v_perm_b32 v22, v62, v59, s11\n"
"v_perm_b32 v23, v62, v59, s12\n"
"v_mov_b32_dpp v63, v36 quad_perm:[0,0,0,0] row_mask:0xf bank_mask:0xf\n"
"v_add_f32_dpp v63, v37, v63 quad_perm:[1,1,1,1] row_mask:0xf bank_mask:0xf\n"
"v_add_f32_dpp v63, v38, v63 quad_perm:[2,2,2,2] row_mask:0xf bank_mask:0xf\n"
"v_add_f32_dpp v63, v39, v63 quad_perm:[3,3,3,3] row_mask:0xf bank_mask:0xf\n"
"v_cndmask_b32_e64 v45, v45, v63, s[62:63]\n"
"v_accvgpr_read_b32 v60, a77\n"
"v_accvgpr_read_b32 v58, a75\n"
"v_mfma_f32_16x16x32_bf16 v[36:39], v[20:23], v[4:7], 0\n"
"v_accvgpr_read_b32 v59, a76\n"
"v_and_b32 v61, s8, v60\n"
"v_alignbit_b32 v62, v60, v60, 4\n"
"v_perm_b32 v28, v61, v58, s11\n"
"v_and_b32 v62, s8, v62\n"
"v_perm_b32 v29, v61, v58, s12\n"
"v_perm_b32 v30, v62, v59, s11\n"
"v_perm_b32 v31, v62, v59, s12\n"
"s_nop 1\n"
"v_mfma_f32_16x16x32_bf16 v[36:39], v[28:31], v[8:11], v[36:39]\n"
"s_add_u32 s52, s53, 0x3a3000\n"
"s_and_b32 s52, s52, s54\n"
"buffer_load_dwordx4 v[160:163], v1, s[28:31], s52 offen offset:0 nt\n"
"s_waitcnt vmcnt(35)\n"
"v_pk_sub_u16 v61, v164, s14 clamp\n"
"v_pk_sub_u16 v62, v165, s14 clamp\n"
"v_perm_b32 v58, v62, v61, s9\n"
"v_perm_b32 v61, v62, v61, s10\n"
"v_pk_sub_u16 v62, v166, s14 clamp\n"
"v_pk_sub_u16 v63, v167, s14 clamp\n"
"v_perm_b32 v59, v63, v62, s9\n"
"v_perm_b32 v62, v63, v62, s10\n"
"v_alignbit_b32 v62, v62, v62, 28\n"
"v_bfi_b32 v60, s8, v61, v62\n"
"v_bfi_b32 v61, s8, v62, v61\n"
"v_pk_mad_u16 v62, v164, 2, -1 op_sel_hi:[1,0,0]\n"
"v_pk_mad_u16 v63, v165, 2, -1 op_sel_hi:[1,0,0]\n"
"v_pk_min_u16 v62, v62, v63\n"
"v_pk_mad_u16 v63, v166, 2, -1 op_sel_hi:[1,0,0]\n"
"v_pk_min_u16 v62, v62, v63\n"
"v_pk_mad_u16 v63, v167, 2, -1 op_sel_hi:[1,0,0]\n"
"v_pk_min_u16 v62, v62, v63\n"
"v_pk_sub_u16 v62, s16, v62 clamp\n"
"v_or_b32 v61, v61, v62\n"
"v_cmp_ne_u32 vcc, 0, v61\n"
"s_cbranch_vccnz Lx_264\n"
"Lb_265:\n"
"ds_write_b32 v3, v60 offset:6152\n"
"ds_write_b64 v3, v[58:59] offset:8192\n"
"v_and_b32 v61, s8, v60\n"
"v_alignbit_b32 v62, v60, v60, 4\n"
"v_perm_b32 v24, v61, v58, s11\n"
"v_and_b32 v62, s8, v62\n"
"v_perm_b32 v25, v61, v58, s12\n"
"v_perm_b32 v26, v62, v59, s11\n"
"v_perm_b32 v27, v62, v59, s12\n"
"s_nop 1\n"
"v_mfma_f32_16x16x32_bf16 v[32:35], v[24:27], v[12:15], v[32:35]\n"
"s_add_u32 s52, s53, 0x3aa000\n"
"s_and_b32 s52, s52, s54\n"
"buffer_load_dwordx4 v[164:167], v1, s[28:31], s52 offen offset:0 nt\n"
"s_waitcnt vmcnt(35)\n"
"v_pk_sub_u16 v61, v168, s14 clamp\n"
"v_pk_sub_u16 v62, v169, s14 clamp\n"
"v_perm_b32 v58, v62, v61, s9\n"
"v_perm_b32 v61, v62, v61, s10\n"
"v_pk_sub_u16 v62, v170, s14 clamp\n"
"v_pk_sub_u16 v63, v171, s14 clamp\n"
"v_perm_b32 v59, v63, v62, s9\n"
"v_perm_b32 v62, v63, v62, s10\n"
"v_alignbit_b32 v62, v62, v62, 28\n"
"v_bfi_b32 v60, s8, v61, v62\n"
"v_bfi_b32 v61, s8, v62, v61\n"
"v_pk_mad_u16 v62, v168, 2, -1 op_sel_hi:[1,0,0]\n"
"v_pk_mad_u16 v63, v169, 2, -1 op_sel_hi:[1,0,0]\n"
"v_pk_min_u16 v62, v62, v63\n"
"v_pk_mad_u16 v63, v170, 2, -1 op_sel_hi:[1,0,0]\n"
"v_pk_min_u16 v62, v62, v63\n"
"v_pk_mad_u16 v63, v171, 2, -1 op_sel_hi:[1,0,0]\n"
"v_pk_min_u16 v62, v62, v63\n"
"v_pk_sub_u16 v62, s16, v62 clamp\n"
"v_or_b32 v61, v61, v62\n"
"v_cmp_ne_u32 vcc, 0, v61\n"
"s_cbranch_vccnz Lx_266\n"
"Lb_267:\n"
"ds_write_b32 v3, v60 offset:6156\n"
"ds_write_b64 v3, v[58:59] offset:8200\n"
"v_and_b32 v61, s8, v60\n"
"v_alignbit_b32 v62, v60, v60, 4\n"
"v_perm_b32 v24, v61, v58, s11\n"
"v_and_b32 v62, s8, v62\n"
"v_perm_b32 v25, v61, v58, s12\n"
"v_perm_b32 v26, v62, v59, s11\n"
"v_perm_b32 v27, v62, v59, s12\n"
"s_nop 1\n"
"v_mfma_f32_16x16x32_bf16 v[32:35], v[24:27], v[16:19], v[32:35]\n"
"v_accvgpr_read_b32 v60, a80\n"
"v_accvgpr_read_b32 v58, a78\n"
"v_accvgpr_read_b32 v59, a79\n"
"v_and_b32 v61, s8, v60\n"
"v_alignbit_b32 v62, v60, v60, 4\n"
"v_perm_b32 v20, v61, v58, s11\n"
"v_and_b32 v62, s8, v62\n"
"v_perm_b32 v21, v61, v58, s12\n"
"v_perm_b32 v22, v62, v59, s11\n"
"v_perm_b32 v23, v62, v59, s12\n"
"v_accvgpr_read_b32 v60, a83\n"
"v_accvgpr_read_b32 v58, a81\n"
"v_mfma_f32_16x16x32_bf16 v[36:39], v[20:23], v[12:15], v[36:39]\n"
"v_accvgpr_read_b32 v59, a82\n"
"v_and_b32 v61, s8, v60\n"
"v_alignbit_b32 v62, v60, v60, 4\n"
"v_perm_b32 v28, v61, v58, s11\n"
"v_and_b32 v62, s8, v62\n"
"v_perm_b32 v29, v61, v58, s12\n"
"v_perm_b32 v30, v62, v59, s11\n"
"v_perm_b32 v31, v62, v59, s12\n"
"s_nop 1\n"
"v_mfma_f32_16x16x32_bf16 v[36:39], v[28:31], v[16:19], v[36:39]\n"
"Lpace_268:\n"
"s_memrealtime s[96:97]\n"
"s_waitcnt lgkmcnt(0)\n"
"s_sub_u32 s96, s96, s98\n"
"s_cmp_ge_u32 s96, 1856\n"
"s_cbranch_scc1 Lpgo_269\n"
"s_sleep 2\n"
"s_branch Lpace_268\n"
"Lpgo_269:\n"
"s_add_u32 s52, s53, 0x3b1000\n"
"s_and_b32 s52, s52, s54\n"
"buffer_load_dwordx4 v[168:171], v1, s[28:31], s52 offen offset:0 nt\n"
"s_waitcnt vmcnt(35)\n"
"v_pk_sub_u16 v61, v184, s14 clamp\n"
"v_pk_sub_u16 v62, v185, s14 clamp\n"
"v_perm_b32 v58, v62, v61, s9\n"
"v_perm_b32 v61, v62, v61, s10\n"
"v_pk_sub_u16 v62, v186, s14 clamp\n"
"v_pk_sub_u16 v63, v187, s14 clamp\n"
"v_perm_b32 v59, v63, v62, s9\n"
"v_perm_b32 v62, v63, v62, s10\n"
"v_alignbit_b32 v62, v62, v62, 28\n"
"v_bfi_b32 v60, s8, v61, v62\n"
"v_bfi_b32 v61, s8, v62, v61\n"
"v_pk_mad_u16 v62, v184, 2, -1 op_sel_hi:[1,0,0]\n"
"v_pk_mad_u16 v63, v185, 2, -1 op_sel_hi:[1,0,0]\n"
"v_pk_min_u16 v62, v62, v63\n"
"v_pk_mad_u16 v63, v186, 2, -1 op_sel_hi:[1,0,0]\n"
"v_pk_min_u16 v62, v62, v63\n"
"v_pk_mad_u16 v63, v187, 2, -1 op_sel_hi:[1,0,0]\n"
"v_pk_min_u16 v62, v62, v63\n"
"v_pk_sub_u16 v62, s16, v62 clamp\n"
"v_or_b32 v61, v61, v62\n"
"v_cmp_ne_u32 vcc, 0, v61\n"
"s_cbranch_vccnz Lx_270\n"
"Lb_271:\n"
"ds_write_b32 v3, v60 offset:9216\n"
"ds_write_b64 v3, v[58:59] offset:10240\n"
"v_and_b32 v61, s8, v60\n"
"v_alignbit_b32 v62, v60, v60, 4\n"
"v_perm_b32 v24, v61, v58, s11\n"
"v_and_b32 v62, s8, v62\n"
"v_perm_b32 v25, v61, v58, s12\n"
"v_perm_b32 v26, v62, v59, s11\n"
"v_perm_b32 v27, v62, v59, s12\n"
"v_mov_b32_dpp v63, v32 quad_perm:[0,0,0,0] row_mask:0xf bank_mask:0xf\n"
"v_add_f32_dpp v63, v33, v63 quad_perm:[1,1,1,1] row_mask:0xf bank_mask:0xf\n"
"v_add_f32_dpp v63, v34, v63 quad_perm:[2,2,2,2] row_mask:0xf bank_mask:0xf\n"
"v_add_f32_dpp v63, v35, v63 quad_perm:[3,3,3,3] row_mask:0xf bank_mask:0xf\n"
"v_cndmask_b32_e64 v50, v50, v63, s[60:61]\n"
"v_mfma_f32_16x16x32_bf16 v[32:35], v[24:27], v[4:7], 0\n"
"v_accvgpr_read_b32 v60, a86\n"
"v_accvgpr_read_b32 v58, a84\n"
"v_accvgpr_read_b32 v59, a85\n"
"v_and_b32 v61, s8, v60\n"
"v_alignbit_b32 v62, v60, v60, 4\n"
"v_perm_b32 v20, v61, v58, s11\n"
"v_and_b32 v62, s8, v62\n"
"v_perm_b32 v21, v61, v58, s12\n"
"v_perm_b32 v22, v62, v59, s11\n"
"v_perm_b32 v23, v62, v59, s12\n"
"v_mov_b32_dpp v63, v36 quad_perm:[0,0,0,0] row_mask:0xf bank_mask:0xf\n"
"v_add_f32_dpp v63, v37, v63 quad_perm:[1,1,1,1] row_mask:0xf bank_mask:0xf\n"
"v_add_f32_dpp v63, v38, v63 quad_perm:[2,2,2,2] row_mask:0xf bank_mask:0xf\n"
"v_add_f32_dpp v63, v39, v63 quad_perm:[3,3,3,3] row_mask:0xf bank_mask:0xf\n"
"v_cndmask_b32_e64 v45, v45, v63, s[64:65]\n"
"v_accvgpr_read_b32 v60, a89\n"
"v_accvgpr_read_b32 v58, a87\n"
"v_mfma_f32_16x16x32_bf16 v[36:39], v[20:23], v[4:7], 0\n"
"v_accvgpr_read_b32 v59, a88\n"
"v_and_b32 v61, s8, v60\n"
"v_alignbit_b32 v62, v60, v60, 4\n"
"v_perm_b32 v28, v61, v58, s11\n"
"v_and_b32 v62, s8, v62\n"
"v_perm_b32 v29, v61, v58, s12\n"
"v_perm_b32 v30, v62, v59, s11\n"
"v_perm_b32 v31, v62, v59, s12\n"
"s_nop 1\n"
"v_mfma_f32_16x16x32_bf16 v[36:39], v[28:31], v[8:11], v[36:39]\n"
"s_add_u32 s52, s53, 0x3b8000\n"
"s_and_b32 s52, s52, s54\n"
"buffer_load_dwordx4 v[184:187], v1, s[28:31], s52 offen offset:0 nt\n"
"s_waitcnt vmcnt(35)\n"
"v_pk_sub_u16 v61, v188, s14 clamp\n"
"v_pk_sub_u16 v62, v189, s14 clamp\n"
"v_perm_b32 v58, v62, v61, s9\n"
"v_perm_b32 v61, v62, v61, s10\n"
"v_pk_sub_u16 v62, v190, s14 clamp\n"
"v_pk_sub_u16 v63, v191, s14 clamp\n"
"v_perm_b32 v59, v63, v62, s9\n"
"v_perm_b32 v62, v63, v62, s10\n"
"v_alignbit_b32 v62, v62, v62, 28\n"
"v_bfi_b32 v60, s8, v61, v62\n"
"v_bfi_b32 v61, s8, v62, v61\n"
"v_pk_mad_u16 v62, v188, 2, -1 op_sel_hi:[1,0,0]\n"
"v_pk_mad_u16 v63, v189, 2, -1 op_sel_hi:[1,0,0]\n"
"v_pk_min_u16 v62, v62, v63\n"
"v_pk_mad_u16 v63, v190, 2, -1 op_sel_hi:[1,0,0]\n"
"v_pk_min_u16 v62, v62, v63\n"
"v_pk_mad_u16 v63, v191, 2, -1 op_sel_hi:[1,0,0]\n"
"v_pk_min_u16 v62, v62, v63\n"
"v_pk_sub_u16 v62, s16, v62 clamp\n"
"v_or_b32 v61, v61, v62\n"
"v_cmp_ne_u32 vcc, 0, v61\n"
"s_cbranch_vccnz Lx_272\n"
"Lb_273:\n"
"ds_write_b32 v3, v60 offset:9220\n"
"ds_write_b64 v3, v[58:59] offset:10248\n"
"v_and_b32 v61, s8, v60\n"
"v_alignbit_b32 v62, v60, v60, 4\n"
"v_perm_b32 v24, v61, v58, s11\n"
"v_and_b32 v62, s8, v62\n"
"v_perm_b32 v25, v61, v58, s12\n"
"v_perm_b32 v26, v62, v59, s11\n"
"v_perm_b32 v27, v62, v59, s12\n"
"s_nop 1\n"
"v_mfma_f32_16x16x32_bf16 v[32:35], v[24:27], v[8:11], v[32:35]\n"
"s_add_u32 s52, s53, 0x3bf000\n"
"s_and_b32 s52, s52, s54\n"
"buffer_load_dwordx4 v[188:191], v1, s[28:31], s52 offen offset:0 nt\n"
"s_waitcnt vmcnt(35)\n"
"v_pk_sub_u16 v61, v192, s14 clamp\n"
"v_pk_sub_u16 v62, v193, s14 clamp\n"
"v_perm_b32 v58, v62, v61, s9\n"
"v_perm_b32 v61, v62, v61, s10\n"
"v_pk_sub_u16 v62, v194, s14 clamp\n"
"v_pk_sub_u16 v63, v195, s14 clamp\n"
"v_perm_b32 v59, v63, v62, s9\n"
"v_perm_b32 v62, v63, v62, s10\n"
"v_alignbit_b32 v62, v62, v62, 28\n"
"v_bfi_b32 v60, s8, v61, v62\n"
"v_bfi_b32 v61, s8, v62, v61\n"
"v_pk_mad_u16 v62, v192, 2, -1 op_sel_hi:[1,0,0]\n"
"v_pk_mad_u16 v63, v193, 2, -1 op_sel_hi:[1,0,0]\n"
"v_pk_min_u16 v62, v62, v63\n"
"v_pk_mad_u16 v63, v194, 2, -1 op_sel_hi:[1,0,0]\n"
"v_pk_min_u16 v62, v62, v63\n"
"v_pk_mad_u16 v63, v195, 2, -1 op_sel_hi:[1,0,0]\n"
"v_pk_min_u16 v62, v62, v63\n"
"v_pk_sub_u16 v62, s16, v62 clamp\n"
"v_or_b32 v61, v61, v62\n"
"v_cmp_ne_u32 vcc, 0, v61\n"
"s_cbranch_vccnz Lx_274\n"
"Lb_275:\n"
"ds_write_b32 v3, v60 offset:9224\n"
"ds_write_b64 v3, v[58:59] offset:11264\n"
"v_and_b32 v61, s8, v60\n"
"v_alignbit_b32 v62, v60, v60, 4\n"
"v_perm_b32 v24, v61, v58, s11\n"
"v_and_b32 v62, s8, v62\n"
"v_perm_b32 v25, v61, v58, s12\n"
"v_perm_b32 v26, v62, v59, s11\n"
"v_perm_b32 v27, v62, v59, s12\n"
"s_nop 1\n"
"v_mfma_f32_16x16x32_bf16 v[32:35], v[24:27], v[12:15], v[32:35]\n"
"v_accvgpr_read_b32 v60, a92\n"
"v_accvgpr_read_b32 v58, a90\n"
"v_accvgpr_read_b32 v59, a91\n"
"v_and_b32 v61, s8, v60\n"
"v_alignbit_b32 v62, v60, v60, 4\n"
"v_perm_b32 v20, v61, v58, s11\n"
"v_and_b32 v62, s8, v62\n"
"v_perm_b32 v21, v61, v58, s12\n"
"v_perm_b32 v22, v62, v59, s11\n"
"v_perm_b32 v23, v62, v59, s12\n"
"v_accvgpr_read_b32 v60, a95\n"
"v_accvgpr_read_b32 v58, a93\n"
"v_mfma_f32_16x16x32_bf16 v[36:39], v[20:23], v[12:15], v[36:39]\n"
"v_accvgpr_read_b32 v59, a94\n"
"v_and_b32 v61, s8, v60\n"
"v_alignbit_b32 v62, v60, v60, 4\n"
"v_perm_b32 v28, v61, v58, s11\n"
"v_and_b32 v62, s8, v62\n"
"v_perm_b32 v29, v61, v58, s12\n"
"v_perm_b32 v30, v62, v59, s11\n"
"v_perm_b32 v31, v62, v59, s12\n"
"s_nop 1\n"
"v_mfma_f32_16x16x32_bf16 v[36:39], v[28:31], v[16:19], v[36:39]\n"
"s_add_u32 s52, s53, 0x3c6000\n"
"s_and_b32 s52, s52, s54\n"
"buffer_load_dwordx4 v[192:195], v1, s[28:31], s52 offen offset:0 nt\n"
"s_waitcnt vmcnt(35)\n"
"v_pk_sub_u16 v61, v208, s14 clamp\n"
"v_pk_sub_u16 v62, v209, s14 clamp\n"
"v_perm_b32 v58, v62, v61, s9\n"
"v_perm_b32 v61, v62, v61, s10\n"
"v_pk_sub_u16 v62, v210, s14 clamp\n"
"v_pk_sub_u16 v63, v211, s14 clamp\n"
"v_perm_b32 v59, v63, v62, s9\n"
"v_perm_b32 v62, v63, v62, s10\n"
"v_alignbit_b32 v62, v62, v62, 28\n"
"v_bfi_b32 v60, s8, v61, v62\n"
"v_bfi_b32 v61, s8, v62, v61\n"
"v_pk_mad_u16 v62, v208, 2, -1 op_sel_hi:[1,0,0]\n"
"v_pk_mad_u16 v63, v209, 2, -1 op_sel_hi:[1,0,0]\n"
"v_pk_min_u16 v62, v62, v63\n"
"v_pk_mad_u16 v63, v210, 2, -1 op_sel_hi:[1,0,0]\n"
"v_pk_min_u16 v62, v62, v63\n"
"v_pk_mad_u16 v63, v211, 2, -1 op_sel_hi:[1,0,0]\n"
"v_pk_min_u16 v62, v62, v63\n"
"v_pk_sub_u16 v62, s16, v62 clamp\n"
"v_or_b32 v61, v61, v62\n"
"v_cmp_ne_u32 vcc, 0, v61\n"
"s_cbranch_vccnz Lx_276\n"
"Lb_277:\n"
"ds_write_b32 v3, v60 offset:9228\n"
"ds_write_b64 v3, v[58:59] offset:11272\n"
"v_and_b32 v61, s8, v60\n"
"v_alignbit_b32 v62, v60, v60, 4\n"
"v_perm_b32 v24, v61, v58, s11\n"
"v_and_b32 v62, s8, v62\n"
"v_perm_b32 v25, v61, v58, s12\n"
"v_perm_b32 v26, v62, v59, s11\n"
"v_perm_b32 v27, v62, v59, s12\n"
"s_nop 1\n"
"v_mfma_f32_16x16x32_bf16 v[32:35], v[24:27], v[16:19], v[32:35]\n"
"v_accvgpr_read_b32 v60, a98\n"
"v_accvgpr_read_b32 v58, a96\n"
"v_accvgpr_read_b32 v59, a97\n"
"v_and_b32 v61, s8, v60\n"
"v_alignbit_b32 v62, v60, v60, 4\n"
"v_perm_b32 v20, v61, v58, s11\n"
"v_and_b32 v62, s8, v62\n"
"v_perm_b32 v21, v61, v58, s12\n"
"v_perm_b32 v22, v62, v59, s11\n"
"v_perm_b32 v23, v62, v59, s12\n"
"v_mov_b32_dpp v63, v36 quad_perm:[0,0,0,0] row_mask:0xf bank_mask:0xf\n"
"v_add_f32_dpp v63, v37, v63 quad_perm:[1,1,1,1] row_mask:0xf bank_mask:0xf\n"
"v_add_f32_dpp v63, v38, v63 quad_perm:[2,2,2,2] row_mask:0xf bank_mask:0xf\n"
"v_add_f32_dpp v63, v39, v63 quad_perm:[3,3,3,3] row_mask:0xf bank_mask:0xf\n"
"v_cndmask_b32_e64 v45, v45, v63, s[66:67]\n"
"v_accvgpr_read_b32 v60, a101\n"
"v_accvgpr_read_b32 v58, a99\n"
"v_mfma_f32_16x16x32_bf16 v[36:39], v[20:23], v[4:7], 0\n"
"v_accvgpr_read_b32 v59, a100\n"
"v_and_b32 v61, s8, v60\n"
"v_alignbit_b32 v62, v60, v60, 4\n"
"v_perm_b32 v28, v61, v58, s11\n"
"v_and_b32 v62, s8, v62\n"
"v_perm_b32 v29, v61, v58, s12\n"
"v_perm_b32 v30, v62, v59, s11\n"
"v_perm_b32 v31, v62, v59, s12\n"
"s_nop 1\n"
"v_mfma_f32_16x16x32_bf16 v[36:39], v[28:31], v[8:11], v[36:39]\n"
"Lpace_278:\n"
"s_memrealtime s[96:97]\n"
"s_waitcnt lgkmcnt(0)\n"
"s_sub_u32 s96, s96, s98\n"
"s_cmp_ge_u32 s96, 1913\n"
"s_cbranch_scc1 Lpgo_279\n"
"s_sleep 2\n"
"s_branch Lpace_278\n"
"Lpgo_279:\n"
"s_add_u32 s52, s53, 0x3cd000\n"
"s_and_b32 s52, s52, s54\n"
"buffer_load_dwordx4 v[208:211], v1, s[28:31], s52 offen offset:0 nt\n"
"s_waitcnt vmcnt(35)\n"
"v_pk_sub_u16 v61, v212, s14 clamp\n"
"v_pk_sub_u16 v62, v213, s14 clamp\n"
"v_perm_b32 v58, v62, v61, s9\n"
"v_perm_b32 v61, v62, v61, s10\n"
"v_pk_sub_u16 v62, v214, s14 clamp\n"
"v_pk_sub_u16 v63, v215, s14 clamp\n"
"v_perm_b32 v59, v63, v62, s9\n"
"v_perm_b32 v62, v63, v62, s10\n"
"v_alignbit_b32 v62, v62, v62, 28\n"
"v_bfi_b32 v60, s8, v61, v62\n"
"v_bfi_b32 v61, s8, v62, v61\n"
"v_pk_mad_u16 v62, v212, 2, -1 op_sel_hi:[1,0,0]\n"
"v_pk_mad_u16 v63, v213, 2, -1 op_sel_hi:[1,0,0]\n"
"v_pk_min_u16 v62, v62, v63\n"
"v_pk_mad_u16 v63, v214, 2, -1 op_sel_hi:[1,0,0]\n"
"v_pk_min_u16 v62, v62, v63\n"
"v_pk_mad_u16 v63, v215, 2, -1 op_sel_hi:[1,0,0]\n"
"v_pk_min_u16 v62, v62, v63\n"
"v_pk_sub_u16 v62, s16, v62 clamp\n"
"v_or_b32 v61, v61, v62\n"
"v_cmp_ne_u32 vcc, 0, v61\n"
"s_cbranch_vccnz Lx_280\n"
"Lb_281:\n"
"ds_write_b32 v3, v60 offset:12288\n"
"ds_write_b64 v3, v[58:59] offset:13312\n"
"v_and_b32 v61, s8, v60\n"
"v_alignbit_b32 v62, v60, v60, 4\n"
"v_perm_b32 v24, v61, v58, s11\n"
"v_and_b32 v62, s8, v62\n"
"v_perm_b32 v25, v61, v58, s12\n"
"v_perm_b32 v26, v62, v59, s11\n"
"v_perm_b32 v27, v62, v59, s12\n"
"v_mov_b32_dpp v63, v32 quad_perm:[0,0,0,0] row_mask:0xf bank_mask:0xf\n"
"v_add_f32_dpp v63, v33, v63 quad_perm:[1,1,1,1] row_mask:0xf bank_mask:0xf\n"
"v_add_f32_dpp v63, v34, v63 quad_perm:[2,2,2,2] row_mask:0xf bank_mask:0xf\n"
"v_add_f32_dpp v63, v35, v63 quad_perm:[3,3,3,3] row_mask:0xf bank_mask:0xf\n"
"v_cndmask_b32_e64 v50, v50, v63, s[62:63]\n"
"v_mfma_f32_16x16x32_bf16 v[32:35], v[24:27], v[4:7], 0\n"
"v_accvgpr_read_b32 v60, a104\n"
"v_accvgpr_read_b32 v58, a102\n"
"v_accvgpr_read_b32 v59, a103\n"
"v_and_b32 v61, s8, v60\n"
"v_alignbit_b32 v62, v60, v60, 4\n"
"v_perm_b32 v20, v61, v58, s11\n"
"v_and_b32 v62, s8, v62\n"
"v_perm_b32 v21, v61, v58, s12\n"
"v_perm_b32 v22, v62, v59, s11\n"
"v_perm_b32 v23, v62, v59, s12\n"
"v_accvgpr_read_b32 v60, a107\n"
"v_accvgpr_read_b32 v58, a105\n"
"v_mfma_f32_16x16x32_bf16 v[36:39], v[20:23], v[12:15], v[36:39]\n"
"v_accvgpr_read_b32 v59, a106\n"
"v_and_b32 v61, s8, v60\n"
"v_alignbit_b32 v62, v60, v60, 4\n"
"v_perm_b32 v28, v61, v58, s11\n"
"v_and_b32 v62, s8, v62\n"
"v_perm_b32 v29, v61, v58, s12\n"
"v_perm_b32 v30, v62, v59, s11\n"
"v_perm_b32 v31, v62, v59, s12\n"
"s_nop 1\n"
"v_mfma_f32_16x16x32_bf16 v[36:39], v[28:31], v[16:19], v[36:39]\n"
"s_add_u32 s52, s53, 0x0\n"
"s_and_b32 s52, s52, s54\n"
"buffer_load_dwordx4 a[216:219], v2, s[32:35], s52 offen offset:0\n"
"s_waitcnt vmcnt(35)\n"
"v_pk_sub_u16 v61, v216, s14 clamp\n"
"v_pk_sub_u16 v62, v217, s14 clamp\n"
"v_perm_b32 v58, v62, v61, s9\n"
"v_perm_b32 v61, v62, v61, s10\n"
"v_pk_sub_u16 v62, v218, s14 clamp\n"
"v_pk_sub_u16 v63, v219, s14 clamp\n"
"v_perm_b32 v59, v63, v62, s9\n"
"v_perm_b32 v62, v63, v62, s10\n"
"v_alignbit_b32 v62, v62, v62, 28\n"
"v_bfi_b32 v60, s8, v61, v62\n"
"v_bfi_b32 v61, s8, v62, v61\n"
"v_pk_mad_u16 v62, v216, 2, -1 op_sel_hi:[1,0,0]\n"
"v_pk_mad_u16 v63, v217, 2, -1 op_sel_hi:[1,0,0]\n"
"v_pk_min_u16 v62, v62, v63\n"
"v_pk_mad_u16 v63, v218, 2, -1 op_sel_hi:[1,0,0]\n"
"v_pk_min_u16 v62, v62, v63\n"
"v_pk_mad_u16 v63, v219, 2, -1 op_sel_hi:[1,0,0]\n"
"v_pk_min_u16 v62, v62, v63\n"
"v_pk_sub_u16 v62, s16, v62 clamp\n"
"v_or_b32 v61, v61, v62\n"
"v_cmp_ne_u32 vcc, 0, v61\n"
"s_cbranch_vccnz Lx_282\n"
"Lb_283:\n"
"ds_write_b32 v3, v60 offset:12292\n"
"ds_write_b64 v3, v[58:59] offset:13320\n"
"v_and_b32 v61, s8, v60\n"
"v_alignbit_b32 v62, v60, v60, 4\n"
"v_perm_b32 v24, v61, v58, s11\n"
"v_and_b32 v62, s8, v62\n"
"v_perm_b32 v25, v61, v58, s12\n"
"v_perm_b32 v26, v62, v59, s11\n"
"v_perm_b32 v27, v62, v59, s12\n"
"s_nop 1\n"
"v_mfma_f32_16x16x32_bf16 v[32:35], v[24:27], v[8:11], v[32:35]\n"
"v_accvgpr_read_b32 v60, a110\n"
"v_accvgpr_read_b32 v58, a108\n"
"v_accvgpr_read_b32 v59, a109\n"
"v_and_b32 v61, s8, v60\n"
"v_alignbit_b32 v62, v60, v60, 4\n"
"v_perm_b32 v20, v61, v58, s11\n"
"v_and_b32 v62, s8, v62\n"
"v_perm_b32 v21, v61, v58, s12\n"
"v_perm_b32 v22, v62, v59, s11\n"
"v_perm_b32 v23, v62, v59, s12\n"
"v_mov_b32_dpp v63, v36 quad_perm:[0,0,0,0] row_mask:0xf bank_mask:0xf\n"
"v_add_f32_dpp v63, v37, v63 quad_perm:[1,1,1,1] row_mask:0xf bank_mask:0xf\n"
"v_add_f32_dpp v63, v38, v63 quad_perm:[2,2,2,2] row_mask:0xf bank_mask:0xf\n"
"v_add_f32_dpp v63, v39, v63 quad_perm:[3,3,3,3] row_mask:0xf bank_mask:0xf\n"
"v_cndmask_b32_e64 v46, v46, v63, s[60:61]\n"
"v_accvgpr_read_b32 v60, a113\n"
"v_accvgpr_read_b32 v58, a111\n"
"v_mfma_f32_16x16x32_bf16 v[36:39], v[20:23], v[4:7], 0\n"
"v_accvgpr_read_b32 v59, a112\n"
"v_and_b32 v61, s8, v60\n"
"v_alignbit_b32 v62, v60, v60, 4\n"
"v_perm_b32 v28, v61, v58, s11\n"
"v_and_b32 v62, s8, v62\n"
"v_perm_b32 v29, v61, v58, s12\n"
"v_perm_b32 v30, v62, v59, s11\n"
"v_perm_b32 v31, v62, v59, s12\n"
"s_nop 1\n"
"v_mfma_f32_16x16x32_bf16 v[36:39], v[28:31], v[8:11], v[36:39]\n"
"s_add_u32 s52, s53, 0x7000\n"
"s_and_b32 s52, s52, s54\n"
"buffer_load_dwordx4 a[220:223], v2, s[32:35], s52 offen offset:0\n"
"s_waitcnt vmcnt(35)\n"
"v_pk_sub_u16 v61, v232, s14 clamp\n"
"v_pk_sub_u16 v62, v233, s14 clamp\n"
"v_perm_b32 v58, v62, v61, s9\n"
"v_perm_b32 v61, v62, v61, s10\n"
"v_pk_sub_u16 v62, v234, s14 clamp\n"
"v_pk_sub_u16 v63, v235, s14 clamp\n"
"v_perm_b32 v59, v63, v62, s9\n"
"v_perm_b32 v62, v63, v62, s10\n"
"v_alignbit_b32 v62, v62, v62, 28\n"
"v_bfi_b32 v60, s8, v61, v62\n"
"v_bfi_b32 v61, s8, v62, v61\n"
"v_pk_mad_u16 v62, v232, 2, -1 op_sel_hi:[1,0,0]\n"
"v_pk_mad_u16 v63, v233, 2, -1 op_sel_hi:[1,0,0]\n"
"v_pk_min_u16 v62, v62, v63\n"
"v_pk_mad_u16 v63, v234, 2, -1 op_sel_hi:[1,0,0]\n"
"v_pk_min_u16 v62, v62, v63\n"
"v_pk_mad_u16 v63, v235, 2, -1 op_sel_hi:[1,0,0]\n"
"v_pk_min_u16 v62, v62, v63\n"
"v_pk_sub_u16 v62, s16, v62 clamp\n"
"v_or_b32 v61, v61, v62\n"
"v_cmp_ne_u32 vcc, 0, v61\n"
"s_cbranch_vccnz Lx_284\n"
"Lb_285:\n"
"ds_write_b32 v3, v60 offset:12296\n"
"ds_write_b64 v3, v[58:59] offset:14336\n"
"v_and_b32 v61, s8, v60\n"
"v_alignbit_b32 v62, v60, v60, 4\n"
"v_perm_b32 v24, v61, v58, s11\n"
"v_and_b32 v62, s8, v62\n"
"v_perm_b32 v25, v61, v58, s12\n"
"v_perm_b32 v26, v62, v59, s11\n"
"v_perm_b32 v27, v62, v59, s12\n"
"s_nop 1\n"
"v_mfma_f32_16x16x32_bf16 v[32:35], v[24:27], v[12:15], v[32:35]\n"
"s_add_u32 s52, s53, 0xe000\n"
"s_and_b32 s52, s52, s54\n"
"buffer_load_dwordx4 a[224:227], v2, s[32:35], s52 offen offset:0\n"
"s_waitcnt vmcnt(35)\n"
"v_pk_sub_u16 v61, v236, s14 clamp\n"
"v_pk_sub_u16 v62, v237, s14 clamp\n"
"v_perm_b32 v58, v62, v61, s9\n"
"v_perm_b32 v61, v62, v61, s10\n"
"v_pk_sub_u16 v62, v238, s14 clamp\n"
"v_pk_sub_u16 v63, v239, s14 clamp\n"
"v_perm_b32 v59, v63, v62, s9\n"
"v_perm_b32 v62, v63, v62, s10\n"
"v_alignbit_b32 v62, v62, v62, 28\n"
"v_bfi_b32 v60, s8, v61, v62\n"
"v_bfi_b32 v61, s8, v62, v61\n"
"v_pk_mad_u16 v62, v236, 2, -1 op_sel_hi:[1,0,0]\n"
"v_pk_mad_u16 v63, v237, 2, -1 op_sel_hi:[1,0,0]\n"
"v_pk_min_u16 v62, v62, v63\n"
"v_pk_mad_u16 v63, v238, 2, -1 op_sel_hi:[1,0,0]\n"
"v_pk_min_u16 v62, v62, v63\n"
"v_pk_mad_u16 v63, v239, 2, -1 op_sel_hi:[1,0,0]\n"
"v_pk_min_u16 v62, v62, v63\n"
"v_pk_sub_u16 v62, s16, v62 clamp\n"
"v_or_b32 v61, v61, v62\n"
"v_cmp_ne_u32 vcc, 0, v61\n"
"s_cbranch_vccnz Lx_286\n"
"Lb_287:\n"
"ds_write_b32 v3, v60 offset:12300\n"
"ds_write_b64 v3, v[58:59] offset:14344\n"
"v_and_b32 v61, s8, v60\n"
"v_alignbit_b32 v62, v60, v60, 4\n"
"v_perm_b32 v24, v61, v58, s11\n"
"v_and_b32 v62, s8, v62\n"
"v_perm_b32 v25, v61, v58, s12\n"
"v_perm_b32 v26, v62, v59, s11\n"
"v_perm_b32 v27, v62, v59, s12\n"
"s_nop 1\n"
"v_mfma_f32_16x16x32_bf16 v[32:35], v[24:27], v[16:19], v[32:35]\n"
"v_accvgpr_read_b32 v60, a116\n"
"v_accvgpr_read_b32 v58, a114\n"
"v_accvgpr_read_b32 v59, a115\n"
"v_and_b32 v61, s8, v60\n"
"v_alignbit_b32 v62, v60, v60, 4\n"
"v_perm_b32 v20, v61, v58, s11\n"
"v_and_b32 v62, s8, v62\n"
"v_perm_b32 v21, v61, v58, s12\n"
"v_perm_b32 v22, v62, v59, s11\n"
"v_perm_b32 v23, v62, v59, s12\n"
"v_accvgpr_read_b32 v60, a119\n"
"v_accvgpr_read_b32 v58, a117\n"
"v_mfma_f32_16x16x32_bf16 v[36:39], v[20:23], v[12:15], v[36:39]\n"
"v_accvgpr_read_b32 v59, a118\n"
"v_and_b32 v61, s8, v60\n"
"v_alignbit_b32 v62, v60, v60, 4\n"
"v_perm_b32 v28, v61, v58, s11\n"
"v_and_b32 v62, s8, v62\n"
"v_perm_b32 v29, v61, v58, s12\n"
"v_perm_b32 v30, v62, v59, s11\n"
"v_perm_b32 v31, v62, v59, s12\n"
"s_nop 1\n"
"v_mfma_f32_16x16x32_bf16 v[36:39], v[28:31], v[16:19], v[36:39]\n"
"Lpace_288:\n"
"s_memrealtime s[96:97]\n"
"s_waitcnt lgkmcnt(0)\n"
"s_sub_u32 s96, s96, s98\n"
"s_cmp_ge_u32 s96, 1969\n"
"s_cbranch_scc1 Lpgo_289\n"
"s_sleep 2\n"
"s_branch Lpace_288\n"
"Lpgo_289:\n"
"s_add_u32 s52, s53, 0x15000\n"
"s_and_b32 s52, s52, s54\n"
"buffer_load_dwordx4 a[228:231], v2, s[32:35], s52 offen offset:0\n"
"s_waitcnt vmcnt(35)\n"
"v_pk_sub_u16 v61, v240, s14 clamp\n"
"v_pk_sub_u16 v62, v241, s14 clamp\n"
"v_perm_b32 v58, v62, v61, s9\n"
"v_perm_b32 v61, v62, v61, s10\n"
"v_pk_sub_u16 v62, v242, s14 clamp\n"
"v_pk_sub_u16 v63, v243, s14 clamp\n"
"v_perm_b32 v59, v63, v62, s9\n"
"v_perm_b32 v62, v63, v62, s10\n"
"v_alignbit_b32 v62, v62, v62, 28\n"
"v_bfi_b32 v60, s8, v61, v62\n"
"v_bfi_b32 v61, s8, v62, v61\n"
"v_pk_mad_u16 v62, v240, 2, -1 op_sel_hi:[1,0,0]\n"
"v_pk_mad_u16 v63, v241, 2, -1 op_sel_hi:[1,0,0]\n"
"v_pk_min_u16 v62, v62, v63\n"
"v_pk_mad_u16 v63, v242, 2, -1 op_sel_hi:[1,0,0]\n"
"v_pk_min_u16 v62, v62, v63\n"
"v_pk_mad_u16 v63, v243, 2, -1 op_sel_hi:[1,0,0]\n"
"v_pk_min_u16 v62, v62, v63\n"
"v_pk_sub_u16 v62, s16, v62 clamp\n"
"v_or_b32 v61, v61, v62\n"
"v_cmp_ne_u32 vcc, 0, v61\n"
"s_cbranch_vccnz Lx_290\n"
"Lb_291:\n"
"ds_write_b32 v3, v60 offset:15360\n"
"ds_write_b64 v3, v[58:59] offset:16384\n"
"v_and_b32 v61, s8, v60\n"
"v_alignbit_b32 v62, v60, v60, 4\n"
"v_perm_b32 v24, v61, v58, s11\n"
"v_and_b32 v62, s8, v62\n"
"v_perm_b32 v25, v61, v58, s12\n"
"v_perm_b32 v26, v62, v59, s11\n"
"v_perm_b32 v27, v62, v59, s12\n"
"v_mov_b32_dpp v63, v32 quad_perm:[0,0,0,0] row_mask:0xf bank_mask:0xf\n"
"v_add_f32_dpp v63, v33, v63 quad_perm:[1,1,1,1] row_mask:0xf bank_mask:0xf\n"
"v_add_f32_dpp v63, v34, v63 quad_perm:[2,2,2,2] row_mask:0xf bank_mask:0xf\n"
"v_add_f32_dpp v63, v35, v63 quad_perm:[3,3,3,3] row_mask:0xf bank_mask:0xf\n"
"v_cndmask_b32_e64 v50, v50, v63, s[64:65]\n"
"v_mfma_f32_16x16x32_bf16 v[32:35], v[24:27], v[4:7], 0\n"
"v_accvgpr_read_b32 v60, a122\n"
"v_accvgpr_read_b32 v58, a120\n"
"v_accvgpr_read_b32 v59, a121\n"
"v_and_b32 v61, s8, v60\n"
"v_alignbit_b32 v62, v60, v60, 4\n"
"v_perm_b32 v20, v61, v58, s11\n"
"v_and_b32 v62, s8, v62\n"
"v_perm_b32 v21, v61, v58, s12\n"
"v_perm_b32 v22, v62, v59, s11\n"
"v_perm_b32 v23, v62, v59, s12\n"
"v_mov_b32_dpp v63, v36 quad_perm:[0,0,0,0] row_mask:0xf bank_mask:0xf\n"
"v_add_f32_dpp v63, v37, v63 quad_perm:[1,1,1,1] row_mask:0xf bank_mask:0xf\n"
"v_add_f32_dpp v63, v38, v63 quad_perm:[2,2,2,2] row_mask:0xf bank_mask:0xf\n"
"v_add_f32_dpp v63, v39, v63 quad_perm:[3,3,3,3] row_mask:0xf bank_mask:0xf\n"
"v_cndmask_b32_e64 v46, v46, v63, s[62:63]\n"
"v_accvgpr_read_b32 v60, a125\n"
"v_accvgpr_read_b32 v58, a123\n"
"v_mfma_f32_16x16x32_bf16 v[36:39], v[20:23], v[4:7], 0\n"
"v_accvgpr_read_b32 v59, a124\n"
"v_and_b32 v61, s8, v60\n"
"v_alignbit_b32 v62, v60, v60, 4\n"
"v_perm_b32 v28, v61, v58, s11\n"
"v_and_b32 v62, s8, v62\n"
"v_perm_b32 v29, v61, v58, s12\n"
"v_perm_b32 v30, v62, v59, s11\n"
"v_perm_b32 v31, v62, v59, s12\n"
"s_nop 1\n"
"v_mfma_f32_16x16x32_bf16 v[36:39], v[28:31], v[8:11], v[36:39]\n"
"s_add_u32 s52, s53, 0x1c000\n"
"s_and_b32 s52, s52, s54\n"
"buffer_load_dwordx4 a[232:235], v2, s[32:35], s52 offen offset:0\n"
"s_waitcnt vmcnt(35)\n"
"v_pk_sub_u16 v61, v172, s14 clamp\n"
"v_pk_sub_u16 v62, v173, s14 clamp\n"
"v_perm_b32 v58, v62, v61, s9\n"
"v_perm_b32 v61, v62, v61, s10\n"
"v_pk_sub_u16 v62, v174, s14 clamp\n"
"v_pk_sub_u16 v63, v175, s14 clamp\n"
"v_perm_b32 v59, v63, v62, s9\n"
"v_perm_b32 v62, v63, v62, s10\n"
"v_alignbit_b32 v62, v62, v62, 28\n"
"v_bfi_b32 v60, s8, v61, v62\n"
"v_bfi_b32 v61, s8, v62, v61\n"
"v_pk_mad_u16 v62, v172, 2, -1 op_sel_hi:[1,0,0]\n"
"v_pk_mad_u16 v63, v173, 2, -1 op_sel_hi:[1,0,0]\n"
"v_pk_min_u16 v62, v62, v63\n"
"v_pk_mad_u16 v63, v174, 2, -1 op_sel_hi:[1,0,0]\n"
"v_pk_min_u16 v62, v62, v63\n"
"v_pk_mad_u16 v63, v175, 2, -1 op_sel_hi:[1,0,0]\n"
"v_pk_min_u16 v62, v62, v63\n"
"v_pk_sub_u16 v62, s16, v62 clamp\n"
"v_or_b32 v61, v61, v62\n"
"v_cmp_ne_u32 vcc, 0, v61\n"
"s_cbranch_vccnz Lx_292\n"
"Lb_293:\n"
"ds_write_b32 v3, v60 offset:15364\n"
"ds_write_b64 v3, v[58:59] offset:16392\n"
"v_and_b32 v61, s8, v60\n"
"v_alignbit_b32 v62, v60, v60, 4\n"
"v_perm_b32 v24, v61, v58, s11\n"
"v_and_b32 v62, s8, v62\n"
"v_perm_b32 v25, v61, v58, s12\n"
"v_perm_b32 v26, v62, v59, s11\n"
"v_perm_b32 v27, v62, v59, s12\n"
"s_nop 1\n"
"v_mfma_f32_16x16x32_bf16 v[32:35], v[24:27], v[8:11], v[32:35]\n"
"v_accvgpr_read_b32 v60, a128\n"
"v_accvgpr_read_b32 v58, a126\n"
"v_accvgpr_read_b32 v59, a127\n"
"v_and_b32 v61, s8, v60\n"
"v_alignbit_b32 v62, v60, v60, 4\n"
"v_perm_b32 v20, v61, v58, s11\n"
"v_and_b32 v62, s8, v62\n"
"v_perm_b32 v21, v61, v58, s12\n"
"v_perm_b32 v22, v62, v59, s11\n"
"v_perm_b32 v23, v62, v59, s12\n"
"v_accvgpr_read_b32 v60, a131\n"
"v_accvgpr_read_b32 v58, a129\n"
"v_mfma_f32_16x16x32_bf16 v[36:39], v[20:23], v[12:15], v[36:39]\n"
"v_accvgpr_read_b32 v59, a130\n"
"v_and_b32 v61, s8, v60\n"
"v_alignbit_b32 v62, v60, v60, 4\n"
"v_perm_b32 v28, v61, v58, s11\n"
"v_and_b32 v62, s8, v62\n"
"v_perm_b32 v29, v61, v58, s12\n"
"v_perm_b32 v30, v62, v59, s11\n"
"v_perm_b32 v31, v62, v59, s12\n"
"s_nop 1\n"
"v_mfma_f32_16x16x32_bf16 v[36:39], v[28:31], v[16:19], v[36:39]\n"
"s_add_u32 s52, s53, 0x23000\n"
"s_and_b32 s52, s52, s54\n"
"buffer_load_dwordx4 a[236:239], v2, s[32:35], s52 offen offset:0\n"
"s_waitcnt vmcnt(35)\n"
"v_pk_sub_u16 v61, v176, s14 clamp\n"
"v_pk_sub_u16 v62, v177, s14 clamp\n"
"v_perm_b32 v58, v62, v61, s9\n"
"v_perm_b32 v61, v62, v61, s10\n"
"v_pk_sub_u16 v62, v178, s14 clamp\n"
"v_pk_sub_u16 v63, v179, s14 clamp\n"
"v_perm_b32 v59, v63, v62, s9\n"
"v_perm_b32 v62, v63, v62, s10\n"
"v_alignbit_b32 v62, v62, v62, 28\n"
"v_bfi_b32 v60, s8, v61, v62\n"
"v_bfi_b32 v61, s8, v62, v61\n"
"v_pk_mad_u16 v62, v176, 2, -1 op_sel_hi:[1,0,0]\n"
"v_pk_mad_u16 v63, v177, 2, -1 op_sel_hi:[1,0,0]\n"
"v_pk_min_u16 v62, v62, v63\n"
"v_pk_mad_u16 v63, v178, 2, -1 op_sel_hi:[1,0,0]\n"
"v_pk_min_u16 v62, v62, v63\n"
"v_pk_mad_u16 v63, v179, 2, -1 op_sel_hi:[1,0,0]\n"
"v_pk_min_u16 v62, v62, v63\n"
"v_pk_sub_u16 v62, s16, v62 clamp\n"
"v_or_b32 v61, v61, v62\n"
"v_cmp_ne_u32 vcc, 0, v61\n"
"s_cbranch_vccnz Lx_294\n"
"Lb_295:\n"
"ds_write_b32 v3, v60 offset:15368\n"
"ds_write_b64 v3, v[58:59] offset:17408\n"
"v_and_b32 v61, s8, v60\n"
"v_alignbit_b32 v62, v60, v60, 4\n"
"v_perm_b32 v24, v61, v58, s11\n"
"v_and_b32 v62, s8, v62\n"
"v_perm_b32 v25, v61, v58, s12\n"
"v_perm_b32 v26, v62, v59, s11\n"
"v_perm_b32 v27, v62, v59, s12\n"
"s_nop 1\n"
"v_mfma_f32_16x16x32_bf16 v[32:35], v[24:27], v[12:15], v[32:35]\n"
"s_add_u32 s52, s53, 0x2a000\n"
"s_and_b32 s52, s52, s54\n"
"buffer_load_dwordx4 a[240:243], v2, s[32:35], s52 offen offset:0\n"
"s_waitcnt vmcnt(35)\n"
"v_pk_sub_u16 v61, v180, s14 clamp\n"
"v_pk_sub_u16 v62, v181, s14 clamp\n"
"v_perm_b32 v58, v62, v61, s9\n"
"v_perm_b32 v61, v62, v61, s10\n"
"v_pk_sub_u16 v62, v182, s14 clamp\n"
"v_pk_sub_u16 v63, v183, s14 clamp\n"
"v_perm_b32 v59, v63, v62, s9\n"
"v_perm_b32 v62, v63, v62, s10\n"
"v_alignbit_b32 v62, v62, v62, 28\n"
"v_bfi_b32 v60, s8, v61, v62\n"
"v_bfi_b32 v61, s8, v62, v61\n"
"v_pk_mad_u16 v62, v180, 2, -1 op_sel_hi:[1,0,0]\n"
"v_pk_mad_u16 v63, v181, 2, -1 op_sel_hi:[1,0,0]\n"
"v_pk_min_u16 v62, v62, v63\n"
"v_pk_mad_u16 v63, v182, 2, -1 op_sel_hi:[1,0,0]\n"
"v_pk_min_u16 v62, v62, v63\n"
"v_pk_mad_u16 v63, v183, 2, -1 op_sel_hi:[1,0,0]\n"
"v_pk_min_u16 v62, v62, v63\n"
"v_pk_sub_u16 v62, s16, v62 clamp\n"
"v_or_b32 v61, v61, v62\n"
"v_cmp_ne_u32 vcc, 0, v61\n"
"s_cbranch_vccnz Lx_296\n"
"Lb_297:\n"
"ds_write_b32 v3, v60 offset:15372\n"
"ds_write_b64 v3, v[58:59] offset:17416\n"
"v_and_b32 v61, s8, v60\n"
"v_alignbit_b32 v62, v60, v60, 4\n"
"v_perm_b32 v24, v61, v58, s11\n"
"v_and_b32 v62, s8, v62\n"
"v_perm_b32 v25, v61, v58, s12\n"
"v_perm_b32 v26, v62, v59, s11\n"
"v_perm_b32 v27, v62, v59, s12\n"
"s_nop 1\n"
"v_mfma_f32_16x16x32_bf16 v[32:35], v[24:27], v[16:19], v[32:35]\n"
"v_accvgpr_read_b32 v60, a134\n"
"v_accvgpr_read_b32 v58, a132\n"
"v_accvgpr_read_b32 v59, a133\n"
"v_and_b32 v61, s8, v60\n"
"v_alignbit_b32 v62, v60, v60, 4\n"
"v_perm_b32 v20, v61, v58, s11\n"
"v_and_b32 v62, s8, v62\n"
"v_perm_b32 v21, v61, v58, s12\n"
"v_perm_b32 v22, v62, v59, s11\n"
"v_perm_b32 v23, v62, v59, s12\n"
"v_mov_b32_dpp v63, v36 quad_perm:[0,0,0,0] row_mask:0xf bank_mask:0xf\n"
"v_add_f32_dpp v63, v37, v63 quad_perm:[1,1,1,1] row_mask:0xf bank_mask:0xf\n"
"v_add_f32_dpp v63, v38, v63 quad_perm:[2,2,2,2] row_mask:0xf bank_mask:0xf\n"
"v_add_f32_dpp v63, v39, v63 quad_perm:[3,3,3,3] row_mask:0xf bank_mask:0xf\n"
"v_cndmask_b32_e64 v46, v46, v63, s[64:65]\n"
"v_accvgpr_read_b32 v60, a137\n"
"v_accvgpr_read_b32 v58, a135\n"
"v_mfma_f32_16x16x32_bf16 v[36:39], v[20:23], v[4:7], 0\n"
"v_accvgpr_read_b32 v59, a136\n"
"v_and_b32 v61, s8, v60\n"
"v_alignbit_b32 v62, v60, v60, 4\n"
"v_perm_b32 v28, v61, v58, s11\n"
"v_and_b32 v62, s8, v62\n"
"v_perm_b32 v29, v61, v58, s12\n"
"v_perm_b32 v30, v62, v59, s11\n"
"v_perm_b32 v31, v62, v59, s12\n"
"s_nop 1\n"
"v_mfma_f32_16x16x32_bf16 v[36:39], v[28:31], v[8:11], v[36:39]\n"
"Lpace_298:\n"
"s_memrealtime s[96:97]\n"
"s_waitcnt lgkmcnt(0)\n"
"s_sub_u32 s96, s96, s98\n"
"s_cmp_ge_u32 s96, 2026\n"
"s_cbranch_scc1 Lpgo_299\n"
"s_sleep 2\n"
"s_branch Lpace_298\n"
"Lpgo_299:\n"
"s_add_u32 s52, s53, 0x31000\n"
"s_and_b32 s52, s52, s54\n"
"buffer_load_dwordx4 a[244:247], v2, s[32:35], s52 offen offset:0\n"
"s_waitcnt vmcnt(35)\n"
"v_pk_sub_u16 v61, v196, s14 clamp\n"
"v_pk_sub_u16 v62, v197, s14 clamp\n"
"v_perm_b32 v58, v62, v61, s9\n"
"v_perm_b32 v61, v62, v61, s10\n"
"v_pk_sub_u16 v62, v198, s14 clamp\n"
"v_pk_sub_u16 v63, v199, s14 clamp\n"
"v_perm_b32 v59, v63, v62, s9\n"
"v_perm_b32 v62, v63, v62, s10\n"
"v_alignbit_b32 v62, v62, v62, 28\n"
"v_bfi_b32 v60, s8, v61, v62\n"
"v_bfi_b32 v61, s8, v62, v61\n"
"v_pk_mad_u16 v62, v196, 2, -1 op_sel_hi:[1,0,0]\n"
"v_pk_mad_u16 v63, v197, 2, -1 op_sel_hi:[1,0,0]\n"
"v_pk_min_u16 v62, v62, v63\n"
"v_pk_mad_u16 v63, v198, 2, -1 op_sel_hi:[1,0,0]\n"
"v_pk_min_u16 v62, v62, v63\n"
"v_pk_mad_u16 v63, v199, 2, -1 op_sel_hi:[1,0,0]\n"
"v_pk_min_u16 v62, v62, v63\n"
"v_pk_sub_u16 v62, s16, v62 clamp\n"
"v_or_b32 v61, v61, v62\n"
"v_cmp_ne_u32 vcc, 0, v61\n"
"s_cbranch_vccnz Lx_300\n"
"Lb_301:\n"
"ds_write_b32 v3, v60 offset:18432\n"
"ds_write_b64 v3, v[58:59] offset:19456\n"
"v_and_b32 v61, s8, v60\n"
"v_alignbit_b32 v62, v60, v60, 4\n"
"v_perm_b32 v24, v61, v58, s11\n"
"v_and_b32 v62, s8, v62\n"
"v_perm_b32 v25, v61, v58, s12\n"
"v_perm_b32 v26, v62, v59, s11\n"
"v_perm_b32 v27, v62, v59, s12\n"
"v_mov_b32_dpp v63, v32 quad_perm:[0,0,0,0] row_mask:0xf bank_mask:0xf\n"
"v_add_f32_dpp v63, v33, v63 quad_perm:[1,1,1,1] row_mask:0xf bank_mask:0xf\n"
"v_add_f32_dpp v63, v34, v63 quad_perm:[2,2,2,2] row_mask:0xf bank_mask:0xf\n"
"v_add_f32_dpp v63, v35, v63 quad_perm:[3,3,3,3] row_mask:0xf bank_mask:0xf\n"
"v_cndmask_b32_e64 v50, v50, v63, s[66:67]\n"
"v_mfma_f32_16x16x32_bf16 v[32:35], v[24:27], v[4:7], 0\n"
"v_accvgpr_read_b32 v60, a140\n"
"v_accvgpr_read_b32 v58, a138\n"
"v_accvgpr_read_b32 v59, a139\n"
"v_and_b32 v61, s8, v60\n"
"v_alignbit_b32 v62, v60, v60, 4\n"
"v_perm_b32 v20, v61, v58, s11\n"
"v_and_b32 v62, s8, v62\n"
"v_perm_b32 v21, v61, v58, s12\n"
"v_perm_b32 v22, v62, v59, s11\n"
"v_perm_b32 v23, v62, v59, s12\n"
"v_accvgpr_read_b32 v60, a143\n"
"v_accvgpr_read_b32 v58, a141\n"
"v_mfma_f32_16x16x32_bf16 v[36:39], v[20:23], v[12:15], v[36:39]\n"
"v_accvgpr_read_b32 v59, a142\n"
"v_and_b32 v61, s8, v60\n"
"v_alignbit_b32 v62, v60, v60, 4\n"
"v_perm_b32 v28, v61, v58, s11\n"
"v_and_b32 v62, s8, v62\n"
"v_perm_b32 v29, v61, v58, s12\n"
"v_perm_b32 v30, v62, v59, s11\n"
"v_perm_b32 v31, v62, v59, s12\n"
"s_nop 1\n"
"v_mfma_f32_16x16x32_bf16 v[36:39], v[28:31], v[16:19], v[36:39]\n"
"s_add_u32 s52, s53, 0x38000\n"
"s_and_b32 s52, s52, s54\n"
"buffer_load_dwordx4 a[248:251], v2, s[32:35], s52 offen offset:0\n"
"s_waitcnt vmcnt(35)\n"
"v_pk_sub_u16 v61, v200, s14 clamp\n"
"v_pk_sub_u16 v62, v201, s14 clamp\n"
"v_perm_b32 v58, v62, v61, s9\n"
"v_perm_b32 v61, v62, v61, s10\n"
"v_pk_sub_u16 v62, v202, s14 clamp\n"
"v_pk_sub_u16 v63, v203, s14 clamp\n"
"v_perm_b32 v59, v63, v62, s9\n"
"v_perm_b32 v62, v63, v62, s10\n"
"v_alignbit_b32 v62, v62, v62, 28\n"
"v_bfi_b32 v60, s8, v61, v62\n"
"v_bfi_b32 v61, s8, v62, v61\n"
"v_pk_mad_u16 v62, v200, 2, -1 op_sel_hi:[1,0,0]\n"
"v_pk_mad_u16 v63, v201, 2, -1 op_sel_hi:[1,0,0]\n"
"v_pk_min_u16 v62, v62, v63\n"
"v_pk_mad_u16 v63, v202, 2, -1 op_sel_hi:[1,0,0]\n"
"v_pk_min_u16 v62, v62, v63\n"
"v_pk_mad_u16 v63, v203, 2, -1 op_sel_hi:[1,0,0]\n"
"v_pk_min_u16 v62, v62, v63\n"
"v_pk_sub_u16 v62, s16, v62 clamp\n"
"v_or_b32 v61, v61, v62\n"
"v_cmp_ne_u32 vcc, 0, v61\n"
"s_cbranch_vccnz Lx_302\n"
"Lb_303:\n"
"ds_write_b32 v3, v60 offset:18436\n"
"ds_write_b64 v3, v[58:59] offset:19464\n"
"v_and_b32 v61, s8, v60\n"
"v_alignbit_b32 v62, v60, v60, 4\n"
"v_perm_b32 v24, v61, v58, s11\n"
"v_and_b32 v62, s8, v62\n"
"v_perm_b32 v25, v61, v58, s12\n"
"v_perm_b32 v26, v62, v59, s11\n"
"v_perm_b32 v27, v62, v59, s12\n"
"s_nop 1\n"
"v_mfma_f32_16x16x32_bf16 v[32:35], v[24:27], v[8:11], v[32:35]\n"
"v_accvgpr_read_b32 v60, a146\n"
"v_accvgpr_read_b32 v58, a144\n"
"v_accvgpr_read_b32 v59, a145\n"
"v_and_b32 v61, s8, v60\n"
"v_alignbit_b32 v62, v60, v60, 4\n"
"v_perm_b32 v20, v61, v58, s11\n"
"v_and_b32 v62, s8, v62\n"
"v_perm_b32 v21, v61, v58, s12\n"
"v_perm_b32 v22, v62, v59, s11\n"
"v_perm_b32 v23, v62, v59, s12\n"
"v_mov_b32_dpp v63, v36 quad_perm:[0,0,0,0] row_mask:0xf bank_mask:0xf\n"
"v_add_f32_dpp v63, v37, v63 quad_perm:[1,1,1,1] row_mask:0xf bank_mask:0xf\n"
"v_add_f32_dpp v63, v38, v63 quad_perm:[2,2,2,2] row_mask:0xf bank_mask:0xf\n"
"v_add_f32_dpp v63, v39, v63 quad_perm:[3,3,3,3] row_mask:0xf bank_mask:0xf\n"
"v_cndmask_b32_e64 v46, v46, v63, s[66:67]\n"
"v_accvgpr_read_b32 v60, a149\n"
"v_accvgpr_read_b32 v58, a147\n"
"v_mfma_f32_16x16x32_bf16 v[36:39], v[20:23], v[4:7], 0\n"
"v_accvgpr_read_b32 v59, a148\n"
"v_and_b32 v61, s8, v60\n"
"v_alignbit_b32 v62, v60, v60, 4\n"
"v_perm_b32 v28, v61, v58, s11\n"
"v_and_b32 v62, s8, v62\n"
"v_perm_b32 v29, v61, v58, s12\n"
"v_perm_b32 v30, v62, v59, s11\n"
"v_perm_b32 v31, v62, v59, s12\n"
"s_nop 1\n"
"v_mfma_f32_16x16x32_bf16 v[36:39], v[28:31], v[8:11], v[36:39]\n"
"s_add_u32 s52, s53, 0x3f000\n"
"s_and_b32 s52, s52, s54\n"
"buffer_load_dwordx4 a[252:255], v2, s[32:35], s52 offen offset:0\n"
"s_waitcnt vmcnt(35)\n"
"v_pk_sub_u16 v61, v204, s14 clamp\n"
"v_pk_sub_u16 v62, v205, s14 clamp\n"
"v_perm_b32 v58, v62, v61, s9\n"
"v_perm_b32 v61, v62, v61, s10\n"
"v_pk_sub_u16 v62, v206, s14 clamp\n"
"v_pk_sub_u16 v63, v207, s14 clamp\n"
"v_perm_b32 v59, v63, v62, s9\n"
"v_perm_b32 v62, v63, v62, s10\n"
"v_alignbit_b32 v62, v62, v62, 28\n"
"v_bfi_b32 v60, s8, v61, v62\n"
"v_bfi_b32 v61, s8, v62, v61\n"
"v_pk_mad_u16 v62, v204, 2, -1 op_sel_hi:[1,0,0]\n"
"v_pk_mad_u16 v63, v205, 2, -1 op_sel_hi:[1,0,0]\n"
"v_pk_min_u16 v62, v62, v63\n"
"v_pk_mad_u16 v63, v206, 2, -1 op_sel_hi:[1,0,0]\n"
"v_pk_min_u16 v62, v62, v63\n"
"v_pk_mad_u16 v63, v207, 2, -1 op_sel_hi:[1,0,0]\n"
"v_pk_min_u16 v62, v62, v63\n"
"v_pk_sub_u16 v62, s16, v62 clamp\n"
"v_or_b32 v61, v61, v62\n"
"v_cmp_ne_u32 vcc, 0, v61\n"
"s_cbranch_vccnz Lx_304\n"
"Lb_305:\n"
"ds_write_b32 v3, v60 offset:18440\n"
"ds_write_b64 v3, v[58:59] offset:20480\n"
"v_and_b32 v61, s8, v60\n"
"v_alignbit_b32 v62, v60, v60, 4\n"
"v_perm_b32 v24, v61, v58, s11\n"
"v_and_b32 v62, s8, v62\n"
"v_perm_b32 v25, v61, v58, s12\n"
"v_perm_b32 v26, v62, v59, s11\n"
"v_perm_b32 v27, v62, v59, s12\n"
"s_nop 1\n"
"v_mfma_f32_16x16x32_bf16 v[32:35], v[24:27], v[12:15], v[32:35]\n"
"s_add_u32 s52, s53, 0x46000\n"
"s_and_b32 s52, s52, s54\n"
"buffer_load_dwordx4 v[212:215], v2, s[32:35], s52 offen offset:0\n"
"s_waitcnt vmcnt(35)\n"
"v_pk_sub_u16 v61, v220, s14 clamp\n"
"v_pk_sub_u16 v62, v221, s14 clamp\n"
"v_perm_b32 v58, v62, v61, s9\n"
"v_perm_b32 v61, v62, v61, s10\n"
"v_pk_sub_u16 v62, v222, s14 clamp\n"
"v_pk_sub_u16 v63, v223, s14 clamp\n"
"v_perm_b32 v59, v63, v62, s9\n"
"v_perm_b32 v62, v63, v62, s10\n"
"v_alignbit_b32 v62, v62, v62, 28\n"
"v_bfi_b32 v60, s8, v61, v62\n"
"v_bfi_b32 v61, s8, v62, v61\n"
"v_pk_mad_u16 v62, v220, 2, -1 op_sel_hi:[1,0,0]\n"
"v_pk_mad_u16 v63, v221, 2, -1 op_sel_hi:[1,0,0]\n"
"v_pk_min_u16 v62, v62, v63\n"
"v_pk_mad_u16 v63, v222, 2, -1 op_sel_hi:[1,0,0]\n"
"v_pk_min_u16 v62, v62, v63\n"
"v_pk_mad_u16 v63, v223, 2, -1 op_sel_hi:[1,0,0]\n"
"v_pk_min_u16 v62, v62, v63\n"
"v_pk_sub_u16 v62, s16, v62 clamp\n"
"v_or_b32 v61, v61, v62\n"
"v_cmp_ne_u32 vcc, 0, v61\n"
"s_cbranch_vccnz Lx_306\n"
"Lb_307:\n"
"ds_write_b32 v3, v60 offset:18444\n"
"ds_write_b64 v3, v[58:59] offset:20488\n"
"v_and_b32 v61, s8, v60\n"
"v_alignbit_b32 v62, v60, v60, 4\n"
"v_perm_b32 v24, v61, v58, s11\n"
"v_and_b32 v62, s8, v62\n"
"v_perm_b32 v25, v61, v58, s12\n"
"v_perm_b32 v26, v62, v59, s11\n"
"v_perm_b32 v27, v62, v59, s12\n"
"s_nop 1\n"
"v_mfma_f32_16x16x32_bf16 v[32:35], v[24:27], v[16:19], v[32:35]\n"
"v_accvgpr_read_b32 v60, a152\n"
"v_accvgpr_read_b32 v58, a150\n"
"v_accvgpr_read_b32 v59, a151\n"
"v_and_b32 v61, s8, v60\n"
"v_alignbit_b32 v62, v60, v60, 4\n"
"v_perm_b32 v20, v61, v58, s11\n"
"v_and_b32 v62, s8, v62\n"
"v_perm_b32 v21, v61, v58, s12\n"
"v_perm_b32 v22, v62, v59, s11\n"
"v_perm_b32 v23, v62, v59, s12\n"
"v_accvgpr_read_b32 v60, a155\n"
"v_accvgpr_read_b32 v58, a153\n"
"v_mfma_f32_16x16x32_bf16 v[36:39], v[20:23], v[12:15], v[36:39]\n"
"v_accvgpr_read_b32 v59, a154\n"
"v_and_b32 v61, s8, v60\n"
"v_alignbit_b32 v62, v60, v60, 4\n"
"v_perm_b32 v28, v61, v58, s11\n"
"v_and_b32 v62, s8, v62\n"
"v_perm_b32 v29, v61, v58, s12\n"
"v_perm_b32 v30, v62, v59, s11\n"
"v_perm_b32 v31, v62, v59, s12\n"
"s_nop 1\n"
"v_mfma_f32_16x16x32_bf16 v[36:39], v[28:31], v[16:19], v[36:39]\n"
"Lpace_308:\n"
"s_memrealtime s[96:97]\n"
"s_waitcnt lgkmcnt(0)\n"
"s_sub_u32 s96, s96, s98\n"
"s_cmp_ge_u32 s96, 2083\n"
"s_cbranch_scc1 Lpgo_309\n"
"s_sleep 2\n"
"s_branch Lpace_308\n"
"Lpgo_309:\n"
"s_add_u32 s52, s53, 0x4d000\n"
"s_and_b32 s52, s52, s54\n"
"buffer_load_dwordx4 v[216:219], v2, s[32:35], s52 offen offset:0\n"
"s_waitcnt vmcnt(35)\n"
"v_pk_sub_u16 v61, v224, s14 clamp\n"
"v_pk_sub_u16 v62, v225, s14 clamp\n"
"v_perm_b32 v58, v62, v61, s9\n"
"v_perm_b32 v61, v62, v61, s10\n"
"v_pk_sub_u16 v62, v226, s14 clamp\n"
"v_pk_sub_u16 v63, v227, s14 clamp\n"
"v_perm_b32 v59, v63, v62, s9\n"
"v_perm_b32 v62, v63, v62, s10\n"
"v_alignbit_b32 v62, v62, v62, 28\n"
"v_bfi_b32 v60, s8, v61, v62\n"
"v_bfi_b32 v61, s8, v62, v61\n"
"v_pk_mad_u16 v62, v224, 2, -1 op_sel_hi:[1,0,0]\n"
"v_pk_mad_u16 v63, v225, 2, -1 op_sel_hi:[1,0,0]\n"
"v_pk_min_u16 v62, v62, v63\n"
"v_pk_mad_u16 v63, v226, 2, -1 op_sel_hi:[1,0,0]\n"
"v_pk_min_u16 v62, v62, v63\n"
"v_pk_mad_u16 v63, v227, 2, -1 op_sel_hi:[1,0,0]\n"
"v_pk_min_u16 v62, v62, v63\n"
"v_pk_sub_u16 v62, s16, v62 clamp\n"
"v_or_b32 v61, v61, v62\n"
"v_cmp_ne_u32 vcc, 0, v61\n"
"s_cbranch_vccnz Lx_310\n"
"Lb_311:\n"
"ds_write_b32 v3, v60 offset:21504\n"
"ds_write_b64 v3, v[58:59] offset:22528\n"
"v_and_b32 v61, s8, v60\n"
"v_alignbit_b32 v62, v60, v60, 4\n"
"v_perm_b32 v24, v61, v58, s11\n"
"v_and_b32 v62, s8, v62\n"
"v_perm_b32 v25, v61, v58, s12\n"
"v_perm_b32 v26, v62, v59, s11\n"
"v_perm_b32 v27, v62, v59, s12\n"
"v_mov_b32_dpp v63, v32 quad_perm:[0,0,0,0] row_mask:0xf bank_mask:0xf\n"
"v_add_f32_dpp v63, v33, v63 quad_perm:[1,1,1,1] row_mask:0xf bank_mask:0xf\n"
"v_add_f32_dpp v63, v34, v63 quad_perm:[2,2,2,2] row_mask:0xf bank_mask:0xf\n"
"v_add_f32_dpp v63, v35, v63 quad_perm:[3,3,3,3] row_mask:0xf bank_mask:0xf\n"
"v_cndmask_b32_e64 v51, v51, v63, s[60:61]\n"
"v_mfma_f32_16x16x32_bf16 v[32:35], v[24:27], v[4:7], 0\n"
"v_accvgpr_read_b32 v60, a158\n"
"v_accvgpr_read_b32 v58, a156\n"
"v_accvgpr_read_b32 v59, a157\n"
"v_and_b32 v61, s8, v60\n"
"v_alignbit_b32 v62, v60, v60, 4\n"
"v_perm_b32 v20, v61, v58, s11\n"
"v_and_b32 v62, s8, v62\n"
"v_perm_b32 v21, v61, v58, s12\n"
"v_perm_b32 v22, v62, v59, s11\n"
"v_perm_b32 v23, v62, v59, s12\n"
"v_mov_b32_dpp v63, v36 quad_perm:[0,0,0,0] row_mask:0xf bank_mask:0xf\n"
"v_add_f32_dpp v63, v37, v63 quad_perm:[1,1,1,1] row_mask:0xf bank_mask:0xf\n"
"v_add_f32_dpp v63, v38, v63 quad_perm:[2,2,2,2] row_mask:0xf bank_mask:0xf\n"
"v_add_f32_dpp v63, v39, v63 quad_perm:[3,3,3,3] row_mask:0xf bank_mask:0xf\n"
"v_cndmask_b32_e64 v47, v47, v63, s[60:61]\n"
"v_accvgpr_read_b32 v60, a161\n"
"v_accvgpr_read_b32 v58, a159\n"
"v_mfma_f32_16x16x32_bf16 v[36:39], v[20:23], v[4:7], 0\n"
"v_accvgpr_read_b32 v59, a160\n"
"v_and_b32 v61, s8, v60\n"
"v_alignbit_b32 v62, v60, v60, 4\n"
"v_perm_b32 v28, v61, v58, s11\n"
"v_and_b32 v62, s8, v62\n"
"v_perm_b32 v29, v61, v58, s12\n"
"v_perm_b32 v30, v62, v59, s11\n"
"v_perm_b32 v31, v62, v59, s12\n"
"s_nop 1\n"
"v_mfma_f32_16x16x32_bf16 v[36:39], v[28:31], v[8:11], v[36:39]\n"
"s_add_u32 s52, s53, 0x54000\n"
"s_and_b32 s52, s52, s54\n"
"buffer_load_dwordx4 v[232:235], v2, s[32:35], s52 offen offset:0\n"
"s_waitcnt vmcnt(35)\n"
"v_pk_sub_u16 v61, v228, s14 clamp\n"
"v_pk_sub_u16 v62, v229, s14 clamp\n"
"v_perm_b32 v58, v62, v61, s9\n"
"v_perm_b32 v61, v62, v61, s10\n"
"v_pk_sub_u16 v62, v230, s14 clamp\n"
"v_pk_sub_u16 v63, v231, s14 clamp\n"
"v_perm_b32 v59, v63, v62, s9\n"
"v_perm_b32 v62, v63, v62, s10\n"
"v_alignbit_b32 v62, v62, v62, 28\n"
"v_bfi_b32 v60, s8, v61, v62\n"
"v_bfi_b32 v61, s8, v62, v61\n"
"v_pk_mad_u16 v62, v228, 2, -1 op_sel_hi:[1,0,0]\n"
"v_pk_mad_u16 v63, v229, 2, -1 op_sel_hi:[1,0,0]\n"
"v_pk_min_u16 v62, v62, v63\n"
"v_pk_mad_u16 v63, v230, 2, -1 op_sel_hi:[1,0,0]\n"
"v_pk_min_u16 v62, v62, v63\n"
"v_pk_mad_u16 v63, v231, 2, -1 op_sel_hi:[1,0,0]\n"
"v_pk_min_u16 v62, v62, v63\n"
"v_pk_sub_u16 v62, s16, v62 clamp\n"
"v_or_b32 v61, v61, v62\n"
"v_cmp_ne_u32 vcc, 0, v61\n"
"s_cbranch_vccnz Lx_312\n"
"Lb_313:\n"
"ds_write_b32 v3, v60 offset:21508\n"
"ds_write_b64 v3, v[58:59] offset:22536\n"
"v_and_b32 v61, s8, v60\n"
"v_alignbit_b32 v62, v60, v60, 4\n"
"v_perm_b32 v24, v61, v58, s11\n"
"v_and_b32 v62, s8, v62\n"
"v_perm_b32 v25, v61, v58, s12\n"
"v_perm_b32 v26, v62, v59, s11\n"
"v_perm_b32 v27, v62, v59, s12\n"
"s_nop 1\n"
"v_mfma_f32_16x16x32_bf16 v[32:35], v[24:27], v[8:11], v[32:35]\n"
"v_accvgpr_read_b32 v60, a164\n"
"v_accvgpr_read_b32 v58, a162\n"
"v_accvgpr_read_b32 v59, a163\n"
"v_and_b32 v61, s8, v60\n"
"v_alignbit_b32 v62, v60, v60, 4\n"
"v_perm_b32 v20, v61, v58, s11\n"
"v_and_b32 v62, s8, v62\n"
"v_perm_b32 v21, v61, v58, s12\n"
"v_perm_b32 v22, v62, v59, s11\n"
"v_perm_b32 v23, v62, v59, s12\n"
"v_accvgpr_read_b32 v60, a167\n"
"v_accvgpr_read_b32 v58, a165\n"
"v_mfma_f32_16x16x32_bf16 v[36:39], v[20:23], v[12:15], v[36:39]\n"
"v_accvgpr_read_b32 v59, a166\n"
"v_and_b32 v61, s8, v60\n"
"v_alignbit_b32 v62, v60, v60, 4\n"
"v_perm_b32 v28, v61, v58, s11\n"
"v_and_b32 v62, s8, v62\n"
"v_perm_b32 v29, v61, v58, s12\n"
"v_perm_b32 v30, v62, v59, s11\n"
"v_perm_b32 v31, v62, v59, s12\n"
"s_nop 1\n"
"v_mfma_f32_16x16x32_bf16 v[36:39], v[28:31], v[16:19], v[36:39]\n"
"s_add_u32 s52, s53, 0x5b000\n"
"s_and_b32 s52, s52, s54\n"
"buffer_load_dwordx4 v[236:239], v2, s[32:35], s52 offen offset:0\n"
"s_waitcnt vmcnt(35)\n"
"v_pk_sub_u16 v61, v244, s14 clamp\n"
"v_pk_sub_u16 v62, v245, s14 clamp\n"
"v_perm_b32 v58, v62, v61, s9\n"
"v_perm_b32 v61, v62, v61, s10\n"
"v_pk_sub_u16 v62, v246, s14 clamp\n"
"v_pk_sub_u16 v63, v247, s14 clamp\n"
"v_perm_b32 v59, v63, v62, s9\n"
"v_perm_b32 v62, v63, v62, s10\n"
"v_alignbit_b32 v62, v62, v62, 28\n"
"v_bfi_b32 v60, s8, v61, v62\n"
"v_bfi_b32 v61, s8, v62, v61\n"
"v_pk_mad_u16 v62, v244, 2, -1 op_sel_hi:[1,0,0]\n"
"v_pk_mad_u16 v63, v245, 2, -1 op_sel_hi:[1,0,0]\n"
"v_pk_min_u16 v62, v62, v63\n"
"v_pk_mad_u16 v63, v246, 2, -1 op_sel_hi:[1,0,0]\n"
"v_pk_min_u16 v62, v62, v63\n"
"v_pk_mad_u16 v63, v247, 2, -1 op_sel_hi:[1,0,0]\n"
"v_pk_min_u16 v62, v62, v63\n"
"v_pk_sub_u16 v62, s16, v62 clamp\n"
"v_or_b32 v61, v61, v62\n"
"v_cmp_ne_u32 vcc, 0, v61\n"
"s_cbranch_vccnz Lx_314\n"
"Lb_315:\n"
"ds_write_b32 v3, v60 offset:21512\n"
"ds_write_b64 v3, v[58:59] offset:23552\n"
"v_and_b32 v61, s8, v60\n"
"v_alignbit_b32 v62, v60, v60, 4\n"
"v_perm_b32 v24, v61, v58, s11\n"
"v_and_b32 v62, s8, v62\n"
"v_perm_b32 v25, v61, v58, s12\n"
"v_perm_b32 v26, v62, v59, s11\n"
"v_perm_b32 v27, v62, v59, s12\n"
"s_nop 1\n"
"v_mfma_f32_16x16x32_bf16 v[32:35], v[24:27], v[12:15], v[32:35]\n"
"v_accvgpr_read_b32 v60, a170\n"
"v_accvgpr_read_b32 v58, a168\n"
"v_accvgpr_read_b32 v59, a169\n"
"v_and_b32 v61, s8, v60\n"
"v_alignbit_b32 v62, v60, v60, 4\n"
"v_perm_b32 v20, v61, v58, s11\n"
"v_and_b32 v62, s8, v62\n"
"v_perm_b32 v21, v61, v58, s12\n"
"v_perm_b32 v22, v62, v59, s11\n"
"v_perm_b32 v23, v62, v59, s12\n"
"v_mov_b32_dpp v63, v36 quad_perm:[0,0,0,0] row_mask:0xf bank_mask:0xf\n"
"v_add_f32_dpp v63, v37, v63 quad_perm:[1,1,1,1] row_mask:0xf bank_mask:0xf\n"
"v_add_f32_dpp v63, v38, v63 quad_perm:[2,2,2,2] row_mask:0xf bank_mask:0xf\n"
"v_add_f32_dpp v63, v39, v63 quad_perm:[3,3,3,3] row_mask:0xf bank_mask:0xf\n"
"v_cndmask_b32_e64 v47, v47, v63, s[62:63]\n"
"v_accvgpr_read_b32 v60, a173\n"
"v_accvgpr_read_b32 v58, a171\n"
"v_mfma_f32_16x16x32_bf16 v[36:39], v[20:23], v[4:7], 0\n"
"v_accvgpr_read_b32 v59, a172\n"
"v_and_b32 v61, s8, v60\n"
"v_alignbit_b32 v62, v60, v60, 4\n"
"v_perm_b32 v28, v61, v58, s11\n"
"v_and_b32 v62, s8, v62\n"
"v_perm_b32 v29, v61, v58, s12\n"
"v_perm_b32 v30, v62, v59, s11\n"
"v_perm_b32 v31, v62, v59, s12\n"
"s_nop 1\n"
"v_mfma_f32_16x16x32_bf16 v[36:39], v[28:31], v[8:11], v[36:39]\n"
"s_add_u32 s52, s53, 0x62000\n"
"s_and_b32 s52, s52, s54\n"
"buffer_load_dwordx4 v[240:243], v2, s[32:35], s52 offen offset:0\n"
"s_waitcnt vmcnt(35)\n"
"v_pk_sub_u16 v61, v248, s14 clamp\n"
"v_pk_sub_u16 v62, v249, s14 clamp\n"
"v_perm_b32 v58, v62, v61, s9\n"
"v_perm_b32 v61, v62, v61, s10\n"
"v_pk_sub_u16 v62, v250, s14 clamp\n"
"v_pk_sub_u16 v63, v251, s14 clamp\n"
"v_perm_b32 v59, v63, v62, s9\n"
"v_perm_b32 v62, v63, v62, s10\n"
"v_alignbit_b32 v62, v62, v62, 28\n"
"v_bfi_b32 v60, s8, v61, v62\n"
"v_bfi_b32 v61, s8, v62, v61\n"
"v_pk_mad_u16 v62, v248, 2, -1 op_sel_hi:[1,0,0]\n"
"v_pk_mad_u16 v63, v249, 2, -1 op_sel_hi:[1,0,0]\n"
"v_pk_min_u16 v62, v62, v63\n"
"v_pk_mad_u16 v63, v250, 2, -1 op_sel_hi:[1,0,0]\n"
"v_pk_min_u16 v62, v62, v63\n"
"v_pk_mad_u16 v63, v251, 2, -1 op_sel_hi:[1,0,0]\n"
"v_pk_min_u16 v62, v62, v63\n"
"v_pk_sub_u16 v62, s16, v62 clamp\n"
"v_or_b32 v61, v61, v62\n"
"v_cmp_ne_u32 vcc, 0, v61\n"
"s_cbranch_vccnz Lx_316\n"
"Lb_317:\n"
"ds_write_b32 v3, v60 offset:21516\n"
"ds_write_b64 v3, v[58:59] offset:23560\n"
"v_and_b32 v61, s8, v60\n"
"v_alignbit_b32 v62, v60, v60, 4\n"
"v_perm_b32 v24, v61, v58, s11\n"
"v_and_b32 v62, s8, v62\n"
"v_perm_b32 v25, v61, v58, s12\n"
"v_perm_b32 v26, v62, v59, s11\n"
"v_perm_b32 v27, v62, v59, s12\n"
"s_nop 1\n"
"v_mfma_f32_16x16x32_bf16 v[32:35], v[24:27], v[16:19], v[32:35]\n"
"Lpace_318:\n"
"s_memrealtime s[96:97]\n"
"s_waitcnt lgkmcnt(0)\n"
"s_sub_u32 s96, s96, s98\n"
"s_cmp_ge_u32 s96, 2140\n"
"s_cbranch_scc1 Lpgo_319\n"
"s_sleep 2\n"
"s_branch Lpace_318\n"
"Lpgo_319:\n"
"s_add_u32 s52, s53, 0x69000\n"
"s_and_b32 s52, s52, s54\n"
"buffer_load_dwordx4 v[172:175], v2, s[32:35], s52 offen offset:0\n"
"s_waitcnt vmcnt(35)\n"
"v_pk_sub_u16 v61, v252, s14 clamp\n"
"v_pk_sub_u16 v62, v253, s14 clamp\n"
"v_perm_b32 v58, v62, v61, s9\n"
"v_perm_b32 v61, v62, v61, s10\n"
"v_pk_sub_u16 v62, v254, s14 clamp\n"
"v_pk_sub_u16 v63, v255, s14 clamp\n"
"v_perm_b32 v59, v63, v62, s9\n"
"v_perm_b32 v62, v63, v62, s10\n"
"v_alignbit_b32 v62, v62, v62, 28\n"
"v_bfi_b32 v60, s8, v61, v62\n"
"v_bfi_b32 v61, s8, v62, v61\n"
"v_pk_mad_u16 v62, v252, 2, -1 op_sel_hi:[1,0,0]\n"
"v_pk_mad_u16 v63, v253, 2, -1 op_sel_hi:[1,0,0]\n"
"v_pk_min_u16 v62, v62, v63\n"
"v_pk_mad_u16 v63, v254, 2, -1 op_sel_hi:[1,0,0]\n"
"v_pk_min_u16 v62, v62, v63\n"
"v_pk_mad_u16 v63, v255, 2, -1 op_sel_hi:[1,0,0]\n"
"v_pk_min_u16 v62, v62, v63\n"
"v_pk_sub_u16 v62, s16, v62 clamp\n"
"v_or_b32 v61, v61, v62\n"
"v_cmp_ne_u32 vcc, 0, v61\n"
"s_cbranch_vccnz Lx_320\n"
"Lb_321:\n"
"ds_write_b32 v3, v60 offset:24576\n"
"ds_write_b64 v3, v[58:59] offset:25600\n"
"v_and_b32 v61, s8, v60\n"
"v_alignbit_b32 v62, v60, v60, 4\n"
"v_perm_b32 v24, v61, v58, s11\n"
"v_and_b32 v62, s8, v62\n"
"v_perm_b32 v25, v61, v58, s12\n"
"v_perm_b32 v26, v62, v59, s11\n"
"v_perm_b32 v27, v62, v59, s12\n"
"v_mov_b32_dpp v63, v32 quad_perm:[0,0,0,0] row_mask:0xf bank_mask:0xf\n"
"v_add_f32_dpp v63, v33, v63 quad_perm:[1,1,1,1] row_mask:0xf bank_mask:0xf\n"
"v_add_f32_dpp v63, v34, v63 quad_perm:[2,2,2,2] row_mask:0xf bank_mask:0xf\n"
"v_add_f32_dpp v63, v35, v63 quad_perm:[3,3,3,3] row_mask:0xf bank_mask:0xf\n"
"v_cndmask_b32_e64 v51, v51, v63, s[62:63]\n"
"v_mfma_f32_16x16x32_bf16 v[32:35], v[24:27], v[4:7], 0\n"
"v_accvgpr_read_b32 v60, a176\n"
"v_accvgpr_read_b32 v58, a174\n"
"v_accvgpr_read_b32 v59, a175\n"
"v_and_b32 v61, s8, v60\n"
"v_alignbit_b32 v62, v60, v60, 4\n"
"v_perm_b32 v20, v61, v58, s11\n"
"v_and_b32 v62, s8, v62\n"
"v_perm_b32 v21, v61, v58, s12\n"
"v_perm_b32 v22, v62, v59, s11\n"
"v_perm_b32 v23, v62, v59, s12\n"
"v_accvgpr_read_b32 v60, a179\n"
"v_accvgpr_read_b32 v58, a177\n"
"v_mfma_f32_16x16x32_bf16 v[36:39], v[20:23], v[12:15], v[36:39]\n"
"v_accvgpr_read_b32 v59, a178\n"
"v_and_b32 v61, s8, v60\n"
"v_alignbit_b32 v62, v60, v60, 4\n"
"v_perm_b32 v28, v61, v58, s11\n"
"v_and_b32 v62, s8, v62\n"
"v_perm_b32 v29, v61, v58, s12\n"
"v_perm_b32 v30, v62, v59, s11\n"
"v_perm_b32 v31, v62, v59, s12\n"
"s_nop 1\n"
"v_mfma_f32_16x16x32_bf16 v[36:39], v[28:31], v[16:19], v[36:39]\n"
"s_add_u32 s52, s53, 0x70000\n"
"s_and_b32 s52, s52, s54\n"
"buffer_load_dwordx4 v[176:179], v2, s[32:35], s52 offen offset:0\n"
"s_waitcnt vmcnt(35)\n"
"v_pk_sub_u16 v61, v112, s14 clamp\n"
"v_pk_sub_u16 v62, v113, s14 clamp\n"
"v_perm_b32 v58, v62, v61, s9\n"
"v_perm_b32 v61, v62, v61, s10\n"
"v_pk_sub_u16 v62, v114, s14 clamp\n"
"v_pk_sub_u16 v63, v115, s14 clamp\n"
"v_perm_b32 v59, v63, v62, s9\n"
"v_perm_b32 v62, v63, v62, s10\n"
"v_alignbit_b32 v62, v62, v62, 28\n"
"v_bfi_b32 v60, s8, v61, v62\n"
"v_bfi_b32 v61, s8, v62, v61\n"
"v_pk_mad_u16 v62, v112, 2, -1 op_sel_hi:[1,0,0]\n"
"v_pk_mad_u16 v63, v113, 2, -1 op_sel_hi:[1,0,0]\n"
"v_pk_min_u16 v62, v62, v63\n"
"v_pk_mad_u16 v63, v114, 2, -1 op_sel_hi:[1,0,0]\n"
"v_pk_min_u16 v62, v62, v63\n"
"v_pk_mad_u16 v63, v115, 2, -1 op_sel_hi:[1,0,0]\n"
"v_pk_min_u16 v62, v62, v63\n"
"v_pk_sub_u16 v62, s16, v62 clamp\n"
"v_or_b32 v61, v61, v62\n"
"v_cmp_ne_u32 vcc, 0, v61\n"
"s_cbranch_vccnz Lx_322\n"
"Lb_323:\n"
"ds_write_b32 v3, v60 offset:24580\n"
"ds_write_b64 v3, v[58:59] offset:25608\n"
"v_and_b32 v61, s8, v60\n"
"v_alignbit_b32 v62, v60, v60, 4\n"
"v_perm_b32 v24, v61, v58, s11\n"
"v_and_b32 v62, s8, v62\n"
"v_perm_b32 v25, v61, v58, s12\n"
"v_perm_b32 v26, v62, v59, s11\n"
"v_perm_b32 v27, v62, v59, s12\n"
"s_nop 1\n"
"v_mfma_f32_16x16x32_bf16 v[32:35], v[24:27], v[8:11], v[32:35]\n"
"v_accvgpr_read_b32 v60, a182\n"
"v_accvgpr_read_b32 v58, a180\n"
"v_accvgpr_read_b32 v59, a181\n"
"v_and_b32 v61, s8, v60\n"
"v_alignbit_b32 v62, v60, v60, 4\n"
"v_perm_b32 v20, v61, v58, s11\n"
"v_and_b32 v62, s8, v62\n"
"v_perm_b32 v21, v61, v58, s12\n"
"v_perm_b32 v22, v62, v59, s11\n"
"v_perm_b32 v23, v62, v59, s12\n"
"v_mov_b32_dpp v63, v36 quad_perm:[0,0,0,0] row_mask:0xf bank_mask:0xf\n"
"v_add_f32_dpp v63, v37, v63 quad_perm:[1,1,1,1] row_mask:0xf bank_mask:0xf\n"
"v_add_f32_dpp v63, v38, v63 quad_perm:[2,2,2,2] row_mask:0xf bank_mask:0xf\n"
"v_add_f32_dpp v63, v39, v63 quad_perm:[3,3,3,3] row_mask:0xf bank_mask:0xf\n"
"v_cndmask_b32_e64 v47, v47, v63, s[64:65]\n"
"v_accvgpr_read_b32 v60, a185\n"
"v_accvgpr_read_b32 v58, a183\n"
"v_mfma_f32_16x16x32_bf16 v[36:39], v[20:23], v[4:7], 0\n"
"v_accvgpr_read_b32 v59, a184\n"
"v_and_b32 v61, s8, v60\n"
"v_alignbit_b32 v62, v60, v60, 4\n"
"v_perm_b32 v28, v61, v58, s11\n"
"v_and_b32 v62, s8, v62\n"
"v_perm_b32 v29, v61, v58, s12\n"
"v_perm_b32 v30, v62, v59, s11\n"
"v_perm_b32 v31, v62, v59, s12\n"
"s_nop 1\n"
"v_mfma_f32_16x16x32_bf16 v[36:39], v[28:31], v[8:11], v[36:39]\n"
"s_add_u32 s52, s53, 0x77000\n"
"s_and_b32 s52, s52, s54\n"
"buffer_load_dwordx4 v[112:115], v2, s[32:35], s52 offen offset:0\n"
"s_waitcnt vmcnt(35)\n"
"v_pk_sub_u16 v61, v116, s14 clamp\n"
"v_pk_sub_u16 v62, v117, s14 clamp\n"
"v_perm_b32 v58, v62, v61, s9\n"
"v_perm_b32 v61, v62, v61, s10\n"
"v_pk_sub_u16 v62, v118, s14 clamp\n"
"v_pk_sub_u16 v63, v119, s14 clamp\n"
"v_perm_b32 v59, v63, v62, s9\n"
"v_perm_b32 v62, v63, v62, s10\n"
"v_alignbit_b32 v62, v62, v62, 28\n"
"v_bfi_b32 v60, s8, v61, v62\n"
"v_bfi_b32 v61, s8, v62, v61\n"
"v_pk_mad_u16 v62, v116, 2, -1 op_sel_hi:[1,0,0]\n"
"v_pk_mad_u16 v63, v117, 2, -1 op_sel_hi:[1,0,0]\n"
"v_pk_min_u16 v62, v62, v63\n"
"v_pk_mad_u16 v63, v118, 2, -1 op_sel_hi:[1,0,0]\n"
"v_pk_min_u16 v62, v62, v63\n"
"v_pk_mad_u16 v63, v119, 2, -1 op_sel_hi:[1,0,0]\n"
"v_pk_min_u16 v62, v62, v63\n"
"v_pk_sub_u16 v62, s16, v62 clamp\n"
"v_or_b32 v61, v61, v62\n"
"v_cmp_ne_u32 vcc, 0, v61\n"
"s_cbranch_vccnz Lx_324\n"
"Lb_325:\n"
"ds_write_b32 v3, v60 offset:24584\n"
"ds_write_b64 v3, v[58:59] offset:26624\n"
"v_and_b32 v61, s8, v60\n"
"v_alignbit_b32 v62, v60, v60, 4\n"
"v_perm_b32 v24, v61, v58, s11\n"
"v_and_b32 v62, s8, v62\n"
"v_perm_b32 v25, v61, v58, s12\n"
"v_perm_b32 v26, v62, v59, s11\n"
"v_perm_b32 v27, v62, v59, s12\n"
"s_nop 1\n"
"v_mfma_f32_16x16x32_bf16 v[32:35], v[24:27], v[12:15], v[32:35]\n"
"s_add_u32 s52, s53, 0x7e000\n"
"s_and_b32 s52, s52, s54\n"
"buffer_load_dwordx4 v[116:119], v2, s[32:35], s52 offen offset:0\n"
"s_waitcnt vmcnt(35)\n"
"v_pk_sub_u16 v61, v120, s14 clamp\n"
"v_pk_sub_u16 v62, v121, s14 clamp\n"
"v_perm_b32 v58, v62, v61, s9\n"
"v_perm_b32 v61, v62, v61, s10\n"
"v_pk_sub_u16 v62, v122, s14 clamp\n"
"v_pk_sub_u16 v63, v123, s14 clamp\n"
"v_perm_b32 v59, v63, v62, s9\n"
"v_perm_b32 v62, v63, v62, s10\n"
"v_alignbit_b32 v62, v62, v62, 28\n"
"v_bfi_b32 v60, s8, v61, v62\n"
"v_bfi_b32 v61, s8, v62, v61\n"
"v_pk_mad_u16 v62, v120, 2, -1 op_sel_hi:[1,0,0]\n"
"v_pk_mad_u16 v63, v121, 2, -1 op_sel_hi:[1,0,0]\n"
"v_pk_min_u16 v62, v62, v63\n"
"v_pk_mad_u16 v63, v122, 2, -1 op_sel_hi:[1,0,0]\n"
"v_pk_min_u16 v62, v62, v63\n"
"v_pk_mad_u16 v63, v123, 2, -1 op_sel_hi:[1,0,0]\n"
"v_pk_min_u16 v62, v62, v63\n"
"v_pk_sub_u16 v62, s16, v62 clamp\n"
"v_or_b32 v61, v61, v62\n"
"v_cmp_ne_u32 vcc, 0, v61\n"
"s_cbranch_vccnz Lx_326\n"
"Lb_327:\n"
"ds_write_b32 v3, v60 offset:24588\n"
"ds_write_b64 v3, v[58:59] offset:26632\n"
"v_and_b32 v61, s8, v60\n"
"v_alignbit_b32 v62, v60, v60, 4\n"
"v_perm_b32 v24, v61, v58, s11\n"
"v_and_b32 v62, s8, v62\n"
"v_perm_b32 v25, v61, v58, s12\n"
"v_perm_b32 v26, v62, v59, s11\n"
"v_perm_b32 v27, v62, v59, s12\n"
"s_nop 1\n"
"v_mfma_f32_16x16x32_bf16 v[32:35], v[24:27], v[16:19], v[32:35]\n"
"v_accvgpr_read_b32 v60, a188\n"
"v_accvgpr_read_b32 v58, a186\n"
"v_accvgpr_read_b32 v59, a187\n"
"v_and_b32 v61, s8, v60\n"
"v_alignbit_b32 v62, v60, v60, 4\n"
"v_perm_b32 v20, v61, v58, s11\n"
"v_and_b32 v62, s8, v62\n"
"v_perm_b32 v21, v61, v58, s12\n"
"v_perm_b32 v22, v62, v59, s11\n"
"v_perm_b32 v23, v62, v59, s12\n"
"v_accvgpr_read_b32 v60, a191\n"
"v_accvgpr_read_b32 v58, a189\n"
"v_mfma_f32_16x16x32_bf16 v[36:39], v[20:23], v[12:15], v[36:39]\n"
"v_accvgpr_read_b32 v59, a190\n"
"v_and_b32 v61, s8, v60\n"
"v_alignbit_b32 v62, v60, v60, 4\n"
"v_perm_b32 v28, v61, v58, s11\n"
"v_and_b32 v62, s8, v62\n"
"v_perm_b32 v29, v61, v58, s12\n"
"v_perm_b32 v30, v62, v59, s11\n"
"v_perm_b32 v31, v62, v59, s12\n"
"s_nop 1\n"
"v_mfma_f32_16x16x32_bf16 v[36:39], v[28:31], v[16:19], v[36:39]\n"
"Lpace_328:\n"
"s_memrealtime s[96:97]\n"
"s_waitcnt lgkmcnt(0)\n"
"s_sub_u32 s96, s96, s98\n"
"s_cmp_ge_u32 s96, 2197\n"
"s_cbranch_scc1 Lpgo_329\n"
"s_sleep 2\n"
"s_branch Lpace_328\n"
"Lpgo_329:\n"
"s_add_u32 s52, s53, 0x85000\n"
"s_and_b32 s52, s52, s54\n"
"buffer_load_dwordx4 v[120:123], v2, s[32:35], s52 offen offset:0\n"
"s_waitcnt vmcnt(35)\n"
"v_pk_sub_u16 v61, v136, s14 clamp\n"
"v_pk_sub_u16 v62, v137, s14 clamp\n"
"v_perm_b32 v58, v62, v61, s9\n"
"v_perm_b32 v61, v62, v61, s10\n"
"v_pk_sub_u16 v62, v138, s14 clamp\n"
"v_pk_sub_u16 v63, v139, s14 clamp\n"
"v_perm_b32 v59, v63, v62, s9\n"
"v_perm_b32 v62, v63, v62, s10\n"
"v_alignbit_b32 v62, v62, v62, 28\n"
"v_bfi_b32 v60, s8, v61, v62\n"
"v_bfi_b32 v61, s8, v62, v61\n"
"v_pk_mad_u16 v62, v136, 2, -1 op_sel_hi:[1,0,0]\n"
"v_pk_mad_u16 v63, v137, 2, -1 op_sel_hi:[1,0,0]\n"
"v_pk_min_u16 v62, v62, v63\n"
"v_pk_mad_u16 v63, v138, 2, -1 op_sel_hi:[1,0,0]\n"
"v_pk_min_u16 v62, v62, v63\n"
"v_pk_mad_u16 v63, v139, 2, -1 op_sel_hi:[1,0,0]\n"
"v_pk_min_u16 v62, v62, v63\n"
"v_pk_sub_u16 v62, s16, v62 clamp\n"
"v_or_b32 v61, v61, v62\n"
"v_cmp_ne_u32 vcc, 0, v61\n"
"s_cbranch_vccnz Lx_330\n"
"Lb_331:\n"
"ds_write_b32 v3, v60 offset:27648\n"
"ds_write_b64 v3, v[58:59] offset:28672\n"
"v_and_b32 v61, s8, v60\n"
"v_alignbit_b32 v62, v60, v60, 4\n"
"v_perm_b32 v24, v61, v58, s11\n"
"v_and_b32 v62, s8, v62\n"
"v_perm_b32 v25, v61, v58, s12\n"
"v_perm_b32 v26, v62, v59, s11\n"
"v_perm_b32 v27, v62, v59, s12\n"
"v_mov_b32_dpp v63, v32 quad_perm:[0,0,0,0] row_mask:0xf bank_mask:0xf\n"
"v_add_f32_dpp v63, v33, v63 quad_perm:[1,1,1,1] row_mask:0xf bank_mask:0xf\n"
"v_add_f32_dpp v63, v34, v63 quad_perm:[2,2,2,2] row_mask:0xf bank_mask:0xf\n"
"v_add_f32_dpp v63, v35, v63 quad_perm:[3,3,3,3] row_mask:0xf bank_mask:0xf\n"
"v_cndmask_b32_e64 v51, v51, v63, s[64:65]\n"
"v_mfma_f32_16x16x32_bf16 v[32:35], v[24:27], v[4:7], 0\n"
"v_accvgpr_read_b32 v60, a194\n"
"v_accvgpr_read_b32 v58, a192\n"
"v_accvgpr_read_b32 v59, a193\n"
"v_and_b32 v61, s8, v60\n"
"v_alignbit_b32 v62, v60, v60, 4\n"
"v_perm_b32 v20, v61, v58, s11\n"
"v_and_b32 v62, s8, v62\n"
"v_perm_b32 v21, v61, v58, s12\n"
"v_perm_b32 v22, v62, v59, s11\n"
"v_perm_b32 v23, v62, v59, s12\n"
"v_mov_b32_dpp v63, v36 quad_perm:[0,0,0,0] row_mask:0xf bank_mask:0xf\n"
"v_add_f32_dpp v63, v37, v63 quad_perm:[1,1,1,1] row_mask:0xf bank_mask:0xf\n"
"v_add_f32_dpp v63, v38, v63 quad_perm:[2,2,2,2] row_mask:0xf bank_mask:0xf\n"
"v_add_f32_dpp v63, v39, v63 quad_perm:[3,3,3,3] row_mask:0xf bank_mask:0xf\n"
"v_cndmask_b32_e64 v47, v47, v63, s[66:67]\n"
"v_accvgpr_read_b32 v60, a197\n"
"v_accvgpr_read_b32 v58, a195\n"
"v_mfma_f32_16x16x32_bf16 v[36:39], v[20:23], v[4:7], 0\n"
"v_accvgpr_read_b32 v59, a196\n"
"v_and_b32 v61, s8, v60\n"
"v_alignbit_b32 v62, v60, v60, 4\n"
"v_perm_b32 v28, v61, v58, s11\n"
"v_and_b32 v62, s8, v62\n"
"v_perm_b32 v29, v61, v58, s12\n"
"v_perm_b32 v30, v62, v59, s11\n"
"v_perm_b32 v31, v62, v59, s12\n"
"s_nop 1\n"
"v_mfma_f32_16x16x32_bf16 v[36:39], v[28:31], v[8:11], v[36:39]\n"
"s_add_u32 s52, s53, 0x8c000\n"
"s_and_b32 s52, s52, s54\n"
"buffer_load_dwordx4 v[136:139], v2, s[32:35], s52 offen offset:0\n"
"s_waitcnt vmcnt(35)\n"
"v_pk_sub_u16 v61, v140, s14 clamp\n"
"v_pk_sub_u16 v62, v141, s14 clamp\n"
"v_perm_b32 v58, v62, v61, s9\n"
"v_perm_b32 v61, v62, v61, s10\n"
"v_pk_sub_u16 v62, v142, s14 clamp\n"
"v_pk_sub_u16 v63, v143, s14 clamp\n"
"v_perm_b32 v59, v63, v62, s9\n"
"v_perm_b32 v62, v63, v62, s10\n"
"v_alignbit_b32 v62, v62, v62, 28\n"
"v_bfi_b32 v60, s8, v61, v62\n"
"v_bfi_b32 v61, s8, v62, v61\n"
"v_pk_mad_u16 v62, v140, 2, -1 op_sel_hi:[1,0,0]\n"
"v_pk_mad_u16 v63, v141, 2, -1 op_sel_hi:[1,0,0]\n"
"v_pk_min_u16 v62, v62, v63\n"
"v_pk_mad_u16 v63, v142, 2, -1 op_sel_hi:[1,0,0]\n"
"v_pk_min_u16 v62, v62, v63\n"
"v_pk_mad_u16 v63, v143, 2, -1 op_sel_hi:[1,0,0]\n"
"v_pk_min_u16 v62, v62, v63\n"
"v_pk_sub_u16 v62, s16, v62 clamp\n"
"v_or_b32 v61, v61, v62\n"
"v_cmp_ne_u32 vcc, 0, v61\n"
"s_cbranch_vccnz Lx_332\n"
"Lb_333:\n"
"ds_write_b32 v3, v60 offset:27652\n"
"ds_write_b64 v3, v[58:59] offset:28680\n"
"v_and_b32 v61, s8, v60\n"
"v_alignbit_b32 v62, v60, v60, 4\n"
"v_perm_b32 v24, v61, v58, s11\n"
"v_and_b32 v62, s8, v62\n"
"v_perm_b32 v25, v61, v58, s12\n"
"v_perm_b32 v26, v62, v59, s11\n"
"v_perm_b32 v27, v62, v59, s12\n"
"s_nop 1\n"
"v_mfma_f32_16x16x32_bf16 v[32:35], v[24:27], v[8:11], v[32:35]\n"
"v_accvgpr_read_b32 v60, a200\n"
"v_accvgpr_read_b32 v58, a198\n"
"v_accvgpr_read_b32 v59, a199\n"
"v_and_b32 v61, s8, v60\n"
"v_alignbit_b32 v62, v60, v60, 4\n"
"v_perm_b32 v20, v61, v58, s11\n"
"v_and_b32 v62, s8, v62\n"
"v_perm_b32 v21, v61, v58, s12\n"
"v_perm_b32 v22, v62, v59, s11\n"
"v_perm_b32 v23, v62, v59, s12\n"
"v_accvgpr_read_b32 v60, a203\n"
"v_accvgpr_read_b32 v58, a201\n"
"v_mfma_f32_16x16x32_bf16 v[36:39], v[20:23], v[12:15], v[36:39]\n"
"v_accvgpr_read_b32 v59, a202\n"
"v_and_b32 v61, s8, v60\n"
"v_alignbit_b32 v62, v60, v60, 4\n"
"v_perm_b32 v28, v61, v58, s11\n"
"v_and_b32 v62, s8, v62\n"
"v_perm_b32 v29, v61, v58, s12\n"
"v_perm_b32 v30, v62, v59, s11\n"
"v_perm_b32 v31, v62, v59, s12\n"
"s_nop 1\n"
"v_mfma_f32_16x16x32_bf16 v[36:39], v[28:31], v[16:19], v[36:39]\n"
"s_add_u32 s52, s53, 0x93000\n"
"s_and_b32 s52, s52, s54\n"
"buffer_load_dwordx4 v[140:143], v2, s[32:35], s52 offen offset:0\n"
"s_waitcnt vmcnt(35)\n"
"v_pk_sub_u16 v61, v144, s14 clamp\n"
"v_pk_sub_u16 v62, v145, s14 clamp\n"
"v_perm_b32 v58, v62, v61, s9\n"
"v_perm_b32 v61, v62, v61, s10\n"
"v_pk_sub_u16 v62, v146, s14 clamp\n"
"v_pk_sub_u16 v63, v147, s14 clamp\n"
"v_perm_b32 v59, v63, v62, s9\n"
"v_perm_b32 v62, v63, v62, s10\n"
"v_alignbit_b32 v62, v62, v62, 28\n"
"v_bfi_b32 v60, s8, v61, v62\n"
"v_bfi_b32 v61, s8, v62, v61\n"
"v_pk_mad_u16 v62, v144, 2, -1 op_sel_hi:[1,0,0]\n"
"v_pk_mad_u16 v63, v145, 2, -1 op_sel_hi:[1,0,0]\n"
"v_pk_min_u16 v62, v62, v63\n"
"v_pk_mad_u16 v63, v146, 2, -1 op_sel_hi:[1,0,0]\n"
"v_pk_min_u16 v62, v62, v63\n"
"v_pk_mad_u16 v63, v147, 2, -1 op_sel_hi:[1,0,0]\n"
"v_pk_min_u16 v62, v62, v63\n"
"v_pk_sub_u16 v62, s16, v62 clamp\n"
"v_or_b32 v61, v61, v62\n"
"v_cmp_ne_u32 vcc, 0, v61\n"
"s_cbranch_vccnz Lx_334\n"
"Lb_335:\n"
"ds_write_b32 v3, v60 offset:27656\n"
"ds_write_b64 v3, v[58:59] offset:29696\n"
"v_and_b32 v61, s8, v60\n"
"v_alignbit_b32 v62, v60, v60, 4\n"
"v_perm_b32 v24, v61, v58, s11\n"
"v_and_b32 v62, s8, v62\n"
"v_perm_b32 v25, v61, v58, s12\n"
"v_perm_b32 v26, v62, v59, s11\n"
"v_perm_b32 v27, v62, v59, s12\n"
"s_nop 1\n"
"v_mfma_f32_16x16x32_bf16 v[32:35], v[24:27], v[12:15], v[32:35]\n"
"v_accvgpr_read_b32 v60, a206\n"
"v_accvgpr_read_b32 v58, a204\n"
"v_accvgpr_read_b32 v59, a205\n"
"v_and_b32 v61, s8, v60\n"
"v_alignbit_b32 v62, v60, v60, 4\n"
"v_perm_b32 v20, v61, v58, s11\n"
"v_and_b32 v62, s8, v62\n"
"v_perm_b32 v21, v61, v58, s12\n"
"v_perm_b32 v22, v62, v59, s11\n"
"v_perm_b32 v23, v62, v59, s12\n"
"v_mov_b32_dpp v63, v36 quad_perm:[0,0,0,0] row_mask:0xf bank_mask:0xf\n"
"v_add_f32_dpp v63, v37, v63 quad_perm:[1,1,1,1] row_mask:0xf bank_mask:0xf\n"
"v_add_f32_dpp v63, v38, v63 quad_perm:[2,2,2,2] row_mask:0xf bank_mask:0xf\n"
"v_add_f32_dpp v63, v39, v63 quad_perm:[3,3,3,3] row_mask:0xf bank_mask:0xf\n"
"v_cndmask_b32_e64 v48, v48, v63, s[60:61]\n"
"v_accvgpr_read_b32 v60, a209\n"
"v_accvgpr_read_b32 v58, a207\n"
"v_mfma_f32_16x16x32_bf16 v[36:39], v[20:23], v[4:7], 0\n"
"v_accvgpr_read_b32 v59, a208\n"
"v_and_b32 v61, s8, v60\n"
"v_alignbit_b32 v62, v60, v60, 4\n"
"v_perm_b32 v28, v61, v58, s11\n"
"v_and_b32 v62, s8, v62\n"
"v_perm_b32 v29, v61, v58, s12\n"
"v_perm_b32 v30, v62, v59, s11\n"
"v_perm_b32 v31, v62, v59, s12\n"
"s_nop 1\n"
"v_mfma_f32_16x16x32_bf16 v[36:39], v[28:31], v[8:11], v[36:39]\n"
"s_add_u32 s52, s53, 0x9a000\n"
"s_and_b32 s52, s52, s54\n"
"buffer_load_dwordx4 v[144:147], v2, s[32:35], s52 offen offset:0\n"
"s_waitcnt vmcnt(35)\n"
"v_pk_sub_u16 v61, v124, s14 clamp\n"
"v_pk_sub_u16 v62, v125, s14 clamp\n"
"v_perm_b32 v58, v62, v61, s9\n"
"v_perm_b32 v61, v62, v61, s10\n"
"v_pk_sub_u16 v62, v126, s14 clamp\n"
"v_pk_sub_u16 v63, v127, s14 clamp\n"
"v_perm_b32 v59, v63, v62, s9\n"
"v_perm_b32 v62, v63, v62, s10\n"
"v_alignbit_b32 v62, v62, v62, 28\n"
"v_bfi_b32 v60, s8, v61, v62\n"
"v_bfi_b32 v61, s8, v62, v61\n"
"v_pk_mad_u16 v62, v124, 2, -1 op_sel_hi:[1,0,0]\n"
"v_pk_mad_u16 v63, v125, 2, -1 op_sel_hi:[1,0,0]\n"
"v_pk_min_u16 v62, v62, v63\n"
"v_pk_mad_u16 v63, v126, 2, -1 op_sel_hi:[1,0,0]\n"
"v_pk_min_u16 v62, v62, v63\n"
"v_pk_mad_u16 v63, v127, 2, -1 op_sel_hi:[1,0,0]\n"
"v_pk_min_u16 v62, v62, v63\n"
"v_pk_sub_u16 v62, s16, v62 clamp\n"
"v_or_b32 v61, v61, v62\n"
"v_cmp_ne_u32 vcc, 0, v61\n"
"s_cbranch_vccnz Lx_336\n"
"Lb_337:\n"
"ds_write_b32 v3, v60 offset:27660\n"
"ds_write_b64 v3, v[58:59] offset:29704\n"
"v_and_b32 v61, s8, v60\n"
"v_alignbit_b32 v62, v60, v60, 4\n"
"v_perm_b32 v24, v61, v58, s11\n"
"v_and_b32 v62, s8, v62\n"
"v_perm_b32 v25, v61, v58, s12\n"
"v_perm_b32 v26, v62, v59, s11\n"
"v_perm_b32 v27, v62, v59, s12\n"
"s_nop 1\n"
"v_mfma_f32_16x16x32_bf16 v[32:35], v[24:27], v[16:19], v[32:35]\n"
"Lpace_338:\n"
"s_memrealtime s[96:97]\n"
"s_waitcnt lgkmcnt(0)\n"
"s_sub_u32 s96, s96, s98\n"
"s_cmp_ge_u32 s96, 2253\n"
"s_cbranch_scc1 Lpgo_339\n"
"s_sleep 2\n"
"s_branch Lpace_338\n"
"Lpgo_339:\n"
"s_add_u32 s52, s53, 0xa1000\n"
"s_and_b32 s52, s52, s54\n"
"buffer_load_dwordx4 v[124:127], v2, s[32:35], s52 offen offset:0\n"
"s_waitcnt vmcnt(35)\n"
"v_pk_sub_u16 v61, v128, s14 clamp\n"
"v_pk_sub_u16 v62, v129, s14 clamp\n"
"v_perm_b32 v58, v62, v61, s9\n"
"v_perm_b32 v61, v62, v61, s10\n"
"v_pk_sub_u16 v62, v130, s14 clamp\n"
"v_pk_sub_u16 v63, v131, s14 clamp\n"
"v_perm_b32 v59, v63, v62, s9\n"
"v_perm_b32 v62, v63, v62, s10\n"
"v_alignbit_b32 v62, v62, v62, 28\n"
"v_bfi_b32 v60, s8, v61, v62\n"
"v_bfi_b32 v61, s8, v62, v61\n"
"v_pk_mad_u16 v62, v128, 2, -1 op_sel_hi:[1,0,0]\n"
"v_pk_mad_u16 v63, v129, 2, -1 op_sel_hi:[1,0,0]\n"
"v_pk_min_u16 v62, v62, v63\n"
"v_pk_mad_u16 v63, v130, 2, -1 op_sel_hi:[1,0,0]\n"
"v_pk_min_u16 v62, v62, v63\n"
"v_pk_mad_u16 v63, v131, 2, -1 op_sel_hi:[1,0,0]\n"
"v_pk_min_u16 v62, v62, v63\n"
"v_pk_sub_u16 v62, s16, v62 clamp\n"
"v_or_b32 v61, v61, v62\n"
"v_cmp_ne_u32 vcc, 0, v61\n"
"s_cbranch_vccnz Lx_340\n"
"Lb_341:\n"
"ds_write_b32 v3, v60 offset:30720\n"
"ds_write_b64 v3, v[58:59] offset:31744\n"
"v_and_b32 v61, s8, v60\n"
"v_alignbit_b32 v62, v60, v60, 4\n"
"v_perm_b32 v24, v61, v58, s11\n"
"v_and_b32 v62, s8, v62\n"
"v_perm_b32 v25, v61, v58, s12\n"
"v_perm_b32 v26, v62, v59, s11\n"
"v_perm_b32 v27, v62, v59, s12\n"
"v_mov_b32_dpp v63, v32 quad_perm:[0,0,0,0] row_mask:0xf bank_mask:0xf\n"
"v_add_f32_dpp v63, v33, v63 quad_perm:[1,1,1,1] row_mask:0xf bank_mask:0xf\n"
"v_add_f32_dpp v63, v34, v63 quad_perm:[2,2,2,2] row_mask:0xf bank_mask:0xf\n"
"v_add_f32_dpp v63, v35, v63 quad_perm:[3,3,3,3] row_mask:0xf bank_mask:0xf\n"
"v_cndmask_b32_e64 v51, v51, v63, s[66:67]\n"
"v_mfma_f32_16x16x32_bf16 v[32:35], v[24:27], v[4:7], 0\n"
"v_accvgpr_read_b32 v60, a212\n"
"v_accvgpr_read_b32 v58, a210\n"
"v_accvgpr_read_b32 v59, a211\n"
"v_and_b32 v61, s8, v60\n"
"v_alignbit_b32 v62, v60, v60, 4\n"
"v_perm_b32 v20, v61, v58, s11\n"
"v_and_b32 v62, s8, v62\n"
"v_perm_b32 v21, v61, v58, s12\n"
"v_perm_b32 v22, v62, v59, s11\n"
"v_perm_b32 v23, v62, v59, s12\n"
"v_accvgpr_read_b32 v60, a215\n"
"v_accvgpr_read_b32 v58, a213\n"
"v_mfma_f32_16x16x32_bf16 v[36:39], v[20:23], v[12:15], v[36:39]\n"
"v_accvgpr_read_b32 v59, a214\n"
"v_and_b32 v61, s8, v60\n"
"v_alignbit_b32 v62, v60, v60, 4\n"
"v_perm_b32 v28, v61, v58, s11\n"
"v_and_b32 v62, s8, v62\n"
"v_perm_b32 v29, v61, v58, s12\n"
"v_perm_b32 v30, v62, v59, s11\n"
"v_perm_b32 v31, v62, v59, s12\n"
"s_nop 1\n"
"v_mfma_f32_16x16x32_bf16 v[36:39], v[28:31], v[16:19], v[36:39]\n"
"s_add_u32 s52, s53, 0xa8000\n"
"s_and_b32 s52, s52, s54\n"
"buffer_load_dwordx4 v[128:131], v2, s[32:35], s52 offen offset:0\n"
"s_waitcnt vmcnt(35)\n"
"v_pk_sub_u16 v61, v132, s14 clamp\n"
"v_pk_sub_u16 v62, v133, s14 clamp\n"
"v_perm_b32 v58, v62, v61, s9\n"
"v_perm_b32 v61, v62, v61, s10\n"
"v_pk_sub_u16 v62, v134, s14 clamp\n"
"v_pk_sub_u16 v63, v135, s14 clamp\n"
"v_perm_b32 v59, v63, v62, s9\n"
"v_perm_b32 v62, v63, v62, s10\n"
"v_alignbit_b32 v62, v62, v62, 28\n"
"v_bfi_b32 v60, s8, v61, v62\n"
"v_bfi_b32 v61, s8, v62, v61\n"
"v_pk_mad_u16 v62, v132, 2, -1 op_sel_hi:[1,0,0]\n"
"v_pk_mad_u16 v63, v133, 2, -1 op_sel_hi:[1,0,0]\n"
"v_pk_min_u16 v62, v62, v63\n"
"v_pk_mad_u16 v63, v134, 2, -1 op_sel_hi:[1,0,0]\n"
"v_pk_min_u16 v62, v62, v63\n"
"v_pk_mad_u16 v63, v135, 2, -1 op_sel_hi:[1,0,0]\n"
"v_pk_min_u16 v62, v62, v63\n"
"v_pk_sub_u16 v62, s16, v62 clamp\n"
"v_or_b32 v61, v61, v62\n"
"v_cmp_ne_u32 vcc, 0, v61\n"
"s_cbranch_vccnz Lx_342\n"
"Lb_343:\n"
"ds_write_b32 v3, v60 offset:30724\n"
"ds_write_b64 v3, v[58:59] offset:31752\n"
"v_and_b32 v61, s8, v60\n"
"v_alignbit_b32 v62, v60, v60, 4\n"
"v_perm_b32 v24, v61, v58, s11\n"
"v_and_b32 v62, s8, v62\n"
"v_perm_b32 v25, v61, v58, s12\n"
"v_perm_b32 v26, v62, v59, s11\n"
"v_perm_b32 v27, v62, v59, s12\n"
"s_nop 1\n"
"v_mfma_f32_16x16x32_bf16 v[32:35], v[24:27], v[8:11], v[32:35]\n"
"v_and_b32 v61, s8, v66\n"
"v_alignbit_b32 v62, v66, v66, 4\n"
"v_perm_b32 v20, v61, v64, s11\n"
"v_and_b32 v62, s8, v62\n"
"v_perm_b32 v21, v61, v64, s12\n"
"v_perm_b32 v22, v62, v65, s11\n"
"v_perm_b32 v23, v62, v65, s12\n"
"v_mov_b32_dpp v63, v36 quad_perm:[0,0,0,0] row_mask:0xf bank_mask:0xf\n"
"v_add_f32_dpp v63, v37, v63 quad_perm:[1,1,1,1] row_mask:0xf bank_mask:0xf\n"
"v_add_f32_dpp v63, v38, v63 quad_perm:[2,2,2,2] row_mask:0xf bank_mask:0xf\n"
"v_add_f32_dpp v63, v39, v63 quad_perm:[3,3,3,3] row_mask:0xf bank_mask:0xf\n"
"v_cndmask_b32_e64 v48, v48, v63, s[62:63]\n"
"v_and_b32 v61, s8, v69\n"
"v_alignbit_b32 v62, v69, v69, 4\n"
"v_mfma_f32_16x16x32_bf16 v[36:39], v[20:23], v[4:7], 0\n"
"v_perm_b32 v28, v61, v67, s11\n"
"v_and_b32 v62, s8, v62\n"
"v_perm_b32 v29, v61, v67, s12\n"
"v_perm_b32 v30, v62, v68, s11\n"
"v_perm_b32 v31, v62, v68, s12\n"
"s_nop 1\n"
"v_mfma_f32_16x16x32_bf16 v[36:39], v[28:31], v[8:11], v[36:39]\n"
"s_add_u32 s52, s53, 0xaf000\n"
"s_and_b32 s52, s52, s54\n"
"buffer_load_dwordx4 v[132:135], v2, s[32:35], s52 offen offset:0\n"
"s_waitcnt vmcnt(35)\n"
"v_pk_sub_u16 v61, v148, s14 clamp\n"
"v_pk_sub_u16 v62, v149, s14 clamp\n"
"v_perm_b32 v58, v62, v61, s9\n"
"v_perm_b32 v61, v62, v61, s10\n"
"v_pk_sub_u16 v62, v150, s14 clamp\n"
"v_pk_sub_u16 v63, v151, s14 clamp\n"
"v_perm_b32 v59, v63, v62, s9\n"
"v_perm_b32 v62, v63, v62, s10\n"
"v_alignbit_b32 v62, v62, v62, 28\n"
"v_bfi_b32 v60, s8, v61, v62\n"
"v_bfi_b32 v61, s8, v62, v61\n"
"v_pk_mad_u16 v62, v148, 2, -1 op_sel_hi:[1,0,0]\n"
"v_pk_mad_u16 v63, v149, 2, -1 op_sel_hi:[1,0,0]\n"
"v_pk_min_u16 v62, v62, v63\n"
"v_pk_mad_u16 v63, v150, 2, -1 op_sel_hi:[1,0,0]\n"
"v_pk_min_u16 v62, v62, v63\n"
"v_pk_mad_u16 v63, v151, 2, -1 op_sel_hi:[1,0,0]\n"
"v_pk_min_u16 v62, v62, v63\n"
"v_pk_sub_u16 v62, s16, v62 clamp\n"
"v_or_b32 v61, v61, v62\n"
"v_cmp_ne_u32 vcc, 0, v61\n"
"s_cbranch_vccnz Lx_344\n"
"Lb_345:\n"
"ds_write_b32 v3, v60 offset:30728\n"
"ds_write_b64 v3, v[58:59] offset:32768\n"
"v_and_b32 v61, s8, v60\n"
"v_alignbit_b32 v62, v60, v60, 4\n"
"v_perm_b32 v24, v61, v58, s11\n"
"v_and_b32 v62, s8, v62\n"
"v_perm_b32 v25, v61, v58, s12\n"
"v_perm_b32 v26, v62, v59, s11\n"
"v_perm_b32 v27, v62, v59, s12\n"
"s_nop 1\n"
"v_mfma_f32_16x16x32_bf16 v[32:35], v[24:27], v[12:15], v[32:35]\n"
"v_and_b32 v61, s8, v72\n"
"v_alignbit_b32 v62, v72, v72, 4\n"
"v_perm_b32 v20, v61, v70, s11\n"
"v_and_b32 v62, s8, v62\n"
"v_perm_b32 v21, v61, v70, s12\n"
"v_perm_b32 v22, v62, v71, s11\n"
"v_perm_b32 v23, v62, v71, s12\n"
"v_and_b32 v61, s8, v75\n"
"v_alignbit_b32 v62, v75, v75, 4\n"
"v_mfma_f32_16x16x32_bf16 v[36:39], v[20:23], v[12:15], v[36:39]\n"
"v_perm_b32 v28, v61, v73, s11\n"
"v_and_b32 v62, s8, v62\n"
"v_perm_b32 v29, v61, v73, s12\n"
"v_perm_b32 v30, v62, v74, s11\n"
"v_perm_b32 v31, v62, v74, s12\n"
"s_nop 1\n"
"v_mfma_f32_16x16x32_bf16 v[36:39], v[28:31], v[16:19], v[36:39]\n"
"s_add_u32 s52, s53, 0xb6000\n"
"s_and_b32 s52, s52, s54\n"
"buffer_load_dwordx4 v[148:151], v2, s[32:35], s52 offen offset:0\n"
"s_waitcnt vmcnt(35)\n"
"v_pk_sub_u16 v61, v152, s14 clamp\n"
"v_pk_sub_u16 v62, v153, s14 clamp\n"
"v_perm_b32 v58, v62, v61, s9\n"
"v_perm_b32 v61, v62, v61, s10\n"
"v_pk_sub_u16 v62, v154, s14 clamp\n"
"v_pk_sub_u16 v63, v155, s14 clamp\n"
"v_perm_b32 v59, v63, v62, s9\n"
"v_perm_b32 v62, v63, v62, s10\n"
"v_alignbit_b32 v62, v62, v62, 28\n"
"v_bfi_b32 v60, s8, v61, v62\n"
"v_bfi_b32 v61, s8, v62, v61\n"
"v_pk_mad_u16 v62, v152, 2, -1 op_sel_hi:[1,0,0]\n"
"v_pk_mad_u16 v63, v153, 2, -1 op_sel_hi:[1,0,0]\n"
"v_pk_min_u16 v62, v62, v63\n"
"v_pk_mad_u16 v63, v154, 2, -1 op_sel_hi:[1,0,0]\n"
"v_pk_min_u16 v62, v62, v63\n"
"v_pk_mad_u16 v63, v155, 2, -1 op_sel_hi:[1,0,0]\n"
"v_pk_min_u16 v62, v62, v63\n"
"v_pk_sub_u16 v62, s16, v62 clamp\n"
"v_or_b32 v61, v61, v62\n"
"v_cmp_ne_u32 vcc, 0, v61\n"
"s_cbranch_vccnz Lx_346\n"
"Lb_347:\n"
"ds_write_b32 v3, v60 offset:30732\n"
"ds_write_b64 v3, v[58:59] offset:32776\n"
"v_and_b32 v61, s8, v60\n"
"v_alignbit_b32 v62, v60, v60, 4\n"
"v_perm_b32 v24, v61, v58, s11\n"
"v_and_b32 v62, s8, v62\n"
"v_perm_b32 v25, v61, v58, s12\n"
"v_perm_b32 v26, v62, v59, s11\n"
"v_perm_b32 v27, v62, v59, s12\n"
"s_nop 1\n"
"v_mfma_f32_16x16x32_bf16 v[32:35], v[24:27], v[16:19], v[32:35]\n"
"v_and_b32 v61, s8, v78\n"
"v_alignbit_b32 v62, v78, v78, 4\n"
"v_perm_b32 v20, v61, v76, s11\n"
"v_and_b32 v62, s8, v62\n"
"v_perm_b32 v21, v61, v76, s12\n"
"v_perm_b32 v22, v62, v77, s11\n"
"v_perm_b32 v23, v62, v77, s12\n"
"v_mov_b32_dpp v63, v36 quad_perm:[0,0,0,0] row_mask:0xf bank_mask:0xf\n"
"v_add_f32_dpp v63, v37, v63 quad_perm:[1,1,1,1] row_mask:0xf bank_mask:0xf\n"
"v_add_f32_dpp v63, v38, v63 quad_perm:[2,2,2,2] row_mask:0xf bank_mask:0xf\n"
"v_add_f32_dpp v63, v39, v63 quad_perm:[3,3,3,3] row_mask:0xf bank_mask:0xf\n"
"v_cndmask_b32_e64 v48, v48, v63, s[64:65]\n"
"v_and_b32 v61, s8, v81\n"
"v_alignbit_b32 v62, v81, v81, 4\n"
"v_mfma_f32_16x16x32_bf16 v[36:39], v[20:23], v[4:7], 0\n"
"v_perm_b32 v28, v61, v79, s11\n"
"v_and_b32 v62, s8, v62\n"
"v_perm_b32 v29, v61, v79, s12\n"
"v_perm_b32 v30, v62, v80, s11\n"
"v_perm_b32 v31, v62, v80, s12\n"
"s_nop 1\n"
"v_mfma_f32_16x16x32_bf16 v[36:39], v[28:31], v[8:11], v[36:39]\n"
"Lpace_348:\n"
"s_memrealtime s[96:97]\n"
"s_waitcnt lgkmcnt(0)\n"
"s_sub_u32 s96, s96, s98\n"
"s_cmp_ge_u32 s96, 2310\n"
"s_cbranch_scc1 Lpgo_349\n"
"s_sleep 2\n"
"s_branch Lpace_348\n"
"Lpgo_349:\n"
"s_add_u32 s52, s53, 0xbd000\n"
"s_and_b32 s52, s52, s54\n"
"buffer_load_dwordx4 v[152:155], v2, s[32:35], s52 offen offset:0\n"
"s_waitcnt vmcnt(35)\n"
"v_pk_sub_u16 v61, v156, s14 clamp\n"
"v_pk_sub_u16 v62, v157, s14 clamp\n"
"v_perm_b32 v58, v62, v61, s9\n"
"v_perm_b32 v61, v62, v61, s10\n"
"v_pk_sub_u16 v62, v158, s14 clamp\n"
"v_pk_sub_u16 v63, v159, s14 clamp\n"
"v_perm_b32 v59, v63, v62, s9\n"
"v_perm_b32 v62, v63, v62, s10\n"
"v_alignbit_b32 v62, v62, v62, 28\n"
"v_bfi_b32 v60, s8, v61, v62\n"
"v_bfi_b32 v61, s8, v62, v61\n"
"v_pk_mad_u16 v62, v156, 2, -1 op_sel_hi:[1,0,0]\n"
"v_pk_mad_u16 v63, v157, 2, -1 op_sel_hi:[1,0,0]\n"
"v_pk_min_u16 v62, v62, v63\n"
"v_pk_mad_u16 v63, v158, 2, -1 op_sel_hi:[1,0,0]\n"
"v_pk_min_u16 v62, v62, v63\n"
"v_pk_mad_u16 v63, v159, 2, -1 op_sel_hi:[1,0,0]\n"
"v_pk_min_u16 v62, v62, v63\n"
"v_pk_sub_u16 v62, s16, v62 clamp\n"
"v_or_b32 v61, v61, v62\n"
"v_cmp_ne_u32 vcc, 0, v61\n"
"s_cbranch_vccnz Lx_350\n"
"Lb_351:\n"
"ds_write_b32 v3, v60 offset:33792\n"
"ds_write_b64 v3, v[58:59] offset:34816\n"
"v_and_b32 v61, s8, v60\n"
"v_alignbit_b32 v62, v60, v60, 4\n"
"v_perm_b32 v24, v61, v58, s11\n"
"v_and_b32 v62, s8, v62\n"
"v_perm_b32 v25, v61, v58, s12\n"
"v_perm_b32 v26, v62, v59, s11\n"
"v_perm_b32 v27, v62, v59, s12\n"
"v_mov_b32_dpp v63, v32 quad_perm:[0,0,0,0] row_mask:0xf bank_mask:0xf\n"
"v_add_f32_dpp v63, v33, v63 quad_perm:[1,1,1,1] row_mask:0xf bank_mask:0xf\n"
"v_add_f32_dpp v63, v34, v63 quad_perm:[2,2,2,2] row_mask:0xf bank_mask:0xf\n"
"v_add_f32_dpp v63, v35, v63 quad_perm:[3,3,3,3] row_mask:0xf bank_mask:0xf\n"
"v_cndmask_b32_e64 v52, v52, v63, s[60:61]\n"
"v_mfma_f32_16x16x32_bf16 v[32:35], v[24:27], v[4:7], 0\n"
"s_add_u32 s52, s53, 0xc4000\n"
"s_and_b32 s52, s52, s54\n"
"buffer_load_dwordx4 v[156:159], v2, s[32:35], s52 offen offset:0\n"
"s_waitcnt vmcnt(35)\n"
"v_pk_sub_u16 v61, v160, s14 clamp\n"
"v_pk_sub_u16 v62, v161, s14 clamp\n"
"v_perm_b32 v58, v62, v61, s9\n"
"v_perm_b32 v61, v62, v61, s10\n"
"v_pk_sub_u16 v62, v162, s14 clamp\n"
"v_pk_sub_u16 v63, v163, s14 clamp\n"
"v_perm_b32 v59, v63, v62, s9\n"
"v_perm_b32 v62, v63, v62, s10\n"
"v_alignbit_b32 v62, v62, v62, 28\n"
"v_bfi_b32 v60, s8, v61, v62\n"
"v_bfi_b32 v61, s8, v62, v61\n"
"v_pk_mad_u16 v62, v160, 2, -1 op_sel_hi:[1,0,0]\n"
"v_pk_mad_u16 v63, v161, 2, -1 op_sel_hi:[1,0,0]\n"
"v_pk_min_u16 v62, v62, v63\n"
"v_pk_mad_u16 v63, v162, 2, -1 op_sel_hi:[1,0,0]\n"
"v_pk_min_u16 v62, v62, v63\n"
"v_pk_mad_u16 v63, v163, 2, -1 op_sel_hi:[1,0,0]\n"
"v_pk_min_u16 v62, v62, v63\n"
"v_pk_sub_u16 v62, s16, v62 clamp\n"
"v_or_b32 v61, v61, v62\n"
"v_cmp_ne_u32 vcc, 0, v61\n"
"s_cbranch_vccnz Lx_352\n"
"Lb_353:\n"
"ds_write_b32 v3, v60 offset:33796\n"
"ds_write_b64 v3, v[58:59] offset:34824\n"
"v_and_b32 v61, s8, v60\n"
"v_alignbit_b32 v62, v60, v60, 4\n"
"v_perm_b32 v24, v61, v58, s11\n"
"v_and_b32 v62, s8, v62\n"
"v_perm_b32 v25, v61, v58, s12\n"
"v_perm_b32 v26, v62, v59, s11\n"
"v_perm_b32 v27, v62, v59, s12\n"
"s_nop 1\n"
"v_mfma_f32_16x16x32_bf16 v[32:35], v[24:27], v[8:11], v[32:35]\n"
"v_and_b32 v61, s8, v84\n"
"v_alignbit_b32 v62, v84, v84, 4\n"
"v_perm_b32 v20, v61, v82, s11\n"
"v_and_b32 v62, s8, v62\n"
"v_perm_b32 v21, v61, v82, s12\n"
"v_perm_b32 v22, v62, v83, s11\n"
"v_perm_b32 v23, v62, v83, s12\n"
"v_and_b32 v61, s8, v87\n"
"v_alignbit_b32 v62, v87, v87, 4\n"
"v_mfma_f32_16x16x32_bf16 v[36:39], v[20:23], v[12:15], v[36:39]\n"
"v_perm_b32 v28, v61, v85, s11\n"
"v_and_b32 v62, s8, v62\n"
"v_perm_b32 v29, v61, v85, s12\n"
"v_perm_b32 v30, v62, v86, s11\n"
"v_perm_b32 v31, v62, v86, s12\n"
"s_nop 1\n"
"v_mfma_f32_16x16x32_bf16 v[36:39], v[28:31], v[16:19], v[36:39]\n"
"s_add_u32 s52, s53, 0xcb000\n"
"s_and_b32 s52, s52, s54\n"
"buffer_load_dwordx4 v[160:163], v2, s[32:35], s52 offen offset:0\n"
"s_waitcnt vmcnt(35)\n"
"v_pk_sub_u16 v61, v164, s14 clamp\n"
"v_pk_sub_u16 v62, v165, s14 clamp\n"
"v_perm_b32 v58, v62, v61, s9\n"
"v_perm_b32 v61, v62, v61, s10\n"
"v_pk_sub_u16 v62, v166, s14 clamp\n"
"v_pk_sub_u16 v63, v167, s14 clamp\n"
"v_perm_b32 v59, v63, v62, s9\n"
"v_perm_b32 v62, v63, v62, s10\n"
"v_alignbit_b32 v62, v62, v62, 28\n"
"v_bfi_b32 v60, s8, v61, v62\n"
"v_bfi_b32 v61, s8, v62, v61\n"
"v_pk_mad_u16 v62, v164, 2, -1 op_sel_hi:[1,0,0]\n"
"v_pk_mad_u16 v63, v165, 2, -1 op_sel_hi:[1,0,0]\n"
"v_pk_min_u16 v62, v62, v63\n"
"v_pk_mad_u16 v63, v166, 2, -1 op_sel_hi:[1,0,0]\n"
"v_pk_min_u16 v62, v62, v63\n"
"v_pk_mad_u16 v63, v167, 2, -1 op_sel_hi:[1,0,0]\n"
"v_pk_min_u16 v62, v62, v63\n"
"v_pk_sub_u16 v62, s16, v62 clamp\n"
"v_or_b32 v61, v61, v62\n"
"v_cmp_ne_u32 vcc, 0, v61\n"
"s_cbranch_vccnz Lx_354\n"
"Lb_355:\n"
"ds_write_b32 v3, v60 offset:33800\n"
"ds_write_b64 v3, v[58:59] offset:35840\n"
"v_and_b32 v61, s8, v60\n"
"v_alignbit_b32 v62, v60, v60, 4\n"
"v_perm_b32 v24, v61, v58, s11\n"
"v_and_b32 v62, s8, v62\n"
"v_perm_b32 v25, v61, v58, s12\n"
"v_perm_b32 v26, v62, v59, s11\n"
"v_perm_b32 v27, v62, v59, s12\n"
"s_nop 1\n"
"v_mfma_f32_16x16x32_bf16 v[32:35], v[24:27], v[12:15], v[32:35]\n"
"s_add_u32 s52, s53, 0xd2000\n"
"s_and_b32 s52, s52, s54\n"
"buffer_load_dwordx4 v[164:167], v2, s[32:35], s52 offen offset:0\n"
"s_waitcnt vmcnt(35)\n"
"v_pk_sub_u16 v61, v168, s14 clamp\n"
"v_pk_sub_u16 v62, v169, s14 clamp\n"
"v_perm_b32 v58, v62, v61, s9\n"
"v_perm_b32 v61, v62, v61, s10\n"
"v_pk_sub_u16 v62, v170, s14 clamp\n"
"v_pk_sub_u16 v63, v171, s14 clamp\n"
"v_perm_b32 v59, v63, v62, s9\n"
"v_perm_b32 v62, v63, v62, s10\n"
"v_alignbit_b32 v62, v62, v62, 28\n"
"v_bfi_b32 v60, s8, v61, v62\n"
"v_bfi_b32 v61, s8, v62, v61\n"
"v_pk_mad_u16 v62, v168, 2, -1 op_sel_hi:[1,0,0]\n"
"v_pk_mad_u16 v63, v169, 2, -1 op_sel_hi:[1,0,0]\n"
"v_pk_min_u16 v62, v62, v63\n"
"v_pk_mad_u16 v63, v170, 2, -1 op_sel_hi:[1,0,0]\n"
"v_pk_min_u16 v62, v62, v63\n"
"v_pk_mad_u16 v63, v171, 2, -1 op_sel_hi:[1,0,0]\n"
"v_pk_min_u16 v62, v62, v63\n"
"v_pk_sub_u16 v62, s16, v62 clamp\n"
"v_or_b32 v61, v61, v62\n"
"v_cmp_ne_u32 vcc, 0, v61\n"
"s_cbranch_vccnz Lx_356\n"
"Lb_357:\n"
"ds_write_b32 v3, v60 offset:33804\n"
"ds_write_b64 v3, v[58:59] offset:35848\n"
"v_and_b32 v61, s8, v60\n"
"v_alignbit_b32 v62, v60, v60, 4\n"
"v_perm_b32 v24, v61, v58, s11\n"
"v_and_b32 v62, s8, v62\n"
"v_perm_b32 v25, v61, v58, s12\n"
"v_perm_b32 v26, v62, v59, s11\n"
"v_perm_b32 v27, v62, v59, s12\n"
"s_nop 1\n"
"v_mfma_f32_16x16x32_bf16 v[32:35], v[24:27], v[16:19], v[32:35]\n"
"Lpace_358:\n"
"s_memrealtime s[96:97]\n"
"s_waitcnt lgkmcnt(0)\n"
"s_sub_u32 s96, s96, s98\n"
"s_cmp_ge_u32 s96, 2367\n"
"s_cbranch_scc1 Lpgo_359\n"
"s_sleep 2\n"
"s_branch Lpace_358\n"
"Lpgo_359:\n"
"s_add_u32 s52, s53, 0xd9000\n"
"s_and_b32 s52, s52, s54\n"
"buffer_load_dwordx4 v[168:171], v2, s[32:35], s52 offen offset:0\n"
"s_waitcnt vmcnt(35)\n"
"v_pk_sub_u16 v61, v184, s14 clamp\n"
"v_pk_sub_u16 v62, v185, s14 clamp\n"
"v_perm_b32 v58, v62, v61, s9\n"
"v_perm_b32 v61, v62, v61, s10\n"
"v_pk_sub_u16 v62, v186, s14 clamp\n"
"v_pk_sub_u16 v63, v187, s14 clamp\n"
"v_perm_b32 v59, v63, v62, s9\n"
"v_perm_b32 v62, v63, v62, s10\n"
"v_alignbit_b32 v62, v62, v62, 28\n"
"v_bfi_b32 v60, s8, v61, v62\n"
"v_bfi_b32 v61, s8, v62, v61\n"
"v_pk_mad_u16 v62, v184, 2, -1 op_sel_hi:[1,0,0]\n"
"v_pk_mad_u16 v63, v185, 2, -1 op_sel_hi:[1,0,0]\n"
"v_pk_min_u16 v62, v62, v63\n"
"v_pk_mad_u16 v63, v186, 2, -1 op_sel_hi:[1,0,0]\n"
"v_pk_min_u16 v62, v62, v63\n"
"v_pk_mad_u16 v63, v187, 2, -1 op_sel_hi:[1,0,0]\n"
"v_pk_min_u16 v62, v62, v63\n"
"v_pk_sub_u16 v62, s16, v62 clamp\n"
"v_or_b32 v61, v61, v62\n"
"v_cmp_ne_u32 vcc, 0, v61\n"
"s_cbranch_vccnz Lx_360\n"
"Lb_361:\n"
"ds_write_b32 v3, v60 offset:36864\n"
"ds_write_b64 v3, v[58:59] offset:37888\n"
"v_and_b32 v61, s8, v60\n"
"v_alignbit_b32 v62, v60, v60, 4\n"
"v_perm_b32 v24, v61, v58, s11\n"
"v_and_b32 v62, s8, v62\n"
"v_perm_b32 v25, v61, v58, s12\n"
"v_perm_b32 v26, v62, v59, s11\n"
"v_perm_b32 v27, v62, v59, s12\n"
"v_mov_b32_dpp v63, v32 quad_perm:[0,0,0,0] row_mask:0xf bank_mask:0xf\n"
"v_add_f32_dpp v63, v33, v63 quad_perm:[1,1,1,1] row_mask:0xf bank_mask:0xf\n"
"v_add_f32_dpp v63, v34, v63 quad_perm:[2,2,2,2] row_mask:0xf bank_mask:0xf\n"
"v_add_f32_dpp v63, v35, v63 quad_perm:[3,3,3,3] row_mask:0xf bank_mask:0xf\n"
"v_cndmask_b32_e64 v52, v52, v63, s[62:63]\n"
"v_mfma_f32_16x16x32_bf16 v[32:35], v[24:27], v[4:7], 0\n"
"s_add_u32 s52, s53, 0xe0000\n"
"s_and_b32 s52, s52, s54\n"
"buffer_load_dwordx4 v[184:187], v2, s[32:35], s52 offen offset:0\n"
"s_waitcnt vmcnt(35)\n"
"v_pk_sub_u16 v61, v188, s14 clamp\n"
"v_pk_sub_u16 v62, v189, s14 clamp\n"
"v_perm_b32 v58, v62, v61, s9\n"
"v_perm_b32 v61, v62, v61, s10\n"
"v_pk_sub_u16 v62, v190, s14 clamp\n"
"v_pk_sub_u16 v63, v191, s14 clamp\n"
"v_perm_b32 v59, v63, v62, s9\n"
"v_perm_b32 v62, v63, v62, s10\n"
"v_alignbit_b32 v62, v62, v62, 28\n"
"v_bfi_b32 v60, s8, v61, v62\n"
"v_bfi_b32 v61, s8, v62, v61\n"
"v_pk_mad_u16 v62, v188, 2, -1 op_sel_hi:[1,0,0]\n"
"v_pk_mad_u16 v63, v189, 2, -1 op_sel_hi:[1,0,0]\n"
"v_pk_min_u16 v62, v62, v63\n"
"v_pk_mad_u16 v63, v190, 2, -1 op_sel_hi:[1,0,0]\n"
"v_pk_min_u16 v62, v62, v63\n"
"v_pk_mad_u16 v63, v191, 2, -1 op_sel_hi:[1,0,0]\n"
"v_pk_min_u16 v62, v62, v63\n"
"v_pk_sub_u16 v62, s16, v62 clamp\n"
"v_or_b32 v61, v61, v62\n"
"v_cmp_ne_u32 vcc, 0, v61\n"
"s_cbranch_vccnz Lx_362\n"
"Lb_363:\n"
"ds_write_b32 v3, v60 offset:36868\n"
"ds_write_b64 v3, v[58:59] offset:37896\n"
"v_and_b32 v61, s8, v60\n"
"v_alignbit_b32 v62, v60, v60, 4\n"
"v_perm_b32 v24, v61, v58, s11\n"
"v_and_b32 v62, s8, v62\n"
"v_perm_b32 v25, v61, v58, s12\n"
"v_perm_b32 v26, v62, v59, s11\n"
"v_perm_b32 v27, v62, v59, s12\n"
"s_nop 1\n"
"v_mfma_f32_16x16x32_bf16 v[32:35], v[24:27], v[8:11], v[32:35]\n"
"s_add_u32 s52, s53, 0xe7000\n"
"s_and_b32 s52, s52, s54\n"
"buffer_load_dwordx4 v[188:191], v2, s[32:35], s52 offen offset:0\n"
"s_waitcnt vmcnt(35)\n"
"v_pk_sub_u16 v61, v192, s14 clamp\n"
"v_pk_sub_u16 v62, v193, s14 clamp\n"
"v_perm_b32 v58, v62, v61, s9\n"
"v_perm_b32 v61, v62, v61, s10\n"
"v_pk_sub_u16 v62, v194, s14 clamp\n"
"v_pk_sub_u16 v63, v195, s14 clamp\n"
"v_perm_b32 v59, v63, v62, s9\n"
"v_perm_b32 v62, v63, v62, s10\n"
"v_alignbit_b32 v62, v62, v62, 28\n"
"v_bfi_b32 v60, s8, v61, v62\n"
"v_bfi_b32 v61, s8, v62, v61\n"
"v_pk_mad_u16 v62, v192, 2, -1 op_sel_hi:[1,0,0]\n"
"v_pk_mad_u16 v63, v193, 2, -1 op_sel_hi:[1,0,0]\n"
"v_pk_min_u16 v62, v62, v63\n"
"v_pk_mad_u16 v63, v194, 2, -1 op_sel_hi:[1,0,0]\n"
"v_pk_min_u16 v62, v62, v63\n"
"v_pk_mad_u16 v63, v195, 2, -1 op_sel_hi:[1,0,0]\n"
"v_pk_min_u16 v62, v62, v63\n"
"v_pk_sub_u16 v62, s16, v62 clamp\n"
"v_or_b32 v61, v61, v62\n"
"v_cmp_ne_u32 vcc, 0, v61\n"
"s_cbranch_vccnz Lx_364\n"
"Lb_365:\n"
"ds_write_b32 v3, v60 offset:36872\n"
"ds_write_b64 v3, v[58:59] offset:38912\n"
"v_and_b32 v61, s8, v60\n"
"v_alignbit_b32 v62, v60, v60, 4\n"
"v_perm_b32 v24, v61, v58, s11\n"
"v_and_b32 v62, s8, v62\n"
"v_perm_b32 v25, v61, v58, s12\n"
"v_perm_b32 v26, v62, v59, s11\n"
"v_perm_b32 v27, v62, v59, s12\n"
"s_nop 1\n"
"v_mfma_f32_16x16x32_bf16 v[32:35], v[24:27], v[12:15], v[32:35]\n"
"s_add_u32 s52, s53, 0xee000\n"
"s_and_b32 s52, s52, s54\n"
"buffer_load_dwordx4 v[192:195], v2, s[32:35], s52 offen offset:0\n"
"s_waitcnt vmcnt(35)\n"
"v_pk_sub_u16 v61, v208, s14 clamp\n"
"v_pk_sub_u16 v62, v209, s14 clamp\n"
"v_perm_b32 v58, v62, v61, s9\n"
"v_perm_b32 v61, v62, v61, s10\n"
"v_pk_sub_u16 v62, v210, s14 clamp\n"
"v_pk_sub_u16 v63, v211, s14 clamp\n"
"v_perm_b32 v59, v63, v62, s9\n"
"v_perm_b32 v62, v63, v62, s10\n"
"v_alignbit_b32 v62, v62, v62, 28\n"
"v_bfi_b32 v60, s8, v61, v62\n"
"v_bfi_b32 v61, s8, v62, v61\n"
"v_pk_mad_u16 v62, v208, 2, -1 op_sel_hi:[1,0,0]\n"
"v_pk_mad_u16 v63, v209, 2, -1 op_sel_hi:[1,0,0]\n"
"v_pk_min_u16 v62, v62, v63\n"
"v_pk_mad_u16 v63, v210, 2, -1 op_sel_hi:[1,0,0]\n"
"v_pk_min_u16 v62, v62, v63\n"
"v_pk_mad_u16 v63, v211, 2, -1 op_sel_hi:[1,0,0]\n"
"v_pk_min_u16 v62, v62, v63\n"
"v_pk_sub_u16 v62, s16, v62 clamp\n"
"v_or_b32 v61, v61, v62\n"
"v_cmp_ne_u32 vcc, 0, v61\n"
"s_cbranch_vccnz Lx_366\n"
"Lb_367:\n"
"ds_write_b32 v3, v60 offset:36876\n"
"ds_write_b64 v3, v[58:59] offset:38920\n"
"v_and_b32 v61, s8, v60\n"
"v_alignbit_b32 v62, v60, v60, 4\n"
"v_perm_b32 v24, v61, v58, s11\n"
"v_and_b32 v62, s8, v62\n"
"v_perm_b32 v25, v61, v58, s12\n"
"v_perm_b32 v26, v62, v59, s11\n"
"v_perm_b32 v27, v62, v59, s12\n"
"s_nop 1\n"
"v_mfma_f32_16x16x32_bf16 v[32:35], v[24:27], v[16:19], v[32:35]\n"
"v_and_b32 v58, 1, v0\n"
"v_lshlrev_b32 v58, 4, v58\n"
"v_mov_b32 v59, 0xffff\n"
"v_lshlrev_b32 v58, v58, v59\n"
"v_bfe_u32 v59, v0, 1, 3\n"
"v_cmp_eq_u32_e64 s[72:73], 0, v59\n"
"v_cmp_eq_u32_e64 s[74:75], 1, v59\n"
"v_cmp_eq_u32_e64 s[76:77], 2, v59\n"
"v_cmp_eq_u32_e64 s[78:79], 3, v59\n"
"v_cndmask_b32_e64 v20, 0, v58, s[72:73]\n"
"v_cndmask_b32_e64 v21, 0, v58, s[74:75]\n"
"v_cndmask_b32_e64 v22, 0, v58, s[76:77]\n"
"v_cndmask_b32_e64 v23, 0, v58, s[78:79]\n"
"v_mov_b32_dpp v63, v36 quad_perm:[0,0,0,0] row_mask:0xf bank_mask:0xf\n"
"v_add_f32_dpp v63, v37, v63 quad_perm:[1,1,1,1] row_mask:0xf bank_mask:0xf\n"
"v_add_f32_dpp v63, v38, v63 quad_perm:[2,2,2,2] row_mask:0xf bank_mask:0xf\n"
"v_add_f32_dpp v63, v39, v63 quad_perm:[3,3,3,3] row_mask:0xf bank_mask:0xf\n"
"v_cndmask_b32_e64 v48, v48, v63, s[66:67]\n"
"v_mov_b32_dpp v63, v32 quad_perm:[0,0,0,0] row_mask:0xf bank_mask:0xf\n"
"v_add_f32_dpp v63, v33, v63 quad_perm:[1,1,1,1] row_mask:0xf bank_mask:0xf\n"
"v_add_f32_dpp v63, v34, v63 quad_perm:[2,2,2,2] row_mask:0xf bank_mask:0xf\n"
"v_add_f32_dpp v63, v35, v63 quad_perm:[3,3,3,3] row_mask:0xf bank_mask:0xf\n"
"v_cndmask_b32_e64 v52, v52, v63, s[64:65]\n"
"s_mov_b32 s20, s19\n"
"s_cmp_lg_u32 s21, 0\n"
"s_cbranch_scc1 LSLOW\n"
"s_call_b64 s[44:45], LKFIX\n"
"v_cvt_pk_bf16_f32 v58, v44, v45\n"
"v_lshlrev_b32 v44, 16, v58\n"
"v_and_b32 v45, 0xffff0000, v58\n"
"v_mul_f32 v44, 0x3db504f3, v44\n"
"v_mul_f32 v45, 0x3db504f3, v45\n"
"v_cvt_pk_bf16_f32 v58, v44, v45\n"
"v_lshlrev_b32 v44, 16, v58\n"
"v_and_b32 v45, 0xffff0000, v58\n"
"v_cvt_pk_bf16_f32 v58, v46, v47\n"
"v_lshlrev_b32 v46, 16, v58\n"
"v_and_b32 v47, 0xffff0000, v58\n"
"v_mul_f32 v46, 0x3db504f3, v46\n"
"v_mul_f32 v47, 0x3db504f3, v47\n"
"v_cvt_pk_bf16_f32 v58, v46, v47\n"
"v_lshlrev_b32 v46, 16, v58\n"
"v_and_b32 v47, 0xffff0000, v58\n"
"v_cvt_pk_bf16_f32 v58, v48, v49\n"
"v_lshlrev_b32 v48, 16, v58\n"
"v_and_b32 v49, 0xffff0000, v58\n"
"v_mul_f32 v48, 0x3db504f3, v48\n"
"v_mul_f32 v49, 0x3db504f3, v49\n"
"v_cvt_pk_bf16_f32 v58, v48, v49\n"
"v_lshlrev_b32 v48, 16, v58\n"
"v_and_b32 v49, 0xffff0000, v58\n"
"v_cvt_pk_bf16_f32 v58, v50, v51\n"
"v_lshlrev_b32 v50, 16, v58\n"
"v_and_b32 v51, 0xffff0000, v58\n"
"v_mul_f32 v50, 0x3db504f3, v50\n"
"v_mul_f32 v51, 0x3db504f3, v51\n"
"v_cvt_pk_bf16_f32 v58, v50, v51\n"
"v_lshlrev_b32 v50, 16, v58\n"
"v_and_b32 v51, 0xffff0000, v58\n"
"v_cvt_pk_bf16_f32 v58, v52, v52\n"
"v_lshlrev_b32 v52, 16, v58\n"
"v_mul_f32 v52, 0x3db504f3, v52\n"
"v_cvt_pk_bf16_f32 v58, v52, v52\n"
"v_lshlrev_b32 v52, 16, v58\n"
"v_max_f32 v58, v44, v45\n"
"v_max3_f32 v58, v58, v46, v47\n"
"v_max3_f32 v58, v58, v48, v49\n"
"v_max3_f32 v58, v58, v50, v51\n"
"v_max_f32 v58, v58, v52\n"
"s_nop 1\n"
"v_max_f32_dpp v58, v58, v58 quad_perm:[1,0,3,2] row_mask:0xf bank_mask:0xf\n"
"s_nop 1\n"
"v_max_f32_dpp v58, v58, v58 quad_perm:[2,3,0,1] row_mask:0xf bank_mask:0xf\n"
"s_nop 1\n"
"v_max_f32_dpp v58, v58, v58 row_half_mirror row_mask:0xf bank_mask:0xf\n"
"s_nop 1\n"
"v_max_f32_dpp v58, v58, v58 row_mirror row_mask:0xf bank_mask:0xf\n"
"s_nop 1\n"
"v_max_f32_dpp v58, v58, v58 row_bcast:15 row_mask:0xa bank_mask:0xf\n"
"s_nop 1\n"
"v_max_f32_dpp v58, v58, v58 row_bcast:31 row_mask:0xc bank_mask:0xf\n"
"v_mul_f32 v58, 0x3fb8aa3b, v58\n"
"s_nop 0\n"
"v_readlane_b32 s58, v58, 63\n"
"v_mov_b32 v59, 0x3fb8aa3b\n"
"s_nop 0\n"
"v_fma_f32 v44, v44, v59, -s58\n"
"v_fma_f32 v45, v45, v59, -s58\n"
"v_fma_f32 v46, v46, v59, -s58\n"
"v_fma_f32 v47, v47, v59, -s58\n"
"v_fma_f32 v48, v48, v59, -s58\n"
"v_fma_f32 v49, v49, v59, -s58\n"
"v_fma_f32 v50, v50, v59, -s58\n"
"v_fma_f32 v51, v51, v59, -s58\n"
"v_fma_f32 v52, v52, v59, -s58\n"
"v_exp_f32 v44, v44\n"
"v_exp_f32 v45, v45\n"
"v_exp_f32 v46, v46\n"
"v_exp_f32 v47, v47\n"
"v_exp_f32 v48, v48\n"
"v_exp_f32 v49, v49\n"
"v_exp_f32 v50, v50\n"
"v_exp_f32 v51, v51\n"
"v_exp_f32 v52, v52\n"
"v_add_f32 v58, v44, v45\n"
"v_add_f32 v58, v58, v46\n"
"v_add_f32 v58, v58, v47\n"
"v_add_f32 v58, v58, v48\n"
"v_add_f32 v58, v58, v49\n"
"v_add_f32 v58, v58, v50\n"
"v_add_f32 v58, v58, v51\n"
"v_add_f32 v58, v58, v52\n"
"v_cvt_pk_bf16_f32 v44, v44, v44\n"
"v_cvt_pk_bf16_f32 v45, v45, v45\n"
"v_cvt_pk_bf16_f32 v46, v46, v46\n"
"v_cvt_pk_bf16_f32 v47, v47, v47\n"
"v_cvt_pk_bf16_f32 v48, v48, v48\n"
"v_cvt_pk_bf16_f32 v49, v49, v49\n"
"v_cvt_pk_bf16_f32 v50, v50, v50\n"
"v_cvt_pk_bf16_f32 v51, v51, v51\n"
"v_cvt_pk_bf16_f32 v52, v52, v52\n"
"v_add_f32_dpp v58, v58, v58 quad_perm:[1,0,3,2] row_mask:0xf bank_mask:0xf\n"
"s_nop 1\n"
"v_add_f32_dpp v58, v58, v58 quad_perm:[2,3,0,1] row_mask:0xf bank_mask:0xf\n"
"s_nop 1\n"
"v_add_f32_dpp v58, v58, v58 row_half_mirror row_mask:0xf bank_mask:0xf\n"
"s_nop 1\n"
"v_add_f32_dpp v58, v58, v58 row_mirror row_mask:0xf bank_mask:0xf\n"
"s_nop 1\n"
"v_add_f32_dpp v58, v58, v58 row_bcast:15 row_mask:0xa bank_mask:0xf\n"
"s_nop 1\n"
"v_add_f32_dpp v58, v58, v58 row_bcast:31 row_mask:0xc bank_mask:0xf\n"
"v_mov_b32 v61, v58\n"
"v_mov_b32 v60, s58\n"
"s_mul_i32 s56, s50, 576\n"
"s_add_u32 s56, s56, 160256\n"
"v_mov_b32 v59, s56\n"
"s_mov_b32 exec_lo, 0\n"
"s_and_b32 exec_hi, exec_hi, 0x80000000\n"
"ds_write_b64 v59, v[60:61]\n"
"s_mov_b64 exec, -1\n"
"Lpace_368:\n"
"s_memrealtime s[96:97]\n"
"s_waitcnt lgkmcnt(0)\n"
"s_sub_u32 s96, s96, s98\n"
"s_cmp_ge_u32 s96, 2424\n"
"s_cbranch_scc1 Lpgo_369\n"
"s_sleep 2\n"
"s_branch Lpace_368\n"
"Lpgo_369:\n"
"s_add_u32 s52, s53, 0xf5000\n"
"s_and_b32 s52, s52, s54\n"
"buffer_load_dwordx4 v[4:7], v2, s[32:35], s52 offen offset:0\n"
"s_add_u32 s52, s53, 0xfc000\n"
"s_and_b32 s52, s52, s54\n"
"buffer_load_dwordx4 v[8:11], v2, s[32:35], s52 offen offset:0\n"
"s_add_u32 s52, s53, 0x103000\n"
"s_and_b32 s52, s52, s54\n"
"buffer_load_dwordx4 v[12:15], v2, s[32:35], s52 offen offset:0\n"
"s_add_u32 s52, s53, 0x10a000\n"
"s_and_b32 s52, s52, s54\n"
"buffer_load_dwordx4 v[16:19], v2, s[32:35], s52 offen offset:0\n"
"Lpace_370:\n"
"s_memrealtime s[96:97]\n"
"s_waitcnt lgkmcnt(0)\n"
"s_sub_u32 s96, s96, s98\n"
"s_cmp_ge_u32 s96, 2481\n"
"s_cbranch_scc1 Lpgo_371\n"
"s_sleep 2\n"
"s_branch Lpace_370\n"
"Lpgo_371:\n"
"s_add_u32 s52, s53, 0x111000\n"
"s_and_b32 s52, s52, s54\n"
"buffer_load_dwordx4 v[208:211], v2, s[32:35], s52 offen offset:0\n"
"v_and_b32_dpp v24, v44, v20 row_newbcast:0 row_mask:0xf bank_mask:0xf\n"
"v_and_b32_dpp v25, v44, v21 row_newbcast:0 row_mask:0xf bank_mask:0xf\n"
"v_and_b32_dpp v26, v44, v22 row_newbcast:0 row_mask:0xf bank_mask:0xf\n"
"v_and_b32_dpp v27, v44, v23 row_newbcast:0 row_mask:0xf bank_mask:0xf\n"
"s_waitcnt vmcnt(39)\n"
"s_nop 0\n"
"v_mfma_f32_16x16x32_bf16 v[40:43], v[24:27], a[216:219], 0\n"
"v_and_b32_dpp v24, v44, v20 row_newbcast:4 row_mask:0xf bank_mask:0xf\n"
"v_and_b32_dpp v25, v44, v21 row_newbcast:4 row_mask:0xf bank_mask:0xf\n"
"v_and_b32_dpp v26, v44, v22 row_newbcast:4 row_mask:0xf bank_mask:0xf\n"
"v_and_b32_dpp v27, v44, v23 row_newbcast:4 row_mask:0xf bank_mask:0xf\n"
"s_waitcnt vmcnt(38)\n"
"s_nop 0\n"
"v_mfma_f32_16x16x32_bf16 v[40:43], v[24:27], a[220:223], v[40:43]\n"
"v_and_b32_dpp v24, v44, v20 row_newbcast:8 row_mask:0xf bank_mask:0xf\n"
"v_and_b32_dpp v25, v44, v21 row_newbcast:8 row_mask:0xf bank_mask:0xf\n"
"v_and_b32_dpp v26, v44, v22 row_newbcast:8 row_mask:0xf bank_mask:0xf\n"
"v_and_b32_dpp v27, v44, v23 row_newbcast:8 row_mask:0xf bank_mask:0xf\n"
"s_waitcnt vmcnt(37)\n"
"s_nop 0\n"
"v_mfma_f32_16x16x32_bf16 v[40:43], v[24:27], a[224:227], v[40:43]\n"
"v_and_b32_dpp v24, v44, v20 row_newbcast:12 row_mask:0xf bank_mask:0xf\n"
"v_and_b32_dpp v25, v44, v21 row_newbcast:12 row_mask:0xf bank_mask:0xf\n"
"v_and_b32_dpp v26, v44, v22 row_newbcast:12 row_mask:0xf bank_mask:0xf\n"
"v_and_b32_dpp v27, v44, v23 row_newbcast:12 row_mask:0xf bank_mask:0xf\n"
"s_waitcnt vmcnt(36)\n"
"s_nop 0\n"
"v_mfma_f32_16x16x32_bf16 v[40:43], v[24:27], a[228:231], v[40:43]\n"
"v_and_b32_dpp v24, v44, v20 row_newbcast:1 row_mask:0xf bank_mask:0xf\n"
"v_and_b32_dpp v25, v44, v21 row_newbcast:1 row_mask:0xf bank_mask:0xf\n"
"v_and_b32_dpp v26, v44, v22 row_newbcast:1 row_mask:0xf bank_mask:0xf\n"
"v_and_b32_dpp v27, v44, v23 row_newbcast:1 row_mask:0xf bank_mask:0xf\n"
"s_waitcnt vmcnt(35)\n"
"s_nop 0\n"
"v_mfma_f32_16x16x32_bf16 v[40:43], v[24:27], a[232:235], v[40:43]\n"
"v_and_b32_dpp v24, v44, v20 row_newbcast:5 row_mask:0xf bank_mask:0xf\n"
"v_and_b32_dpp v25, v44, v21 row_newbcast:5 row_mask:0xf bank_mask:0xf\n"
"v_and_b32_dpp v26, v44, v22 row_newbcast:5 row_mask:0xf bank_mask:0xf\n"
"v_and_b32_dpp v27, v44, v23 row_newbcast:5 row_mask:0xf bank_mask:0xf\n"
"s_waitcnt vmcnt(34)\n"
"s_nop 0\n"
"v_mfma_f32_16x16x32_bf16 v[40:43], v[24:27], a[236:239], v[40:43]\n"
"s_add_u32 s52, s53, 0x118000\n"
"s_and_b32 s52, s52, s54\n"
"buffer_load_dwordx4 a[216:219], v2, s[32:35], s52 offen offset:0\n"
"v_and_b32_dpp v24, v44, v20 row_newbcast:9 row_mask:0xf bank_mask:0xf\n"
"v_and_b32_dpp v25, v44, v21 row_newbcast:9 row_mask:0xf bank_mask:0xf\n"
"v_and_b32_dpp v26, v44, v22 row_newbcast:9 row_mask:0xf bank_mask:0xf\n"
"v_and_b32_dpp v27, v44, v23 row_newbcast:9 row_mask:0xf bank_mask:0xf\n"
"s_waitcnt vmcnt(34)\n"
"s_nop 0\n"
"v_mfma_f32_16x16x32_bf16 v[40:43], v[24:27], a[240:243], v[40:43]\n"
"v_and_b32_dpp v24, v44, v20 row_newbcast:13 row_mask:0xf bank_mask:0xf\n"
"v_and_b32_dpp v25, v44, v21 row_newbcast:13 row_mask:0xf bank_mask:0xf\n"
"v_and_b32_dpp v26, v44, v22 row_newbcast:13 row_mask:0xf bank_mask:0xf\n"
"v_and_b32_dpp v27, v44, v23 row_newbcast:13 row_mask:0xf bank_mask:0xf\n"
"s_waitcnt vmcnt(33)\n"
"s_nop 0\n"
"v_mfma_f32_16x16x32_bf16 v[40:43], v[24:27], a[244:247], v[40:43]\n"
"v_and_b32_dpp v24, v44, v20 row_newbcast:2 row_mask:0xf bank_mask:0xf\n"
"v_and_b32_dpp v25, v44, v21 row_newbcast:2 row_mask:0xf bank_mask:0xf\n"
"v_and_b32_dpp v26, v44, v22 row_newbcast:2 row_mask:0xf bank_mask:0xf\n"
"v_and_b32_dpp v27, v44, v23 row_newbcast:2 row_mask:0xf bank_mask:0xf\n"
"s_waitcnt vmcnt(32)\n"
"s_nop 0\n"
"v_mfma_f32_16x16x32_bf16 v[40:43], v[24:27], a[248:251], v[40:43]\n"
"v_and_b32_dpp v24, v44, v20 row_newbcast:6 row_mask:0xf bank_mask:0xf\n"
"v_and_b32_dpp v25, v44, v21 row_newbcast:6 row_mask:0xf bank_mask:0xf\n"
"v_and_b32_dpp v26, v44, v22 row_newbcast:6 row_mask:0xf bank_mask:0xf\n"
"v_and_b32_dpp v27, v44, v23 row_newbcast:6 row_mask:0xf bank_mask:0xf\n"
"s_waitcnt vmcnt(31)\n"
"s_nop 0\n"
"v_mfma_f32_16x16x32_bf16 v[40:43], v[24:27], a[252:255], v[40:43]\n"
"v_and_b32_dpp v24, v44, v20 row_newbcast:10 row_mask:0xf bank_mask:0xf\n"
"v_and_b32_dpp v25, v44, v21 row_newbcast:10 row_mask:0xf bank_mask:0xf\n"
"v_and_b32_dpp v26, v44, v22 row_newbcast:10 row_mask:0xf bank_mask:0xf\n"
"v_and_b32_dpp v27, v44, v23 row_newbcast:10 row_mask:0xf bank_mask:0xf\n"
"s_waitcnt vmcnt(30)\n"
"s_nop 0\n"
"v_mfma_f32_16x16x32_bf16 v[40:43], v[24:27], v[212:215], v[40:43]\n"
"v_and_b32_dpp v24, v44, v20 row_newbcast:14 row_mask:0xf bank_mask:0xf\n"
"v_and_b32_dpp v25, v44, v21 row_newbcast:14 row_mask:0xf bank_mask:0xf\n"
"v_and_b32_dpp v26, v44, v22 row_newbcast:14 row_mask:0xf bank_mask:0xf\n"
"v_and_b32_dpp v27, v44, v23 row_newbcast:14 row_mask:0xf bank_mask:0xf\n"
"s_waitcnt vmcnt(29)\n"
"s_nop 0\n"
"v_mfma_f32_16x16x32_bf16 v[40:43], v[24:27], v[216:219], v[40:43]\n"
"v_and_b32_dpp v24, v44, v20 row_newbcast:3 row_mask:0xf bank_mask:0xf\n"
"v_and_b32_dpp v25, v44, v21 row_newbcast:3 row_mask:0xf bank_mask:0xf\n"
"v_and_b32_dpp v26, v44, v22 row_newbcast:3 row_mask:0xf bank_mask:0xf\n"
"v_and_b32_dpp v27, v44, v23 row_newbcast:3 row_mask:0xf bank_mask:0xf\n"
"s_waitcnt vmcnt(28)\n"
"s_nop 0\n"
"v_mfma_f32_16x16x32_bf16 v[40:43], v[24:27], v[232:235], v[40:43]\n"
"v_and_b32_dpp v24, v44, v20 row_newbcast:7 row_mask:0xf bank_mask:0xf\n"
"v_and_b32_dpp v25, v44, v21 row_newbcast:7 row_mask:0xf bank_mask:0xf\n"
"v_and_b32_dpp v26, v44, v22 row_newbcast:7 row_mask:0xf bank_mask:0xf\n"
"v_and_b32_dpp v27, v44, v23 row_newbcast:7 row_mask:0xf bank_mask:0xf\n"
"s_waitcnt vmcnt(27)\n"
"s_nop 0\n"
"v_mfma_f32_16x16x32_bf16 v[40:43], v[24:27], v[236:239], v[40:43]\n"
"s_add_u32 s52, s53, 0x150000\n"
"s_and_b32 s52, s52, s54\n"
"buffer_load_dwordx4 a[220:223], v2, s[32:35], s52 offen offset:0\n"
"v_and_b32_dpp v24, v44, v20 row_newbcast:11 row_mask:0xf bank_mask:0xf\n"
"v_and_b32_dpp v25, v44, v21 row_newbcast:11 row_mask:0xf bank_mask:0xf\n"
"v_and_b32_dpp v26, v44, v22 row_newbcast:11 row_mask:0xf bank_mask:0xf\n"
"v_and_b32_dpp v27, v44, v23 row_newbcast:11 row_mask:0xf bank_mask:0xf\n"
"s_waitcnt vmcnt(27)\n"
"s_nop 0\n"
"v_mfma_f32_16x16x32_bf16 v[40:43], v[24:27], v[240:243], v[40:43]\n"
"v_and_b32_dpp v24, v44, v20 row_newbcast:15 row_mask:0xf bank_mask:0xf\n"
"v_and_b32_dpp v25, v44, v21 row_newbcast:15 row_mask:0xf bank_mask:0xf\n"
"v_and_b32_dpp v26, v44, v22 row_newbcast:15 row_mask:0xf bank_mask:0xf\n"
"v_and_b32_dpp v27, v44, v23 row_newbcast:15 row_mask:0xf bank_mask:0xf\n"
"s_waitcnt vmcnt(26)\n"
"s_nop 0\n"
"v_mfma_f32_16x16x32_bf16 v[40:43], v[24:27], v[172:175], v[40:43]\n"
"v_and_b32_dpp v24, v45, v20 row_newbcast:0 row_mask:0xf bank_mask:0xf\n"
"v_and_b32_dpp v25, v45, v21 row_newbcast:0 row_mask:0xf bank_mask:0xf\n"
"v_and_b32_dpp v26, v45, v22 row_newbcast:0 row_mask:0xf bank_mask:0xf\n"
"v_and_b32_dpp v27, v45, v23 row_newbcast:0 row_mask:0xf bank_mask:0xf\n"
"s_waitcnt vmcnt(25)\n"
"s_nop 0\n"
"v_mfma_f32_16x16x32_bf16 v[40:43], v[24:27], v[176:179], v[40:43]\n"
"v_and_b32_dpp v24, v45, v20 row_newbcast:4 row_mask:0xf bank_mask:0xf\n"
"v_and_b32_dpp v25, v45, v21 row_newbcast:4 row_mask:0xf bank_mask:0xf\n"
"v_and_b32_dpp v26, v45, v22 row_newbcast:4 row_mask:0xf bank_mask:0xf\n"
"v_and_b32_dpp v27, v45, v23 row_newbcast:4 row_mask:0xf bank_mask:0xf\n"
"s_waitcnt vmcnt(24)\n"
"s_nop 0\n"
"v_mfma_f32_16x16x32_bf16 v[40:43], v[24:27], v[112:115], v[40:43]\n"
"v_and_b32_dpp v24, v45, v20 row_newbcast:8 row_mask:0xf bank_mask:0xf\n"
"v_and_b32_dpp v25, v45, v21 row_newbcast:8 row_mask:0xf bank_mask:0xf\n"
"v_and_b32_dpp v26, v45, v22 row_newbcast:8 row_mask:0xf bank_mask:0xf\n"
"v_and_b32_dpp v27, v45, v23 row_newbcast:8 row_mask:0xf bank_mask:0xf\n"
"s_waitcnt vmcnt(23)\n"
"s_nop 0\n"
"v_mfma_f32_16x16x32_bf16 v[40:43], v[24:27], v[116:119], v[40:43]\n"
"s_add_u32 s52, s53, 0x157000\n"
"s_and_b32 s52, s52, s54\n"
"buffer_load_dwordx4 v[112:115], v2, s[32:35], s52 offen offset:0\n"
"v_and_b32_dpp v24, v45, v20 row_newbcast:12 row_mask:0xf bank_mask:0xf\n"
"v_and_b32_dpp v25, v45, v21 row_newbcast:12 row_mask:0xf bank_mask:0xf\n"
"v_and_b32_dpp v26, v45, v22 row_newbcast:12 row_mask:0xf bank_mask:0xf\n"
"v_and_b32_dpp v27, v45, v23 row_newbcast:12 row_mask:0xf bank_mask:0xf\n"
"s_waitcnt vmcnt(23)\n"
"s_nop 0\n"
"v_mfma_f32_16x16x32_bf16 v[40:43], v[24:27], v[120:123], v[40:43]\n"
"Lpace_372:\n"
"s_memrealtime s[96:97]\n"
"s_waitcnt lgkmcnt(0)\n"
"s_sub_u32 s96, s96, s98\n"
"s_cmp_ge_u32 s96, 2537\n"
"s_cbranch_scc1 Lpgo_373\n"
"s_sleep 2\n"
"s_branch Lpace_372\n"
"Lpgo_373:\n"
"s_add_u32 s52, s53, 0x15e000\n"
"s_and_b32 s52, s52, s54\n"
"buffer_load_dwordx4 v[116:119], v2, s[32:35], s52 offen offset:0\n"
"v_and_b32_dpp v24, v45, v20 row_newbcast:1 row_mask:0xf bank_mask:0xf\n"
"v_and_b32_dpp v25, v45, v21 row_newbcast:1 row_mask:0xf bank_mask:0xf\n"
"v_and_b32_dpp v26, v45, v22 row_newbcast:1 row_mask:0xf bank_mask:0xf\n"
"v_and_b32_dpp v27, v45, v23 row_newbcast:1 row_mask:0xf bank_mask:0xf\n"
"s_waitcnt vmcnt(23)\n"
"s_nop 0\n"
"v_mfma_f32_16x16x32_bf16 v[40:43], v[24:27], v[136:139], v[40:43]\n"
"s_add_u32 s52, s53, 0x165000\n"
"s_and_b32 s52, s52, s54\n"
"buffer_load_dwordx4 v[120:123], v2, s[32:35], s52 offen offset:0\n"
"v_and_b32_dpp v24, v45, v20 row_newbcast:5 row_mask:0xf bank_mask:0xf\n"
"v_and_b32_dpp v25, v45, v21 row_newbcast:5 row_mask:0xf bank_mask:0xf\n"
"v_and_b32_dpp v26, v45, v22 row_newbcast:5 row_mask:0xf bank_mask:0xf\n"
"v_and_b32_dpp v27, v45, v23 row_newbcast:5 row_mask:0xf bank_mask:0xf\n"
"s_waitcnt vmcnt(23)\n"
"s_nop 0\n"
"v_mfma_f32_16x16x32_bf16 v[40:43], v[24:27], v[140:143], v[40:43]\n"
"s_add_u32 s52, s53, 0x16c000\n"
"s_and_b32 s52, s52, s54\n"
"buffer_load_dwordx4 v[136:139], v2, s[32:35], s52 offen offset:0\n"
"v_and_b32_dpp v24, v45, v20 row_newbcast:9 row_mask:0xf bank_mask:0xf\n"
"v_and_b32_dpp v25, v45, v21 row_newbcast:9 row_mask:0xf bank_mask:0xf\n"
"v_and_b32_dpp v26, v45, v22 row_newbcast:9 row_mask:0xf bank_mask:0xf\n"
"v_and_b32_dpp v27, v45, v23 row_newbcast:9 row_mask:0xf bank_mask:0xf\n"
"s_waitcnt vmcnt(23)\n"
"s_nop 0\n"
"v_mfma_f32_16x16x32_bf16 v[40:43], v[24:27], v[144:147], v[40:43]\n"
"s_add_u32 s52, s53, 0x173000\n"
"s_and_b32 s52, s52, s54\n"
"buffer_load_dwordx4 v[140:143], v2, s[32:35], s52 offen offset:0\n"
"v_and_b32_dpp v24, v45, v20 row_newbcast:13 row_mask:0xf bank_mask:0xf\n"
"v_and_b32_dpp v25, v45, v21 row_newbcast:13 row_mask:0xf bank_mask:0xf\n"
"v_and_b32_dpp v26, v45, v22 row_newbcast:13 row_mask:0xf bank_mask:0xf\n"
"v_and_b32_dpp v27, v45, v23 row_newbcast:13 row_mask:0xf bank_mask:0xf\n"
"s_waitcnt vmcnt(23)\n"
"s_nop 0\n"
"v_mfma_f32_16x16x32_bf16 v[40:43], v[24:27], v[124:127], v[40:43]\n"
"Lpace_374:\n"
"s_memrealtime s[96:97]\n"
"s_waitcnt lgkmcnt(0)\n"
"s_sub_u32 s96, s96, s98\n"
"s_cmp_ge_u32 s96, 2594\n"
"s_cbranch_scc1 Lpgo_375\n"
"s_sleep 2\n"
"s_branch Lpace_374\n"
"Lpgo_375:\n"
"s_add_u32 s52, s53, 0x17a000\n"
"s_and_b32 s52, s52, s54\n"
"buffer_load_dwordx4 v[144:147], v2, s[32:35], s52 offen offset:0\n"
"v_and_b32_dpp v24, v45, v20 row_newbcast:2 row_mask:0xf bank_mask:0xf\n"
"v_and_b32_dpp v25, v45, v21 row_newbcast:2 row_mask:0xf bank_mask:0xf\n"
"v_and_b32_dpp v26, v45, v22 row_newbcast:2 row_mask:0xf bank_mask:0xf\n"
"v_and_b32_dpp v27, v45, v23 row_newbcast:2 row_mask:0xf bank_mask:0xf\n"
"s_waitcnt vmcnt(23)\n"
"s_nop 0\n"
"v_mfma_f32_16x16x32_bf16 v[40:43], v[24:27], v[128:131], v[40:43]\n"
"s_add_u32 s52, s53, 0x181000\n"
"s_and_b32 s52, s52, s54\n"
"buffer_load_dwordx4 v[124:127], v2, s[32:35], s52 offen offset:0\n"
"v_and_b32_dpp v24, v45, v20 row_newbcast:6 row_mask:0xf bank_mask:0xf\n"
"v_and_b32_dpp v25, v45, v21 row_newbcast:6 row_mask:0xf bank_mask:0xf\n"
"v_and_b32_dpp v26, v45, v22 row_newbcast:6 row_mask:0xf bank_mask:0xf\n"
"v_and_b32_dpp v27, v45, v23 row_newbcast:6 row_mask:0xf bank_mask:0xf\n"
"s_waitcnt vmcnt(23)\n"
"s_nop 0\n"
"v_mfma_f32_16x16x32_bf16 v[40:43], v[24:27], v[132:135], v[40:43]\n"
"s_add_u32 s52, s53, 0x188000\n"
"s_and_b32 s52, s52, s54\n"
"buffer_load_dwordx4 v[128:131], v2, s[32:35], s52 offen offset:0\n"
"v_and_b32_dpp v24, v45, v20 row_newbcast:10 row_mask:0xf bank_mask:0xf\n"
"v_and_b32_dpp v25, v45, v21 row_newbcast:10 row_mask:0xf bank_mask:0xf\n"
"v_and_b32_dpp v26, v45, v22 row_newbcast:10 row_mask:0xf bank_mask:0xf\n"
"v_and_b32_dpp v27, v45, v23 row_newbcast:10 row_mask:0xf bank_mask:0xf\n"
"s_waitcnt vmcnt(23)\n"
"s_nop 0\n"
"v_mfma_f32_16x16x32_bf16 v[40:43], v[24:27], v[148:151], v[40:43]\n"
"s_add_u32 s52, s53, 0x1f8000\n"
"s_and_b32 s52, s52, s54\n"
"buffer_load_dwordx4 v[132:135], v2, s[32:35], s52 offen offset:0 nt\n"
"v_and_b32_dpp v24, v45, v20 row_newbcast:14 row_mask:0xf bank_mask:0xf\n"
"v_and_b32_dpp v25, v45, v21 row_newbcast:14 row_mask:0xf bank_mask:0xf\n"
"v_and_b32_dpp v26, v45, v22 row_newbcast:14 row_mask:0xf bank_mask:0xf\n"
"v_and_b32_dpp v27, v45, v23 row_newbcast:14 row_mask:0xf bank_mask:0xf\n"
"s_waitcnt vmcnt(23)\n"
"s_nop 0\n"
"v_mfma_f32_16x16x32_bf16 v[40:43], v[24:27], v[152:155], v[40:43]\n"
"Lpace_376:\n"
"s_memrealtime s[96:97]\n"
"s_waitcnt lgkmcnt(0)\n"
"s_sub_u32 s96, s96, s98\n"
"s_cmp_ge_u32 s96, 2650\n"
"s_cbranch_scc1 Lpgo_377\n"
"s_sleep 2\n"
"s_branch Lpace_376\n"
"Lpgo_377:\n"
"s_add_u32 s52, s53, 0x1ff000\n"
"s_and_b32 s52, s52, s54\n"
"buffer_load_dwordx4 v[148:151], v2, s[32:35], s52 offen offset:0 nt\n"
"v_and_b32_dpp v24, v45, v20 row_newbcast:3 row_mask:0xf bank_mask:0xf\n"
"v_and_b32_dpp v25, v45, v21 row_newbcast:3 row_mask:0xf bank_mask:0xf\n"
"v_and_b32_dpp v26, v45, v22 row_newbcast:3 row_mask:0xf bank_mask:0xf\n"
"v_and_b32_dpp v27, v45, v23 row_newbcast:3 row_mask:0xf bank_mask:0xf\n"
"s_waitcnt vmcnt(23)\n"
"s_nop 0\n"
"v_mfma_f32_16x16x32_bf16 v[40:43], v[24:27], v[156:159], v[40:43]\n"
"s_add_u32 s52, s53, 0x206000\n"
"s_and_b32 s52, s52, s54\n"
"buffer_load_dwordx4 v[152:155], v2, s[32:35], s52 offen offset:0 nt\n"
"v_and_b32_dpp v24, v45, v20 row_newbcast:7 row_mask:0xf bank_mask:0xf\n"
"v_and_b32_dpp v25, v45, v21 row_newbcast:7 row_mask:0xf bank_mask:0xf\n"
"v_and_b32_dpp v26, v45, v22 row_newbcast:7 row_mask:0xf bank_mask:0xf\n"
"v_and_b32_dpp v27, v45, v23 row_newbcast:7 row_mask:0xf bank_mask:0xf\n"
"s_waitcnt vmcnt(23)\n"
"s_nop 0\n"
"v_mfma_f32_16x16x32_bf16 v[40:43], v[24:27], v[160:163], v[40:43]\n"
"s_add_u32 s52, s53, 0x20d000\n"
"s_and_b32 s52, s52, s54\n"
"buffer_load_dwordx4 v[156:159], v2, s[32:35], s52 offen offset:0 nt\n"
"v_and_b32_dpp v24, v45, v20 row_newbcast:11 row_mask:0xf bank_mask:0xf\n"
"v_and_b32_dpp v25, v45, v21 row_newbcast:11 row_mask:0xf bank_mask:0xf\n"
"v_and_b32_dpp v26, v45, v22 row_newbcast:11 row_mask:0xf bank_mask:0xf\n"
"v_and_b32_dpp v27, v45, v23 row_newbcast:11 row_mask:0xf bank_mask:0xf\n"
"s_waitcnt vmcnt(23)\n"
"s_nop 0\n"
"v_mfma_f32_16x16x32_bf16 v[40:43], v[24:27], v[164:167], v[40:43]\n"
"s_add_u32 s52, s53, 0x214000\n"
"s_and_b32 s52, s52, s54\n"
"buffer_load_dwordx4 v[160:163], v2, s[32:35], s52 offen offset:0 nt\n"
"v_and_b32_dpp v24, v45, v20 row_newbcast:15 row_mask:0xf bank_mask:0xf\n"
"v_and_b32_dpp v25, v45, v21 row_newbcast:15 row_mask:0xf bank_mask:0xf\n"
"v_and_b32_dpp v26, v45, v22 row_newbcast:15 row_mask:0xf bank_mask:0xf\n"
"v_and_b32_dpp v27, v45, v23 row_newbcast:15 row_mask:0xf bank_mask:0xf\n"
"s_waitcnt vmcnt(23)\n"
"s_nop 0\n"
"v_mfma_f32_16x16x32_bf16 v[40:43], v[24:27], v[168:171], v[40:43]\n"
"Lpace_378:\n"
"s_memrealtime s[96:97]\n"
"s_waitcnt lgkmcnt(0)\n"
"s_sub_u32 s96, s96, s98\n"
"s_cmp_ge_u32 s96, 2704\n"
"s_cbranch_scc1 Lpgo_379\n"
"s_sleep 2\n"
"s_branch Lpace_378\n"
"Lpgo_379:\n"
"s_add_u32 s52, s53, 0x21b000\n"
"s_and_b32 s52, s52, s54\n"
"buffer_load_dwordx4 v[164:167], v2, s[32:35], s52 offen offset:0 nt\n"
"v_and_b32_dpp v24, v46, v20 row_newbcast:0 row_mask:0xf bank_mask:0xf\n"
"v_and_b32_dpp v25, v46, v21 row_newbcast:0 row_mask:0xf bank_mask:0xf\n"
"v_and_b32_dpp v26, v46, v22 row_newbcast:0 row_mask:0xf bank_mask:0xf\n"
"v_and_b32_dpp v27, v46, v23 row_newbcast:0 row_mask:0xf bank_mask:0xf\n"
"s_waitcnt vmcnt(23)\n"
"s_nop 0\n"
"v_mfma_f32_16x16x32_bf16 v[40:43], v[24:27], v[184:187], v[40:43]\n"
"s_add_u32 s52, s53, 0x222000\n"
"s_and_b32 s52, s52, s54\n"
"buffer_load_dwordx4 v[168:171], v2, s[32:35], s52 offen offset:0 nt\n"
"v_and_b32_dpp v24, v46, v20 row_newbcast:4 row_mask:0xf bank_mask:0xf\n"
"v_and_b32_dpp v25, v46, v21 row_newbcast:4 row_mask:0xf bank_mask:0xf\n"
"v_and_b32_dpp v26, v46, v22 row_newbcast:4 row_mask:0xf bank_mask:0xf\n"
"v_and_b32_dpp v27, v46, v23 row_newbcast:4 row_mask:0xf bank_mask:0xf\n"
"s_waitcnt vmcnt(23)\n"
"s_nop 0\n"
"v_mfma_f32_16x16x32_bf16 v[40:43], v[24:27], v[188:191], v[40:43]\n"
"s_add_u32 s52, s53, 0x229000\n"
"s_and_b32 s52, s52, s54\n"
"buffer_load_dwordx4 v[184:187], v2, s[32:35], s52 offen offset:0 nt\n"
"v_and_b32_dpp v24, v46, v20 row_newbcast:8 row_mask:0xf bank_mask:0xf\n"
"v_and_b32_dpp v25, v46, v21 row_newbcast:8 row_mask:0xf bank_mask:0xf\n"
"v_and_b32_dpp v26, v46, v22 row_newbcast:8 row_mask:0xf bank_mask:0xf\n"
"v_and_b32_dpp v27, v46, v23 row_newbcast:8 row_mask:0xf bank_mask:0xf\n"
"s_waitcnt vmcnt(23)\n"
"s_nop 0\n"
"v_mfma_f32_16x16x32_bf16 v[40:43], v[24:27], v[192:195], v[40:43]\n"
"s_add_u32 s52, s53, 0x268000\n"
"s_and_b32 s52, s52, s54\n"
"buffer_load_dwordx4 v[188:191], v2, s[32:35], s52 offen offset:0 nt\n"
"v_and_b32_dpp v24, v46, v20 row_newbcast:12 row_mask:0xf bank_mask:0xf\n"
"v_and_b32_dpp v25, v46, v21 row_newbcast:12 row_mask:0xf bank_mask:0xf\n"
"v_and_b32_dpp v26, v46, v22 row_newbcast:12 row_mask:0xf bank_mask:0xf\n"
"v_and_b32_dpp v27, v46, v23 row_newbcast:12 row_mask:0xf bank_mask:0xf\n"
"s_waitcnt vmcnt(23)\n"
"s_nop 0\n"
"v_mfma_f32_16x16x32_bf16 v[40:43], v[24:27], v[4:7], v[40:43]\n"
"Lpace_380:\n"
"s_memrealtime s[96:97]\n"
"s_waitcnt lgkmcnt(0)\n"
"s_sub_u32 s96, s96, s98\n"
"s_cmp_ge_u32 s96, 2759\n"
"s_cbranch_scc1 Lpgo_381\n"
"s_sleep 2\n"
"s_branch Lpace_380\n"
"Lpgo_381:\n"
"s_add_u32 s52, s53, 0x26f000\n"
"s_and_b32 s52, s52, s54\n"
"buffer_load_dwordx4 v[192:195], v2, s[32:35], s52 offen offset:0 nt\n"
"v_and_b32_dpp v24, v46, v20 row_newbcast:1 row_mask:0xf bank_mask:0xf\n"
"v_and_b32_dpp v25, v46, v21 row_newbcast:1 row_mask:0xf bank_mask:0xf\n"
"v_and_b32_dpp v26, v46, v22 row_newbcast:1 row_mask:0xf bank_mask:0xf\n"
"v_and_b32_dpp v27, v46, v23 row_newbcast:1 row_mask:0xf bank_mask:0xf\n"
"s_waitcnt vmcnt(23)\n"
"s_nop 0\n"
"v_mfma_f32_16x16x32_bf16 v[40:43], v[24:27], v[8:11], v[40:43]\n"
"s_add_u32 s52, s53, 0x276000\n"
"s_and_b32 s52, s52, s54\n"
"buffer_load_dwordx4 v[212:215], v2, s[32:35], s52 offen offset:0 nt\n"
"v_and_b32_dpp v24, v46, v20 row_newbcast:5 row_mask:0xf bank_mask:0xf\n"
"v_and_b32_dpp v25, v46, v21 row_newbcast:5 row_mask:0xf bank_mask:0xf\n"
"v_and_b32_dpp v26, v46, v22 row_newbcast:5 row_mask:0xf bank_mask:0xf\n"
"v_and_b32_dpp v27, v46, v23 row_newbcast:5 row_mask:0xf bank_mask:0xf\n"
"s_waitcnt vmcnt(23)\n"
"s_nop 0\n"
"v_mfma_f32_16x16x32_bf16 v[40:43], v[24:27], v[12:15], v[40:43]\n"
"s_add_u32 s52, s53, 0x27d000\n"
"s_and_b32 s52, s52, s54\n"
"buffer_load_dwordx4 v[216:219], v2, s[32:35], s52 offen offset:0 nt\n"
"v_and_b32_dpp v24, v46, v20 row_newbcast:9 row_mask:0xf bank_mask:0xf\n"
"v_and_b32_dpp v25, v46, v21 row_newbcast:9 row_mask:0xf bank_mask:0xf\n"
"v_and_b32_dpp v26, v46, v22 row_newbcast:9 row_mask:0xf bank_mask:0xf\n"
"v_and_b32_dpp v27, v46, v23 row_newbcast:9 row_mask:0xf bank_mask:0xf\n"
"s_waitcnt vmcnt(23)\n"
"s_nop 0\n"
"v_mfma_f32_16x16x32_bf16 v[40:43], v[24:27], v[16:19], v[40:43]\n"
"s_add_u32 s52, s53, 0x284000\n"
"s_and_b32 s52, s52, s54\n"
"buffer_load_dwordx4 v[232:235], v2, s[32:35], s52 offen offset:0 nt\n"
"v_and_b32_dpp v24, v46, v20 row_newbcast:13 row_mask:0xf bank_mask:0xf\n"
"v_and_b32_dpp v25, v46, v21 row_newbcast:13 row_mask:0xf bank_mask:0xf\n"
"v_and_b32_dpp v26, v46, v22 row_newbcast:13 row_mask:0xf bank_mask:0xf\n"
"v_and_b32_dpp v27, v46, v23 row_newbcast:13 row_mask:0xf bank_mask:0xf\n"
"s_waitcnt vmcnt(23)\n"
"s_nop 0\n"
"v_mfma_f32_16x16x32_bf16 v[40:43], v[24:27], v[208:211], v[40:43]\n"
"Lpace_382:\n"
"s_memrealtime s[96:97]\n"
"s_waitcnt lgkmcnt(0)\n"
"s_sub_u32 s96, s96, s98\n"
"s_cmp_ge_u32 s96, 2813\n"
"s_cbranch_scc1 Lpgo_383\n"
"s_sleep 2\n"
"s_branch Lpace_382\n"
"Lpgo_383:\n"
"s_add_u32 s52, s53, 0x28b000\n"
"s_and_b32 s52, s52, s54\n"
"buffer_load_dwordx4 v[208:211], v2, s[32:35], s52 offen offset:0 nt\n"
"v_and_b32_dpp v24, v46, v20 row_newbcast:2 row_mask:0xf bank_mask:0xf\n"
"v_and_b32_dpp v25, v46, v21 row_newbcast:2 row_mask:0xf bank_mask:0xf\n"
"v_and_b32_dpp v26, v46, v22 row_newbcast:2 row_mask:0xf bank_mask:0xf\n"
"v_and_b32_dpp v27, v46, v23 row_newbcast:2 row_mask:0xf bank_mask:0xf\n"
"s_waitcnt vmcnt(23)\n"
"s_nop 0\n"
"v_mfma_f32_16x16x32_bf16 v[40:43], v[24:27], a[216:219], v[40:43]\n"
"s_add_u32 s52, s53, 0x292000\n"
"s_and_b32 s52, s52, s54\n"
"buffer_load_dwordx4 v[236:239], v2, s[32:35], s52 offen offset:0 nt\n"
"v_and_b32_dpp v24, v47, v20 row_newbcast:0 row_mask:0xf bank_mask:0xf\n"
"v_and_b32_dpp v25, v47, v21 row_newbcast:0 row_mask:0xf bank_mask:0xf\n"
"v_and_b32_dpp v26, v47, v22 row_newbcast:0 row_mask:0xf bank_mask:0xf\n"
"v_and_b32_dpp v27, v47, v23 row_newbcast:0 row_mask:0xf bank_mask:0xf\n"
"s_waitcnt vmcnt(23)\n"
"s_nop 0\n"
"v_mfma_f32_16x16x32_bf16 v[40:43], v[24:27], a[220:223], v[40:43]\n"
"s_add_u32 s52, s53, 0x299000\n"
"s_and_b32 s52, s52, s54\n"
"buffer_load_dwordx4 v[240:243], v2, s[32:35], s52 offen offset:0 nt\n"
"v_and_b32_dpp v24, v47, v20 row_newbcast:4 row_mask:0xf bank_mask:0xf\n"
"v_and_b32_dpp v25, v47, v21 row_newbcast:4 row_mask:0xf bank_mask:0xf\n"
"v_and_b32_dpp v26, v47, v22 row_newbcast:4 row_mask:0xf bank_mask:0xf\n"
"v_and_b32_dpp v27, v47, v23 row_newbcast:4 row_mask:0xf bank_mask:0xf\n"
"s_waitcnt vmcnt(23)\n"
"s_nop 0\n"
"v_mfma_f32_16x16x32_bf16 v[40:43], v[24:27], v[112:115], v[40:43]\n"
"v_pk_lshlrev_b16 v63, 1, v112 op_sel_hi:[0,1]\n"
"v_pk_lshlrev_b16 v62, 1, v113 op_sel_hi:[0,1]\n"
"v_pk_max_u16 v63, v63, v62\n"
"v_pk_lshlrev_b16 v62, 1, v114 op_sel_hi:[0,1]\n"
"v_pk_max_u16 v63, v63, v62\n"
"v_pk_lshlrev_b16 v62, 1, v115 op_sel_hi:[0,1]\n"
"v_pk_max_u16 v63, v63, v62\n"
"s_add_u32 s52, s53, 0x2a0000\n"
"s_and_b32 s52, s52, s54\n"
"buffer_load_dwordx4 v[172:175], v2, s[32:35], s52 offen offset:0 nt\n"
"v_and_b32_dpp v24, v47, v20 row_newbcast:8 row_mask:0xf bank_mask:0xf\n"
"v_and_b32_dpp v25, v47, v21 row_newbcast:8 row_mask:0xf bank_mask:0xf\n"
"v_and_b32_dpp v26, v47, v22 row_newbcast:8 row_mask:0xf bank_mask:0xf\n"
"v_and_b32_dpp v27, v47, v23 row_newbcast:8 row_mask:0xf bank_mask:0xf\n"
"s_waitcnt vmcnt(23)\n"
"s_nop 0\n"
"v_mfma_f32_16x16x32_bf16 v[40:43], v[24:27], v[116:119], v[40:43]\n"
"v_pk_lshlrev_b16 v62, 1, v116 op_sel_hi:[0,1]\n"
"v_pk_max_u16 v63, v63, v62\n"
"v_pk_lshlrev_b16 v62, 1, v117 op_sel_hi:[0,1]\n"
"v_pk_max_u16 v63, v63, v62\n"
"v_pk_lshlrev_b16 v62, 1, v118 op_sel_hi:[0,1]\n"
"v_pk_max_u16 v63, v63, v62\n"
"v_pk_lshlrev_b16 v62, 1, v119 op_sel_hi:[0,1]\n"
"v_pk_max_u16 v63, v63, v62\n"
"Lpace_384:\n"
"s_memrealtime s[96:97]\n"
"s_waitcnt lgkmcnt(0)\n"
"s_sub_u32 s96, s96, s98\n"
"s_cmp_ge_u32 s96, 2867\n"
"s_cbranch_scc1 Lpgo_385\n"
"s_sleep 2\n"
"s_branch Lpace_384\n"
"Lpgo_385:\n"
"s_add_u32 s52, s53, 0x2a7000\n"
"s_and_b32 s52, s52, s54\n"
"buffer_load_dwordx4 v[176:179], v2, s[32:35], s52 offen offset:0 nt\n"
"v_and_b32_dpp v24, v47, v20 row_newbcast:12 row_mask:0xf bank_mask:0xf\n"
"v_and_b32_dpp v25, v47, v21 row_newbcast:12 row_mask:0xf bank_mask:0xf\n"
"v_and_b32_dpp v26, v47, v22 row_newbcast:12 row_mask:0xf bank_mask:0xf\n"
"v_and_b32_dpp v27, v47, v23 row_newbcast:12 row_mask:0xf bank_mask:0xf\n"
"s_waitcnt vmcnt(23)\n"
"s_nop 0\n"
"v_mfma_f32_16x16x32_bf16 v[40:43], v[24:27], v[120:123], v[40:43]\n"
"v_pk_lshlrev_b16 v62, 1, v120 op_sel_hi:[0,1]\n"
"v_pk_max_u16 v63, v63, v62\n"
"v_pk_lshlrev_b16 v62, 1, v121 op_sel_hi:[0,1]\n"
"v_pk_max_u16 v63, v63, v62\n"
"v_pk_lshlrev_b16 v62, 1, v122 op_sel_hi:[0,1]\n"
"v_pk_max_u16 v63, v63, v62\n"
"v_pk_lshlrev_b16 v62, 1, v123 op_sel_hi:[0,1]\n"
"v_pk_max_u16 v63, v63, v62\n"
"s_add_u32 s52, s53, 0x2ae000\n"
"s_and_b32 s52, s52, s54\n"
"buffer_load_dwordx4 v[180:183], v2, s[32:35], s52 offen offset:0 nt\n"
"v_and_b32_dpp v24, v47, v20 row_newbcast:1 row_mask:0xf bank_mask:0xf\n"
"v_and_b32_dpp v25, v47, v21 row_newbcast:1 row_mask:0xf bank_mask:0xf\n"
"v_and_b32_dpp v26, v47, v22 row_newbcast:1 row_mask:0xf bank_mask:0xf\n"
"v_and_b32_dpp v27, v47, v23 row_newbcast:1 row_mask:0xf bank_mask:0xf\n"
"s_waitcnt vmcnt(23)\n"
"s_nop 0\n"
"v_mfma_f32_16x16x32_bf16 v[40:43], v[24:27], v[136:139], v[40:43]\n"
"v_pk_lshlrev_b16 v62, 1, v136 op_sel_hi:[0,1]\n"
"v_pk_max_u16 v63, v63, v62\n"
"v_pk_lshlrev_b16 v62, 1, v137 op_sel_hi:[0,1]\n"
"v_pk_max_u16 v63, v63, v62\n"
"v_pk_lshlrev_b16 v62, 1, v138 op_sel_hi:[0,1]\n"
"v_pk_max_u16 v63, v63, v62\n"
"v_pk_lshlrev_b16 v62, 1, v139 op_sel_hi:[0,1]\n"
"v_pk_max_u16 v63, v63, v62\n"
"s_add_u32 s52, s53, 0x2b5000\n"
"s_and_b32 s52, s52, s54\n"
"buffer_load_dwordx4 v[196:199], v2, s[32:35], s52 offen offset:0 nt\n"
"v_and_b32_dpp v24, v47, v20 row_newbcast:5 row_mask:0xf bank_mask:0xf\n"
"v_and_b32_dpp v25, v47, v21 row_newbcast:5 row_mask:0xf bank_mask:0xf\n"
"v_and_b32_dpp v26, v47, v22 row_newbcast:5 row_mask:0xf bank_mask:0xf\n"
"v_and_b32_dpp v27, v47, v23 row_newbcast:5 row_mask:0xf bank_mask:0xf\n"
"s_waitcnt vmcnt(23)\n"
"s_nop 0\n"
"v_mfma_f32_16x16x32_bf16 v[40:43], v[24:27], v[140:143], v[40:43]\n"
"v_pk_lshlrev_b16 v62, 1, v140 op_sel_hi:[0,1]\n"
"v_pk_max_u16 v63, v63, v62\n"
"v_pk_lshlrev_b16 v62, 1, v141 op_sel_hi:[0,1]\n"
"v_pk_max_u16 v63, v63, v62\n"
"v_pk_lshlrev_b16 v62, 1, v142 op_sel_hi:[0,1]\n"
"v_pk_max_u16 v63, v63, v62\n"
"v_pk_lshlrev_b16 v62, 1, v143 op_sel_hi:[0,1]\n"
"v_pk_max_u16 v63, v63, v62\n"
"s_add_u32 s52, s53, 0x2bc000\n"
"s_and_b32 s52, s52, s54\n"
"buffer_load_dwordx4 v[200:203], v2, s[32:35], s52 offen offset:0 nt\n"
"v_and_b32_dpp v24, v47, v20 row_newbcast:9 row_mask:0xf bank_mask:0xf\n"
"v_and_b32_dpp v25, v47, v21 row_newbcast:9 row_mask:0xf bank_mask:0xf\n"
"v_and_b32_dpp v26, v47, v22 row_newbcast:9 row_mask:0xf bank_mask:0xf\n"
"v_and_b32_dpp v27, v47, v23 row_newbcast:9 row_mask:0xf bank_mask:0xf\n"
"s_waitcnt vmcnt(23)\n"
"s_nop 0\n"
"v_mfma_f32_16x16x32_bf16 v[40:43], v[24:27], v[144:147], v[40:43]\n"
"v_pk_lshlrev_b16 v62, 1, v144 op_sel_hi:[0,1]\n"
"v_pk_max_u16 v63, v63, v62\n"
"v_pk_lshlrev_b16 v62, 1, v145 op_sel_hi:[0,1]\n"
"v_pk_max_u16 v63, v63, v62\n"
"v_pk_lshlrev_b16 v62, 1, v146 op_sel_hi:[0,1]\n"
"v_pk_max_u16 v63, v63, v62\n"
"v_pk_lshlrev_b16 v62, 1, v147 op_sel_hi:[0,1]\n"
"v_pk_max_u16 v63, v63, v62\n"
"Lpace_386:\n"
"s_memrealtime s[96:97]\n"
"s_waitcnt lgkmcnt(0)\n"
"s_sub_u32 s96, s96, s98\n"
"s_cmp_ge_u32 s96, 2922\n"
"s_cbranch_scc1 Lpgo_387\n"
"s_sleep 2\n"
"s_branch Lpace_386\n"
"Lpgo_387:\n"
"s_add_u32 s52, s53, 0x2c3000\n"
"s_and_b32 s52, s52, s54\n"
"buffer_load_dwordx4 v[204:207], v2, s[32:35], s52 offen offset:0 nt\n"
"v_and_b32_dpp v24, v47, v20 row_newbcast:13 row_mask:0xf bank_mask:0xf\n"
"v_and_b32_dpp v25, v47, v21 row_newbcast:13 row_mask:0xf bank_mask:0xf\n"
"v_and_b32_dpp v26, v47, v22 row_newbcast:13 row_mask:0xf bank_mask:0xf\n"
"v_and_b32_dpp v27, v47, v23 row_newbcast:13 row_mask:0xf bank_mask:0xf\n"
"s_waitcnt vmcnt(23)\n"
"s_nop 0\n"
"v_mfma_f32_16x16x32_bf16 v[40:43], v[24:27], v[124:127], v[40:43]\n"
"v_pk_lshlrev_b16 v62, 1, v124 op_sel_hi:[0,1]\n"
"v_pk_max_u16 v63, v63, v62\n"
"v_pk_lshlrev_b16 v62, 1, v125 op_sel_hi:[0,1]\n"
"v_pk_max_u16 v63, v63, v62\n"
"v_pk_lshlrev_b16 v62, 1, v126 op_sel_hi:[0,1]\n"
"v_pk_max_u16 v63, v63, v62\n"
"v_pk_lshlrev_b16 v62, 1, v127 op_sel_hi:[0,1]\n"
"v_pk_max_u16 v63, v63, v62\n"
"s_add_u32 s52, s53, 0x2ca000\n"
"s_and_b32 s52, s52, s54\n"
"buffer_load_dwordx4 v[220:223], v2, s[32:35], s52 offen offset:0 nt\n"
"v_and_b32_dpp v24, v47, v20 row_newbcast:2 row_mask:0xf bank_mask:0xf\n"
"v_and_b32_dpp v25, v47, v21 row_newbcast:2 row_mask:0xf bank_mask:0xf\n"
"v_and_b32_dpp v26, v47, v22 row_newbcast:2 row_mask:0xf bank_mask:0xf\n"
"v_and_b32_dpp v27, v47, v23 row_newbcast:2 row_mask:0xf bank_mask:0xf\n"
"s_waitcnt vmcnt(23)\n"
"s_nop 0\n"
"v_mfma_f32_16x16x32_bf16 v[40:43], v[24:27], v[128:131], v[40:43]\n"
"v_pk_lshlrev_b16 v62, 1, v128 op_sel_hi:[0,1]\n"
"v_pk_max_u16 v63, v63, v62\n"
"v_pk_lshlrev_b16 v62, 1, v129 op_sel_hi:[0,1]\n"
"v_pk_max_u16 v63, v63, v62\n"
"v_pk_lshlrev_b16 v62, 1, v130 op_sel_hi:[0,1]\n"
"v_pk_max_u16 v63, v63, v62\n"
"v_pk_lshlrev_b16 v62, 1, v131 op_sel_hi:[0,1]\n"
"v_pk_max_u16 v63, v63, v62\n"
"v_lshrrev_b32 v62, 16, v63\n"
"v_and_b32 v63, 0xffff, v63\n"
"v_max_u32 v63, v63, v62\n"
"s_nop 1\n"
"v_max_u32_dpp v63, v63, v63 quad_perm:[1,0,3,2] row_mask:0xf bank_mask:0xf\n"
"s_nop 1\n"
"v_max_u32_dpp v63, v63, v63 quad_perm:[2,3,0,1] row_mask:0xf bank_mask:0xf\n"
"s_nop 1\n"
"v_max_u32_dpp v63, v63, v63 row_half_mirror row_mask:0xf bank_mask:0xf\n"
"s_nop 1\n"
"v_max_u32_dpp v63, v63, v63 row_mirror row_mask:0xf bank_mask:0xf\n"
"s_nop 1\n"
"v_max_u32_dpp v63, v63, v63 row_bcast:15 row_mask:0xa bank_mask:0xf\n"
"s_nop 1\n"
"v_max_u32_dpp v63, v63, v63 row_bcast:31 row_mask:0xc bank_mask:0xf\n"
"s_nop 0\n"
"v_readlane_b32 s72, v63, 63\n"
"s_nop 3\n"
"s_lshr_b32 s72, s72, 8\n"
"s_sub_i32 s72, s72, 15\n"
"s_max_i32 s72, s72, 0\n"
"s_min_i32 s72, s72, 126\n"
"s_mul_i32 s14, s72, 0x00800080\n"
"s_lshl_b32 s16, s14, 1\n"
"s_add_u32 s16, s16, 0x00ff00ff\n"
"s_lshl_b32 s72, s72, 23\n"
"s_add_u32 s18, s72, 0x3f800000\n"
"s_add_u32 s52, s53, 0x2d1000\n"
"s_and_b32 s52, s52, s54\n"
"buffer_load_dwordx4 v[224:227], v2, s[32:35], s52 offen offset:0 nt\n"
"v_and_b32_dpp v24, v48, v20 row_newbcast:2 row_mask:0xf bank_mask:0xf\n"
"v_and_b32_dpp v25, v48, v21 row_newbcast:2 row_mask:0xf bank_mask:0xf\n"
"v_and_b32_dpp v26, v48, v22 row_newbcast:2 row_mask:0xf bank_mask:0xf\n"
"v_and_b32_dpp v27, v48, v23 row_newbcast:2 row_mask:0xf bank_mask:0xf\n"
"s_waitcnt vmcnt(23)\n"
"s_nop 0\n"
"v_mfma_f32_16x16x32_bf16 v[40:43], v[24:27], v[132:135], v[40:43]\n"
"v_pk_sub_u16 v61, v132, s14 clamp\n"
"v_pk_sub_u16 v62, v133, s14 clamp\n"
"v_perm_b32 v112, v62, v61, s9\n"
"v_perm_b32 v61, v62, v61, s10\n"
"v_pk_sub_u16 v62, v134, s14 clamp\n"
"v_pk_sub_u16 v63, v135, s14 clamp\n"
"v_perm_b32 v113, v63, v62, s9\n"
"v_perm_b32 v62, v63, v62, s10\n"
"v_alignbit_b32 v62, v62, v62, 28\n"
"v_bfi_b32 v114, s8, v61, v62\n"
"v_bfi_b32 v61, s8, v62, v61\n"
"v_pk_mad_u16 v62, v132, 2, -1 op_sel_hi:[1,0,0]\n"
"v_pk_mad_u16 v63, v133, 2, -1 op_sel_hi:[1,0,0]\n"
"v_pk_min_u16 v62, v62, v63\n"
"v_pk_mad_u16 v63, v134, 2, -1 op_sel_hi:[1,0,0]\n"
"v_pk_min_u16 v62, v62, v63\n"
"v_pk_mad_u16 v63, v135, 2, -1 op_sel_hi:[1,0,0]\n"
"v_pk_min_u16 v62, v62, v63\n"
"v_pk_sub_u16 v62, s16, v62 clamp\n"
"v_or_b32 v61, v61, v62\n"
"v_cmp_ne_u32 vcc, 0, v61\n"
"s_cbranch_vccnz Lx_388\n"
"Lb_389:\n"
"s_add_u32 s52, s53, 0x2d8000\n"
"s_and_b32 s52, s52, s54\n"
"buffer_load_dwordx4 v[228:231], v2, s[32:35], s52 offen offset:0 nt\n"
"v_and_b32_dpp v24, v48, v20 row_newbcast:6 row_mask:0xf bank_mask:0xf\n"
"v_and_b32_dpp v25, v48, v21 row_newbcast:6 row_mask:0xf bank_mask:0xf\n"
"v_and_b32_dpp v26, v48, v22 row_newbcast:6 row_mask:0xf bank_mask:0xf\n"
"v_and_b32_dpp v27, v48, v23 row_newbcast:6 row_mask:0xf bank_mask:0xf\n"
"s_waitcnt vmcnt(23)\n"
"s_nop 0\n"
"v_mfma_f32_16x16x32_bf16 v[40:43], v[24:27], v[148:151], v[40:43]\n"
"v_pk_sub_u16 v61, v148, s14 clamp\n"
"v_pk_sub_u16 v62, v149, s14 clamp\n"
"v_perm_b32 v115, v62, v61, s9\n"
"v_perm_b32 v61, v62, v61, s10\n"
"v_pk_sub_u16 v62, v150, s14 clamp\n"
"v_pk_sub_u16 v63, v151, s14 clamp\n"
"v_perm_b32 v116, v63, v62, s9\n"
"v_perm_b32 v62, v63, v62, s10\n"
"v_alignbit_b32 v62, v62, v62, 28\n"
"v_bfi_b32 v117, s8, v61, v62\n"
"v_bfi_b32 v61, s8, v62, v61\n"
"v_pk_mad_u16 v62, v148, 2, -1 op_sel_hi:[1,0,0]\n"
"v_pk_mad_u16 v63, v149, 2, -1 op_sel_hi:[1,0,0]\n"
"v_pk_min_u16 v62, v62, v63\n"
"v_pk_mad_u16 v63, v150, 2, -1 op_sel_hi:[1,0,0]\n"
"v_pk_min_u16 v62, v62, v63\n"
"v_pk_mad_u16 v63, v151, 2, -1 op_sel_hi:[1,0,0]\n"
"v_pk_min_u16 v62, v62, v63\n"
"v_pk_sub_u16 v62, s16, v62 clamp\n"
"v_or_b32 v61, v61, v62\n"
"v_cmp_ne_u32 vcc, 0, v61\n"
"s_cbranch_vccnz Lx_390\n"
"Lb_391:\n"
"Lpace_392:\n"
"s_memrealtime s[96:97]\n"
"s_waitcnt lgkmcnt(0)\n"
"s_sub_u32 s96, s96, s98\n"
"s_cmp_ge_u32 s96, 2976\n"
"s_cbranch_scc1 Lpgo_393\n"
"s_sleep 2\n"
"s_branch Lpace_392\n"
"Lpgo_393:\n"
"s_add_u32 s52, s53, 0x2df000\n"
"s_and_b32 s52, s52, s54\n"
"buffer_load_dwordx4 v[244:247], v2, s[32:35], s52 offen offset:0 nt\n"
"v_and_b32_dpp v24, v48, v20 row_newbcast:10 row_mask:0xf bank_mask:0xf\n"
"v_and_b32_dpp v25, v48, v21 row_newbcast:10 row_mask:0xf bank_mask:0xf\n"
"v_and_b32_dpp v26, v48, v22 row_newbcast:10 row_mask:0xf bank_mask:0xf\n"
"v_and_b32_dpp v27, v48, v23 row_newbcast:10 row_mask:0xf bank_mask:0xf\n"
"s_waitcnt vmcnt(23)\n"
"s_nop 0\n"
"v_mfma_f32_16x16x32_bf16 v[40:43], v[24:27], v[152:155], v[40:43]\n"
"v_pk_sub_u16 v61, v152, s14 clamp\n"
"v_pk_sub_u16 v62, v153, s14 clamp\n"
"v_perm_b32 v118, v62, v61, s9\n"
"v_perm_b32 v61, v62, v61, s10\n"
"v_pk_sub_u16 v62, v154, s14 clamp\n"
"v_pk_sub_u16 v63, v155, s14 clamp\n"
"v_perm_b32 v119, v63, v62, s9\n"
"v_perm_b32 v62, v63, v62, s10\n"
"v_alignbit_b32 v62, v62, v62, 28\n"
"v_bfi_b32 v120, s8, v61, v62\n"
"v_bfi_b32 v61, s8, v62, v61\n"
"v_pk_mad_u16 v62, v152, 2, -1 op_sel_hi:[1,0,0]\n"
"v_pk_mad_u16 v63, v153, 2, -1 op_sel_hi:[1,0,0]\n"
"v_pk_min_u16 v62, v62, v63\n"
"v_pk_mad_u16 v63, v154, 2, -1 op_sel_hi:[1,0,0]\n"
"v_pk_min_u16 v62, v62, v63\n"
"v_pk_mad_u16 v63, v155, 2, -1 op_sel_hi:[1,0,0]\n"
"v_pk_min_u16 v62, v62, v63\n"
"v_pk_sub_u16 v62, s16, v62 clamp\n"
"v_or_b32 v61, v61, v62\n"
"v_cmp_ne_u32 vcc, 0, v61\n"
"s_cbranch_vccnz Lx_394\n"
"Lb_395:\n"
"s_add_u32 s52, s53, 0x2e6000\n"
"s_and_b32 s52, s52, s54\n"
"buffer_load_dwordx4 v[248:251], v2, s[32:35], s52 offen offset:0 nt\n"
"v_and_b32_dpp v24, v48, v20 row_newbcast:14 row_mask:0xf bank_mask:0xf\n"
"v_and_b32_dpp v25, v48, v21 row_newbcast:14 row_mask:0xf bank_mask:0xf\n"
"v_and_b32_dpp v26, v48, v22 row_newbcast:14 row_mask:0xf bank_mask:0xf\n"
"v_and_b32_dpp v27, v48, v23 row_newbcast:14 row_mask:0xf bank_mask:0xf\n"
"s_waitcnt vmcnt(23)\n"
"s_nop 0\n"
"v_mfma_f32_16x16x32_bf16 v[40:43], v[24:27], v[156:159], v[40:43]\n"
"v_pk_sub_u16 v61, v156, s14 clamp\n"
"v_pk_sub_u16 v62, v157, s14 clamp\n"
"v_perm_b32 v121, v62, v61, s9\n"
"v_perm_b32 v61, v62, v61, s10\n"
"v_pk_sub_u16 v62, v158, s14 clamp\n"
"v_pk_sub_u16 v63, v159, s14 clamp\n"
"v_perm_b32 v122, v63, v62, s9\n"
"v_perm_b32 v62, v63, v62, s10\n"
"v_alignbit_b32 v62, v62, v62, 28\n"
"v_bfi_b32 v123, s8, v61, v62\n"
"v_bfi_b32 v61, s8, v62, v61\n"
"v_pk_mad_u16 v62, v156, 2, -1 op_sel_hi:[1,0,0]\n"
"v_pk_mad_u16 v63, v157, 2, -1 op_sel_hi:[1,0,0]\n"
"v_pk_min_u16 v62, v62, v63\n"
"v_pk_mad_u16 v63, v158, 2, -1 op_sel_hi:[1,0,0]\n"
"v_pk_min_u16 v62, v62, v63\n"
"v_pk_mad_u16 v63, v159, 2, -1 op_sel_hi:[1,0,0]\n"
"v_pk_min_u16 v62, v62, v63\n"
"v_pk_sub_u16 v62, s16, v62 clamp\n"
"v_or_b32 v61, v61, v62\n"
"v_cmp_ne_u32 vcc, 0, v61\n"
"s_cbranch_vccnz Lx_396\n"
"Lb_397:\n"
"s_add_u32 s52, s53, 0x2ed000\n"
"s_and_b32 s52, s52, s54\n"
"buffer_load_dwordx4 v[252:255], v2, s[32:35], s52 offen offset:0 nt\n"
"v_and_b32_dpp v24, v48, v20 row_newbcast:3 row_mask:0xf bank_mask:0xf\n"
"v_and_b32_dpp v25, v48, v21 row_newbcast:3 row_mask:0xf bank_mask:0xf\n"
"v_and_b32_dpp v26, v48, v22 row_newbcast:3 row_mask:0xf bank_mask:0xf\n"
"v_and_b32_dpp v27, v48, v23 row_newbcast:3 row_mask:0xf bank_mask:0xf\n"
"s_waitcnt vmcnt(23)\n"
"s_nop 0\n"
"v_mfma_f32_16x16x32_bf16 v[40:43], v[24:27], v[160:163], v[40:43]\n"
"v_pk_sub_u16 v61, v160, s14 clamp\n"
"v_pk_sub_u16 v62, v161, s14 clamp\n"
"v_perm_b32 v136, v62, v61, s9\n"
"v_perm_b32 v61, v62, v61, s10\n"
"v_pk_sub_u16 v62, v162, s14 clamp\n"
"v_pk_sub_u16 v63, v163, s14 clamp\n"
"v_perm_b32 v137, v63, v62, s9\n"
"v_perm_b32 v62, v63, v62, s10\n"
"v_alignbit_b32 v62, v62, v62, 28\n"
"v_bfi_b32 v138, s8, v61, v62\n"
"v_bfi_b32 v61, s8, v62, v61\n"
"v_pk_mad_u16 v62, v160, 2, -1 op_sel_hi:[1,0,0]\n"
"v_pk_mad_u16 v63, v161, 2, -1 op_sel_hi:[1,0,0]\n"
"v_pk_min_u16 v62, v62, v63\n"
"v_pk_mad_u16 v63, v162, 2, -1 op_sel_hi:[1,0,0]\n"
"v_pk_min_u16 v62, v62, v63\n"
"v_pk_mad_u16 v63, v163, 2, -1 op_sel_hi:[1,0,0]\n"
"v_pk_min_u16 v62, v62, v63\n"
"v_pk_sub_u16 v62, s16, v62 clamp\n"
"v_or_b32 v61, v61, v62\n"
"v_cmp_ne_u32 vcc, 0, v61\n"
"s_cbranch_vccnz Lx_398\n"
"Lb_399:\n"
"s_add_u32 s52, s53, 0x2f4000\n"
"s_and_b32 s52, s52, s54\n"
"buffer_load_dwordx4 a[224:227], v2, s[32:35], s52 offen offset:0 nt\n"
"v_and_b32_dpp v24, v48, v20 row_newbcast:7 row_mask:0xf bank_mask:0xf\n"
"v_and_b32_dpp v25, v48, v21 row_newbcast:7 row_mask:0xf bank_mask:0xf\n"
"v_and_b32_dpp v26, v48, v22 row_newbcast:7 row_mask:0xf bank_mask:0xf\n"
"v_and_b32_dpp v27, v48, v23 row_newbcast:7 row_mask:0xf bank_mask:0xf\n"
"s_waitcnt vmcnt(23)\n"
"s_nop 0\n"
"v_mfma_f32_16x16x32_bf16 v[40:43], v[24:27], v[164:167], v[40:43]\n"
"v_pk_sub_u16 v61, v164, s14 clamp\n"
"v_pk_sub_u16 v62, v165, s14 clamp\n"
"v_perm_b32 v139, v62, v61, s9\n"
"v_perm_b32 v61, v62, v61, s10\n"
"v_pk_sub_u16 v62, v166, s14 clamp\n"
"v_pk_sub_u16 v63, v167, s14 clamp\n"
"v_perm_b32 v140, v63, v62, s9\n"
"v_perm_b32 v62, v63, v62, s10\n"
"v_alignbit_b32 v62, v62, v62, 28\n"
"v_bfi_b32 v141, s8, v61, v62\n"
"v_bfi_b32 v61, s8, v62, v61\n"
"v_pk_mad_u16 v62, v164, 2, -1 op_sel_hi:[1,0,0]\n"
"v_pk_mad_u16 v63, v165, 2, -1 op_sel_hi:[1,0,0]\n"
"v_pk_min_u16 v62, v62, v63\n"
"v_pk_mad_u16 v63, v166, 2, -1 op_sel_hi:[1,0,0]\n"
"v_pk_min_u16 v62, v62, v63\n"
"v_pk_mad_u16 v63, v167, 2, -1 op_sel_hi:[1,0,0]\n"
"v_pk_min_u16 v62, v62, v63\n"
"v_pk_sub_u16 v62, s16, v62 clamp\n"
"v_or_b32 v61, v61, v62\n"
"v_cmp_ne_u32 vcc, 0, v61\n"
"s_cbranch_vccnz Lx_400\n"
"Lb_401:\n"
"Lpace_402:\n"
"s_memrealtime s[96:97]\n"
"s_waitcnt lgkmcnt(0)\n"
"s_sub_u32 s96, s96, s98\n"
"s_cmp_ge_u32 s96, 3031\n"
"s_cbranch_scc1 Lpgo_403\n"
"s_sleep 2\n"
"s_branch Lpace_402\n"
"Lpgo_403:\n"
"s_add_u32 s52, s53, 0x2fb000\n"
"s_and_b32 s52, s52, s54\n"
"buffer_load_dwordx4 a[228:231], v2, s[32:35], s52 offen offset:0 nt\n"
"v_and_b32_dpp v24, v48, v20 row_newbcast:11 row_mask:0xf bank_mask:0xf\n"
"v_and_b32_dpp v25, v48, v21 row_newbcast:11 row_mask:0xf bank_mask:0xf\n"
"v_and_b32_dpp v26, v48, v22 row_newbcast:11 row_mask:0xf bank_mask:0xf\n"
"v_and_b32_dpp v27, v48, v23 row_newbcast:11 row_mask:0xf bank_mask:0xf\n"
"s_waitcnt vmcnt(23)\n"
"s_nop 0\n"
"v_mfma_f32_16x16x32_bf16 v[40:43], v[24:27], v[168:171], v[40:43]\n"
"v_pk_sub_u16 v61, v168, s14 clamp\n"
"v_pk_sub_u16 v62, v169, s14 clamp\n"
"v_perm_b32 v142, v62, v61, s9\n"
"v_perm_b32 v61, v62, v61, s10\n"
"v_pk_sub_u16 v62, v170, s14 clamp\n"
"v_pk_sub_u16 v63, v171, s14 clamp\n"
"v_perm_b32 v143, v63, v62, s9\n"
"v_perm_b32 v62, v63, v62, s10\n"
"v_alignbit_b32 v62, v62, v62, 28\n"
"v_bfi_b32 v144, s8, v61, v62\n"
"v_bfi_b32 v61, s8, v62, v61\n"
"v_pk_mad_u16 v62, v168, 2, -1 op_sel_hi:[1,0,0]\n"
"v_pk_mad_u16 v63, v169, 2, -1 op_sel_hi:[1,0,0]\n"
"v_pk_min_u16 v62, v62, v63\n"
"v_pk_mad_u16 v63, v170, 2, -1 op_sel_hi:[1,0,0]\n"
"v_pk_min_u16 v62, v62, v63\n"
"v_pk_mad_u16 v63, v171, 2, -1 op_sel_hi:[1,0,0]\n"
"v_pk_min_u16 v62, v62, v63\n"
"v_pk_sub_u16 v62, s16, v62 clamp\n"
"v_or_b32 v61, v61, v62\n"
"v_cmp_ne_u32 vcc, 0, v61\n"
"s_cbranch_vccnz Lx_404\n"
"Lb_405:\n"
"s_add_u32 s52, s53, 0x302000\n"
"s_and_b32 s52, s52, s54\n"
"buffer_load_dwordx4 a[232:235], v2, s[32:35], s52 offen offset:0 nt\n"
"v_and_b32_dpp v24, v48, v20 row_newbcast:15 row_mask:0xf bank_mask:0xf\n"
"v_and_b32_dpp v25, v48, v21 row_newbcast:15 row_mask:0xf bank_mask:0xf\n"
"v_and_b32_dpp v26, v48, v22 row_newbcast:15 row_mask:0xf bank_mask:0xf\n"
"v_and_b32_dpp v27, v48, v23 row_newbcast:15 row_mask:0xf bank_mask:0xf\n"
"s_waitcnt vmcnt(23)\n"
"s_nop 0\n"
"v_mfma_f32_16x16x32_bf16 v[40:43], v[24:27], v[184:187], v[40:43]\n"
"v_pk_sub_u16 v61, v184, s14 clamp\n"
"v_pk_sub_u16 v62, v185, s14 clamp\n"
"v_perm_b32 v145, v62, v61, s9\n"
"v_perm_b32 v61, v62, v61, s10\n"
"v_pk_sub_u16 v62, v186, s14 clamp\n"
"v_pk_sub_u16 v63, v187, s14 clamp\n"
"v_perm_b32 v146, v63, v62, s9\n"
"v_perm_b32 v62, v63, v62, s10\n"
"v_alignbit_b32 v62, v62, v62, 28\n"
"v_bfi_b32 v147, s8, v61, v62\n"
"v_bfi_b32 v61, s8, v62, v61\n"
"v_pk_mad_u16 v62, v184, 2, -1 op_sel_hi:[1,0,0]\n"
"v_pk_mad_u16 v63, v185, 2, -1 op_sel_hi:[1,0,0]\n"
"v_pk_min_u16 v62, v62, v63\n"
"v_pk_mad_u16 v63, v186, 2, -1 op_sel_hi:[1,0,0]\n"
"v_pk_min_u16 v62, v62, v63\n"
"v_pk_mad_u16 v63, v187, 2, -1 op_sel_hi:[1,0,0]\n"
"v_pk_min_u16 v62, v62, v63\n"
"v_pk_sub_u16 v62, s16, v62 clamp\n"
"v_or_b32 v61, v61, v62\n"
"v_cmp_ne_u32 vcc, 0, v61\n"
"s_cbranch_vccnz Lx_406\n"
"Lb_407:\n"
"s_add_u32 s52, s53, 0x309000\n"
"s_and_b32 s52, s52, s54\n"
"buffer_load_dwordx4 a[236:239], v2, s[32:35], s52 offen offset:0 nt\n"
"v_and_b32_dpp v24, v49, v20 row_newbcast:2 row_mask:0xf bank_mask:0xf\n"
"v_and_b32_dpp v25, v49, v21 row_newbcast:2 row_mask:0xf bank_mask:0xf\n"
"v_and_b32_dpp v26, v49, v22 row_newbcast:2 row_mask:0xf bank_mask:0xf\n"
"v_and_b32_dpp v27, v49, v23 row_newbcast:2 row_mask:0xf bank_mask:0xf\n"
"s_waitcnt vmcnt(23)\n"
"s_nop 0\n"
"v_mfma_f32_16x16x32_bf16 v[40:43], v[24:27], v[188:191], v[40:43]\n"
"v_pk_sub_u16 v61, v188, s14 clamp\n"
"v_pk_sub_u16 v62, v189, s14 clamp\n"
"v_perm_b32 v124, v62, v61, s9\n"
"v_perm_b32 v61, v62, v61, s10\n"
"v_pk_sub_u16 v62, v190, s14 clamp\n"
"v_pk_sub_u16 v63, v191, s14 clamp\n"
"v_perm_b32 v125, v63, v62, s9\n"
"v_perm_b32 v62, v63, v62, s10\n"
"v_alignbit_b32 v62, v62, v62, 28\n"
"v_bfi_b32 v126, s8, v61, v62\n"
"v_bfi_b32 v61, s8, v62, v61\n"
"v_pk_mad_u16 v62, v188, 2, -1 op_sel_hi:[1,0,0]\n"
"v_pk_mad_u16 v63, v189, 2, -1 op_sel_hi:[1,0,0]\n"
"v_pk_min_u16 v62, v62, v63\n"
"v_pk_mad_u16 v63, v190, 2, -1 op_sel_hi:[1,0,0]\n"
"v_pk_min_u16 v62, v62, v63\n"
"v_pk_mad_u16 v63, v191, 2, -1 op_sel_hi:[1,0,0]\n"
"v_pk_min_u16 v62, v62, v63\n"
"v_pk_sub_u16 v62, s16, v62 clamp\n"
"v_or_b32 v61, v61, v62\n"
"v_cmp_ne_u32 vcc, 0, v61\n"
"s_cbranch_vccnz Lx_408\n"
"Lb_409:\n"
"s_add_u32 s52, s53, 0x310000\n"
"s_and_b32 s52, s52, s54\n"
"buffer_load_dwordx4 a[240:243], v2, s[32:35], s52 offen offset:0 nt\n"
"v_and_b32_dpp v24, v49, v20 row_newbcast:6 row_mask:0xf bank_mask:0xf\n"
"v_and_b32_dpp v25, v49, v21 row_newbcast:6 row_mask:0xf bank_mask:0xf\n"
"v_and_b32_dpp v26, v49, v22 row_newbcast:6 row_mask:0xf bank_mask:0xf\n"
"v_and_b32_dpp v27, v49, v23 row_newbcast:6 row_mask:0xf bank_mask:0xf\n"
"s_waitcnt vmcnt(23)\n"
"s_nop 0\n"
"v_mfma_f32_16x16x32_bf16 v[40:43], v[24:27], v[192:195], v[40:43]\n"
"v_pk_sub_u16 v61, v192, s14 clamp\n"
"v_pk_sub_u16 v62, v193, s14 clamp\n"
"v_perm_b32 v127, v62, v61, s9\n"
"v_perm_b32 v61, v62, v61, s10\n"
"v_pk_sub_u16 v62, v194, s14 clamp\n"
"v_pk_sub_u16 v63, v195, s14 clamp\n"
"v_perm_b32 v128, v63, v62, s9\n"
"v_perm_b32 v62, v63, v62, s10\n"
"v_alignbit_b32 v62, v62, v62, 28\n"
"v_bfi_b32 v129, s8, v61, v62\n"
"v_bfi_b32 v61, s8, v62, v61\n"
"v_pk_mad_u16 v62, v192, 2, -1 op_sel_hi:[1,0,0]\n"
"v_pk_mad_u16 v63, v193, 2, -1 op_sel_hi:[1,0,0]\n"
"v_pk_min_u16 v62, v62, v63\n"
"v_pk_mad_u16 v63, v194, 2, -1 op_sel_hi:[1,0,0]\n"
"v_pk_min_u16 v62, v62, v63\n"
"v_pk_mad_u16 v63, v195, 2, -1 op_sel_hi:[1,0,0]\n"
"v_pk_min_u16 v62, v62, v63\n"
"v_pk_sub_u16 v62, s16, v62 clamp\n"
"v_or_b32 v61, v61, v62\n"
"v_cmp_ne_u32 vcc, 0, v61\n"
"s_cbranch_vccnz Lx_410\n"
"Lb_411:\n"
"Lpace_412:\n"
"s_memrealtime s[96:97]\n"
"s_waitcnt lgkmcnt(0)\n"
"s_sub_u32 s96, s96, s98\n"
"s_cmp_ge_u32 s96, 3085\n"
"s_cbranch_scc1 Lpgo_413\n"
"s_sleep 2\n"
"s_branch Lpace_412\n"
"Lpgo_413:\n"
"s_add_u32 s52, s53, 0x317000\n"
"s_and_b32 s52, s52, s54\n"
"buffer_load_dwordx4 a[244:247], v2, s[32:35], s52 offen offset:0 nt\n"
"v_and_b32_dpp v24, v49, v20 row_newbcast:10 row_mask:0xf bank_mask:0xf\n"
"v_and_b32_dpp v25, v49, v21 row_newbcast:10 row_mask:0xf bank_mask:0xf\n"
"v_and_b32_dpp v26, v49, v22 row_newbcast:10 row_mask:0xf bank_mask:0xf\n"
"v_and_b32_dpp v27, v49, v23 row_newbcast:10 row_mask:0xf bank_mask:0xf\n"
"s_waitcnt vmcnt(23)\n"
"s_nop 0\n"
"v_mfma_f32_16x16x32_bf16 v[40:43], v[24:27], v[212:215], v[40:43]\n"
"v_pk_sub_u16 v61, v212, s14 clamp\n"
"v_pk_sub_u16 v62, v213, s14 clamp\n"
"v_perm_b32 v130, v62, v61, s9\n"
"v_perm_b32 v61, v62, v61, s10\n"
"v_pk_sub_u16 v62, v214, s14 clamp\n"
"v_pk_sub_u16 v63, v215, s14 clamp\n"
"v_perm_b32 v131, v63, v62, s9\n"
"v_perm_b32 v62, v63, v62, s10\n"
"v_alignbit_b32 v62, v62, v62, 28\n"
"v_bfi_b32 v132, s8, v61, v62\n"
"v_bfi_b32 v61, s8, v62, v61\n"
"v_pk_mad_u16 v62, v212, 2, -1 op_sel_hi:[1,0,0]\n"
"v_pk_mad_u16 v63, v213, 2, -1 op_sel_hi:[1,0,0]\n"
"v_pk_min_u16 v62, v62, v63\n"
"v_pk_mad_u16 v63, v214, 2, -1 op_sel_hi:[1,0,0]\n"
"v_pk_min_u16 v62, v62, v63\n"
"v_pk_mad_u16 v63, v215, 2, -1 op_sel_hi:[1,0,0]\n"
"v_pk_min_u16 v62, v62, v63\n"
"v_pk_sub_u16 v62, s16, v62 clamp\n"
"v_or_b32 v61, v61, v62\n"
"v_cmp_ne_u32 vcc, 0, v61\n"
"s_cbranch_vccnz Lx_414\n"
"Lb_415:\n"
"s_add_u32 s52, s53, 0x31e000\n"
"s_and_b32 s52, s52, s54\n"
"buffer_load_dwordx4 a[248:251], v2, s[32:35], s52 offen offset:0 nt\n"
"v_and_b32_dpp v24, v49, v20 row_newbcast:14 row_mask:0xf bank_mask:0xf\n"
"v_and_b32_dpp v25, v49, v21 row_newbcast:14 row_mask:0xf bank_mask:0xf\n"
"v_and_b32_dpp v26, v49, v22 row_newbcast:14 row_mask:0xf bank_mask:0xf\n"
"v_and_b32_dpp v27, v49, v23 row_newbcast:14 row_mask:0xf bank_mask:0xf\n"
"s_waitcnt vmcnt(23)\n"
"s_nop 0\n"
"v_mfma_f32_16x16x32_bf16 v[40:43], v[24:27], v[216:219], v[40:43]\n"
"v_pk_sub_u16 v61, v216, s14 clamp\n"
"v_pk_sub_u16 v62, v217, s14 clamp\n"
"v_perm_b32 v133, v62, v61, s9\n"
"v_perm_b32 v61, v62, v61, s10\n"
"v_pk_sub_u16 v62, v218, s14 clamp\n"
"v_pk_sub_u16 v63, v219, s14 clamp\n"
"v_perm_b32 v134, v63, v62, s9\n"
"v_perm_b32 v62, v63, v62, s10\n"
"v_alignbit_b32 v62, v62, v62, 28\n"
"v_bfi_b32 v135, s8, v61, v62\n"
"v_bfi_b32 v61, s8, v62, v61\n"
"v_pk_mad_u16 v62, v216, 2, -1 op_sel_hi:[1,0,0]\n"
"v_pk_mad_u16 v63, v217, 2, -1 op_sel_hi:[1,0,0]\n"
"v_pk_min_u16 v62, v62, v63\n"
"v_pk_mad_u16 v63, v218, 2, -1 op_sel_hi:[1,0,0]\n"
"v_pk_min_u16 v62, v62, v63\n"
"v_pk_mad_u16 v63, v219, 2, -1 op_sel_hi:[1,0,0]\n"
"v_pk_min_u16 v62, v62, v63\n"
"v_pk_sub_u16 v62, s16, v62 clamp\n"
"v_or_b32 v61, v61, v62\n"
"v_cmp_ne_u32 vcc, 0, v61\n"
"s_cbranch_vccnz Lx_416\n"
"Lb_417:\n"
"s_add_u32 s52, s53, 0x325000\n"
"s_and_b32 s52, s52, s54\n"
"buffer_load_dwordx4 a[252:255], v2, s[32:35], s52 offen offset:0 nt\n"
"v_and_b32_dpp v24, v49, v20 row_newbcast:3 row_mask:0xf bank_mask:0xf\n"
"v_and_b32_dpp v25, v49, v21 row_newbcast:3 row_mask:0xf bank_mask:0xf\n"
"v_and_b32_dpp v26, v49, v22 row_newbcast:3 row_mask:0xf bank_mask:0xf\n"
"v_and_b32_dpp v27, v49, v23 row_newbcast:3 row_mask:0xf bank_mask:0xf\n"
"s_waitcnt vmcnt(23)\n"
"s_nop 0\n"
"v_mfma_f32_16x16x32_bf16 v[40:43], v[24:27], v[232:235], v[40:43]\n"
"v_pk_sub_u16 v61, v232, s14 clamp\n"
"v_pk_sub_u16 v62, v233, s14 clamp\n"
"v_perm_b32 v148, v62, v61, s9\n"
"v_perm_b32 v61, v62, v61, s10\n"
"v_pk_sub_u16 v62, v234, s14 clamp\n"
"v_pk_sub_u16 v63, v235, s14 clamp\n"
"v_perm_b32 v149, v63, v62, s9\n"
"v_perm_b32 v62, v63, v62, s10\n"
"v_alignbit_b32 v62, v62, v62, 28\n"
"v_bfi_b32 v150, s8, v61, v62\n"
"v_bfi_b32 v61, s8, v62, v61\n"
"v_pk_mad_u16 v62, v232, 2, -1 op_sel_hi:[1,0,0]\n"
"v_pk_mad_u16 v63, v233, 2, -1 op_sel_hi:[1,0,0]\n"
"v_pk_min_u16 v62, v62, v63\n"
"v_pk_mad_u16 v63, v234, 2, -1 op_sel_hi:[1,0,0]\n"
"v_pk_min_u16 v62, v62, v63\n"
"v_pk_mad_u16 v63, v235, 2, -1 op_sel_hi:[1,0,0]\n"
"v_pk_min_u16 v62, v62, v63\n"
"v_pk_sub_u16 v62, s16, v62 clamp\n"
"v_or_b32 v61, v61, v62\n"
"v_cmp_ne_u32 vcc, 0, v61\n"
"s_cbranch_vccnz Lx_418\n"
"Lb_419:\n"
"s_add_u32 s52, s53, 0x32c000\n"
"s_and_b32 s52, s52, s54\n"
"buffer_load_dwordx4 a[216:219], v2, s[32:35], s52 offen offset:0 nt\n"
"v_and_b32_dpp v24, v49, v20 row_newbcast:7 row_mask:0xf bank_mask:0xf\n"
"v_and_b32_dpp v25, v49, v21 row_newbcast:7 row_mask:0xf bank_mask:0xf\n"
"v_and_b32_dpp v26, v49, v22 row_newbcast:7 row_mask:0xf bank_mask:0xf\n"
"v_and_b32_dpp v27, v49, v23 row_newbcast:7 row_mask:0xf bank_mask:0xf\n"
"s_waitcnt vmcnt(23)\n"
"s_nop 0\n"
"v_mfma_f32_16x16x32_bf16 v[40:43], v[24:27], v[208:211], v[40:43]\n"
"v_pk_sub_u16 v61, v208, s14 clamp\n"
"v_pk_sub_u16 v62, v209, s14 clamp\n"
"v_perm_b32 v151, v62, v61, s9\n"
"v_perm_b32 v61, v62, v61, s10\n"
"v_pk_sub_u16 v62, v210, s14 clamp\n"
"v_pk_sub_u16 v63, v211, s14 clamp\n"
"v_perm_b32 v152, v63, v62, s9\n"
"v_perm_b32 v62, v63, v62, s10\n"
"v_alignbit_b32 v62, v62, v62, 28\n"
"v_bfi_b32 v153, s8, v61, v62\n"
"v_bfi_b32 v61, s8, v62, v61\n"
"v_pk_mad_u16 v62, v208, 2, -1 op_sel_hi:[1,0,0]\n"
"v_pk_mad_u16 v63, v209, 2, -1 op_sel_hi:[1,0,0]\n"
"v_pk_min_u16 v62, v62, v63\n"
"v_pk_mad_u16 v63, v210, 2, -1 op_sel_hi:[1,0,0]\n"
"v_pk_min_u16 v62, v62, v63\n"
"v_pk_mad_u16 v63, v211, 2, -1 op_sel_hi:[1,0,0]\n"
"v_pk_min_u16 v62, v62, v63\n"
"v_pk_sub_u16 v62, s16, v62 clamp\n"
"v_or_b32 v61, v61, v62\n"
"v_cmp_ne_u32 vcc, 0, v61\n"
"s_cbranch_vccnz Lx_420\n"
"Lb_421:\n"
"Lpace_422:\n"
"s_memrealtime s[96:97]\n"
"s_waitcnt lgkmcnt(0)\n"
"s_sub_u32 s96, s96, s98\n"
"s_cmp_ge_u32 s96, 3139\n"
"s_cbranch_scc1 Lpgo_423\n"
"s_sleep 2\n"
"s_branch Lpace_422\n"
"Lpgo_423:\n"
"s_add_u32 s52, s53, 0x333000\n"
"s_and_b32 s52, s52, s54\n"
"buffer_load_dwordx4 a[220:223], v2, s[32:35], s52 offen offset:0 nt\n"
"v_and_b32_dpp v24, v49, v20 row_newbcast:11 row_mask:0xf bank_mask:0xf\n"
"v_and_b32_dpp v25, v49, v21 row_newbcast:11 row_mask:0xf bank_mask:0xf\n"
"v_and_b32_dpp v26, v49, v22 row_newbcast:11 row_mask:0xf bank_mask:0xf\n"
"v_and_b32_dpp v27, v49, v23 row_newbcast:11 row_mask:0xf bank_mask:0xf\n"
"s_waitcnt vmcnt(23)\n"
"s_nop 0\n"
"v_mfma_f32_16x16x32_bf16 v[40:43], v[24:27], v[236:239], v[40:43]\n"
"v_pk_sub_u16 v61, v236, s14 clamp\n"
"v_pk_sub_u16 v62, v237, s14 clamp\n"
"v_perm_b32 v154, v62, v61, s9\n"
"v_perm_b32 v61, v62, v61, s10\n"
"v_pk_sub_u16 v62, v238, s14 clamp\n"
"v_pk_sub_u16 v63, v239, s14 clamp\n"
"v_perm_b32 v155, v63, v62, s9\n"
"v_perm_b32 v62, v63, v62, s10\n"
"v_alignbit_b32 v62, v62, v62, 28\n"
"v_bfi_b32 v156, s8, v61, v62\n"
"v_bfi_b32 v61, s8, v62, v61\n"
"v_pk_mad_u16 v62, v236, 2, -1 op_sel_hi:[1,0,0]\n"
"v_pk_mad_u16 v63, v237, 2, -1 op_sel_hi:[1,0,0]\n"
"v_pk_min_u16 v62, v62, v63\n"
"v_pk_mad_u16 v63, v238, 2, -1 op_sel_hi:[1,0,0]\n"
"v_pk_min_u16 v62, v62, v63\n"
"v_pk_mad_u16 v63, v239, 2, -1 op_sel_hi:[1,0,0]\n"
"v_pk_min_u16 v62, v62, v63\n"
"v_pk_sub_u16 v62, s16, v62 clamp\n"
"v_or_b32 v61, v61, v62\n"
"v_cmp_ne_u32 vcc, 0, v61\n"
"s_cbranch_vccnz Lx_424\n"
"Lb_425:\n"
"v_and_b32_dpp v24, v49, v20 row_newbcast:15 row_mask:0xf bank_mask:0xf\n"
"v_and_b32_dpp v25, v49, v21 row_newbcast:15 row_mask:0xf bank_mask:0xf\n"
"v_and_b32_dpp v26, v49, v22 row_newbcast:15 row_mask:0xf bank_mask:0xf\n"
"v_and_b32_dpp v27, v49, v23 row_newbcast:15 row_mask:0xf bank_mask:0xf\n"
"s_waitcnt vmcnt(22)\n"
"s_nop 0\n"
"v_mfma_f32_16x16x32_bf16 v[40:43], v[24:27], v[240:243], v[40:43]\n"
"v_pk_sub_u16 v61, v240, s14 clamp\n"
"v_pk_sub_u16 v62, v241, s14 clamp\n"
"v_perm_b32 v157, v62, v61, s9\n"
"v_perm_b32 v61, v62, v61, s10\n"
"v_pk_sub_u16 v62, v242, s14 clamp\n"
"v_pk_sub_u16 v63, v243, s14 clamp\n"
"v_perm_b32 v158, v63, v62, s9\n"
"v_perm_b32 v62, v63, v62, s10\n"
"v_alignbit_b32 v62, v62, v62, 28\n"
"v_bfi_b32 v159, s8, v61, v62\n"
"v_bfi_b32 v61, s8, v62, v61\n"
"v_pk_mad_u16 v62, v240, 2, -1 op_sel_hi:[1,0,0]\n"
"v_pk_mad_u16 v63, v241, 2, -1 op_sel_hi:[1,0,0]\n"
"v_pk_min_u16 v62, v62, v63\n"
"v_pk_mad_u16 v63, v242, 2, -1 op_sel_hi:[1,0,0]\n"
"v_pk_min_u16 v62, v62, v63\n"
"v_pk_mad_u16 v63, v243, 2, -1 op_sel_hi:[1,0,0]\n"
"v_pk_min_u16 v62, v62, v63\n"
"v_pk_sub_u16 v62, s16, v62 clamp\n"
"v_or_b32 v61, v61, v62\n"
"v_cmp_ne_u32 vcc, 0, v61\n"
"s_cbranch_vccnz Lx_426\n"
"Lb_427:\n"
"v_and_b32_dpp v24, v50, v20 row_newbcast:0 row_mask:0xf bank_mask:0xf\n"
"v_and_b32_dpp v25, v50, v21 row_newbcast:0 row_mask:0xf bank_mask:0xf\n"
"v_and_b32_dpp v26, v50, v22 row_newbcast:0 row_mask:0xf bank_mask:0xf\n"
"v_and_b32_dpp v27, v50, v23 row_newbcast:0 row_mask:0xf bank_mask:0xf\n"
"s_waitcnt vmcnt(21)\n"
"s_nop 0\n"
"v_mfma_f32_16x16x32_bf16 v[40:43], v[24:27], v[172:175], v[40:43]\n"
"v_pk_sub_u16 v61, v172, s14 clamp\n"
"v_pk_sub_u16 v62, v173, s14 clamp\n"
"v_perm_b32 v160, v62, v61, s9\n"
"v_perm_b32 v61, v62, v61, s10\n"
"v_pk_sub_u16 v62, v174, s14 clamp\n"
"v_pk_sub_u16 v63, v175, s14 clamp\n"
"v_perm_b32 v161, v63, v62, s9\n"
"v_perm_b32 v62, v63, v62, s10\n"
"v_alignbit_b32 v62, v62, v62, 28\n"
"v_bfi_b32 v162, s8, v61, v62\n"
"v_bfi_b32 v61, s8, v62, v61\n"
"v_pk_mad_u16 v62, v172, 2, -1 op_sel_hi:[1,0,0]\n"
"v_pk_mad_u16 v63, v173, 2, -1 op_sel_hi:[1,0,0]\n"
"v_pk_min_u16 v62, v62, v63\n"
"v_pk_mad_u16 v63, v174, 2, -1 op_sel_hi:[1,0,0]\n"
"v_pk_min_u16 v62, v62, v63\n"
"v_pk_mad_u16 v63, v175, 2, -1 op_sel_hi:[1,0,0]\n"
"v_pk_min_u16 v62, v62, v63\n"
"v_pk_sub_u16 v62, s16, v62 clamp\n"
"v_or_b32 v61, v61, v62\n"
"v_cmp_ne_u32 vcc, 0, v61\n"
"s_cbranch_vccnz Lx_428\n"
"Lb_429:\n"
"v_and_b32_dpp v24, v50, v20 row_newbcast:4 row_mask:0xf bank_mask:0xf\n"
"v_and_b32_dpp v25, v50, v21 row_newbcast:4 row_mask:0xf bank_mask:0xf\n"
"v_and_b32_dpp v26, v50, v22 row_newbcast:4 row_mask:0xf bank_mask:0xf\n"
"v_and_b32_dpp v27, v50, v23 row_newbcast:4 row_mask:0xf bank_mask:0xf\n"
"s_waitcnt vmcnt(20)\n"
"s_nop 0\n"
"v_mfma_f32_16x16x32_bf16 v[40:43], v[24:27], v[176:179], v[40:43]\n"
"v_pk_sub_u16 v61, v176, s14 clamp\n"
"v_pk_sub_u16 v62, v177, s14 clamp\n"
"v_perm_b32 v163, v62, v61, s9\n"
"v_perm_b32 v61, v62, v61, s10\n"
"v_pk_sub_u16 v62, v178, s14 clamp\n"
"v_pk_sub_u16 v63, v179, s14 clamp\n"
"v_perm_b32 v164, v63, v62, s9\n"
"v_perm_b32 v62, v63, v62, s10\n"
"v_alignbit_b32 v62, v62, v62, 28\n"
"v_bfi_b32 v165, s8, v61, v62\n"
"v_bfi_b32 v61, s8, v62, v61\n"
"v_pk_mad_u16 v62, v176, 2, -1 op_sel_hi:[1,0,0]\n"
"v_pk_mad_u16 v63, v177, 2, -1 op_sel_hi:[1,0,0]\n"
"v_pk_min_u16 v62, v62, v63\n"
"v_pk_mad_u16 v63, v178, 2, -1 op_sel_hi:[1,0,0]\n"
"v_pk_min_u16 v62, v62, v63\n"
"v_pk_mad_u16 v63, v179, 2, -1 op_sel_hi:[1,0,0]\n"
"v_pk_min_u16 v62, v62, v63\n"
"v_pk_sub_u16 v62, s16, v62 clamp\n"
"v_or_b32 v61, v61, v62\n"
"v_cmp_ne_u32 vcc, 0, v61\n"
"s_cbranch_vccnz Lx_430\n"
"Lb_431:\n"
"v_and_b32_dpp v24, v50, v20 row_newbcast:8 row_mask:0xf bank_mask:0xf\n"
"v_and_b32_dpp v25, v50, v21 row_newbcast:8 row_mask:0xf bank_mask:0xf\n"
"v_and_b32_dpp v26, v50, v22 row_newbcast:8 row_mask:0xf bank_mask:0xf\n"
"v_and_b32_dpp v27, v50, v23 row_newbcast:8 row_mask:0xf bank_mask:0xf\n"
"s_waitcnt vmcnt(19)\n"
"s_nop 0\n"
"v_mfma_f32_16x16x32_bf16 v[40:43], v[24:27], v[180:183], v[40:43]\n"
"v_pk_sub_u16 v61, v180, s14 clamp\n"
"v_pk_sub_u16 v62, v181, s14 clamp\n"
"v_perm_b32 v166, v62, v61, s9\n"
"v_perm_b32 v61, v62, v61, s10\n"
"v_pk_sub_u16 v62, v182, s14 clamp\n"
"v_pk_sub_u16 v63, v183, s14 clamp\n"
"v_perm_b32 v167, v63, v62, s9\n"
"v_perm_b32 v62, v63, v62, s10\n"
"v_alignbit_b32 v62, v62, v62, 28\n"
"v_bfi_b32 v168, s8, v61, v62\n"
"v_bfi_b32 v61, s8, v62, v61\n"
"v_pk_mad_u16 v62, v180, 2, -1 op_sel_hi:[1,0,0]\n"
"v_pk_mad_u16 v63, v181, 2, -1 op_sel_hi:[1,0,0]\n"
"v_pk_min_u16 v62, v62, v63\n"
"v_pk_mad_u16 v63, v182, 2, -1 op_sel_hi:[1,0,0]\n"
"v_pk_min_u16 v62, v62, v63\n"
"v_pk_mad_u16 v63, v183, 2, -1 op_sel_hi:[1,0,0]\n"
"v_pk_min_u16 v62, v62, v63\n"
"v_pk_sub_u16 v62, s16, v62 clamp\n"
"v_or_b32 v61, v61, v62\n"
"v_cmp_ne_u32 vcc, 0, v61\n"
"s_cbranch_vccnz Lx_432\n"
"Lb_433:\n"
"v_and_b32_dpp v24, v50, v20 row_newbcast:12 row_mask:0xf bank_mask:0xf\n"
"v_and_b32_dpp v25, v50, v21 row_newbcast:12 row_mask:0xf bank_mask:0xf\n"
"v_and_b32_dpp v26, v50, v22 row_newbcast:12 row_mask:0xf bank_mask:0xf\n"
"v_and_b32_dpp v27, v50, v23 row_newbcast:12 row_mask:0xf bank_mask:0xf\n"
"s_waitcnt vmcnt(18)\n"
"s_nop 0\n"
"v_mfma_f32_16x16x32_bf16 v[40:43], v[24:27], v[196:199], v[40:43]\n"
"v_pk_sub_u16 v61, v196, s14 clamp\n"
"v_pk_sub_u16 v62, v197, s14 clamp\n"
"v_perm_b32 v169, v62, v61, s9\n"
"v_perm_b32 v61, v62, v61, s10\n"
"v_pk_sub_u16 v62, v198, s14 clamp\n"
"v_pk_sub_u16 v63, v199, s14 clamp\n"
"v_perm_b32 v170, v63, v62, s9\n"
"v_perm_b32 v62, v63, v62, s10\n"
"v_alignbit_b32 v62, v62, v62, 28\n"
"v_bfi_b32 v171, s8, v61, v62\n"
"v_bfi_b32 v61, s8, v62, v61\n"
"v_pk_mad_u16 v62, v196, 2, -1 op_sel_hi:[1,0,0]\n"
"v_pk_mad_u16 v63, v197, 2, -1 op_sel_hi:[1,0,0]\n"
"v_pk_min_u16 v62, v62, v63\n"
"v_pk_mad_u16 v63, v198, 2, -1 op_sel_hi:[1,0,0]\n"
"v_pk_min_u16 v62, v62, v63\n"
"v_pk_mad_u16 v63, v199, 2, -1 op_sel_hi:[1,0,0]\n"
"v_pk_min_u16 v62, v62, v63\n"
"v_pk_sub_u16 v62, s16, v62 clamp\n"
"v_or_b32 v61, v61, v62\n"
"v_cmp_ne_u32 vcc, 0, v61\n"
"s_cbranch_vccnz Lx_434\n"
"Lb_435:\n"
"v_and_b32_dpp v24, v50, v20 row_newbcast:1 row_mask:0xf bank_mask:0xf\n"
"v_and_b32_dpp v25, v50, v21 row_newbcast:1 row_mask:0xf bank_mask:0xf\n"
"v_and_b32_dpp v26, v50, v22 row_newbcast:1 row_mask:0xf bank_mask:0xf\n"
"v_and_b32_dpp v27, v50, v23 row_newbcast:1 row_mask:0xf bank_mask:0xf\n"
"s_waitcnt vmcnt(17)\n"
"s_nop 0\n"
"v_mfma_f32_16x16x32_bf16 v[40:43], v[24:27], v[200:203], v[40:43]\n"
"v_pk_sub_u16 v61, v200, s14 clamp\n"
"v_pk_sub_u16 v62, v201, s14 clamp\n"
"v_perm_b32 v184, v62, v61, s9\n"
"v_perm_b32 v61, v62, v61, s10\n"
"v_pk_sub_u16 v62, v202, s14 clamp\n"
"v_pk_sub_u16 v63, v203, s14 clamp\n"
"v_perm_b32 v185, v63, v62, s9\n"
"v_perm_b32 v62, v63, v62, s10\n"
"v_alignbit_b32 v62, v62, v62, 28\n"
"v_bfi_b32 v186, s8, v61, v62\n"
"v_bfi_b32 v61, s8, v62, v61\n"
"v_pk_mad_u16 v62, v200, 2, -1 op_sel_hi:[1,0,0]\n"
"v_pk_mad_u16 v63, v201, 2, -1 op_sel_hi:[1,0,0]\n"
"v_pk_min_u16 v62, v62, v63\n"
"v_pk_mad_u16 v63, v202, 2, -1 op_sel_hi:[1,0,0]\n"
"v_pk_min_u16 v62, v62, v63\n"
"v_pk_mad_u16 v63, v203, 2, -1 op_sel_hi:[1,0,0]\n"
"v_pk_min_u16 v62, v62, v63\n"
"v_pk_sub_u16 v62, s16, v62 clamp\n"
"v_or_b32 v61, v61, v62\n"
"v_cmp_ne_u32 vcc, 0, v61\n"
"s_cbranch_vccnz Lx_436\n"
"Lb_437:\n"
"v_and_b32_dpp v24, v50, v20 row_newbcast:5 row_mask:0xf bank_mask:0xf\n"
"v_and_b32_dpp v25, v50, v21 row_newbcast:5 row_mask:0xf bank_mask:0xf\n"
"v_and_b32_dpp v26, v50, v22 row_newbcast:5 row_mask:0xf bank_mask:0xf\n"
"v_and_b32_dpp v27, v50, v23 row_newbcast:5 row_mask:0xf bank_mask:0xf\n"
"s_waitcnt vmcnt(16)\n"
"s_nop 0\n"
"v_mfma_f32_16x16x32_bf16 v[40:43], v[24:27], v[204:207], v[40:43]\n"
"v_pk_sub_u16 v61, v204, s14 clamp\n"
"v_pk_sub_u16 v62, v205, s14 clamp\n"
"v_perm_b32 v187, v62, v61, s9\n"
"v_perm_b32 v61, v62, v61, s10\n"
"v_pk_sub_u16 v62, v206, s14 clamp\n"
"v_pk_sub_u16 v63, v207, s14 clamp\n"
"v_perm_b32 v188, v63, v62, s9\n"
"v_perm_b32 v62, v63, v62, s10\n"
"v_alignbit_b32 v62, v62, v62, 28\n"
"v_bfi_b32 v189, s8, v61, v62\n"
"v_bfi_b32 v61, s8, v62, v61\n"
"v_pk_mad_u16 v62, v204, 2, -1 op_sel_hi:[1,0,0]\n"
"v_pk_mad_u16 v63, v205, 2, -1 op_sel_hi:[1,0,0]\n"
"v_pk_min_u16 v62, v62, v63\n"
"v_pk_mad_u16 v63, v206, 2, -1 op_sel_hi:[1,0,0]\n"
"v_pk_min_u16 v62, v62, v63\n"
"v_pk_mad_u16 v63, v207, 2, -1 op_sel_hi:[1,0,0]\n"
"v_pk_min_u16 v62, v62, v63\n"
"v_pk_sub_u16 v62, s16, v62 clamp\n"
"v_or_b32 v61, v61, v62\n"
"v_cmp_ne_u32 vcc, 0, v61\n"
"s_cbranch_vccnz Lx_438\n"
"Lb_439:\n"
"v_and_b32_dpp v24, v50, v20 row_newbcast:9 row_mask:0xf bank_mask:0xf\n"
"v_and_b32_dpp v25, v50, v21 row_newbcast:9 row_mask:0xf bank_mask:0xf\n"
"v_and_b32_dpp v26, v50, v22 row_newbcast:9 row_mask:0xf bank_mask:0xf\n"
"v_and_b32_dpp v27, v50, v23 row_newbcast:9 row_mask:0xf bank_mask:0xf\n"
"s_waitcnt vmcnt(15)\n"
"s_nop 0\n"
"v_mfma_f32_16x16x32_bf16 v[40:43], v[24:27], v[220:223], v[40:43]\n"
"v_pk_sub_u16 v61, v220, s14 clamp\n"
"v_pk_sub_u16 v62, v221, s14 clamp\n"
"v_perm_b32 v190, v62, v61, s9\n"
"v_perm_b32 v61, v62, v61, s10\n"
"v_pk_sub_u16 v62, v222, s14 clamp\n"
"v_pk_sub_u16 v63, v223, s14 clamp\n"
"v_perm_b32 v191, v63, v62, s9\n"
"v_perm_b32 v62, v63, v62, s10\n"
"v_alignbit_b32 v62, v62, v62, 28\n"
"v_bfi_b32 v192, s8, v61, v62\n"
"v_bfi_b32 v61, s8, v62, v61\n"
"v_pk_mad_u16 v62, v220, 2, -1 op_sel_hi:[1,0,0]\n"
"v_pk_mad_u16 v63, v221, 2, -1 op_sel_hi:[1,0,0]\n"
"v_pk_min_u16 v62, v62, v63\n"
"v_pk_mad_u16 v63, v222, 2, -1 op_sel_hi:[1,0,0]\n"
"v_pk_min_u16 v62, v62, v63\n"
"v_pk_mad_u16 v63, v223, 2, -1 op_sel_hi:[1,0,0]\n"
"v_pk_min_u16 v62, v62, v63\n"
"v_pk_sub_u16 v62, s16, v62 clamp\n"
"v_or_b32 v61, v61, v62\n"
"v_cmp_ne_u32 vcc, 0, v61\n"
"s_cbranch_vccnz Lx_440\n"
"Lb_441:\n"
"s_add_u32 s52, s53, 0x33a000\n"
"s_and_b32 s52, s52, s54\n"
"buffer_load_dwordx4 v[220:223], v2, s[32:35], s52 offen offset:0 nt\n"
"v_and_b32_dpp v24, v50, v20 row_newbcast:13 row_mask:0xf bank_mask:0xf\n"
"v_and_b32_dpp v25, v50, v21 row_newbcast:13 row_mask:0xf bank_mask:0xf\n"
"v_and_b32_dpp v26, v50, v22 row_newbcast:13 row_mask:0xf bank_mask:0xf\n"
"v_and_b32_dpp v27, v50, v23 row_newbcast:13 row_mask:0xf bank_mask:0xf\n"
"s_waitcnt vmcnt(15)\n"
"s_nop 0\n"
"v_mfma_f32_16x16x32_bf16 v[40:43], v[24:27], v[224:227], v[40:43]\n"
"v_pk_sub_u16 v61, v224, s14 clamp\n"
"v_pk_sub_u16 v62, v225, s14 clamp\n"
"v_perm_b32 v193, v62, v61, s9\n"
"v_perm_b32 v61, v62, v61, s10\n"
"v_pk_sub_u16 v62, v226, s14 clamp\n"
"v_pk_sub_u16 v63, v227, s14 clamp\n"
"v_perm_b32 v194, v63, v62, s9\n"
"v_perm_b32 v62, v63, v62, s10\n"
"v_alignbit_b32 v62, v62, v62, 28\n"
"v_bfi_b32 v195, s8, v61, v62\n"
"v_bfi_b32 v61, s8, v62, v61\n"
"v_pk_mad_u16 v62, v224, 2, -1 op_sel_hi:[1,0,0]\n"
"v_pk_mad_u16 v63, v225, 2, -1 op_sel_hi:[1,0,0]\n"
"v_pk_min_u16 v62, v62, v63\n"
"v_pk_mad_u16 v63, v226, 2, -1 op_sel_hi:[1,0,0]\n"
"v_pk_min_u16 v62, v62, v63\n"
"v_pk_mad_u16 v63, v227, 2, -1 op_sel_hi:[1,0,0]\n"
"v_pk_min_u16 v62, v62, v63\n"
"v_pk_sub_u16 v62, s16, v62 clamp\n"
"v_or_b32 v61, v61, v62\n"
"v_cmp_ne_u32 vcc, 0, v61\n"
"s_cbranch_vccnz Lx_442\n"
"Lb_443:\n"
"s_add_u32 s52, s53, 0x341000\n"
"s_and_b32 s52, s52, s54\n"
"buffer_load_dwordx4 v[224:227], v2, s[32:35], s52 offen offset:0 nt\n"
"v_and_b32_dpp v24, v50, v20 row_newbcast:2 row_mask:0xf bank_mask:0xf\n"
"v_and_b32_dpp v25, v50, v21 row_newbcast:2 row_mask:0xf bank_mask:0xf\n"
"v_and_b32_dpp v26, v50, v22 row_newbcast:2 row_mask:0xf bank_mask:0xf\n"
"v_and_b32_dpp v27, v50, v23 row_newbcast:2 row_mask:0xf bank_mask:0xf\n"
"s_waitcnt vmcnt(15)\n"
"s_nop 0\n"
"v_mfma_f32_16x16x32_bf16 v[40:43], v[24:27], v[228:231], v[40:43]\n"
"v_pk_sub_u16 v61, v228, s14 clamp\n"
"v_pk_sub_u16 v62, v229, s14 clamp\n"
"v_perm_b32 v208, v62, v61, s9\n"
"v_perm_b32 v61, v62, v61, s10\n"
"v_pk_sub_u16 v62, v230, s14 clamp\n"
"v_pk_sub_u16 v63, v231, s14 clamp\n"
"v_perm_b32 v209, v63, v62, s9\n"
"v_perm_b32 v62, v63, v62, s10\n"
"v_alignbit_b32 v62, v62, v62, 28\n"
"v_bfi_b32 v210, s8, v61, v62\n"
"v_bfi_b32 v61, s8, v62, v61\n"
"v_pk_mad_u16 v62, v228, 2, -1 op_sel_hi:[1,0,0]\n"
"v_pk_mad_u16 v63, v229, 2, -1 op_sel_hi:[1,0,0]\n"
"v_pk_min_u16 v62, v62, v63\n"
"v_pk_mad_u16 v63, v230, 2, -1 op_sel_hi:[1,0,0]\n"
"v_pk_min_u16 v62, v62, v63\n"
"v_pk_mad_u16 v63, v231, 2, -1 op_sel_hi:[1,0,0]\n"
"v_pk_min_u16 v62, v62, v63\n"
"v_pk_sub_u16 v62, s16, v62 clamp\n"
"v_or_b32 v61, v61, v62\n"
"v_cmp_ne_u32 vcc, 0, v61\n"
"s_cbranch_vccnz Lx_444\n"
"Lb_445:\n"
"s_add_u32 s52, s53, 0x348000\n"
"s_and_b32 s52, s52, s54\n"
"buffer_load_dwordx4 v[228:231], v2, s[32:35], s52 offen offset:0 nt\n"
"v_and_b32_dpp v24, v50, v20 row_newbcast:6 row_mask:0xf bank_mask:0xf\n"
"v_and_b32_dpp v25, v50, v21 row_newbcast:6 row_mask:0xf bank_mask:0xf\n"
"v_and_b32_dpp v26, v50, v22 row_newbcast:6 row_mask:0xf bank_mask:0xf\n"
"v_and_b32_dpp v27, v50, v23 row_newbcast:6 row_mask:0xf bank_mask:0xf\n"
"s_waitcnt vmcnt(15)\n"
"s_nop 0\n"
"v_mfma_f32_16x16x32_bf16 v[40:43], v[24:27], v[244:247], v[40:43]\n"
"v_pk_sub_u16 v61, v244, s14 clamp\n"
"v_pk_sub_u16 v62, v245, s14 clamp\n"
"v_perm_b32 v211, v62, v61, s9\n"
"v_perm_b32 v61, v62, v61, s10\n"
"v_pk_sub_u16 v62, v246, s14 clamp\n"
"v_pk_sub_u16 v63, v247, s14 clamp\n"
"v_perm_b32 v212, v63, v62, s9\n"
"v_perm_b32 v62, v63, v62, s10\n"
"v_alignbit_b32 v62, v62, v62, 28\n"
"v_bfi_b32 v213, s8, v61, v62\n"
"v_bfi_b32 v61, s8, v62, v61\n"
"v_pk_mad_u16 v62, v244, 2, -1 op_sel_hi:[1,0,0]\n"
"v_pk_mad_u16 v63, v245, 2, -1 op_sel_hi:[1,0,0]\n"
"v_pk_min_u16 v62, v62, v63\n"
"v_pk_mad_u16 v63, v246, 2, -1 op_sel_hi:[1,0,0]\n"
"v_pk_min_u16 v62, v62, v63\n"
"v_pk_mad_u16 v63, v247, 2, -1 op_sel_hi:[1,0,0]\n"
"v_pk_min_u16 v62, v62, v63\n"
"v_pk_sub_u16 v62, s16, v62 clamp\n"
"v_or_b32 v61, v61, v62\n"
"v_cmp_ne_u32 vcc, 0, v61\n"
"s_cbranch_vccnz Lx_446\n"
"Lb_447:\n"
"Lpace_448:\n"
"s_memrealtime s[96:97]\n"
"s_waitcnt lgkmcnt(0)\n"
"s_sub_u32 s96, s96, s98\n"
"s_cmp_ge_u32 s96, 3194\n"
"s_cbranch_scc1 Lpgo_449\n"
"s_sleep 2\n"
"s_branch Lpace_448\n"
"Lpgo_449:\n"
"s_add_u32 s52, s53, 0x34f000\n"
"s_and_b32 s52, s52, s54\n"
"buffer_load_dwordx4 v[244:247], v2, s[32:35], s52 offen offset:0 nt\n"
"v_and_b32_dpp v24, v50, v20 row_newbcast:10 row_mask:0xf bank_mask:0xf\n"
"v_and_b32_dpp v25, v50, v21 row_newbcast:10 row_mask:0xf bank_mask:0xf\n"
"v_and_b32_dpp v26, v50, v22 row_newbcast:10 row_mask:0xf bank_mask:0xf\n"
"v_and_b32_dpp v27, v50, v23 row_newbcast:10 row_mask:0xf bank_mask:0xf\n"
"s_waitcnt vmcnt(15)\n"
"s_nop 0\n"
"v_mfma_f32_16x16x32_bf16 v[40:43], v[24:27], v[248:251], v[40:43]\n"
"v_pk_sub_u16 v61, v248, s14 clamp\n"
"v_pk_sub_u16 v62, v249, s14 clamp\n"
"v_perm_b32 v214, v62, v61, s9\n"
"v_perm_b32 v61, v62, v61, s10\n"
"v_pk_sub_u16 v62, v250, s14 clamp\n"
"v_pk_sub_u16 v63, v251, s14 clamp\n"
"v_perm_b32 v215, v63, v62, s9\n"
"v_perm_b32 v62, v63, v62, s10\n"
"v_alignbit_b32 v62, v62, v62, 28\n"
"v_bfi_b32 v216, s8, v61, v62\n"
"v_bfi_b32 v61, s8, v62, v61\n"
"v_pk_mad_u16 v62, v248, 2, -1 op_sel_hi:[1,0,0]\n"
"v_pk_mad_u16 v63, v249, 2, -1 op_sel_hi:[1,0,0]\n"
"v_pk_min_u16 v62, v62, v63\n"
"v_pk_mad_u16 v63, v250, 2, -1 op_sel_hi:[1,0,0]\n"
"v_pk_min_u16 v62, v62, v63\n"
"v_pk_mad_u16 v63, v251, 2, -1 op_sel_hi:[1,0,0]\n"
"v_pk_min_u16 v62, v62, v63\n"
"v_pk_sub_u16 v62, s16, v62 clamp\n"
"v_or_b32 v61, v61, v62\n"
"v_cmp_ne_u32 vcc, 0, v61\n"
"s_cbranch_vccnz Lx_450\n"
"Lb_451:\n"
"s_add_u32 s52, s53, 0x356000\n"
"s_and_b32 s52, s52, s54\n"
"buffer_load_dwordx4 v[248:251], v2, s[32:35], s52 offen offset:0 nt\n"
"v_and_b32_dpp v24, v50, v20 row_newbcast:14 row_mask:0xf bank_mask:0xf\n"
"v_and_b32_dpp v25, v50, v21 row_newbcast:14 row_mask:0xf bank_mask:0xf\n"
"v_and_b32_dpp v26, v50, v22 row_newbcast:14 row_mask:0xf bank_mask:0xf\n"
"v_and_b32_dpp v27, v50, v23 row_newbcast:14 row_mask:0xf bank_mask:0xf\n"
"s_waitcnt vmcnt(15)\n"
"s_nop 0\n"
"v_mfma_f32_16x16x32_bf16 v[40:43], v[24:27], v[252:255], v[40:43]\n"
"v_pk_sub_u16 v61, v252, s14 clamp\n"
"v_pk_sub_u16 v62, v253, s14 clamp\n"
"v_perm_b32 v217, v62, v61, s9\n"
"v_perm_b32 v61, v62, v61, s10\n"
"v_pk_sub_u16 v62, v254, s14 clamp\n"
"v_pk_sub_u16 v63, v255, s14 clamp\n"
"v_perm_b32 v218, v63, v62, s9\n"
"v_perm_b32 v62, v63, v62, s10\n"
"v_alignbit_b32 v62, v62, v62, 28\n"
"v_bfi_b32 v219, s8, v61, v62\n"
"v_bfi_b32 v61, s8, v62, v61\n"
"v_pk_mad_u16 v62, v252, 2, -1 op_sel_hi:[1,0,0]\n"
"v_pk_mad_u16 v63, v253, 2, -1 op_sel_hi:[1,0,0]\n"
"v_pk_min_u16 v62, v62, v63\n"
"v_pk_mad_u16 v63, v254, 2, -1 op_sel_hi:[1,0,0]\n"
"v_pk_min_u16 v62, v62, v63\n"
"v_pk_mad_u16 v63, v255, 2, -1 op_sel_hi:[1,0,0]\n"
"v_pk_min_u16 v62, v62, v63\n"
"v_pk_sub_u16 v62, s16, v62 clamp\n"
"v_or_b32 v61, v61, v62\n"
"v_cmp_ne_u32 vcc, 0, v61\n"
"s_cbranch_vccnz Lx_452\n"
"Lb_453:\n"
"s_add_u32 s52, s53, 0x35d000\n"
"s_and_b32 s52, s52, s54\n"
"buffer_load_dwordx4 v[252:255], v2, s[32:35], s52 offen offset:0 nt\n"
"v_and_b32_dpp v24, v50, v20 row_newbcast:3 row_mask:0xf bank_mask:0xf\n"
"v_and_b32_dpp v25, v50, v21 row_newbcast:3 row_mask:0xf bank_mask:0xf\n"
"v_and_b32_dpp v26, v50, v22 row_newbcast:3 row_mask:0xf bank_mask:0xf\n"
"v_and_b32_dpp v27, v50, v23 row_newbcast:3 row_mask:0xf bank_mask:0xf\n"
"s_waitcnt vmcnt(15)\n"
"s_nop 0\n"
"v_mfma_f32_16x16x32_bf16 v[40:43], v[24:27], a[224:227], v[40:43]\n"
"v_accvgpr_read_b32 v28, a224\n"
"v_accvgpr_read_b32 v29, a225\n"
"v_accvgpr_read_b32 v30, a226\n"
"v_accvgpr_read_b32 v31, a227\n"
"v_pk_sub_u16 v61, v28, s14 clamp\n"
"v_pk_sub_u16 v62, v29, s14 clamp\n"
"v_perm_b32 v232, v62, v61, s9\n"
"v_perm_b32 v61, v62, v61, s10\n"
"v_pk_sub_u16 v62, v30, s14 clamp\n"
"v_pk_sub_u16 v63, v31, s14 clamp\n"
"v_perm_b32 v233, v63, v62, s9\n"
"v_perm_b32 v62, v63, v62, s10\n"
"v_alignbit_b32 v62, v62, v62, 28\n"
"v_bfi_b32 v234, s8, v61, v62\n"
"v_bfi_b32 v61, s8, v62, v61\n"
"v_pk_mad_u16 v62, v28, 2, -1 op_sel_hi:[1,0,0]\n"
"v_pk_mad_u16 v63, v29, 2, -1 op_sel_hi:[1,0,0]\n"
"v_pk_min_u16 v62, v62, v63\n"
"v_pk_mad_u16 v63, v30, 2, -1 op_sel_hi:[1,0,0]\n"
"v_pk_min_u16 v62, v62, v63\n"
"v_pk_mad_u16 v63, v31, 2, -1 op_sel_hi:[1,0,0]\n"
"v_pk_min_u16 v62, v62, v63\n"
"v_pk_sub_u16 v62, s16, v62 clamp\n"
"v_or_b32 v61, v61, v62\n"
"v_cmp_ne_u32 vcc, 0, v61\n"
"s_cbranch_vccnz Lx_454\n"
"Lb_455:\n"
"s_add_u32 s52, s53, 0x364000\n"
"s_and_b32 s52, s52, s54\n"
"buffer_load_dwordx4 a[224:227], v2, s[32:35], s52 offen offset:0 nt\n"
"v_and_b32_dpp v24, v50, v20 row_newbcast:7 row_mask:0xf bank_mask:0xf\n"
"v_and_b32_dpp v25, v50, v21 row_newbcast:7 row_mask:0xf bank_mask:0xf\n"
"v_and_b32_dpp v26, v50, v22 row_newbcast:7 row_mask:0xf bank_mask:0xf\n"
"v_and_b32_dpp v27, v50, v23 row_newbcast:7 row_mask:0xf bank_mask:0xf\n"
"s_waitcnt vmcnt(15)\n"
"s_nop 0\n"
"v_mfma_f32_16x16x32_bf16 v[40:43], v[24:27], a[228:231], v[40:43]\n"
"v_accvgpr_read_b32 v28, a228\n"
"v_accvgpr_read_b32 v29, a229\n"
"v_accvgpr_read_b32 v30, a230\n"
"v_accvgpr_read_b32 v31, a231\n"
"v_pk_sub_u16 v61, v28, s14 clamp\n"
"v_pk_sub_u16 v62, v29, s14 clamp\n"
"v_perm_b32 v235, v62, v61, s9\n"
"v_perm_b32 v61, v62, v61, s10\n"
"v_pk_sub_u16 v62, v30, s14 clamp\n"
"v_pk_sub_u16 v63, v31, s14 clamp\n"
"v_perm_b32 v236, v63, v62, s9\n"
"v_perm_b32 v62, v63, v62, s10\n"
"v_alignbit_b32 v62, v62, v62, 28\n"
"v_bfi_b32 v237, s8, v61, v62\n"
"v_bfi_b32 v61, s8, v62, v61\n"
"v_pk_mad_u16 v62, v28, 2, -1 op_sel_hi:[1,0,0]\n"
"v_pk_mad_u16 v63, v29, 2, -1 op_sel_hi:[1,0,0]\n"
"v_pk_min_u16 v62, v62, v63\n"
"v_pk_mad_u16 v63, v30, 2, -1 op_sel_hi:[1,0,0]\n"
"v_pk_min_u16 v62, v62, v63\n"
"v_pk_mad_u16 v63, v31, 2, -1 op_sel_hi:[1,0,0]\n"
"v_pk_min_u16 v62, v62, v63\n"
"v_pk_sub_u16 v62, s16, v62 clamp\n"
"v_or_b32 v61, v61, v62\n"
"v_cmp_ne_u32 vcc, 0, v61\n"
"s_cbranch_vccnz Lx_456\n"
"Lb_457:\n"
"Lpace_458:\n"
"s_memrealtime s[96:97]\n"
"s_waitcnt lgkmcnt(0)\n"
"s_sub_u32 s96, s96, s98\n"
"s_cmp_ge_u32 s96, 3248\n"
"s_cbranch_scc1 Lpgo_459\n"
"s_sleep 2\n"
"s_branch Lpace_458\n"
"Lpgo_459:\n"
"s_add_u32 s52, s53, 0x36b000\n"
"s_and_b32 s52, s52, s54\n"
"buffer_load_dwordx4 a[228:231], v2, s[32:35], s52 offen offset:0 nt\n"
"v_and_b32_dpp v24, v50, v20 row_newbcast:11 row_mask:0xf bank_mask:0xf\n"
"v_and_b32_dpp v25, v50, v21 row_newbcast:11 row_mask:0xf bank_mask:0xf\n"
"v_and_b32_dpp v26, v50, v22 row_newbcast:11 row_mask:0xf bank_mask:0xf\n"
"v_and_b32_dpp v27, v50, v23 row_newbcast:11 row_mask:0xf bank_mask:0xf\n"
"s_waitcnt vmcnt(15)\n"
"s_nop 0\n"
"v_mfma_f32_16x16x32_bf16 v[40:43], v[24:27], a[232:235], v[40:43]\n"
"v_accvgpr_read_b32 v28, a232\n"
"v_accvgpr_read_b32 v29, a233\n"
"v_accvgpr_read_b32 v30, a234\n"
"v_accvgpr_read_b32 v31, a235\n"
"v_pk_sub_u16 v61, v28, s14 clamp\n"
"v_pk_sub_u16 v62, v29, s14 clamp\n"
"v_perm_b32 v238, v62, v61, s9\n"
"v_perm_b32 v61, v62, v61, s10\n"
"v_pk_sub_u16 v62, v30, s14 clamp\n"
"v_pk_sub_u16 v63, v31, s14 clamp\n"
"v_perm_b32 v239, v63, v62, s9\n"
"v_perm_b32 v62, v63, v62, s10\n"
"v_alignbit_b32 v62, v62, v62, 28\n"
"v_bfi_b32 v240, s8, v61, v62\n"
"v_bfi_b32 v61, s8, v62, v61\n"
"v_pk_mad_u16 v62, v28, 2, -1 op_sel_hi:[1,0,0]\n"
"v_pk_mad_u16 v63, v29, 2, -1 op_sel_hi:[1,0,0]\n"
"v_pk_min_u16 v62, v62, v63\n"
"v_pk_mad_u16 v63, v30, 2, -1 op_sel_hi:[1,0,0]\n"
"v_pk_min_u16 v62, v62, v63\n"
"v_pk_mad_u16 v63, v31, 2, -1 op_sel_hi:[1,0,0]\n"
"v_pk_min_u16 v62, v62, v63\n"
"v_pk_sub_u16 v62, s16, v62 clamp\n"
"v_or_b32 v61, v61, v62\n"
"v_cmp_ne_u32 vcc, 0, v61\n"
"s_cbranch_vccnz Lx_460\n"
"Lb_461:\n"
"s_add_u32 s52, s53, 0x372000\n"
"s_and_b32 s52, s52, s54\n"
"buffer_load_dwordx4 a[232:235], v2, s[32:35], s52 offen offset:0 nt\n"
"v_and_b32_dpp v24, v50, v20 row_newbcast:15 row_mask:0xf bank_mask:0xf\n"
"v_and_b32_dpp v25, v50, v21 row_newbcast:15 row_mask:0xf bank_mask:0xf\n"
"v_and_b32_dpp v26, v50, v22 row_newbcast:15 row_mask:0xf bank_mask:0xf\n"
"v_and_b32_dpp v27, v50, v23 row_newbcast:15 row_mask:0xf bank_mask:0xf\n"
"s_waitcnt vmcnt(15)\n"
"s_nop 0\n"
"v_mfma_f32_16x16x32_bf16 v[40:43], v[24:27], a[236:239], v[40:43]\n"
"v_accvgpr_read_b32 v28, a236\n"
"v_accvgpr_read_b32 v29, a237\n"
"v_accvgpr_read_b32 v30, a238\n"
"v_accvgpr_read_b32 v31, a239\n"
"v_pk_sub_u16 v61, v28, s14 clamp\n"
"v_pk_sub_u16 v62, v29, s14 clamp\n"
"v_perm_b32 v241, v62, v61, s9\n"
"v_perm_b32 v61, v62, v61, s10\n"
"v_pk_sub_u16 v62, v30, s14 clamp\n"
"v_pk_sub_u16 v63, v31, s14 clamp\n"
"v_perm_b32 v242, v63, v62, s9\n"
"v_perm_b32 v62, v63, v62, s10\n"
"v_alignbit_b32 v62, v62, v62, 28\n"
"v_bfi_b32 v243, s8, v61, v62\n"
"v_bfi_b32 v61, s8, v62, v61\n"
"v_pk_mad_u16 v62, v28, 2, -1 op_sel_hi:[1,0,0]\n"
"v_pk_mad_u16 v63, v29, 2, -1 op_sel_hi:[1,0,0]\n"
"v_pk_min_u16 v62, v62, v63\n"
"v_pk_mad_u16 v63, v30, 2, -1 op_sel_hi:[1,0,0]\n"
"v_pk_min_u16 v62, v62, v63\n"
"v_pk_mad_u16 v63, v31, 2, -1 op_sel_hi:[1,0,0]\n"
"v_pk_min_u16 v62, v62, v63\n"
"v_pk_sub_u16 v62, s16, v62 clamp\n"
"v_or_b32 v61, v61, v62\n"
"v_cmp_ne_u32 vcc, 0, v61\n"
"s_cbranch_vccnz Lx_462\n"
"Lb_463:\n"
"s_add_u32 s52, s53, 0x379000\n"
"s_and_b32 s52, s52, s54\n"
"buffer_load_dwordx4 a[236:239], v2, s[32:35], s52 offen offset:0 nt\n"
"s_add_u32 s52, s53, 0x11f000\n"
"s_and_b32 s52, s52, s54\n"
"buffer_load_dwordx4 v[4:7], v2, s[32:35], s52 offen offset:0\n"
"Lpace_464:\n"
"s_memrealtime s[96:97]\n"
"s_waitcnt lgkmcnt(0)\n"
"s_sub_u32 s96, s96, s98\n"
"s_cmp_ge_u32 s96, 3304\n"
"s_cbranch_scc1 Lpgo_465\n"
"s_sleep 2\n"
"s_branch Lpace_464\n"
"Lpgo_465:\n"
"s_add_u32 s52, s53, 0x126000\n"
"s_and_b32 s52, s52, s54\n"
"buffer_load_dwordx4 v[8:11], v2, s[32:35], s52 offen offset:0\n"
"s_add_u32 s52, s53, 0x12d000\n"
"s_and_b32 s52, s52, s54\n"
"buffer_load_dwordx4 v[12:15], v2, s[32:35], s52 offen offset:0\n"
"s_add_u32 s52, s53, 0x134000\n"
"s_and_b32 s52, s52, s54\n"
"buffer_load_dwordx4 v[16:19], v2, s[32:35], s52 offen offset:0\n"
"v_and_b32_dpp v24, v51, v20 row_newbcast:0 row_mask:0xf bank_mask:0xf\n"
"v_and_b32_dpp v25, v51, v21 row_newbcast:0 row_mask:0xf bank_mask:0xf\n"
"v_and_b32_dpp v26, v51, v22 row_newbcast:0 row_mask:0xf bank_mask:0xf\n"
"v_and_b32_dpp v27, v51, v23 row_newbcast:0 row_mask:0xf bank_mask:0xf\n"
"s_waitcnt vmcnt(19)\n"
"s_nop 0\n"
"v_mfma_f32_16x16x32_bf16 v[40:43], v[24:27], a[240:243], v[40:43]\n"
"v_accvgpr_read_b32 v28, a240\n"
"v_accvgpr_read_b32 v29, a241\n"
"v_accvgpr_read_b32 v30, a242\n"
"v_accvgpr_read_b32 v31, a243\n"
"v_pk_sub_u16 v61, v28, s14 clamp\n"
"v_pk_sub_u16 v62, v29, s14 clamp\n"
"v_perm_b32 v172, v62, v61, s9\n"
"v_perm_b32 v61, v62, v61, s10\n"
"v_pk_sub_u16 v62, v30, s14 clamp\n"
"v_pk_sub_u16 v63, v31, s14 clamp\n"
"v_perm_b32 v173, v63, v62, s9\n"
"v_perm_b32 v62, v63, v62, s10\n"
"v_alignbit_b32 v62, v62, v62, 28\n"
"v_bfi_b32 v174, s8, v61, v62\n"
"v_bfi_b32 v61, s8, v62, v61\n"
"v_pk_mad_u16 v62, v28, 2, -1 op_sel_hi:[1,0,0]\n"
"v_pk_mad_u16 v63, v29, 2, -1 op_sel_hi:[1,0,0]\n"
"v_pk_min_u16 v62, v62, v63\n"
"v_pk_mad_u16 v63, v30, 2, -1 op_sel_hi:[1,0,0]\n"
"v_pk_min_u16 v62, v62, v63\n"
"v_pk_mad_u16 v63, v31, 2, -1 op_sel_hi:[1,0,0]\n"
"v_pk_min_u16 v62, v62, v63\n"
"v_pk_sub_u16 v62, s16, v62 clamp\n"
"v_or_b32 v61, v61, v62\n"
"v_cmp_ne_u32 vcc, 0, v61\n"
"s_cbranch_vccnz Lx_466\n"
"Lb_467:\n"
"s_add_u32 s52, s53, 0x13b000\n"
"s_and_b32 s52, s52, s54\n"
"buffer_load_dwordx4 a[240:243], v2, s[32:35], s52 offen offset:0\n"
"v_and_b32_dpp v24, v51, v20 row_newbcast:4 row_mask:0xf bank_mask:0xf\n"
"v_and_b32_dpp v25, v51, v21 row_newbcast:4 row_mask:0xf bank_mask:0xf\n"
"v_and_b32_dpp v26, v51, v22 row_newbcast:4 row_mask:0xf bank_mask:0xf\n"
"v_and_b32_dpp v27, v51, v23 row_newbcast:4 row_mask:0xf bank_mask:0xf\n"
"s_waitcnt vmcnt(19)\n"
"s_nop 0\n"
"v_mfma_f32_16x16x32_bf16 v[40:43], v[24:27], a[244:247], v[40:43]\n"
"v_accvgpr_read_b32 v28, a244\n"
"v_accvgpr_read_b32 v29, a245\n"
"v_accvgpr_read_b32 v30, a246\n"
"v_accvgpr_read_b32 v31, a247\n"
"v_pk_sub_u16 v61, v28, s14 clamp\n"
"v_pk_sub_u16 v62, v29, s14 clamp\n"
"v_perm_b32 v175, v62, v61, s9\n"
"v_perm_b32 v61, v62, v61, s10\n"
"v_pk_sub_u16 v62, v30, s14 clamp\n"
"v_pk_sub_u16 v63, v31, s14 clamp\n"
"v_perm_b32 v176, v63, v62, s9\n"
"v_perm_b32 v62, v63, v62, s10\n"
"v_alignbit_b32 v62, v62, v62, 28\n"
"v_bfi_b32 v177, s8, v61, v62\n"
"v_bfi_b32 v61, s8, v62, v61\n"
"v_pk_mad_u16 v62, v28, 2, -1 op_sel_hi:[1,0,0]\n"
"v_pk_mad_u16 v63, v29, 2, -1 op_sel_hi:[1,0,0]\n"
"v_pk_min_u16 v62, v62, v63\n"
"v_pk_mad_u16 v63, v30, 2, -1 op_sel_hi:[1,0,0]\n"
"v_pk_min_u16 v62, v62, v63\n"
"v_pk_mad_u16 v63, v31, 2, -1 op_sel_hi:[1,0,0]\n"
"v_pk_min_u16 v62, v62, v63\n"
"v_pk_sub_u16 v62, s16, v62 clamp\n"
"v_or_b32 v61, v61, v62\n"
"v_cmp_ne_u32 vcc, 0, v61\n"
"s_cbranch_vccnz Lx_468\n"
"Lb_469:\n"
"Lpace_470:\n"
"s_memrealtime s[96:97]\n"
"s_waitcnt lgkmcnt(0)\n"
"s_sub_u32 s96, s96, s98\n"
"s_cmp_ge_u32 s96, 3361\n"
"s_cbranch_scc1 Lpgo_471\n"
"s_sleep 2\n"
"s_branch Lpace_470\n"
"Lpgo_471:\n"
"s_add_u32 s52, s53, 0x142000\n"
"s_and_b32 s52, s52, s54\n"
"buffer_load_dwordx4 a[244:247], v2, s[32:35], s52 offen offset:0\n"
"v_and_b32_dpp v24, v51, v20 row_newbcast:8 row_mask:0xf bank_mask:0xf\n"
"v_and_b32_dpp v25, v51, v21 row_newbcast:8 row_mask:0xf bank_mask:0xf\n"
"v_and_b32_dpp v26, v51, v22 row_newbcast:8 row_mask:0xf bank_mask:0xf\n"
"v_and_b32_dpp v27, v51, v23 row_newbcast:8 row_mask:0xf bank_mask:0xf\n"
"s_waitcnt vmcnt(19)\n"
"s_nop 0\n"
"v_mfma_f32_16x16x32_bf16 v[40:43], v[24:27], a[248:251], v[40:43]\n"
"v_accvgpr_read_b32 v28, a248\n"
"v_accvgpr_read_b32 v29, a249\n"
"v_accvgpr_read_b32 v30, a250\n"
"v_accvgpr_read_b32 v31, a251\n"
"v_pk_sub_u16 v61, v28, s14 clamp\n"
"v_pk_sub_u16 v62, v29, s14 clamp\n"
"v_perm_b32 v178, v62, v61, s9\n"
"v_perm_b32 v61, v62, v61, s10\n"
"v_pk_sub_u16 v62, v30, s14 clamp\n"
"v_pk_sub_u16 v63, v31, s14 clamp\n"
"v_perm_b32 v179, v63, v62, s9\n"
"v_perm_b32 v62, v63, v62, s10\n"
"v_alignbit_b32 v62, v62, v62, 28\n"
"v_bfi_b32 v180, s8, v61, v62\n"
"v_bfi_b32 v61, s8, v62, v61\n"
"v_pk_mad_u16 v62, v28, 2, -1 op_sel_hi:[1,0,0]\n"
"v_pk_mad_u16 v63, v29, 2, -1 op_sel_hi:[1,0,0]\n"
"v_pk_min_u16 v62, v62, v63\n"
"v_pk_mad_u16 v63, v30, 2, -1 op_sel_hi:[1,0,0]\n"
"v_pk_min_u16 v62, v62, v63\n"
"v_pk_mad_u16 v63, v31, 2, -1 op_sel_hi:[1,0,0]\n"
"v_pk_min_u16 v62, v62, v63\n"
"v_pk_sub_u16 v62, s16, v62 clamp\n"
"v_or_b32 v61, v61, v62\n"
"v_cmp_ne_u32 vcc, 0, v61\n"
"s_cbranch_vccnz Lx_472\n"
"Lb_473:\n"
"s_add_u32 s52, s53, 0x149000\n"
"s_and_b32 s52, s52, s54\n"
"buffer_load_dwordx4 a[248:251], v2, s[32:35], s52 offen offset:0\n"
"v_and_b32_dpp v24, v51, v20 row_newbcast:12 row_mask:0xf bank_mask:0xf\n"
"v_and_b32_dpp v25, v51, v21 row_newbcast:12 row_mask:0xf bank_mask:0xf\n"
"v_and_b32_dpp v26, v51, v22 row_newbcast:12 row_mask:0xf bank_mask:0xf\n"
"v_and_b32_dpp v27, v51, v23 row_newbcast:12 row_mask:0xf bank_mask:0xf\n"
"s_waitcnt vmcnt(19)\n"
"s_nop 0\n"
"v_mfma_f32_16x16x32_bf16 v[40:43], v[24:27], a[252:255], v[40:43]\n"
"v_accvgpr_read_b32 v28, a252\n"
"v_accvgpr_read_b32 v29, a253\n"
"v_accvgpr_read_b32 v30, a254\n"
"v_accvgpr_read_b32 v31, a255\n"
"v_pk_sub_u16 v61, v28, s14 clamp\n"
"v_pk_sub_u16 v62, v29, s14 clamp\n"
"v_perm_b32 v181, v62, v61, s9\n"
"v_perm_b32 v61, v62, v61, s10\n"
"v_pk_sub_u16 v62, v30, s14 clamp\n"
"v_pk_sub_u16 v63, v31, s14 clamp\n"
"v_perm_b32 v182, v63, v62, s9\n"
"v_perm_b32 v62, v63, v62, s10\n"
"v_alignbit_b32 v62, v62, v62, 28\n"
"v_bfi_b32 v183, s8, v61, v62\n"
"v_bfi_b32 v61, s8, v62, v61\n"
"v_pk_mad_u16 v62, v28, 2, -1 op_sel_hi:[1,0,0]\n"
"v_pk_mad_u16 v63, v29, 2, -1 op_sel_hi:[1,0,0]\n"
"v_pk_min_u16 v62, v62, v63\n"
"v_pk_mad_u16 v63, v30, 2, -1 op_sel_hi:[1,0,0]\n"
"v_pk_min_u16 v62, v62, v63\n"
"v_pk_mad_u16 v63, v31, 2, -1 op_sel_hi:[1,0,0]\n"
"v_pk_min_u16 v62, v62, v63\n"
"v_pk_sub_u16 v62, s16, v62 clamp\n"
"v_or_b32 v61, v61, v62\n"
"v_cmp_ne_u32 vcc, 0, v61\n"
"s_cbranch_vccnz Lx_474\n"
"Lb_475:\n"
"s_add_u32 s52, s53, 0x18f000\n"
"s_and_b32 s52, s52, s54\n"
"buffer_load_dwordx4 a[252:255], v2, s[32:35], s52 offen offset:0\n"
"v_and_b32_dpp v24, v51, v20 row_newbcast:1 row_mask:0xf bank_mask:0xf\n"
"v_and_b32_dpp v25, v51, v21 row_newbcast:1 row_mask:0xf bank_mask:0xf\n"
"v_and_b32_dpp v26, v51, v22 row_newbcast:1 row_mask:0xf bank_mask:0xf\n"
"v_and_b32_dpp v27, v51, v23 row_newbcast:1 row_mask:0xf bank_mask:0xf\n"
"s_waitcnt vmcnt(19)\n"
"s_nop 0\n"
"v_mfma_f32_16x16x32_bf16 v[40:43], v[24:27], a[216:219], v[40:43]\n"
"v_accvgpr_read_b32 v28, a216\n"
"v_accvgpr_read_b32 v29, a217\n"
"v_accvgpr_read_b32 v30, a218\n"
"v_accvgpr_read_b32 v31, a219\n"
"v_pk_sub_u16 v61, v28, s14 clamp\n"
"v_pk_sub_u16 v62, v29, s14 clamp\n"
"v_perm_b32 v196, v62, v61, s9\n"
"v_perm_b32 v61, v62, v61, s10\n"
"v_pk_sub_u16 v62, v30, s14 clamp\n"
"v_pk_sub_u16 v63, v31, s14 clamp\n"
"v_perm_b32 v197, v63, v62, s9\n"
"v_perm_b32 v62, v63, v62, s10\n"
"v_alignbit_b32 v62, v62, v62, 28\n"
"v_bfi_b32 v198, s8, v61, v62\n"
"v_bfi_b32 v61, s8, v62, v61\n"
"v_pk_mad_u16 v62, v28, 2, -1 op_sel_hi:[1,0,0]\n"
"v_pk_mad_u16 v63, v29, 2, -1 op_sel_hi:[1,0,0]\n"
"v_pk_min_u16 v62, v62, v63\n"
"v_pk_mad_u16 v63, v30, 2, -1 op_sel_hi:[1,0,0]\n"
"v_pk_min_u16 v62, v62, v63\n"
"v_pk_mad_u16 v63, v31, 2, -1 op_sel_hi:[1,0,0]\n"
"v_pk_min_u16 v62, v62, v63\n"
"v_pk_sub_u16 v62, s16, v62 clamp\n"
"v_or_b32 v61, v61, v62\n"
"v_cmp_ne_u32 vcc, 0, v61\n"
"s_cbranch_vccnz Lx_476\n"
"Lb_477:\n"
"s_add_u32 s52, s53, 0x196000\n"
"s_and_b32 s52, s52, s54\n"
"buffer_load_dwordx4 a[216:219], v2, s[32:35], s52 offen offset:0\n"
"v_and_b32_dpp v24, v51, v20 row_newbcast:5 row_mask:0xf bank_mask:0xf\n"
"v_and_b32_dpp v25, v51, v21 row_newbcast:5 row_mask:0xf bank_mask:0xf\n"
"v_and_b32_dpp v26, v51, v22 row_newbcast:5 row_mask:0xf bank_mask:0xf\n"
"v_and_b32_dpp v27, v51, v23 row_newbcast:5 row_mask:0xf bank_mask:0xf\n"
"s_waitcnt vmcnt(19)\n"
"s_nop 0\n"
"v_mfma_f32_16x16x32_bf16 v[40:43], v[24:27], a[220:223], v[40:43]\n"
"v_accvgpr_read_b32 v28, a220\n"
"v_accvgpr_read_b32 v29, a221\n"
"v_accvgpr_read_b32 v30, a222\n"
"v_accvgpr_read_b32 v31, a223\n"
"v_pk_sub_u16 v61, v28, s14 clamp\n"
"v_pk_sub_u16 v62, v29, s14 clamp\n"
"v_perm_b32 v199, v62, v61, s9\n"
"v_perm_b32 v61, v62, v61, s10\n"
"v_pk_sub_u16 v62, v30, s14 clamp\n"
"v_pk_sub_u16 v63, v31, s14 clamp\n"
"v_perm_b32 v200, v63, v62, s9\n"
"v_perm_b32 v62, v63, v62, s10\n"
"v_alignbit_b32 v62, v62, v62, 28\n"
"v_bfi_b32 v201, s8, v61, v62\n"
"v_bfi_b32 v61, s8, v62, v61\n"
"v_pk_mad_u16 v62, v28, 2, -1 op_sel_hi:[1,0,0]\n"
"v_pk_mad_u16 v63, v29, 2, -1 op_sel_hi:[1,0,0]\n"
"v_pk_min_u16 v62, v62, v63\n"
"v_pk_mad_u16 v63, v30, 2, -1 op_sel_hi:[1,0,0]\n"
"v_pk_min_u16 v62, v62, v63\n"
"v_pk_mad_u16 v63, v31, 2, -1 op_sel_hi:[1,0,0]\n"
"v_pk_min_u16 v62, v62, v63\n"
"v_pk_sub_u16 v62, s16, v62 clamp\n"
"v_or_b32 v61, v61, v62\n"
"v_cmp_ne_u32 vcc, 0, v61\n"
"s_cbranch_vccnz Lx_478\n"
"Lb_479:\n"
"Lpace_480:\n"
"s_memrealtime s[96:97]\n"
"s_waitcnt lgkmcnt(0)\n"
"s_sub_u32 s96, s96, s98\n"
"s_cmp_ge_u32 s96, 3417\n"
"s_cbranch_scc1 Lpgo_481\n"
"s_sleep 2\n"
"s_branch Lpace_480\n"
"Lpgo_481:\n"
"s_add_u32 s52, s53, 0x19d000\n"
"s_and_b32 s52, s52, s54\n"
"buffer_load_dwordx4 a[220:223], v2, s[32:35], s52 offen offset:0\n"
"v_and_b32_dpp v24, v51, v20 row_newbcast:9 row_mask:0xf bank_mask:0xf\n"
"v_and_b32_dpp v25, v51, v21 row_newbcast:9 row_mask:0xf bank_mask:0xf\n"
"v_and_b32_dpp v26, v51, v22 row_newbcast:9 row_mask:0xf bank_mask:0xf\n"
"v_and_b32_dpp v27, v51, v23 row_newbcast:9 row_mask:0xf bank_mask:0xf\n"
"s_waitcnt vmcnt(19)\n"
"s_nop 0\n"
"v_mfma_f32_16x16x32_bf16 v[40:43], v[24:27], v[220:223], v[40:43]\n"
"v_pk_sub_u16 v61, v220, s14 clamp\n"
"v_pk_sub_u16 v62, v221, s14 clamp\n"
"v_perm_b32 v202, v62, v61, s9\n"
"v_perm_b32 v61, v62, v61, s10\n"
"v_pk_sub_u16 v62, v222, s14 clamp\n"
"v_pk_sub_u16 v63, v223, s14 clamp\n"
"v_perm_b32 v203, v63, v62, s9\n"
"v_perm_b32 v62, v63, v62, s10\n"
"v_alignbit_b32 v62, v62, v62, 28\n"
"v_bfi_b32 v204, s8, v61, v62\n"
"v_bfi_b32 v61, s8, v62, v61\n"
"v_pk_mad_u16 v62, v220, 2, -1 op_sel_hi:[1,0,0]\n"
"v_pk_mad_u16 v63, v221, 2, -1 op_sel_hi:[1,0,0]\n"
"v_pk_min_u16 v62, v62, v63\n"
"v_pk_mad_u16 v63, v222, 2, -1 op_sel_hi:[1,0,0]\n"
"v_pk_min_u16 v62, v62, v63\n"
"v_pk_mad_u16 v63, v223, 2, -1 op_sel_hi:[1,0,0]\n"
"v_pk_min_u16 v62, v62, v63\n"
"v_pk_sub_u16 v62, s16, v62 clamp\n"
"v_or_b32 v61, v61, v62\n"
"v_cmp_ne_u32 vcc, 0, v61\n"
"s_cbranch_vccnz Lx_482\n"
"Lb_483:\n"
"v_and_b32_dpp v24, v51, v20 row_newbcast:13 row_mask:0xf bank_mask:0xf\n"
"v_and_b32_dpp v25, v51, v21 row_newbcast:13 row_mask:0xf bank_mask:0xf\n"
"v_and_b32_dpp v26, v51, v22 row_newbcast:13 row_mask:0xf bank_mask:0xf\n"
"v_and_b32_dpp v27, v51, v23 row_newbcast:13 row_mask:0xf bank_mask:0xf\n"
"s_waitcnt vmcnt(18)\n"
"s_nop 0\n"
"v_mfma_f32_16x16x32_bf16 v[40:43], v[24:27], v[224:227], v[40:43]\n"
"v_pk_sub_u16 v61, v224, s14 clamp\n"
"v_pk_sub_u16 v62, v225, s14 clamp\n"
"v_perm_b32 v205, v62, v61, s9\n"
"v_perm_b32 v61, v62, v61, s10\n"
"v_pk_sub_u16 v62, v226, s14 clamp\n"
"v_pk_sub_u16 v63, v227, s14 clamp\n"
"v_perm_b32 v206, v63, v62, s9\n"
"v_perm_b32 v62, v63, v62, s10\n"
"v_alignbit_b32 v62, v62, v62, 28\n"
"v_bfi_b32 v207, s8, v61, v62\n"
"v_bfi_b32 v61, s8, v62, v61\n"
"v_pk_mad_u16 v62, v224, 2, -1 op_sel_hi:[1,0,0]\n"
"v_pk_mad_u16 v63, v225, 2, -1 op_sel_hi:[1,0,0]\n"
"v_pk_min_u16 v62, v62, v63\n"
"v_pk_mad_u16 v63, v226, 2, -1 op_sel_hi:[1,0,0]\n"
"v_pk_min_u16 v62, v62, v63\n"
"v_pk_mad_u16 v63, v227, 2, -1 op_sel_hi:[1,0,0]\n"
"v_pk_min_u16 v62, v62, v63\n"
"v_pk_sub_u16 v62, s16, v62 clamp\n"
"v_or_b32 v61, v61, v62\n"
"v_cmp_ne_u32 vcc, 0, v61\n"
"s_cbranch_vccnz Lx_484\n"
"Lb_485:\n"
"v_and_b32_dpp v24, v51, v20 row_newbcast:2 row_mask:0xf bank_mask:0xf\n"
"v_and_b32_dpp v25, v51, v21 row_newbcast:2 row_mask:0xf bank_mask:0xf\n"
"v_and_b32_dpp v26, v51, v22 row_newbcast:2 row_mask:0xf bank_mask:0xf\n"
"v_and_b32_dpp v27, v51, v23 row_newbcast:2 row_mask:0xf bank_mask:0xf\n"
"s_waitcnt vmcnt(17)\n"
"s_nop 0\n"
"v_mfma_f32_16x16x32_bf16 v[40:43], v[24:27], v[228:231], v[40:43]\n"
"v_pk_sub_u16 v61, v228, s14 clamp\n"
"v_pk_sub_u16 v62, v229, s14 clamp\n"
"v_perm_b32 v220, v62, v61, s9\n"
"v_perm_b32 v61, v62, v61, s10\n"
"v_pk_sub_u16 v62, v230, s14 clamp\n"
"v_pk_sub_u16 v63, v231, s14 clamp\n"
"v_perm_b32 v221, v63, v62, s9\n"
"v_perm_b32 v62, v63, v62, s10\n"
"v_alignbit_b32 v62, v62, v62, 28\n"
"v_bfi_b32 v222, s8, v61, v62\n"
"v_bfi_b32 v61, s8, v62, v61\n"
"v_pk_mad_u16 v62, v228, 2, -1 op_sel_hi:[1,0,0]\n"
"v_pk_mad_u16 v63, v229, 2, -1 op_sel_hi:[1,0,0]\n"
"v_pk_min_u16 v62, v62, v63\n"
"v_pk_mad_u16 v63, v230, 2, -1 op_sel_hi:[1,0,0]\n"
"v_pk_min_u16 v62, v62, v63\n"
"v_pk_mad_u16 v63, v231, 2, -1 op_sel_hi:[1,0,0]\n"
"v_pk_min_u16 v62, v62, v63\n"
"v_pk_sub_u16 v62, s16, v62 clamp\n"
"v_or_b32 v61, v61, v62\n"
"v_cmp_ne_u32 vcc, 0, v61\n"
"s_cbranch_vccnz Lx_486\n"
"Lb_487:\n"
"v_and_b32_dpp v24, v51, v20 row_newbcast:6 row_mask:0xf bank_mask:0xf\n"
"v_and_b32_dpp v25, v51, v21 row_newbcast:6 row_mask:0xf bank_mask:0xf\n"
"v_and_b32_dpp v26, v51, v22 row_newbcast:6 row_mask:0xf bank_mask:0xf\n"
"v_and_b32_dpp v27, v51, v23 row_newbcast:6 row_mask:0xf bank_mask:0xf\n"
"s_waitcnt vmcnt(16)\n"
"s_nop 0\n"
"v_mfma_f32_16x16x32_bf16 v[40:43], v[24:27], v[244:247], v[40:43]\n"
"v_pk_sub_u16 v61, v244, s14 clamp\n"
"v_pk_sub_u16 v62, v245, s14 clamp\n"
"v_perm_b32 v223, v62, v61, s9\n"
"v_perm_b32 v61, v62, v61, s10\n"
"v_pk_sub_u16 v62, v246, s14 clamp\n"
"v_pk_sub_u16 v63, v247, s14 clamp\n"
"v_perm_b32 v224, v63, v62, s9\n"
"v_perm_b32 v62, v63, v62, s10\n"
"v_alignbit_b32 v62, v62, v62, 28\n"
"v_bfi_b32 v225, s8, v61, v62\n"
"v_bfi_b32 v61, s8, v62, v61\n"
"v_pk_mad_u16 v62, v244, 2, -1 op_sel_hi:[1,0,0]\n"
"v_pk_mad_u16 v63, v245, 2, -1 op_sel_hi:[1,0,0]\n"
"v_pk_min_u16 v62, v62, v63\n"
"v_pk_mad_u16 v63, v246, 2, -1 op_sel_hi:[1,0,0]\n"
"v_pk_min_u16 v62, v62, v63\n"
"v_pk_mad_u16 v63, v247, 2, -1 op_sel_hi:[1,0,0]\n"
"v_pk_min_u16 v62, v62, v63\n"
"v_pk_sub_u16 v62, s16, v62 clamp\n"
"v_or_b32 v61, v61, v62\n"
"v_cmp_ne_u32 vcc, 0, v61\n"
"s_cbranch_vccnz Lx_488\n"
"Lb_489:\n"
"v_and_b32_dpp v24, v51, v20 row_newbcast:10 row_mask:0xf bank_mask:0xf\n"
"v_and_b32_dpp v25, v51, v21 row_newbcast:10 row_mask:0xf bank_mask:0xf\n"
"v_and_b32_dpp v26, v51, v22 row_newbcast:10 row_mask:0xf bank_mask:0xf\n"
"v_and_b32_dpp v27, v51, v23 row_newbcast:10 row_mask:0xf bank_mask:0xf\n"
"s_waitcnt vmcnt(15)\n"
"s_nop 0\n"
"v_mfma_f32_16x16x32_bf16 v[40:43], v[24:27], v[248:251], v[40:43]\n"
"v_pk_sub_u16 v61, v248, s14 clamp\n"
"v_pk_sub_u16 v62, v249, s14 clamp\n"
"v_perm_b32 v226, v62, v61, s9\n"
"v_perm_b32 v61, v62, v61, s10\n"
"v_pk_sub_u16 v62, v250, s14 clamp\n"
"v_pk_sub_u16 v63, v251, s14 clamp\n"
"v_perm_b32 v227, v63, v62, s9\n"
"v_perm_b32 v62, v63, v62, s10\n"
"v_alignbit_b32 v62, v62, v62, 28\n"
"v_bfi_b32 v228, s8, v61, v62\n"
"v_bfi_b32 v61, s8, v62, v61\n"
"v_pk_mad_u16 v62, v248, 2, -1 op_sel_hi:[1,0,0]\n"
"v_pk_mad_u16 v63, v249, 2, -1 op_sel_hi:[1,0,0]\n"
"v_pk_min_u16 v62, v62, v63\n"
"v_pk_mad_u16 v63, v250, 2, -1 op_sel_hi:[1,0,0]\n"
"v_pk_min_u16 v62, v62, v63\n"
"v_pk_mad_u16 v63, v251, 2, -1 op_sel_hi:[1,0,0]\n"
"v_pk_min_u16 v62, v62, v63\n"
"v_pk_sub_u16 v62, s16, v62 clamp\n"
"v_or_b32 v61, v61, v62\n"
"v_cmp_ne_u32 vcc, 0, v61\n"
"s_cbranch_vccnz Lx_490\n"
"Lb_491:\n"
"v_and_b32_dpp v24, v51, v20 row_newbcast:14 row_mask:0xf bank_mask:0xf\n"
"v_and_b32_dpp v25, v51, v21 row_newbcast:14 row_mask:0xf bank_mask:0xf\n"
"v_and_b32_dpp v26, v51, v22 row_newbcast:14 row_mask:0xf bank_mask:0xf\n"
"v_and_b32_dpp v27, v51, v23 row_newbcast:14 row_mask:0xf bank_mask:0xf\n"
"s_waitcnt vmcnt(14)\n"
"s_nop 0\n"
"v_mfma_f32_16x16x32_bf16 v[40:43], v[24:27], v[252:255], v[40:43]\n"
"v_pk_sub_u16 v61, v252, s14 clamp\n"
"v_pk_sub_u16 v62, v253, s14 clamp\n"
"v_perm_b32 v229, v62, v61, s9\n"
"v_perm_b32 v61, v62, v61, s10\n"
"v_pk_sub_u16 v62, v254, s14 clamp\n"
"v_pk_sub_u16 v63, v255, s14 clamp\n"
"v_perm_b32 v230, v63, v62, s9\n"
"v_perm_b32 v62, v63, v62, s10\n"
"v_alignbit_b32 v62, v62, v62, 28\n"
"v_bfi_b32 v231, s8, v61, v62\n"
"v_bfi_b32 v61, s8, v62, v61\n"
"v_pk_mad_u16 v62, v252, 2, -1 op_sel_hi:[1,0,0]\n"
"v_pk_mad_u16 v63, v253, 2, -1 op_sel_hi:[1,0,0]\n"
"v_pk_min_u16 v62, v62, v63\n"
"v_pk_mad_u16 v63, v254, 2, -1 op_sel_hi:[1,0,0]\n"
"v_pk_min_u16 v62, v62, v63\n"
"v_pk_mad_u16 v63, v255, 2, -1 op_sel_hi:[1,0,0]\n"
"v_pk_min_u16 v62, v62, v63\n"
"v_pk_sub_u16 v62, s16, v62 clamp\n"
"v_or_b32 v61, v61, v62\n"
"v_cmp_ne_u32 vcc, 0, v61\n"
"s_cbranch_vccnz Lx_492\n"
"Lb_493:\n"
"v_and_b32_dpp v24, v51, v20 row_newbcast:3 row_mask:0xf bank_mask:0xf\n"
"v_and_b32_dpp v25, v51, v21 row_newbcast:3 row_mask:0xf bank_mask:0xf\n"
"v_and_b32_dpp v26, v51, v22 row_newbcast:3 row_mask:0xf bank_mask:0xf\n"
"v_and_b32_dpp v27, v51, v23 row_newbcast:3 row_mask:0xf bank_mask:0xf\n"
"s_waitcnt vmcnt(13)\n"
"s_nop 0\n"
"v_mfma_f32_16x16x32_bf16 v[40:43], v[24:27], a[224:227], v[40:43]\n"
"v_accvgpr_read_b32 v28, a224\n"
"v_accvgpr_read_b32 v29, a225\n"
"v_accvgpr_read_b32 v30, a226\n"
"v_accvgpr_read_b32 v31, a227\n"
"v_pk_sub_u16 v61, v28, s14 clamp\n"
"v_pk_sub_u16 v62, v29, s14 clamp\n"
"v_perm_b32 v244, v62, v61, s9\n"
"v_perm_b32 v61, v62, v61, s10\n"
"v_pk_sub_u16 v62, v30, s14 clamp\n"
"v_pk_sub_u16 v63, v31, s14 clamp\n"
"v_perm_b32 v245, v63, v62, s9\n"
"v_perm_b32 v62, v63, v62, s10\n"
"v_alignbit_b32 v62, v62, v62, 28\n"
"v_bfi_b32 v246, s8, v61, v62\n"
"v_bfi_b32 v61, s8, v62, v61\n"
"v_pk_mad_u16 v62, v28, 2, -1 op_sel_hi:[1,0,0]\n"
"v_pk_mad_u16 v63, v29, 2, -1 op_sel_hi:[1,0,0]\n"
"v_pk_min_u16 v62, v62, v63\n"
"v_pk_mad_u16 v63, v30, 2, -1 op_sel_hi:[1,0,0]\n"
"v_pk_min_u16 v62, v62, v63\n"
"v_pk_mad_u16 v63, v31, 2, -1 op_sel_hi:[1,0,0]\n"
"v_pk_min_u16 v62, v62, v63\n"
"v_pk_sub_u16 v62, s16, v62 clamp\n"
"v_or_b32 v61, v61, v62\n"
"v_cmp_ne_u32 vcc, 0, v61\n"
"s_cbranch_vccnz Lx_494\n"
"Lb_495:\n"
"s_add_u32 s52, s53, 0x1a4000\n"
"s_and_b32 s52, s52, s54\n"
"buffer_load_dwordx4 a[224:227], v2, s[32:35], s52 offen offset:0\n"
"v_and_b32_dpp v24, v51, v20 row_newbcast:7 row_mask:0xf bank_mask:0xf\n"
"v_and_b32_dpp v25, v51, v21 row_newbcast:7 row_mask:0xf bank_mask:0xf\n"
"v_and_b32_dpp v26, v51, v22 row_newbcast:7 row_mask:0xf bank_mask:0xf\n"
"v_and_b32_dpp v27, v51, v23 row_newbcast:7 row_mask:0xf bank_mask:0xf\n"
"s_waitcnt vmcnt(13)\n"
"s_nop 0\n"
"v_mfma_f32_16x16x32_bf16 v[40:43], v[24:27], a[228:231], v[40:43]\n"
"v_accvgpr_read_b32 v28, a228\n"
"v_accvgpr_read_b32 v29, a229\n"
"v_accvgpr_read_b32 v30, a230\n"
"v_accvgpr_read_b32 v31, a231\n"
"v_pk_sub_u16 v61, v28, s14 clamp\n"
"v_pk_sub_u16 v62, v29, s14 clamp\n"
"v_perm_b32 v247, v62, v61, s9\n"
"v_perm_b32 v61, v62, v61, s10\n"
"v_pk_sub_u16 v62, v30, s14 clamp\n"
"v_pk_sub_u16 v63, v31, s14 clamp\n"
"v_perm_b32 v248, v63, v62, s9\n"
"v_perm_b32 v62, v63, v62, s10\n"
"v_alignbit_b32 v62, v62, v62, 28\n"
"v_bfi_b32 v249, s8, v61, v62\n"
"v_bfi_b32 v61, s8, v62, v61\n"
"v_pk_mad_u16 v62, v28, 2, -1 op_sel_hi:[1,0,0]\n"
"v_pk_mad_u16 v63, v29, 2, -1 op_sel_hi:[1,0,0]\n"
"v_pk_min_u16 v62, v62, v63\n"
"v_pk_mad_u16 v63, v30, 2, -1 op_sel_hi:[1,0,0]\n"
"v_pk_min_u16 v62, v62, v63\n"
"v_pk_mad_u16 v63, v31, 2, -1 op_sel_hi:[1,0,0]\n"
"v_pk_min_u16 v62, v62, v63\n"
"v_pk_sub_u16 v62, s16, v62 clamp\n"
"v_or_b32 v61, v61, v62\n"
"v_cmp_ne_u32 vcc, 0, v61\n"
"s_cbranch_vccnz Lx_496\n"
"Lb_497:\n"
"s_add_u32 s52, s53, 0x1ab000\n"
"s_and_b32 s52, s52, s54\n"
"buffer_load_dwordx4 a[228:231], v2, s[32:35], s52 offen offset:0\n"
"v_and_b32_dpp v24, v51, v20 row_newbcast:11 row_mask:0xf bank_mask:0xf\n"
"v_and_b32_dpp v25, v51, v21 row_newbcast:11 row_mask:0xf bank_mask:0xf\n"
"v_and_b32_dpp v26, v51, v22 row_newbcast:11 row_mask:0xf bank_mask:0xf\n"
"v_and_b32_dpp v27, v51, v23 row_newbcast:11 row_mask:0xf bank_mask:0xf\n"
"s_waitcnt vmcnt(13)\n"
"s_nop 0\n"
"v_mfma_f32_16x16x32_bf16 v[40:43], v[24:27], a[232:235], v[40:43]\n"
"v_accvgpr_read_b32 v28, a232\n"
"v_accvgpr_read_b32 v29, a233\n"
"v_accvgpr_read_b32 v30, a234\n"
"v_accvgpr_read_b32 v31, a235\n"
"v_pk_sub_u16 v61, v28, s14 clamp\n"
"v_pk_sub_u16 v62, v29, s14 clamp\n"
"v_perm_b32 v250, v62, v61, s9\n"
"v_perm_b32 v61, v62, v61, s10\n"
"v_pk_sub_u16 v62, v30, s14 clamp\n"
"v_pk_sub_u16 v63, v31, s14 clamp\n"
"v_perm_b32 v251, v63, v62, s9\n"
"v_perm_b32 v62, v63, v62, s10\n"
"v_alignbit_b32 v62, v62, v62, 28\n"
"v_bfi_b32 v252, s8, v61, v62\n"
"v_bfi_b32 v61, s8, v62, v61\n"
"v_pk_mad_u16 v62, v28, 2, -1 op_sel_hi:[1,0,0]\n"
"v_pk_mad_u16 v63, v29, 2, -1 op_sel_hi:[1,0,0]\n"
"v_pk_min_u16 v62, v62, v63\n"
"v_pk_mad_u16 v63, v30, 2, -1 op_sel_hi:[1,0,0]\n"
"v_pk_min_u16 v62, v62, v63\n"
"v_pk_mad_u16 v63, v31, 2, -1 op_sel_hi:[1,0,0]\n"
"v_pk_min_u16 v62, v62, v63\n"
"v_pk_sub_u16 v62, s16, v62 clamp\n"
"v_or_b32 v61, v61, v62\n"
"v_cmp_ne_u32 vcc, 0, v61\n"
"s_cbranch_vccnz Lx_498\n"
"Lb_499:\n"
"s_add_u32 s52, s53, 0x1b2000\n"
"s_and_b32 s52, s52, s54\n"
"buffer_load_dwordx4 a[232:235], v2, s[32:35], s52 offen offset:0\n"
"v_and_b32_dpp v24, v51, v20 row_newbcast:15 row_mask:0xf bank_mask:0xf\n"
"v_and_b32_dpp v25, v51, v21 row_newbcast:15 row_mask:0xf bank_mask:0xf\n"
"v_and_b32_dpp v26, v51, v22 row_newbcast:15 row_mask:0xf bank_mask:0xf\n"
"v_and_b32_dpp v27, v51, v23 row_newbcast:15 row_mask:0xf bank_mask:0xf\n"
"s_waitcnt vmcnt(13)\n"
"s_nop 0\n"
"v_mfma_f32_16x16x32_bf16 v[40:43], v[24:27], a[236:239], v[40:43]\n"
"v_accvgpr_read_b32 v28, a236\n"
"v_accvgpr_read_b32 v29, a237\n"
"v_accvgpr_read_b32 v30, a238\n"
"v_accvgpr_read_b32 v31, a239\n"
"v_pk_sub_u16 v61, v28, s14 clamp\n"
"v_pk_sub_u16 v62, v29, s14 clamp\n"
"v_perm_b32 v253, v62, v61, s9\n"
"v_perm_b32 v61, v62, v61, s10\n"
"v_pk_sub_u16 v62, v30, s14 clamp\n"
"v_pk_sub_u16 v63, v31, s14 clamp\n"
"v_perm_b32 v254, v63, v62, s9\n"
"v_perm_b32 v62, v63, v62, s10\n"
"v_alignbit_b32 v62, v62, v62, 28\n"
"v_bfi_b32 v255, s8, v61, v62\n"
"v_bfi_b32 v61, s8, v62, v61\n"
"v_pk_mad_u16 v62, v28, 2, -1 op_sel_hi:[1,0,0]\n"
"v_pk_mad_u16 v63, v29, 2, -1 op_sel_hi:[1,0,0]\n"
"v_pk_min_u16 v62, v62, v63\n"
"v_pk_mad_u16 v63, v30, 2, -1 op_sel_hi:[1,0,0]\n"
"v_pk_min_u16 v62, v62, v63\n"
"v_pk_mad_u16 v63, v31, 2, -1 op_sel_hi:[1,0,0]\n"
"v_pk_min_u16 v62, v62, v63\n"
"v_pk_sub_u16 v62, s16, v62 clamp\n"
"v_or_b32 v61, v61, v62\n"
"v_cmp_ne_u32 vcc, 0, v61\n"
"s_cbranch_vccnz Lx_500\n"
"Lb_501:\n"
"s_add_u32 s52, s53, 0x1b9000\n"
"s_and_b32 s52, s52, s54\n"
"buffer_load_dwordx4 a[236:239], v2, s[32:35], s52 offen offset:0\n"
"v_and_b32_dpp v24, v46, v20 row_newbcast:6 row_mask:0xf bank_mask:0xf\n"
"v_and_b32_dpp v25, v46, v21 row_newbcast:6 row_mask:0xf bank_mask:0xf\n"
"v_and_b32_dpp v26, v46, v22 row_newbcast:6 row_mask:0xf bank_mask:0xf\n"
"v_and_b32_dpp v27, v46, v23 row_newbcast:6 row_mask:0xf bank_mask:0xf\n"
"s_waitcnt vmcnt(13)\n"
"s_nop 0\n"
"v_mfma_f32_16x16x32_bf16 v[40:43], v[24:27], v[4:7], v[40:43]\n"
"s_add_u32 s52, s53, 0x1c0000\n"
"s_and_b32 s52, s52, s54\n"
"buffer_load_dwordx4 v[4:7], v2, s[32:35], s52 offen offset:0\n"
"v_and_b32_dpp v24, v46, v20 row_newbcast:10 row_mask:0xf bank_mask:0xf\n"
"v_and_b32_dpp v25, v46, v21 row_newbcast:10 row_mask:0xf bank_mask:0xf\n"
"v_and_b32_dpp v26, v46, v22 row_newbcast:10 row_mask:0xf bank_mask:0xf\n"
"v_and_b32_dpp v27, v46, v23 row_newbcast:10 row_mask:0xf bank_mask:0xf\n"
"s_waitcnt vmcnt(13)\n"
"s_nop 0\n"
"v_mfma_f32_16x16x32_bf16 v[40:43], v[24:27], v[8:11], v[40:43]\n"
"s_add_u32 s52, s53, 0x1c7000\n"
"s_and_b32 s52, s52, s54\n"
"buffer_load_dwordx4 v[8:11], v2, s[32:35], s52 offen offset:0\n"
"v_and_b32_dpp v24, v46, v20 row_newbcast:14 row_mask:0xf bank_mask:0xf\n"
"v_and_b32_dpp v25, v46, v21 row_newbcast:14 row_mask:0xf bank_mask:0xf\n"
"v_and_b32_dpp v26, v46, v22 row_newbcast:14 row_mask:0xf bank_mask:0xf\n"
"v_and_b32_dpp v27, v46, v23 row_newbcast:14 row_mask:0xf bank_mask:0xf\n"
"s_waitcnt vmcnt(13)\n"
"s_nop 0\n"
"v_mfma_f32_16x16x32_bf16 v[40:43], v[24:27], v[12:15], v[40:43]\n"
"s_add_u32 s52, s53, 0x1ce000\n"
"s_and_b32 s52, s52, s54\n"
"buffer_load_dwordx4 v[12:15], v2, s[32:35], s52 offen offset:0\n"
"v_and_b32_dpp v24, v46, v20 row_newbcast:3 row_mask:0xf bank_mask:0xf\n"
"v_and_b32_dpp v25, v46, v21 row_newbcast:3 row_mask:0xf bank_mask:0xf\n"
"v_and_b32_dpp v26, v46, v22 row_newbcast:3 row_mask:0xf bank_mask:0xf\n"
"v_and_b32_dpp v27, v46, v23 row_newbcast:3 row_mask:0xf bank_mask:0xf\n"
"s_waitcnt vmcnt(13)\n"
"s_nop 0\n"
"v_mfma_f32_16x16x32_bf16 v[40:43], v[24:27], v[16:19], v[40:43]\n"
"s_add_u32 s52, s53, 0x1d5000\n"
"s_and_b32 s52, s52, s54\n"
"buffer_load_dwordx4 v[16:19], v2, s[32:35], s52 offen offset:0\n"
"v_and_b32_dpp v24, v46, v20 row_newbcast:7 row_mask:0xf bank_mask:0xf\n"
"v_and_b32_dpp v25, v46, v21 row_newbcast:7 row_mask:0xf bank_mask:0xf\n"
"v_and_b32_dpp v26, v46, v22 row_newbcast:7 row_mask:0xf bank_mask:0xf\n"
"v_and_b32_dpp v27, v46, v23 row_newbcast:7 row_mask:0xf bank_mask:0xf\n"
"s_waitcnt vmcnt(13)\n"
"s_nop 0\n"
"v_mfma_f32_16x16x32_bf16 v[40:43], v[24:27], a[240:243], v[40:43]\n"
"s_add_u32 s52, s53, 0x1dc000\n"
"s_and_b32 s52, s52, s54\n"
"buffer_load_dwordx4 a[240:243], v2, s[32:35], s52 offen offset:0\n"
"v_and_b32_dpp v24, v46, v20 row_newbcast:11 row_mask:0xf bank_mask:0xf\n"
"v_and_b32_dpp v25, v46, v21 row_newbcast:11 row_mask:0xf bank_mask:0xf\n"
"v_and_b32_dpp v26, v46, v22 row_newbcast:11 row_mask:0xf bank_mask:0xf\n"
"v_and_b32_dpp v27, v46, v23 row_newbcast:11 row_mask:0xf bank_mask:0xf\n"
"s_waitcnt vmcnt(13)\n"
"s_nop 0\n"
"v_mfma_f32_16x16x32_bf16 v[40:43], v[24:27], a[244:247], v[40:43]\n"
"s_add_u32 s52, s53, 0x1e3000\n"
"s_and_b32 s52, s52, s54\n"
"buffer_load_dwordx4 a[244:247], v2, s[32:35], s52 offen offset:0\n"
"v_and_b32_dpp v24, v46, v20 row_newbcast:15 row_mask:0xf bank_mask:0xf\n"
"v_and_b32_dpp v25, v46, v21 row_newbcast:15 row_mask:0xf bank_mask:0xf\n"
"v_and_b32_dpp v26, v46, v22 row_newbcast:15 row_mask:0xf bank_mask:0xf\n"
"v_and_b32_dpp v27, v46, v23 row_newbcast:15 row_mask:0xf bank_mask:0xf\n"
"s_waitcnt vmcnt(13)\n"
"s_nop 0\n"
"v_mfma_f32_16x16x32_bf16 v[40:43], v[24:27], a[248:251], v[40:43]\n"
"s_add_u32 s52, s53, 0x1ea000\n"
"s_and_b32 s52, s52, s54\n"
"buffer_load_dwordx4 a[248:251], v2, s[32:35], s52 offen offset:0\n"
"v_and_b32_dpp v24, v47, v20 row_newbcast:6 row_mask:0xf bank_mask:0xf\n"
"v_and_b32_dpp v25, v47, v21 row_newbcast:6 row_mask:0xf bank_mask:0xf\n"
"v_and_b32_dpp v26, v47, v22 row_newbcast:6 row_mask:0xf bank_mask:0xf\n"
"v_and_b32_dpp v27, v47, v23 row_newbcast:6 row_mask:0xf bank_mask:0xf\n"
"s_waitcnt vmcnt(13)\n"
"s_nop 0\n"
"v_mfma_f32_16x16x32_bf16 v[40:43], v[24:27], a[252:255], v[40:43]\n"
"s_add_u32 s52, s53, 0x230000\n"
"s_and_b32 s52, s52, s54\n"
"buffer_load_dwordx4 a[252:255], v2, s[32:35], s52 offen offset:0\n"
"v_and_b32_dpp v24, v47, v20 row_newbcast:10 row_mask:0xf bank_mask:0xf\n"
"v_and_b32_dpp v25, v47, v21 row_newbcast:10 row_mask:0xf bank_mask:0xf\n"
"v_and_b32_dpp v26, v47, v22 row_newbcast:10 row_mask:0xf bank_mask:0xf\n"
"v_and_b32_dpp v27, v47, v23 row_newbcast:10 row_mask:0xf bank_mask:0xf\n"
"s_waitcnt vmcnt(13)\n"
"s_nop 0\n"
"v_mfma_f32_16x16x32_bf16 v[40:43], v[24:27], a[216:219], v[40:43]\n"
"s_add_u32 s52, s53, 0x237000\n"
"s_and_b32 s52, s52, s54\n"
"buffer_load_dwordx4 a[216:219], v2, s[32:35], s52 offen offset:0\n"
"v_and_b32_dpp v24, v47, v20 row_newbcast:14 row_mask:0xf bank_mask:0xf\n"
"v_and_b32_dpp v25, v47, v21 row_newbcast:14 row_mask:0xf bank_mask:0xf\n"
"v_and_b32_dpp v26, v47, v22 row_newbcast:14 row_mask:0xf bank_mask:0xf\n"
"v_and_b32_dpp v27, v47, v23 row_newbcast:14 row_mask:0xf bank_mask:0xf\n"
"s_waitcnt vmcnt(13)\n"
"s_nop 0\n"
"v_mfma_f32_16x16x32_bf16 v[40:43], v[24:27], a[220:223], v[40:43]\n"
"s_add_u32 s52, s53, 0x23e000\n"
"s_and_b32 s52, s52, s54\n"
"buffer_load_dwordx4 a[220:223], v2, s[32:35], s52 offen offset:0\n"
"v_and_b32_dpp v24, v47, v20 row_newbcast:3 row_mask:0xf bank_mask:0xf\n"
"v_and_b32_dpp v25, v47, v21 row_newbcast:3 row_mask:0xf bank_mask:0xf\n"
"v_and_b32_dpp v26, v47, v22 row_newbcast:3 row_mask:0xf bank_mask:0xf\n"
"v_and_b32_dpp v27, v47, v23 row_newbcast:3 row_mask:0xf bank_mask:0xf\n"
"s_waitcnt vmcnt(13)\n"
"s_nop 0\n"
"v_mfma_f32_16x16x32_bf16 v[40:43], v[24:27], a[224:227], v[40:43]\n"
"s_add_u32 s52, s53, 0x245000\n"
"s_and_b32 s52, s52, s54\n"
"buffer_load_dwordx4 a[224:227], v2, s[32:35], s52 offen offset:0\n"
"v_and_b32_dpp v24, v47, v20 row_newbcast:7 row_mask:0xf bank_mask:0xf\n"
"v_and_b32_dpp v25, v47, v21 row_newbcast:7 row_mask:0xf bank_mask:0xf\n"
"v_and_b32_dpp v26, v47, v22 row_newbcast:7 row_mask:0xf bank_mask:0xf\n"
"v_and_b32_dpp v27, v47, v23 row_newbcast:7 row_mask:0xf bank_mask:0xf\n"
"s_waitcnt vmcnt(13)\n"
"s_nop 0\n"
"v_mfma_f32_16x16x32_bf16 v[40:43], v[24:27], a[228:231], v[40:43]\n"
"s_add_u32 s52, s53, 0x24c000\n"
"s_and_b32 s52, s52, s54\n"
"buffer_load_dwordx4 a[228:231], v2, s[32:35], s52 offen offset:0\n"
"v_and_b32_dpp v24, v47, v20 row_newbcast:11 row_mask:0xf bank_mask:0xf\n"
"v_and_b32_dpp v25, v47, v21 row_newbcast:11 row_mask:0xf bank_mask:0xf\n"
"v_and_b32_dpp v26, v47, v22 row_newbcast:11 row_mask:0xf bank_mask:0xf\n"
"v_and_b32_dpp v27, v47, v23 row_newbcast:11 row_mask:0xf bank_mask:0xf\n"
"s_waitcnt vmcnt(13)\n"
"s_nop 0\n"
"v_mfma_f32_16x16x32_bf16 v[40:43], v[24:27], a[232:235], v[40:43]\n"
"s_add_u32 s52, s53, 0x253000\n"
"s_and_b32 s52, s52, s54\n"
"buffer_load_dwordx4 a[232:235], v2, s[32:35], s52 offen offset:0\n"
"v_and_b32_dpp v24, v47, v20 row_newbcast:15 row_mask:0xf bank_mask:0xf\n"
"v_and_b32_dpp v25, v47, v21 row_newbcast:15 row_mask:0xf bank_mask:0xf\n"
"v_and_b32_dpp v26, v47, v22 row_newbcast:15 row_mask:0xf bank_mask:0xf\n"
"v_and_b32_dpp v27, v47, v23 row_newbcast:15 row_mask:0xf bank_mask:0xf\n"
"s_waitcnt vmcnt(13)\n"
"s_nop 0\n"
"v_mfma_f32_16x16x32_bf16 v[40:43], v[24:27], a[236:239], v[40:43]\n"
"s_add_u32 s52, s53, 0x25a000\n"
"s_and_b32 s52, s52, s54\n"
"buffer_load_dwordx4 a[236:239], v2, s[32:35], s52 offen offset:0\n"
"v_and_b32_dpp v24, v48, v20 row_newbcast:0 row_mask:0xf bank_mask:0xf\n"
"v_and_b32_dpp v25, v48, v21 row_newbcast:0 row_mask:0xf bank_mask:0xf\n"
"v_and_b32_dpp v26, v48, v22 row_newbcast:0 row_mask:0xf bank_mask:0xf\n"
"v_and_b32_dpp v27, v48, v23 row_newbcast:0 row_mask:0xf bank_mask:0xf\n"
"s_waitcnt vmcnt(13)\n"
"s_nop 0\n"
"v_mfma_f32_16x16x32_bf16 v[40:43], v[24:27], v[4:7], v[40:43]\n"
"s_add_u32 s52, s53, 0x3b8000\n"
"s_and_b32 s52, s52, s54\n"
"buffer_load_dwordx4 v[4:7], v2, s[32:35], s52 offen offset:0 nt\n"
"v_and_b32_dpp v24, v48, v20 row_newbcast:4 row_mask:0xf bank_mask:0xf\n"
"v_and_b32_dpp v25, v48, v21 row_newbcast:4 row_mask:0xf bank_mask:0xf\n"
"v_and_b32_dpp v26, v48, v22 row_newbcast:4 row_mask:0xf bank_mask:0xf\n"
"v_and_b32_dpp v27, v48, v23 row_newbcast:4 row_mask:0xf bank_mask:0xf\n"
"s_waitcnt vmcnt(13)\n"
"s_nop 0\n"
"v_mfma_f32_16x16x32_bf16 v[40:43], v[24:27], v[8:11], v[40:43]\n"
"s_add_u32 s52, s53, 0x3bf000\n"
"s_and_b32 s52, s52, s54\n"
"buffer_load_dwordx4 v[8:11], v2, s[32:35], s52 offen offset:0 nt\n"
"v_and_b32_dpp v24, v48, v20 row_newbcast:8 row_mask:0xf bank_mask:0xf\n"
"v_and_b32_dpp v25, v48, v21 row_newbcast:8 row_mask:0xf bank_mask:0xf\n"
"v_and_b32_dpp v26, v48, v22 row_newbcast:8 row_mask:0xf bank_mask:0xf\n"
"v_and_b32_dpp v27, v48, v23 row_newbcast:8 row_mask:0xf bank_mask:0xf\n"
"s_waitcnt vmcnt(13)\n"
"s_nop 0\n"
"v_mfma_f32_16x16x32_bf16 v[40:43], v[24:27], v[12:15], v[40:43]\n"
"s_add_u32 s52, s53, 0x3c6000\n"
"s_and_b32 s52, s52, s54\n"
"buffer_load_dwordx4 v[12:15], v2, s[32:35], s52 offen offset:0 nt\n"
"v_and_b32_dpp v24, v48, v20 row_newbcast:12 row_mask:0xf bank_mask:0xf\n"
"v_and_b32_dpp v25, v48, v21 row_newbcast:12 row_mask:0xf bank_mask:0xf\n"
"v_and_b32_dpp v26, v48, v22 row_newbcast:12 row_mask:0xf bank_mask:0xf\n"
"v_and_b32_dpp v27, v48, v23 row_newbcast:12 row_mask:0xf bank_mask:0xf\n"
"s_waitcnt vmcnt(13)\n"
"s_nop 0\n"
"v_mfma_f32_16x16x32_bf16 v[40:43], v[24:27], v[16:19], v[40:43]\n"
"s_add_u32 s52, s53, 0x3cd000\n"
"s_and_b32 s52, s52, s54\n"
"buffer_load_dwordx4 v[16:19], v2, s[32:35], s52 offen offset:0 nt\n"
"v_and_b32_dpp v24, v48, v20 row_newbcast:1 row_mask:0xf bank_mask:0xf\n"
"v_and_b32_dpp v25, v48, v21 row_newbcast:1 row_mask:0xf bank_mask:0xf\n"
"v_and_b32_dpp v26, v48, v22 row_newbcast:1 row_mask:0xf bank_mask:0xf\n"
"v_and_b32_dpp v27, v48, v23 row_newbcast:1 row_mask:0xf bank_mask:0xf\n"
"s_waitcnt vmcnt(13)\n"
"s_nop 0\n"
"v_mfma_f32_16x16x32_bf16 v[40:43], v[24:27], a[240:243], v[40:43]\n"
"s_add_u32 s52, s53, 0x39c000\n"
"s_and_b32 s52, s52, s54\n"
"buffer_load_dwordx4 a[240:243], v2, s[32:35], s52 offen offset:0 nt\n"
"v_and_b32_dpp v24, v48, v20 row_newbcast:5 row_mask:0xf bank_mask:0xf\n"
"v_and_b32_dpp v25, v48, v21 row_newbcast:5 row_mask:0xf bank_mask:0xf\n"
"v_and_b32_dpp v26, v48, v22 row_newbcast:5 row_mask:0xf bank_mask:0xf\n"
"v_and_b32_dpp v27, v48, v23 row_newbcast:5 row_mask:0xf bank_mask:0xf\n"
"s_waitcnt vmcnt(13)\n"
"s_nop 0\n"
"v_mfma_f32_16x16x32_bf16 v[40:43], v[24:27], a[244:247], v[40:43]\n"
"s_add_u32 s52, s53, 0x3a3000\n"
"s_and_b32 s52, s52, s54\n"
"buffer_load_dwordx4 a[244:247], v2, s[32:35], s52 offen offset:0 nt\n"
"v_and_b32_dpp v24, v48, v20 row_newbcast:9 row_mask:0xf bank_mask:0xf\n"
"v_and_b32_dpp v25, v48, v21 row_newbcast:9 row_mask:0xf bank_mask:0xf\n"
"v_and_b32_dpp v26, v48, v22 row_newbcast:9 row_mask:0xf bank_mask:0xf\n"
"v_and_b32_dpp v27, v48, v23 row_newbcast:9 row_mask:0xf bank_mask:0xf\n"
"s_waitcnt vmcnt(13)\n"
"s_nop 0\n"
"v_mfma_f32_16x16x32_bf16 v[40:43], v[24:27], a[248:251], v[40:43]\n"
"s_add_u32 s52, s53, 0x3aa000\n"
"s_and_b32 s52, s52, s54\n"
"buffer_load_dwordx4 a[248:251], v2, s[32:35], s52 offen offset:0 nt\n"
"v_and_b32_dpp v24, v49, v20 row_newbcast:0 row_mask:0xf bank_mask:0xf\n"
"v_and_b32_dpp v25, v49, v21 row_newbcast:0 row_mask:0xf bank_mask:0xf\n"
"v_and_b32_dpp v26, v49, v22 row_newbcast:0 row_mask:0xf bank_mask:0xf\n"
"v_and_b32_dpp v27, v49, v23 row_newbcast:0 row_mask:0xf bank_mask:0xf\n"
"s_waitcnt vmcnt(13)\n"
"s_nop 0\n"
"v_mfma_f32_16x16x32_bf16 v[40:43], v[24:27], a[252:255], v[40:43]\n"
"s_add_u32 s52, s53, 0x3b1000\n"
"s_and_b32 s52, s52, s54\n"
"buffer_load_dwordx4 a[252:255], v2, s[32:35], s52 offen offset:0 nt\n"
"v_and_b32_dpp v24, v49, v20 row_newbcast:4 row_mask:0xf bank_mask:0xf\n"
"v_and_b32_dpp v25, v49, v21 row_newbcast:4 row_mask:0xf bank_mask:0xf\n"
"v_and_b32_dpp v26, v49, v22 row_newbcast:4 row_mask:0xf bank_mask:0xf\n"
"v_and_b32_dpp v27, v49, v23 row_newbcast:4 row_mask:0xf bank_mask:0xf\n"
"s_waitcnt vmcnt(13)\n"
"s_nop 0\n"
"v_mfma_f32_16x16x32_bf16 v[40:43], v[24:27], a[216:219], v[40:43]\n"
"s_add_u32 s52, s53, 0x1f1000\n"
"s_and_b32 s52, s52, s54\n"
"buffer_load_dwordx4 a[216:219], v2, s[32:35], s52 offen offset:0 nt\n"
"v_and_b32_dpp v24, v49, v20 row_newbcast:8 row_mask:0xf bank_mask:0xf\n"
"v_and_b32_dpp v25, v49, v21 row_newbcast:8 row_mask:0xf bank_mask:0xf\n"
"v_and_b32_dpp v26, v49, v22 row_newbcast:8 row_mask:0xf bank_mask:0xf\n"
"v_and_b32_dpp v27, v49, v23 row_newbcast:8 row_mask:0xf bank_mask:0xf\n"
"s_waitcnt vmcnt(13)\n"
"s_nop 0\n"
"v_mfma_f32_16x16x32_bf16 v[40:43], v[24:27], a[220:223], v[40:43]\n"
"s_add_u32 s52, s53, 0x261000\n"
"s_and_b32 s52, s52, s54\n"
"buffer_load_dwordx4 a[220:223], v2, s[32:35], s52 offen offset:0 nt\n"
"v_and_b32_dpp v24, v49, v20 row_newbcast:12 row_mask:0xf bank_mask:0xf\n"
"v_and_b32_dpp v25, v49, v21 row_newbcast:12 row_mask:0xf bank_mask:0xf\n"
"v_and_b32_dpp v26, v49, v22 row_newbcast:12 row_mask:0xf bank_mask:0xf\n"
"v_and_b32_dpp v27, v49, v23 row_newbcast:12 row_mask:0xf bank_mask:0xf\n"
"s_waitcnt vmcnt(13)\n"
"s_nop 0\n"
"v_mfma_f32_16x16x32_bf16 v[40:43], v[24:27], a[224:227], v[40:43]\n"
"s_add_u32 s52, s53, 0x380000\n"
"s_and_b32 s52, s52, s54\n"
"buffer_load_dwordx4 a[224:227], v2, s[32:35], s52 offen offset:0 nt\n"
"v_and_b32_dpp v24, v49, v20 row_newbcast:1 row_mask:0xf bank_mask:0xf\n"
"v_and_b32_dpp v25, v49, v21 row_newbcast:1 row_mask:0xf bank_mask:0xf\n"
"v_and_b32_dpp v26, v49, v22 row_newbcast:1 row_mask:0xf bank_mask:0xf\n"
"v_and_b32_dpp v27, v49, v23 row_newbcast:1 row_mask:0xf bank_mask:0xf\n"
"s_waitcnt vmcnt(13)\n"
"s_nop 0\n"
"v_mfma_f32_16x16x32_bf16 v[40:43], v[24:27], a[228:231], v[40:43]\n"
"s_add_u32 s52, s53, 0x387000\n"
"s_and_b32 s52, s52, s54\n"
"buffer_load_dwordx4 a[228:231], v2, s[32:35], s52 offen offset:0 nt\n"
"v_and_b32_dpp v24, v49, v20 row_newbcast:5 row_mask:0xf bank_mask:0xf\n"
"v_and_b32_dpp v25, v49, v21 row_newbcast:5 row_mask:0xf bank_mask:0xf\n"
"v_and_b32_dpp v26, v49, v22 row_newbcast:5 row_mask:0xf bank_mask:0xf\n"
"v_and_b32_dpp v27, v49, v23 row_newbcast:5 row_mask:0xf bank_mask:0xf\n"
"s_waitcnt vmcnt(13)\n"
"s_nop 0\n"
"v_mfma_f32_16x16x32_bf16 v[40:43], v[24:27], a[232:235], v[40:43]\n"
"s_add_u32 s52, s53, 0x38e000\n"
"s_and_b32 s52, s52, s54\n"
"buffer_load_dwordx4 a[232:235], v2, s[32:35], s52 offen offset:0 nt\n"
"v_and_b32_dpp v24, v49, v20 row_newbcast:9 row_mask:0xf bank_mask:0xf\n"
"v_and_b32_dpp v25, v49, v21 row_newbcast:9 row_mask:0xf bank_mask:0xf\n"
"v_and_b32_dpp v26, v49, v22 row_newbcast:9 row_mask:0xf bank_mask:0xf\n"
"v_and_b32_dpp v27, v49, v23 row_newbcast:9 row_mask:0xf bank_mask:0xf\n"
"s_waitcnt vmcnt(13)\n"
"s_nop 0\n"
"v_mfma_f32_16x16x32_bf16 v[40:43], v[24:27], a[236:239], v[40:43]\n"
"s_add_u32 s52, s53, 0x395000\n"
"s_and_b32 s52, s52, s54\n"
"buffer_load_dwordx4 a[236:239], v2, s[32:35], s52 offen offset:0 nt\n"
"v_and_b32_dpp v24, v52, v20 row_newbcast:2 row_mask:0xf bank_mask:0xf\n"
"v_and_b32_dpp v25, v52, v21 row_newbcast:2 row_mask:0xf bank_mask:0xf\n"
"v_and_b32_dpp v26, v52, v22 row_newbcast:2 row_mask:0xf bank_mask:0xf\n"
"v_and_b32_dpp v27, v52, v23 row_newbcast:2 row_mask:0xf bank_mask:0xf\n"
"s_waitcnt vmcnt(13)\n"
"s_nop 0\n"
"v_mfma_f32_16x16x32_bf16 v[40:43], v[24:27], v[4:7], v[40:43]\n"
"v_and_b32_dpp v24, v52, v20 row_newbcast:6 row_mask:0xf bank_mask:0xf\n"
"v_and_b32_dpp v25, v52, v21 row_newbcast:6 row_mask:0xf bank_mask:0xf\n"
"v_and_b32_dpp v26, v52, v22 row_newbcast:6 row_mask:0xf bank_mask:0xf\n"
"v_and_b32_dpp v27, v52, v23 row_newbcast:6 row_mask:0xf bank_mask:0xf\n"
"s_waitcnt vmcnt(12)\n"
"s_nop 0\n"
"v_mfma_f32_16x16x32_bf16 v[40:43], v[24:27], v[8:11], v[40:43]\n"
"v_and_b32_dpp v24, v52, v20 row_newbcast:10 row_mask:0xf bank_mask:0xf\n"
"v_and_b32_dpp v25, v52, v21 row_newbcast:10 row_mask:0xf bank_mask:0xf\n"
"v_and_b32_dpp v26, v52, v22 row_newbcast:10 row_mask:0xf bank_mask:0xf\n"
"v_and_b32_dpp v27, v52, v23 row_newbcast:10 row_mask:0xf bank_mask:0xf\n"
"s_waitcnt vmcnt(11)\n"
"s_nop 0\n"
"v_mfma_f32_16x16x32_bf16 v[40:43], v[24:27], v[12:15], v[40:43]\n"
"v_and_b32_dpp v24, v52, v20 row_newbcast:14 row_mask:0xf bank_mask:0xf\n"
"v_and_b32_dpp v25, v52, v21 row_newbcast:14 row_mask:0xf bank_mask:0xf\n"
"v_and_b32_dpp v26, v52, v22 row_newbcast:14 row_mask:0xf bank_mask:0xf\n"
"v_and_b32_dpp v27, v52, v23 row_newbcast:14 row_mask:0xf bank_mask:0xf\n"
"s_waitcnt vmcnt(10)\n"
"s_nop 0\n"
"v_mfma_f32_16x16x32_bf16 v[40:43], v[24:27], v[16:19], v[40:43]\n"
"v_and_b32_dpp v24, v52, v20 row_newbcast:1 row_mask:0xf bank_mask:0xf\n"
"v_and_b32_dpp v25, v52, v21 row_newbcast:1 row_mask:0xf bank_mask:0xf\n"
"v_and_b32_dpp v26, v52, v22 row_newbcast:1 row_mask:0xf bank_mask:0xf\n"
"v_and_b32_dpp v27, v52, v23 row_newbcast:1 row_mask:0xf bank_mask:0xf\n"
"s_waitcnt vmcnt(9)\n"
"s_nop 0\n"
"v_mfma_f32_16x16x32_bf16 v[40:43], v[24:27], a[240:243], v[40:43]\n"
"v_and_b32_dpp v24, v52, v20 row_newbcast:5 row_mask:0xf bank_mask:0xf\n"
"v_and_b32_dpp v25, v52, v21 row_newbcast:5 row_mask:0xf bank_mask:0xf\n"
"v_and_b32_dpp v26, v52, v22 row_newbcast:5 row_mask:0xf bank_mask:0xf\n"
"v_and_b32_dpp v27, v52, v23 row_newbcast:5 row_mask:0xf bank_mask:0xf\n"
"s_waitcnt vmcnt(8)\n"
"s_nop 0\n"
"v_mfma_f32_16x16x32_bf16 v[40:43], v[24:27], a[244:247], v[40:43]\n"
"v_and_b32_dpp v24, v52, v20 row_newbcast:9 row_mask:0xf bank_mask:0xf\n"
"v_and_b32_dpp v25, v52, v21 row_newbcast:9 row_mask:0xf bank_mask:0xf\n"
"v_and_b32_dpp v26, v52, v22 row_newbcast:9 row_mask:0xf bank_mask:0xf\n"
"v_and_b32_dpp v27, v52, v23 row_newbcast:9 row_mask:0xf bank_mask:0xf\n"
"s_waitcnt vmcnt(7)\n"
"s_nop 0\n"
"v_mfma_f32_16x16x32_bf16 v[40:43], v[24:27], a[248:251], v[40:43]\n"
"v_and_b32_dpp v24, v52, v20 row_newbcast:13 row_mask:0xf bank_mask:0xf\n"
"v_and_b32_dpp v25, v52, v21 row_newbcast:13 row_mask:0xf bank_mask:0xf\n"
"v_and_b32_dpp v26, v52, v22 row_newbcast:13 row_mask:0xf bank_mask:0xf\n"
"v_and_b32_dpp v27, v52, v23 row_newbcast:13 row_mask:0xf bank_mask:0xf\n"
"s_waitcnt vmcnt(6)\n"
"s_nop 0\n"
"v_mfma_f32_16x16x32_bf16 v[40:43], v[24:27], a[252:255], v[40:43]\n"
"v_min_u32 v62, 32, v0\n"
"v_lshlrev_b32 v62, 4, v62\n"
"v_mov_b32 v61, 0x7ff00000\n"
"v_cmp_gt_u32 vcc, 33, v0\n"
"s_nop 1\n"
"v_cndmask_b32 v61, v61, v62, vcc\n"
"v_add_u32 v62, 159744, v62\n"
"v_mov_b32 v63, 160256\n"
"s_mul_i32 s70, s48, 32768\n"
"s_mul_i32 s56, s49, 640\n"
"s_add_u32 s70, s70, s56\n"
"s_add_u32 s70, s70, 0x100000\n"
"s_add_u32 s70, s70, s99\n"
"v_and_b32 v58, 15, v0\n"
"v_lshlrev_b32 v58, 5, v58\n"
"v_lshrrev_b32 v59, 4, v0\n"
"v_lshl_add_u32 v58, v59, 4, v58\n"
"s_mul_i32 s56, s50, 576\n"
"s_add_u32 s56, s56, 159744\n"
"v_add_u32 v58, s56, v58\n"
"v_mov_b32 v60, v58\n"
"v_and_b32_dpp v24, v48, v20 row_newbcast:13 row_mask:0xf bank_mask:0xf\n"
"v_and_b32_dpp v25, v48, v21 row_newbcast:13 row_mask:0xf bank_mask:0xf\n"
"v_and_b32_dpp v26, v48, v22 row_newbcast:13 row_mask:0xf bank_mask:0xf\n"
"v_and_b32_dpp v27, v48, v23 row_newbcast:13 row_mask:0xf bank_mask:0xf\n"
"s_waitcnt vmcnt(5)\n"
"s_nop 0\n"
"v_mfma_f32_16x16x32_bf16 v[40:43], v[24:27], a[216:219], v[40:43]\n"
"v_and_b32_dpp v24, v49, v20 row_newbcast:13 row_mask:0xf bank_mask:0xf\n"
"v_and_b32_dpp v25, v49, v21 row_newbcast:13 row_mask:0xf bank_mask:0xf\n"
"v_and_b32_dpp v26, v49, v22 row_newbcast:13 row_mask:0xf bank_mask:0xf\n"
"v_and_b32_dpp v27, v49, v23 row_newbcast:13 row_mask:0xf bank_mask:0xf\n"
"s_waitcnt vmcnt(4)\n"
"s_nop 0\n"
"v_mfma_f32_16x16x32_bf16 v[40:43], v[24:27], a[220:223], v[40:43]\n"
"v_and_b32_dpp v24, v52, v20 row_newbcast:0 row_mask:0xf bank_mask:0xf\n"
"v_and_b32_dpp v25, v52, v21 row_newbcast:0 row_mask:0xf bank_mask:0xf\n"
"v_and_b32_dpp v26, v52, v22 row_newbcast:0 row_mask:0xf bank_mask:0xf\n"
"v_and_b32_dpp v27, v52, v23 row_newbcast:0 row_mask:0xf bank_mask:0xf\n"
"s_waitcnt vmcnt(3)\n"
"s_nop 0\n"
"v_mfma_f32_16x16x32_bf16 v[40:43], v[24:27], a[224:227], v[40:43]\n"
"v_and_b32_dpp v24, v52, v20 row_newbcast:4 row_mask:0xf bank_mask:0xf\n"
"v_and_b32_dpp v25, v52, v21 row_newbcast:4 row_mask:0xf bank_mask:0xf\n"
"v_and_b32_dpp v26, v52, v22 row_newbcast:4 row_mask:0xf bank_mask:0xf\n"
"v_and_b32_dpp v27, v52, v23 row_newbcast:4 row_mask:0xf bank_mask:0xf\n"
"s_waitcnt vmcnt(2)\n"
"s_nop 0\n"
"v_mfma_f32_16x16x32_bf16 v[40:43], v[24:27], a[228:231], v[40:43]\n"
"v_and_b32_dpp v24, v52, v20 row_newbcast:8 row_mask:0xf bank_mask:0xf\n"
"v_and_b32_dpp v25, v52, v21 row_newbcast:8 row_mask:0xf bank_mask:0xf\n"
"v_and_b32_dpp v26, v52, v22 row_newbcast:8 row_mask:0xf bank_mask:0xf\n"
"v_and_b32_dpp v27, v52, v23 row_newbcast:8 row_mask:0xf bank_mask:0xf\n"
"s_waitcnt vmcnt(1)\n"
"s_nop 0\n"
"v_mfma_f32_16x16x32_bf16 v[40:43], v[24:27], a[232:235], v[40:43]\n"
"v_and_b32_dpp v24, v52, v20 row_newbcast:12 row_mask:0xf bank_mask:0xf\n"
"v_and_b32_dpp v25, v52, v21 row_newbcast:12 row_mask:0xf bank_mask:0xf\n"
"v_and_b32_dpp v26, v52, v22 row_newbcast:12 row_mask:0xf bank_mask:0xf\n"
"v_and_b32_dpp v27, v52, v23 row_newbcast:12 row_mask:0xf bank_mask:0xf\n"
"s_waitcnt vmcnt(0)\n"
"s_nop 0\n"
"v_mfma_f32_16x16x32_bf16 v[40:43], v[24:27], a[236:239], v[40:43]\n"
"LEPI:\n"
"s_mov_b32 exec_hi, 0\n"
"s_nop 6\n"
"ds_write_b128 v60, v[40:43]\n"
"s_mov_b64 exec, -1\n"
"s_waitcnt lgkmcnt(0)\n"
"s_barrier\n"
"s_cmp_lg_u32 s50, 0\n"
"s_cbranch_scc1 Lnomerge_502\n"
"ds_read_b32 v44, v63 offset:0\n"
"ds_read_b32 v45, v63 offset:576\n"
"ds_read_b32 v46, v63 offset:1152\n"
"ds_read_b32 v47, v63 offset:1728\n"
"ds_read_b128 v[24:27], v62 offset:0\n"
"ds_read_b128 v[28:31], v62 offset:576\n"
"ds_read_b128 v[32:35], v62 offset:1152\n"
"ds_read_b128 v[36:39], v62 offset:1728\n"
"s_waitcnt lgkmcnt(4)\n"
"v_max3_f32 v48, v44, v45, v46\n"
"v_max_f32 v48, v48, v47\n"
"v_sub_f32 v44, v44, v48\n"
"v_sub_f32 v45, v45, v48\n"
"v_sub_f32 v46, v46, v48\n"
"v_sub_f32 v47, v47, v48\n"
"v_exp_f32 v44, v44\n"
"v_exp_f32 v45, v45\n"
"v_exp_f32 v46, v46\n"
"v_exp_f32 v47, v47\n"
"s_waitcnt lgkmcnt(0)\n"
"v_mul_f32 v24, v24, v44\n"
"v_mul_f32 v25, v25, v44\n"
"v_mul_f32 v26, v26, v44\n"
"v_mul_f32 v27, v27, v44\n"
"v_fmac_f32 v24, v28, v45\n"
"v_fmac_f32 v25, v29, v45\n"
"v_fmac_f32 v26, v30, v45\n"
"v_fmac_f32 v27, v31, v45\n"
"v_fmac_f32 v24, v32, v46\n"
"v_fmac_f32 v25, v33, v46\n"
"v_fmac_f32 v26, v34, v46\n"
"v_fmac_f32 v27, v35, v46\n"
"v_fmac_f32 v24, v36, v47\n"
"v_fmac_f32 v25, v37, v47\n"
"v_fmac_f32 v26, v38, v47\n"
"v_fmac_f32 v27, v39, v47\n"
"v_cmp_eq_u32 vcc, 32, v0\n"
"s_nop 1\n"
"v_cndmask_b32 v24, v24, v48, vcc\n"
"buffer_store_dwordx4 v[24:27], v61, s[36:39], s70 offen sc1\n"
"s_xor_b32 s70, s70, 8192\n"
"buffer_store_dwordx4 v[24:27], v61, s[36:39], s70 offen sc1\n"
"Lnomerge_502:\n"
"s_branch LA_P2\n"
"Lx_17:\n"
"v_mov_b32 v28, v64\n"
"v_mov_b32 v29, v65\n"
"v_mov_b32 v30, v66\n"
"v_mov_b32 v31, v67\n"
"s_movk_i32 s78, 0x0\n"
"s_call_b64 s[42:43], LEXC\n"
"s_branch Lb_18\n"
"Lx_19:\n"
"v_mov_b32 v28, v68\n"
"v_mov_b32 v29, v69\n"
"v_mov_b32 v30, v70\n"
"v_mov_b32 v31, v71\n"
"s_movk_i32 s78, 0x100\n"
"s_call_b64 s[42:43], LEXC\n"
"s_branch Lb_20\n"
"Lx_21:\n"
"v_mov_b32 v28, v72\n"
"v_mov_b32 v29, v73\n"
"v_mov_b32 v30, v74\n"
"v_mov_b32 v31, v75\n"
"s_movk_i32 s78, 0x200\n"
"s_call_b64 s[42:43], LEXC\n"
"s_branch Lb_22\n"
"Lx_23:\n"
"v_mov_b32 v28, v76\n"
"v_mov_b32 v29, v77\n"
"v_mov_b32 v30, v78\n"
"v_mov_b32 v31, v79\n"
"s_movk_i32 s78, 0x300\n"
"s_call_b64 s[42:43], LEXC\n"
"s_branch Lb_24\n"
"Lx_27:\n"
"v_mov_b32 v28, v80\n"
"v_mov_b32 v29, v81\n"
"v_mov_b32 v30, v82\n"
"v_mov_b32 v31, v83\n"
"s_movk_i32 s78, 0x40\n"
"s_call_b64 s[42:43], LEXC\n"
"s_branch Lb_28\n"
"Lx_29:\n"
"v_mov_b32 v28, v84\n"
"v_mov_b32 v29, v85\n"
"v_mov_b32 v30, v86\n"
"v_mov_b32 v31, v87\n"
"s_movk_i32 s78, 0x140\n"
"s_call_b64 s[42:43], LEXC\n"
"s_branch Lb_30\n"
"Lx_31:\n"
"v_mov_b32 v28, v88\n"
"v_mov_b32 v29, v89\n"
"v_mov_b32 v30, v90\n"
"v_mov_b32 v31, v91\n"
"s_movk_i32 s78, 0x240\n"
"s_call_b64 s[42:43], LEXC\n"
"s_branch Lb_32\n"
"Lx_33:\n"
"v_mov_b32 v28, v92\n"
"v_mov_b32 v29, v93\n"
"v_mov_b32 v30, v94\n"
"v_mov_b32 v31, v95\n"
"s_movk_i32 s78, 0x340\n"
"s_call_b64 s[42:43], LEXC\n"
"s_branch Lb_34\n"
"Lx_37:\n"
"v_mov_b32 v28, v96\n"
"v_mov_b32 v29, v97\n"
"v_mov_b32 v30, v98\n"
"v_mov_b32 v31, v99\n"
"s_movk_i32 s78, 0x80\n"
"s_call_b64 s[42:43], LEXC\n"
"s_branch Lb_38\n"
"Lx_39:\n"
"v_mov_b32 v28, v100\n"
"v_mov_b32 v29, v101\n"
"v_mov_b32 v30, v102\n"
"v_mov_b32 v31, v103\n"
"s_movk_i32 s78, 0x180\n"
"s_call_b64 s[42:43], LEXC\n"
"s_branch Lb_40\n"
"Lx_41:\n"
"v_mov_b32 v28, v104\n"
"v_mov_b32 v29, v105\n"
"v_mov_b32 v30, v106\n"
"v_mov_b32 v31, v107\n"
"s_movk_i32 s78, 0x280\n"
"s_call_b64 s[42:43], LEXC\n"
"s_branch Lb_42\n"
"Lx_43:\n"
"v_mov_b32 v28, v108\n"
"v_mov_b32 v29, v109\n"
"v_mov_b32 v30, v110\n"
"v_mov_b32 v31, v111\n"
"s_movk_i32 s78, 0x380\n"
"s_call_b64 s[42:43], LEXC\n"
"s_branch Lb_44\n"
"Lx_47:\n"
"v_mov_b32 v28, v112\n"
"v_mov_b32 v29, v113\n"
"v_mov_b32 v30, v114\n"
"v_mov_b32 v31, v115\n"
"s_movk_i32 s78, 0xc0\n"
"s_call_b64 s[42:43], LEXC\n"
"s_branch Lb_48\n"
"Lx_49:\n"
"v_mov_b32 v28, v116\n"
"v_mov_b32 v29, v117\n"
"v_mov_b32 v30, v118\n"
"v_mov_b32 v31, v119\n"
"s_movk_i32 s78, 0x1c0\n"
"s_call_b64 s[42:43], LEXC\n"
"s_branch Lb_50\n"
"Lx_51:\n"
"v_mov_b32 v28, v120\n"
"v_mov_b32 v29, v121\n"
"v_mov_b32 v30, v122\n"
"v_mov_b32 v31, v123\n"
"s_movk_i32 s78, 0x2c0\n"
"s_call_b64 s[42:43], LEXC\n"
"s_branch Lb_52\n"
"Lx_53:\n"
"v_mov_b32 v28, v136\n"
"v_mov_b32 v29, v137\n"
"v_mov_b32 v30, v138\n"
"v_mov_b32 v31, v139\n"
"s_movk_i32 s78, 0x3c0\n"
"s_call_b64 s[42:43], LEXC\n"
"s_branch Lb_54\n"
"Lx_57:\n"
"v_mov_b32 v28, v140\n"
"v_mov_b32 v29, v141\n"
"v_mov_b32 v30, v142\n"
"v_mov_b32 v31, v143\n"
"s_movk_i32 s78, 0x400\n"
"s_call_b64 s[42:43], LEXC\n"
"s_branch Lb_58\n"
"Lx_59:\n"
"v_mov_b32 v28, v144\n"
"v_mov_b32 v29, v145\n"
"v_mov_b32 v30, v146\n"
"v_mov_b32 v31, v147\n"
"s_movk_i32 s78, 0x500\n"
"s_call_b64 s[42:43], LEXC\n"
"s_branch Lb_60\n"
"Lx_61:\n"
"v_mov_b32 v28, v124\n"
"v_mov_b32 v29, v125\n"
"v_mov_b32 v30, v126\n"
"v_mov_b32 v31, v127\n"
"s_movk_i32 s78, 0x600\n"
"s_call_b64 s[42:43], LEXC\n"
"s_branch Lb_62\n"
"Lx_63:\n"
"v_mov_b32 v28, v128\n"
"v_mov_b32 v29, v129\n"
"v_mov_b32 v30, v130\n"
"v_mov_b32 v31, v131\n"
"s_movk_i32 s78, 0x700\n"
"s_call_b64 s[42:43], LEXC\n"
"s_branch Lb_64\n"
"Lx_67:\n"
"v_mov_b32 v28, v132\n"
"v_mov_b32 v29, v133\n"
"v_mov_b32 v30, v134\n"
"v_mov_b32 v31, v135\n"
"s_movk_i32 s78, 0x440\n"
"s_call_b64 s[42:43], LEXC\n"
"s_branch Lb_68\n"
"Lx_69:\n"
"v_mov_b32 v28, v148\n"
"v_mov_b32 v29, v149\n"
"v_mov_b32 v30, v150\n"
"v_mov_b32 v31, v151\n"
"s_movk_i32 s78, 0x540\n"
"s_call_b64 s[42:43], LEXC\n"
"s_branch Lb_70\n"
"Lx_71:\n"
"v_mov_b32 v28, v152\n"
"v_mov_b32 v29, v153\n"
"v_mov_b32 v30, v154\n"
"v_mov_b32 v31, v155\n"
"s_movk_i32 s78, 0x640\n"
"s_call_b64 s[42:43], LEXC\n"
"s_branch Lb_72\n"
"Lx_73:\n"
"v_mov_b32 v28, v156\n"
"v_mov_b32 v29, v157\n"
"v_mov_b32 v30, v158\n"
"v_mov_b32 v31, v159\n"
"s_movk_i32 s78, 0x740\n"
"s_call_b64 s[42:43], LEXC\n"
"s_branch Lb_74\n"
"Lx_77:\n"
"v_mov_b32 v28, v160\n"
"v_mov_b32 v29, v161\n"
"v_mov_b32 v30, v162\n"
"v_mov_b32 v31, v163\n"
"s_movk_i32 s78, 0x480\n"
"s_call_b64 s[42:43], LEXC\n"
"s_branch Lb_78\n"
"Lx_79:\n"
"v_mov_b32 v28, v164\n"
"v_mov_b32 v29, v165\n"
"v_mov_b32 v30, v166\n"
"v_mov_b32 v31, v167\n"
"s_movk_i32 s78, 0x580\n"
"s_call_b64 s[42:43], LEXC\n"
"s_branch Lb_80\n"
"Lx_81:\n"
"v_mov_b32 v28, v168\n"
"v_mov_b32 v29, v169\n"
"v_mov_b32 v30, v170\n"
"v_mov_b32 v31, v171\n"
"s_movk_i32 s78, 0x680\n"
"s_call_b64 s[42:43], LEXC\n"
"s_branch Lb_82\n"
"Lx_83:\n"
"v_mov_b32 v28, v184\n"
"v_mov_b32 v29, v185\n"
"v_mov_b32 v30, v186\n"
"v_mov_b32 v31, v187\n"
"s_movk_i32 s78, 0x780\n"
"s_call_b64 s[42:43], LEXC\n"
"s_branch Lb_84\n"
"Lx_87:\n"
"v_mov_b32 v28, v188\n"
"v_mov_b32 v29, v189\n"
"v_mov_b32 v30, v190\n"
"v_mov_b32 v31, v191\n"
"s_movk_i32 s78, 0x4c0\n"
"s_call_b64 s[42:43], LEXC\n"
"s_branch Lb_88\n"
"Lx_89:\n"
"v_mov_b32 v28, v192\n"
"v_mov_b32 v29, v193\n"
"v_mov_b32 v30, v194\n"
"v_mov_b32 v31, v195\n"
"s_movk_i32 s78, 0x5c0\n"
"s_call_b64 s[42:43], LEXC\n"
"s_branch Lb_90\n"
"Lx_91:\n"
"v_mov_b32 v28, v208\n"
"v_mov_b32 v29, v209\n"
"v_mov_b32 v30, v210\n"
"v_mov_b32 v31, v211\n"
"s_movk_i32 s78, 0x6c0\n"
"s_call_b64 s[42:43], LEXC\n"
"s_branch Lb_92\n"
"Lx_93:\n"
"v_mov_b32 v28, v212\n"
"v_mov_b32 v29, v213\n"
"v_mov_b32 v30, v214\n"
"v_mov_b32 v31, v215\n"
"s_movk_i32 s78, 0x7c0\n"
"s_call_b64 s[42:43], LEXC\n"
"s_branch Lb_94\n"
"Lx_97:\n"
"v_mov_b32 v28, v216\n"
"v_mov_b32 v29, v217\n"
"v_mov_b32 v30, v218\n"
"v_mov_b32 v31, v219\n"
"s_movk_i32 s78, 0x800\n"
"s_call_b64 s[42:43], LEXC\n"
"s_branch Lb_98\n"
"Lx_99:\n"
"v_mov_b32 v28, v232\n"
"v_mov_b32 v29, v233\n"
"v_mov_b32 v30, v234\n"
"v_mov_b32 v31, v235\n"
"s_movk_i32 s78, 0x900\n"
"s_call_b64 s[42:43], LEXC\n"
"s_branch Lb_100\n"
"Lx_101:\n"
"v_mov_b32 v28, v236\n"
"v_mov_b32 v29, v237\n"
"v_mov_b32 v30, v238\n"
"v_mov_b32 v31, v239\n"
"s_movk_i32 s78, 0xa00\n"
"s_call_b64 s[42:43], LEXC\n"
"s_branch Lb_102\n"
"Lx_103:\n"
"v_mov_b32 v28, v240\n"
"v_mov_b32 v29, v241\n"
"v_mov_b32 v30, v242\n"
"v_mov_b32 v31, v243\n"
"s_movk_i32 s78, 0xb00\n"
"s_call_b64 s[42:43], LEXC\n"
"s_branch Lb_104\n"
"Lx_107:\n"
"v_mov_b32 v28, v64\n"
"v_mov_b32 v29, v65\n"
"v_mov_b32 v30, v66\n"
"v_mov_b32 v31, v67\n"
"s_movk_i32 s78, 0x840\n"
"s_call_b64 s[42:43], LEXC\n"
"s_branch Lb_108\n"
"Lx_109:\n"
"v_mov_b32 v28, v172\n"
"v_mov_b32 v29, v173\n"
"v_mov_b32 v30, v174\n"
"v_mov_b32 v31, v175\n"
"s_movk_i32 s78, 0x940\n"
"s_call_b64 s[42:43], LEXC\n"
"s_branch Lb_110\n"
"Lx_111:\n"
"v_mov_b32 v28, v68\n"
"v_mov_b32 v29, v69\n"
"v_mov_b32 v30, v70\n"
"v_mov_b32 v31, v71\n"
"s_movk_i32 s78, 0xa40\n"
"s_call_b64 s[42:43], LEXC\n"
"s_branch Lb_112\n"
"Lx_113:\n"
"v_mov_b32 v28, v72\n"
"v_mov_b32 v29, v73\n"
"v_mov_b32 v30, v74\n"
"v_mov_b32 v31, v75\n"
"s_movk_i32 s78, 0xb40\n"
"s_call_b64 s[42:43], LEXC\n"
"s_branch Lb_114\n"
"Lx_117:\n"
"v_mov_b32 v28, v76\n"
"v_mov_b32 v29, v77\n"
"v_mov_b32 v30, v78\n"
"v_mov_b32 v31, v79\n"
"s_movk_i32 s78, 0x880\n"
"s_call_b64 s[42:43], LEXC\n"
"s_branch Lb_118\n"
"Lx_119:\n"
"v_mov_b32 v28, v80\n"
"v_mov_b32 v29, v81\n"
"v_mov_b32 v30, v82\n"
"v_mov_b32 v31, v83\n"
"s_movk_i32 s78, 0x980\n"
"s_call_b64 s[42:43], LEXC\n"
"s_branch Lb_120\n"
"Lx_121:\n"
"v_mov_b32 v28, v84\n"
"v_mov_b32 v29, v85\n"
"v_mov_b32 v30, v86\n"
"v_mov_b32 v31, v87\n"
"s_movk_i32 s78, 0xa80\n"
"s_call_b64 s[42:43], LEXC\n"
"s_branch Lb_122\n"
"Lx_123:\n"
"v_mov_b32 v28, v88\n"
"v_mov_b32 v29, v89\n"
"v_mov_b32 v30, v90\n"
"v_mov_b32 v31, v91\n"
"s_movk_i32 s78, 0xb80\n"
"s_call_b64 s[42:43], LEXC\n"
"s_branch Lb_124\n"
"Lx_127:\n"
"v_mov_b32 v28, v92\n"
"v_mov_b32 v29, v93\n"
"v_mov_b32 v30, v94\n"
"v_mov_b32 v31, v95\n"
"s_movk_i32 s78, 0x8c0\n"
"s_call_b64 s[42:43], LEXC\n"
"s_branch Lb_128\n"
"Lx_129:\n"
"v_mov_b32 v28, v96\n"
"v_mov_b32 v29, v97\n"
"v_mov_b32 v30, v98\n"
"v_mov_b32 v31, v99\n"
"s_movk_i32 s78, 0x9c0\n"
"s_call_b64 s[42:43], LEXC\n"
"s_branch Lb_130\n"
"Lx_131:\n"
"v_mov_b32 v28, v100\n"
"v_mov_b32 v29, v101\n"
"v_mov_b32 v30, v102\n"
"v_mov_b32 v31, v103\n"
"s_movk_i32 s78, 0xac0\n"
"s_call_b64 s[42:43], LEXC\n"
"s_branch Lb_132\n"
"Lx_133:\n"
"v_mov_b32 v28, v104\n"
"v_mov_b32 v29, v105\n"
"v_mov_b32 v30, v106\n"
"v_mov_b32 v31, v107\n"
"s_movk_i32 s78, 0xbc0\n"
"s_call_b64 s[42:43], LEXC\n"
"s_branch Lb_134\n"
"Lx_137:\n"
"v_mov_b32 v28, v108\n"
"v_mov_b32 v29, v109\n"
"v_mov_b32 v30, v110\n"
"v_mov_b32 v31, v111\n"
"s_movk_i32 s78, 0xc00\n"
"s_call_b64 s[42:43], LEXC\n"
"s_branch Lb_138\n"
"Lx_139:\n"
"v_mov_b32 v28, v112\n"
"v_mov_b32 v29, v113\n"
"v_mov_b32 v30, v114\n"
"v_mov_b32 v31, v115\n"
"s_movk_i32 s78, 0xd00\n"
"s_call_b64 s[42:43], LEXC\n"
"s_branch Lb_140\n"
"Lx_141:\n"
"v_mov_b32 v28, v116\n"
"v_mov_b32 v29, v117\n"
"v_mov_b32 v30, v118\n"
"v_mov_b32 v31, v119\n"
"s_movk_i32 s78, 0xe00\n"
"s_call_b64 s[42:43], LEXC\n"
"s_branch Lb_142\n"
"Lx_143:\n"
"v_mov_b32 v28, v120\n"
"v_mov_b32 v29, v121\n"
"v_mov_b32 v30, v122\n"
"v_mov_b32 v31, v123\n"
"s_movk_i32 s78, 0xf00\n"
"s_call_b64 s[42:43], LEXC\n"
"s_branch Lb_144\n"
"Lx_147:\n"
"v_mov_b32 v28, v136\n"
"v_mov_b32 v29, v137\n"
"v_mov_b32 v30, v138\n"
"v_mov_b32 v31, v139\n"
"s_movk_i32 s78, 0xc40\n"
"s_call_b64 s[42:43], LEXC\n"
"s_branch Lb_148\n"
"Lx_149:\n"
"v_mov_b32 v28, v140\n"
"v_mov_b32 v29, v141\n"
"v_mov_b32 v30, v142\n"
"v_mov_b32 v31, v143\n"
"s_movk_i32 s78, 0xd40\n"
"s_call_b64 s[42:43], LEXC\n"
"s_branch Lb_150\n"
"Lx_151:\n"
"v_mov_b32 v28, v144\n"
"v_mov_b32 v29, v145\n"
"v_mov_b32 v30, v146\n"
"v_mov_b32 v31, v147\n"
"s_movk_i32 s78, 0xe40\n"
"s_call_b64 s[42:43], LEXC\n"
"s_branch Lb_152\n"
"Lx_153:\n"
"v_mov_b32 v28, v124\n"
"v_mov_b32 v29, v125\n"
"v_mov_b32 v30, v126\n"
"v_mov_b32 v31, v127\n"
"s_movk_i32 s78, 0xf40\n"
"s_call_b64 s[42:43], LEXC\n"
"s_branch Lb_154\n"
"Lx_157:\n"
"v_mov_b32 v28, v128\n"
"v_mov_b32 v29, v129\n"
"v_mov_b32 v30, v130\n"
"v_mov_b32 v31, v131\n"
"s_movk_i32 s78, 0xc80\n"
"s_call_b64 s[42:43], LEXC\n"
"s_branch Lb_158\n"
"Lx_159:\n"
"v_mov_b32 v28, v132\n"
"v_mov_b32 v29, v133\n"
"v_mov_b32 v30, v134\n"
"v_mov_b32 v31, v135\n"
"s_movk_i32 s78, 0xd80\n"
"s_call_b64 s[42:43], LEXC\n"
"s_branch Lb_160\n"
"Lx_161:\n"
"v_mov_b32 v28, v148\n"
"v_mov_b32 v29, v149\n"
"v_mov_b32 v30, v150\n"
"v_mov_b32 v31, v151\n"
"s_movk_i32 s78, 0xe80\n"
"s_call_b64 s[42:43], LEXC\n"
"s_branch Lb_162\n"
"Lx_163:\n"
"v_mov_b32 v28, v152\n"
"v_mov_b32 v29, v153\n"
"v_mov_b32 v30, v154\n"
"v_mov_b32 v31, v155\n"
"s_movk_i32 s78, 0xf80\n"
"s_call_b64 s[42:43], LEXC\n"
"s_branch Lb_164\n"
"Lx_167:\n"
"v_mov_b32 v28, v156\n"
"v_mov_b32 v29, v157\n"
"v_mov_b32 v30, v158\n"
"v_mov_b32 v31, v159\n"
"s_movk_i32 s78, 0xcc0\n"
"s_call_b64 s[42:43], LEXC\n"
"s_branch Lb_168\n"
"Lx_169:\n"
"v_mov_b32 v28, v160\n"
"v_mov_b32 v29, v161\n"
"v_mov_b32 v30, v162\n"
"v_mov_b32 v31, v163\n"
"s_movk_i32 s78, 0xdc0\n"
"s_call_b64 s[42:43], LEXC\n"
"s_branch Lb_170\n"
"Lx_171:\n"
"v_mov_b32 v28, v164\n"
"v_mov_b32 v29, v165\n"
"v_mov_b32 v30, v166\n"
"v_mov_b32 v31, v167\n"
"s_movk_i32 s78, 0xec0\n"
"s_call_b64 s[42:43], LEXC\n"
"s_branch Lb_172\n"
"Lx_173:\n"
"v_mov_b32 v28, v168\n"
"v_mov_b32 v29, v169\n"
"v_mov_b32 v30, v170\n"
"v_mov_b32 v31, v171\n"
"s_movk_i32 s78, 0xfc0\n"
"s_call_b64 s[42:43], LEXC\n"
"s_branch Lb_174\n"
"Lx_177:\n"
"v_mov_b32 v28, v184\n"
"v_mov_b32 v29, v185\n"
"v_mov_b32 v30, v186\n"
"v_mov_b32 v31, v187\n"
"s_movk_i32 s78, 0x1000\n"
"s_call_b64 s[42:43], LEXC\n"
"s_branch Lb_178\n"
"Lx_179:\n"
"v_mov_b32 v28, v188\n"
"v_mov_b32 v29, v189\n"
"v_mov_b32 v30, v190\n"
"v_mov_b32 v31, v191\n"
"s_movk_i32 s78, 0x1100\n"
"s_call_b64 s[42:43], LEXC\n"
"s_branch Lb_180\n"
"Lx_181:\n"
"v_mov_b32 v28, v192\n"
"v_mov_b32 v29, v193\n"
"v_mov_b32 v30, v194\n"
"v_mov_b32 v31, v195\n"
"s_movk_i32 s78, 0x1200\n"
"s_call_b64 s[42:43], LEXC\n"
"s_branch Lb_182\n"
"Lx_183:\n"
"v_mov_b32 v28, v208\n"
"v_mov_b32 v29, v209\n"
"v_mov_b32 v30, v210\n"
"v_mov_b32 v31, v211\n"
"s_movk_i32 s78, 0x1300\n"
"s_call_b64 s[42:43], LEXC\n"
"s_branch Lb_184\n"
"Lx_187:\n"
"v_mov_b32 v28, v212\n"
"v_mov_b32 v29, v213\n"
"v_mov_b32 v30, v214\n"
"v_mov_b32 v31, v215\n"
"s_movk_i32 s78, 0x1040\n"
"s_call_b64 s[42:43], LEXC\n"
"s_branch Lb_188\n"
"Lx_189:\n"
"v_mov_b32 v28, v216\n"
"v_mov_b32 v29, v217\n"
"v_mov_b32 v30, v218\n"
"v_mov_b32 v31, v219\n"
"s_movk_i32 s78, 0x1140\n"
"s_call_b64 s[42:43], LEXC\n"
"s_branch Lb_190\n"
"Lx_191:\n"
"v_mov_b32 v28, v232\n"
"v_mov_b32 v29, v233\n"
"v_mov_b32 v30, v234\n"
"v_mov_b32 v31, v235\n"
"s_movk_i32 s78, 0x1240\n"
"s_call_b64 s[42:43], LEXC\n"
"s_branch Lb_192\n"
"Lx_193:\n"
"v_mov_b32 v28, v236\n"
"v_mov_b32 v29, v237\n"
"v_mov_b32 v30, v238\n"
"v_mov_b32 v31, v239\n"
"s_movk_i32 s78, 0x1340\n"
"s_call_b64 s[42:43], LEXC\n"
"s_branch Lb_194\n"
"Lx_197:\n"
"v_mov_b32 v28, v240\n"
"v_mov_b32 v29, v241\n"
"v_mov_b32 v30, v242\n"
"v_mov_b32 v31, v243\n"
"s_movk_i32 s78, 0x1080\n"
"s_call_b64 s[42:43], LEXC\n"
"v_cndmask_b32 v64, v64, v58, vcc\n"
"v_cndmask_b32 v65, v65, v58, vcc\n"
"v_cndmask_b32 v66, v66, v58, vcc\n"
"s_branch Lb_198\n"
"Lx_199:\n"
"v_mov_b32 v28, v172\n"
"v_mov_b32 v29, v173\n"
"v_mov_b32 v30, v174\n"
"v_mov_b32 v31, v175\n"
"s_movk_i32 s78, 0x1180\n"
"s_call_b64 s[42:43], LEXC\n"
"v_cndmask_b32 v67, v67, v58, vcc\n"
"v_cndmask_b32 v68, v68, v58, vcc\n"
"v_cndmask_b32 v69, v69, v58, vcc\n"
"s_branch Lb_200\n"
"Lx_201:\n"
"v_mov_b32 v28, v176\n"
"v_mov_b32 v29, v177\n"
"v_mov_b32 v30, v178\n"
"v_mov_b32 v31, v179\n"
"s_movk_i32 s78, 0x1280\n"
"s_call_b64 s[42:43], LEXC\n"
"v_cndmask_b32 v70, v70, v58, vcc\n"
"v_cndmask_b32 v71, v71, v58, vcc\n"
"v_cndmask_b32 v72, v72, v58, vcc\n"
"s_branch Lb_202\n"
"Lx_203:\n"
"v_mov_b32 v28, v180\n"
"v_mov_b32 v29, v181\n"
"v_mov_b32 v30, v182\n"
"v_mov_b32 v31, v183\n"
"s_movk_i32 s78, 0x1380\n"
"s_call_b64 s[42:43], LEXC\n"
"v_cndmask_b32 v73, v73, v58, vcc\n"
"v_cndmask_b32 v74, v74, v58, vcc\n"
"v_cndmask_b32 v75, v75, v58, vcc\n"
"s_branch Lb_204\n"
"Lx_207:\n"
"v_mov_b32 v28, v196\n"
"v_mov_b32 v29, v197\n"
"v_mov_b32 v30, v198\n"
"v_mov_b32 v31, v199\n"
"s_movk_i32 s78, 0x10c0\n"
"s_call_b64 s[42:43], LEXC\n"
"v_cndmask_b32 v76, v76, v58, vcc\n"
"v_cndmask_b32 v77, v77, v58, vcc\n"
"v_cndmask_b32 v78, v78, v58, vcc\n"
"s_branch Lb_208\n"
"Lx_209:\n"
"v_mov_b32 v28, v200\n"
"v_mov_b32 v29, v201\n"
"v_mov_b32 v30, v202\n"
"v_mov_b32 v31, v203\n"
"s_movk_i32 s78, 0x11c0\n"
"s_call_b64 s[42:43], LEXC\n"
"v_cndmask_b32 v79, v79, v58, vcc\n"
"v_cndmask_b32 v80, v80, v58, vcc\n"
"v_cndmask_b32 v81, v81, v58, vcc\n"
"s_branch Lb_210\n"
"Lx_211:\n"
"v_mov_b32 v28, v204\n"
"v_mov_b32 v29, v205\n"
"v_mov_b32 v30, v206\n"
"v_mov_b32 v31, v207\n"
"s_movk_i32 s78, 0x12c0\n"
"s_call_b64 s[42:43], LEXC\n"
"v_cndmask_b32 v82, v82, v58, vcc\n"
"v_cndmask_b32 v83, v83, v58, vcc\n"
"v_cndmask_b32 v84, v84, v58, vcc\n"
"s_branch Lb_212\n"
"Lx_213:\n"
"v_mov_b32 v28, v88\n"
"v_mov_b32 v29, v89\n"
"v_mov_b32 v30, v90\n"
"v_mov_b32 v31, v91\n"
"s_movk_i32 s78, 0x13c0\n"
"s_call_b64 s[42:43], LEXC\n"
"v_cndmask_b32 v85, v85, v58, vcc\n"
"v_cndmask_b32 v86, v86, v58, vcc\n"
"v_cndmask_b32 v87, v87, v58, vcc\n"
"s_branch Lb_214\n"
"Lx_220:\n"
"v_mov_b32 v28, v92\n"
"v_mov_b32 v29, v93\n"
"v_mov_b32 v30, v94\n"
"v_mov_b32 v31, v95\n"
"s_movk_i32 s78, 0x1400\n"
"s_call_b64 s[42:43], LEXC\n"
"v_cndmask_b32 v88, v88, v58, vcc\n"
"v_cndmask_b32 v89, v89, v58, vcc\n"
"v_cndmask_b32 v90, v90, v58, vcc\n"
"s_branch Lb_221\n"
"Lx_222:\n"
"v_mov_b32 v28, v96\n"
"v_mov_b32 v29, v97\n"
"v_mov_b32 v30, v98\n"
"v_mov_b32 v31, v99\n"
"s_movk_i32 s78, 0x1500\n"
"s_call_b64 s[42:43], LEXC\n"
"v_cndmask_b32 v91, v91, v58, vcc\n"
"v_cndmask_b32 v92, v92, v58, vcc\n"
"v_cndmask_b32 v93, v93, v58, vcc\n"
"s_branch Lb_223\n"
"Lx_224:\n"
"v_mov_b32 v28, v100\n"
"v_mov_b32 v29, v101\n"
"v_mov_b32 v30, v102\n"
"v_mov_b32 v31, v103\n"
"s_movk_i32 s78, 0x1600\n"
"s_call_b64 s[42:43], LEXC\n"
"v_cndmask_b32 v94, v94, v58, vcc\n"
"v_cndmask_b32 v95, v95, v58, vcc\n"
"v_cndmask_b32 v96, v96, v58, vcc\n"
"s_branch Lb_225\n"
"Lx_226:\n"
"v_mov_b32 v28, v104\n"
"v_mov_b32 v29, v105\n"
"v_mov_b32 v30, v106\n"
"v_mov_b32 v31, v107\n"
"s_movk_i32 s78, 0x1700\n"
"s_call_b64 s[42:43], LEXC\n"
"v_cndmask_b32 v97, v97, v58, vcc\n"
"v_cndmask_b32 v98, v98, v58, vcc\n"
"v_cndmask_b32 v99, v99, v58, vcc\n"
"s_branch Lb_227\n"
"Lx_230:\n"
"v_mov_b32 v28, v108\n"
"v_mov_b32 v29, v109\n"
"v_mov_b32 v30, v110\n"
"v_mov_b32 v31, v111\n"
"s_movk_i32 s78, 0x1440\n"
"s_call_b64 s[42:43], LEXC\n"
"v_cndmask_b32 v100, v100, v58, vcc\n"
"v_cndmask_b32 v101, v101, v58, vcc\n"
"v_cndmask_b32 v102, v102, v58, vcc\n"
"s_branch Lb_231\n"
"Lx_232:\n"
"v_mov_b32 v28, v112\n"
"v_mov_b32 v29, v113\n"
"v_mov_b32 v30, v114\n"
"v_mov_b32 v31, v115\n"
"s_movk_i32 s78, 0x1540\n"
"s_call_b64 s[42:43], LEXC\n"
"v_cndmask_b32 v103, v103, v58, vcc\n"
"v_cndmask_b32 v104, v104, v58, vcc\n"
"v_cndmask_b32 v105, v105, v58, vcc\n"
"s_branch Lb_233\n"
"Lx_234:\n"
"v_mov_b32 v28, v116\n"
"v_mov_b32 v29, v117\n"
"v_mov_b32 v30, v118\n"
"v_mov_b32 v31, v119\n"
"s_movk_i32 s78, 0x1640\n"
"s_call_b64 s[42:43], LEXC\n"
"v_cndmask_b32 v106, v106, v58, vcc\n"
"v_cndmask_b32 v107, v107, v58, vcc\n"
"v_cndmask_b32 v108, v108, v58, vcc\n"
"s_branch Lb_235\n"
"Lx_236:\n"
"v_mov_b32 v28, v120\n"
"v_mov_b32 v29, v121\n"
"v_mov_b32 v30, v122\n"
"v_mov_b32 v31, v123\n"
"s_movk_i32 s78, 0x1740\n"
"s_call_b64 s[42:43], LEXC\n"
"v_cndmask_b32 v109, v109, v58, vcc\n"
"v_cndmask_b32 v110, v110, v58, vcc\n"
"v_cndmask_b32 v111, v111, v58, vcc\n"
"s_branch Lb_237\n"
"Lx_240:\n"
"v_mov_b32 v28, v136\n"
"v_mov_b32 v29, v137\n"
"v_mov_b32 v30, v138\n"
"v_mov_b32 v31, v139\n"
"s_movk_i32 s78, 0x1480\n"
"s_call_b64 s[42:43], LEXC\n"
"s_branch Lb_241\n"
"Lx_242:\n"
"v_mov_b32 v28, v140\n"
"v_mov_b32 v29, v141\n"
"v_mov_b32 v30, v142\n"
"v_mov_b32 v31, v143\n"
"s_movk_i32 s78, 0x1580\n"
"s_call_b64 s[42:43], LEXC\n"
"s_branch Lb_243\n"
"Lx_244:\n"
"v_mov_b32 v28, v144\n"
"v_mov_b32 v29, v145\n"
"v_mov_b32 v30, v146\n"
"v_mov_b32 v31, v147\n"
"s_movk_i32 s78, 0x1680\n"
"s_call_b64 s[42:43], LEXC\n"
"s_branch Lb_245\n"
"Lx_246:\n"
"v_mov_b32 v28, v124\n"
"v_mov_b32 v29, v125\n"
"v_mov_b32 v30, v126\n"
"v_mov_b32 v31, v127\n"
"s_movk_i32 s78, 0x1780\n"
"s_call_b64 s[42:43], LEXC\n"
"s_branch Lb_247\n"
"Lx_250:\n"
"v_mov_b32 v28, v128\n"
"v_mov_b32 v29, v129\n"
"v_mov_b32 v30, v130\n"
"v_mov_b32 v31, v131\n"
"s_movk_i32 s78, 0x14c0\n"
"s_call_b64 s[42:43], LEXC\n"
"s_branch Lb_251\n"
"Lx_252:\n"
"v_mov_b32 v28, v132\n"
"v_mov_b32 v29, v133\n"
"v_mov_b32 v30, v134\n"
"v_mov_b32 v31, v135\n"
"s_movk_i32 s78, 0x15c0\n"
"s_call_b64 s[42:43], LEXC\n"
"s_branch Lb_253\n"
"Lx_254:\n"
"v_mov_b32 v28, v148\n"
"v_mov_b32 v29, v149\n"
"v_mov_b32 v30, v150\n"
"v_mov_b32 v31, v151\n"
"s_movk_i32 s78, 0x16c0\n"
"s_call_b64 s[42:43], LEXC\n"
"s_branch Lb_255\n"
"Lx_256:\n"
"v_mov_b32 v28, v152\n"
"v_mov_b32 v29, v153\n"
"v_mov_b32 v30, v154\n"
"v_mov_b32 v31, v155\n"
"s_movk_i32 s78, 0x17c0\n"
"s_call_b64 s[42:43], LEXC\n"
"s_branch Lb_257\n"
"Lx_260:\n"
"v_mov_b32 v28, v156\n"
"v_mov_b32 v29, v157\n"
"v_mov_b32 v30, v158\n"
"v_mov_b32 v31, v159\n"
"s_movk_i32 s78, 0x1800\n"
"s_call_b64 s[42:43], LEXC\n"
"s_branch Lb_261\n"
"Lx_262:\n"
"v_mov_b32 v28, v160\n"
"v_mov_b32 v29, v161\n"
"v_mov_b32 v30, v162\n"
"v_mov_b32 v31, v163\n"
"s_movk_i32 s78, 0x1900\n"
"s_call_b64 s[42:43], LEXC\n"
"s_branch Lb_263\n"
"Lx_264:\n"
"v_mov_b32 v28, v164\n"
"v_mov_b32 v29, v165\n"
"v_mov_b32 v30, v166\n"
"v_mov_b32 v31, v167\n"
"s_movk_i32 s78, 0x1a00\n"
"s_call_b64 s[42:43], LEXC\n"
"s_branch Lb_265\n"
"Lx_266:\n"
"v_mov_b32 v28, v168\n"
"v_mov_b32 v29, v169\n"
"v_mov_b32 v30, v170\n"
"v_mov_b32 v31, v171\n"
"s_movk_i32 s78, 0x1b00\n"
"s_call_b64 s[42:43], LEXC\n"
"s_branch Lb_267\n"
"Lx_270:\n"
"v_mov_b32 v28, v184\n"
"v_mov_b32 v29, v185\n"
"v_mov_b32 v30, v186\n"
"v_mov_b32 v31, v187\n"
"s_movk_i32 s78, 0x1840\n"
"s_call_b64 s[42:43], LEXC\n"
"s_branch Lb_271\n"
"Lx_272:\n"
"v_mov_b32 v28, v188\n"
"v_mov_b32 v29, v189\n"
"v_mov_b32 v30, v190\n"
"v_mov_b32 v31, v191\n"
"s_movk_i32 s78, 0x1940\n"
"s_call_b64 s[42:43], LEXC\n"
"s_branch Lb_273\n"
"Lx_274:\n"
"v_mov_b32 v28, v192\n"
"v_mov_b32 v29, v193\n"
"v_mov_b32 v30, v194\n"
"v_mov_b32 v31, v195\n"
"s_movk_i32 s78, 0x1a40\n"
"s_call_b64 s[42:43], LEXC\n"
"s_branch Lb_275\n"
"Lx_276:\n"
"v_mov_b32 v28, v208\n"
"v_mov_b32 v29, v209\n"
"v_mov_b32 v30, v210\n"
"v_mov_b32 v31, v211\n"
"s_movk_i32 s78, 0x1b40\n"
"s_call_b64 s[42:43], LEXC\n"
"s_branch Lb_277\n"
"Lx_280:\n"
"v_mov_b32 v28, v212\n"
"v_mov_b32 v29, v213\n"
"v_mov_b32 v30, v214\n"
"v_mov_b32 v31, v215\n"
"s_movk_i32 s78, 0x1880\n"
"s_call_b64 s[42:43], LEXC\n"
"s_branch Lb_281\n"
"Lx_282:\n"
"v_mov_b32 v28, v216\n"
"v_mov_b32 v29, v217\n"
"v_mov_b32 v30, v218\n"
"v_mov_b32 v31, v219\n"
"s_movk_i32 s78, 0x1980\n"
"s_call_b64 s[42:43], LEXC\n"
"s_branch Lb_283\n"
"Lx_284:\n"
"v_mov_b32 v28, v232\n"
"v_mov_b32 v29, v233\n"
"v_mov_b32 v30, v234\n"
"v_mov_b32 v31, v235\n"
"s_movk_i32 s78, 0x1a80\n"
"s_call_b64 s[42:43], LEXC\n"
"s_branch Lb_285\n"
"Lx_286:\n"
"v_mov_b32 v28, v236\n"
"v_mov_b32 v29, v237\n"
"v_mov_b32 v30, v238\n"
"v_mov_b32 v31, v239\n"
"s_movk_i32 s78, 0x1b80\n"
"s_call_b64 s[42:43], LEXC\n"
"s_branch Lb_287\n"
"Lx_290:\n"
"v_mov_b32 v28, v240\n"
"v_mov_b32 v29, v241\n"
"v_mov_b32 v30, v242\n"
"v_mov_b32 v31, v243\n"
"s_movk_i32 s78, 0x18c0\n"
"s_call_b64 s[42:43], LEXC\n"
"s_branch Lb_291\n"
"Lx_292:\n"
"v_mov_b32 v28, v172\n"
"v_mov_b32 v29, v173\n"
"v_mov_b32 v30, v174\n"
"v_mov_b32 v31, v175\n"
"s_movk_i32 s78, 0x19c0\n"
"s_call_b64 s[42:43], LEXC\n"
"s_branch Lb_293\n"
"Lx_294:\n"
"v_mov_b32 v28, v176\n"
"v_mov_b32 v29, v177\n"
"v_mov_b32 v30, v178\n"
"v_mov_b32 v31, v179\n"
"s_movk_i32 s78, 0x1ac0\n"
"s_call_b64 s[42:43], LEXC\n"
"s_branch Lb_295\n"
"Lx_296:\n"
"v_mov_b32 v28, v180\n"
"v_mov_b32 v29, v181\n"
"v_mov_b32 v30, v182\n"
"v_mov_b32 v31, v183\n"
"s_movk_i32 s78, 0x1bc0\n"
"s_call_b64 s[42:43], LEXC\n"
"s_branch Lb_297\n"
"Lx_300:\n"
"v_mov_b32 v28, v196\n"
"v_mov_b32 v29, v197\n"
"v_mov_b32 v30, v198\n"
"v_mov_b32 v31, v199\n"
"s_movk_i32 s78, 0x1c00\n"
"s_call_b64 s[42:43], LEXC\n"
"s_branch Lb_301\n"
"Lx_302:\n"
"v_mov_b32 v28, v200\n"
"v_mov_b32 v29, v201\n"
"v_mov_b32 v30, v202\n"
"v_mov_b32 v31, v203\n"
"s_movk_i32 s78, 0x1d00\n"
"s_call_b64 s[42:43], LEXC\n"
"s_branch Lb_303\n"
"Lx_304:\n"
"v_mov_b32 v28, v204\n"
"v_mov_b32 v29, v205\n"
"v_mov_b32 v30, v206\n"
"v_mov_b32 v31, v207\n"
"s_movk_i32 s78, 0x1e00\n"
"s_call_b64 s[42:43], LEXC\n"
"s_branch Lb_305\n"
"Lx_306:\n"
"v_mov_b32 v28, v220\n"
"v_mov_b32 v29, v221\n"
"v_mov_b32 v30, v222\n"
"v_mov_b32 v31, v223\n"
"s_movk_i32 s78, 0x1f00\n"
"s_call_b64 s[42:43], LEXC\n"
"s_branch Lb_307\n"
"Lx_310:\n"
"v_mov_b32 v28, v224\n"
"v_mov_b32 v29, v225\n"
"v_mov_b32 v30, v226\n"
"v_mov_b32 v31, v227\n"
"s_movk_i32 s78, 0x1c40\n"
"s_call_b64 s[42:43], LEXC\n"
"s_branch Lb_311\n"
"Lx_312:\n"
"v_mov_b32 v28, v228\n"
"v_mov_b32 v29, v229\n"
"v_mov_b32 v30, v230\n"
"v_mov_b32 v31, v231\n"
"s_movk_i32 s78, 0x1d40\n"
"s_call_b64 s[42:43], LEXC\n"
"s_branch Lb_313\n"
"Lx_314:\n"
"v_mov_b32 v28, v244\n"
"v_mov_b32 v29, v245\n"
"v_mov_b32 v30, v246\n"
"v_mov_b32 v31, v247\n"
"s_movk_i32 s78, 0x1e40\n"
"s_call_b64 s[42:43], LEXC\n"
"s_branch Lb_315\n"
"Lx_316:\n"
"v_mov_b32 v28, v248\n"
"v_mov_b32 v29, v249\n"
"v_mov_b32 v30, v250\n"
"v_mov_b32 v31, v251\n"
"s_movk_i32 s78, 0x1f40\n"
"s_call_b64 s[42:43], LEXC\n"
"s_branch Lb_317\n"
"Lx_320:\n"
"v_mov_b32 v28, v252\n"
"v_mov_b32 v29, v253\n"
"v_mov_b32 v30, v254\n"
"v_mov_b32 v31, v255\n"
"s_movk_i32 s78, 0x1c80\n"
"s_call_b64 s[42:43], LEXC\n"
"s_branch Lb_321\n"
"Lx_322:\n"
"v_mov_b32 v28, v112\n"
"v_mov_b32 v29, v113\n"
"v_mov_b32 v30, v114\n"
"v_mov_b32 v31, v115\n"
"s_movk_i32 s78, 0x1d80\n"
"s_call_b64 s[42:43], LEXC\n"
"s_branch Lb_323\n"
"Lx_324:\n"
"v_mov_b32 v28, v116\n"
"v_mov_b32 v29, v117\n"
"v_mov_b32 v30, v118\n"
"v_mov_b32 v31, v119\n"
"s_movk_i32 s78, 0x1e80\n"
"s_call_b64 s[42:43], LEXC\n"
"s_branch Lb_325\n"
"Lx_326:\n"
"v_mov_b32 v28, v120\n"
"v_mov_b32 v29, v121\n"
"v_mov_b32 v30, v122\n"
"v_mov_b32 v31, v123\n"
"s_movk_i32 s78, 0x1f80\n"
"s_call_b64 s[42:43], LEXC\n"
"s_branch Lb_327\n"
"Lx_330:\n"
"v_mov_b32 v28, v136\n"
"v_mov_b32 v29, v137\n"
"v_mov_b32 v30, v138\n"
"v_mov_b32 v31, v139\n"
"s_movk_i32 s78, 0x1cc0\n"
"s_call_b64 s[42:43], LEXC\n"
"s_branch Lb_331\n"
"Lx_332:\n"
"v_mov_b32 v28, v140\n"
"v_mov_b32 v29, v141\n"
"v_mov_b32 v30, v142\n"
"v_mov_b32 v31, v143\n"
"s_movk_i32 s78, 0x1dc0\n"
"s_call_b64 s[42:43], LEXC\n"
"s_branch Lb_333\n"
"Lx_334:\n"
"v_mov_b32 v28, v144\n"
"v_mov_b32 v29, v145\n"
"v_mov_b32 v30, v146\n"
"v_mov_b32 v31, v147\n"
"s_movk_i32 s78, 0x1ec0\n"
"s_call_b64 s[42:43], LEXC\n"
"s_branch Lb_335\n"
"Lx_336:\n"
"v_mov_b32 v28, v124\n"
"v_mov_b32 v29, v125\n"
"v_mov_b32 v30, v126\n"
"v_mov_b32 v31, v127\n"
"s_movk_i32 s78, 0x1fc0\n"
"s_call_b64 s[42:43], LEXC\n"
"s_branch Lb_337\n"
"Lx_340:\n"
"v_mov_b32 v28, v128\n"
"v_mov_b32 v29, v129\n"
"v_mov_b32 v30, v130\n"
"v_mov_b32 v31, v131\n"
"s_movk_i32 s78, 0x2000\n"
"s_call_b64 s[42:43], LEXC\n"
"s_branch Lb_341\n"
"Lx_342:\n"
"v_mov_b32 v28, v132\n"
"v_mov_b32 v29, v133\n"
"v_mov_b32 v30, v134\n"
"v_mov_b32 v31, v135\n"
"s_movk_i32 s78, 0x2100\n"
"s_call_b64 s[42:43], LEXC\n"
"s_branch Lb_343\n"
"Lx_344:\n"
"v_mov_b32 v28, v148\n"
"v_mov_b32 v29, v149\n"
"v_mov_b32 v30, v150\n"
"v_mov_b32 v31, v151\n"
"s_movk_i32 s78, 0x2200\n"
"s_call_b64 s[42:43], LEXC\n"
"s_branch Lb_345\n"
"Lx_346:\n"
"v_mov_b32 v28, v152\n"
"v_mov_b32 v29, v153\n"
"v_mov_b32 v30, v154\n"
"v_mov_b32 v31, v155\n"
"s_movk_i32 s78, 0x2300\n"
"s_call_b64 s[42:43], LEXC\n"
"s_branch Lb_347\n"
"Lx_350:\n"
"v_mov_b32 v28, v156\n"
"v_mov_b32 v29, v157\n"
"v_mov_b32 v30, v158\n"
"v_mov_b32 v31, v159\n"
"s_movk_i32 s78, 0x2040\n"
"s_call_b64 s[42:43], LEXC\n"
"s_branch Lb_351\n"
"Lx_352:\n"
"v_mov_b32 v28, v160\n"
"v_mov_b32 v29, v161\n"
"v_mov_b32 v30, v162\n"
"v_mov_b32 v31, v163\n"
"s_movk_i32 s78, 0x2140\n"
"s_call_b64 s[42:43], LEXC\n"
"s_branch Lb_353\n"
"Lx_354:\n"
"v_mov_b32 v28, v164\n"
"v_mov_b32 v29, v165\n"
"v_mov_b32 v30, v166\n"
"v_mov_b32 v31, v167\n"
"s_movk_i32 s78, 0x2240\n"
"s_call_b64 s[42:43], LEXC\n"
"s_branch Lb_355\n"
"Lx_356:\n"
"v_mov_b32 v28, v168\n"
"v_mov_b32 v29, v169\n"
"v_mov_b32 v30, v170\n"
"v_mov_b32 v31, v171\n"
"s_movk_i32 s78, 0x2340\n"
"s_call_b64 s[42:43], LEXC\n"
"s_branch Lb_357\n"
"Lx_360:\n"
"v_mov_b32 v28, v184\n"
"v_mov_b32 v29, v185\n"
"v_mov_b32 v30, v186\n"
"v_mov_b32 v31, v187\n"
"s_movk_i32 s78, 0x2080\n"
"s_call_b64 s[42:43], LEXC\n"
"s_branch Lb_361\n"
"Lx_362:\n"
"v_mov_b32 v28, v188\n"
"v_mov_b32 v29, v189\n"
"v_mov_b32 v30, v190\n"
"v_mov_b32 v31, v191\n"
"s_movk_i32 s78, 0x2180\n"
"s_call_b64 s[42:43], LEXC\n"
"s_branch Lb_363\n"
"Lx_364:\n"
"v_mov_b32 v28, v192\n"
"v_mov_b32 v29, v193\n"
"v_mov_b32 v30, v194\n"
"v_mov_b32 v31, v195\n"
"s_movk_i32 s78, 0x2280\n"
"s_call_b64 s[42:43], LEXC\n"
"s_branch Lb_365\n"
"Lx_366:\n"
"v_mov_b32 v28, v208\n"
"v_mov_b32 v29, v209\n"
"v_mov_b32 v30, v210\n"
"v_mov_b32 v31, v211\n"
"s_movk_i32 s78, 0x2380\n"
"s_call_b64 s[42:43], LEXC\n"
"s_branch Lb_367\n"
"Lx_388:\n"
"v_mov_b32 v28, v132\n"
"v_mov_b32 v29, v133\n"
"v_mov_b32 v30, v134\n"
"v_mov_b32 v31, v135\n"
"s_movk_i32 s78, 0x1080\n"
"s_call_b64 s[42:43], LEXC\n"
"v_cndmask_b32 v112, v112, v58, vcc\n"
"v_cndmask_b32 v113, v113, v58, vcc\n"
"v_cndmask_b32 v114, v114, v58, vcc\n"
"s_branch Lb_389\n"
"Lx_390:\n"
"v_mov_b32 v28, v148\n"
"v_mov_b32 v29, v149\n"
"v_mov_b32 v30, v150\n"
"v_mov_b32 v31, v151\n"
"s_movk_i32 s78, 0x1180\n"
"s_call_b64 s[42:43], LEXC\n"
"v_cndmask_b32 v115, v115, v58, vcc\n"
"v_cndmask_b32 v116, v116, v58, vcc\n"
"v_cndmask_b32 v117, v117, v58, vcc\n"
"s_branch Lb_391\n"
"Lx_394:\n"
"v_mov_b32 v28, v152\n"
"v_mov_b32 v29, v153\n"
"v_mov_b32 v30, v154\n"
"v_mov_b32 v31, v155\n"
"s_movk_i32 s78, 0x1280\n"
"s_call_b64 s[42:43], LEXC\n"
"v_cndmask_b32 v118, v118, v58, vcc\n"
"v_cndmask_b32 v119, v119, v58, vcc\n"
"v_cndmask_b32 v120, v120, v58, vcc\n"
"s_branch Lb_395\n"
"Lx_396:\n"
"v_mov_b32 v28, v156\n"
"v_mov_b32 v29, v157\n"
"v_mov_b32 v30, v158\n"
"v_mov_b32 v31, v159\n"
"s_movk_i32 s78, 0x1380\n"
"s_call_b64 s[42:43], LEXC\n"
"v_cndmask_b32 v121, v121, v58, vcc\n"
"v_cndmask_b32 v122, v122, v58, vcc\n"
"v_cndmask_b32 v123, v123, v58, vcc\n"
"s_branch Lb_397\n"
"Lx_398:\n"
"v_mov_b32 v28, v160\n"
"v_mov_b32 v29, v161\n"
"v_mov_b32 v30, v162\n"
"v_mov_b32 v31, v163\n"
"s_movk_i32 s78, 0x10c0\n"
"s_call_b64 s[42:43], LEXC\n"
"v_cndmask_b32 v136, v136, v58, vcc\n"
"v_cndmask_b32 v137, v137, v58, vcc\n"
"v_cndmask_b32 v138, v138, v58, vcc\n"
"s_branch Lb_399\n"
"Lx_400:\n"
"v_mov_b32 v28, v164\n"
"v_mov_b32 v29, v165\n"
"v_mov_b32 v30, v166\n"
"v_mov_b32 v31, v167\n"
"s_movk_i32 s78, 0x11c0\n"
"s_call_b64 s[42:43], LEXC\n"
"v_cndmask_b32 v139, v139, v58, vcc\n"
"v_cndmask_b32 v140, v140, v58, vcc\n"
"v_cndmask_b32 v141, v141, v58, vcc\n"
"s_branch Lb_401\n"
"Lx_404:\n"
"v_mov_b32 v28, v168\n"
"v_mov_b32 v29, v169\n"
"v_mov_b32 v30, v170\n"
"v_mov_b32 v31, v171\n"
"s_movk_i32 s78, 0x12c0\n"
"s_call_b64 s[42:43], LEXC\n"
"v_cndmask_b32 v142, v142, v58, vcc\n"
"v_cndmask_b32 v143, v143, v58, vcc\n"
"v_cndmask_b32 v144, v144, v58, vcc\n"
"s_branch Lb_405\n"
"Lx_406:\n"
"v_mov_b32 v28, v184\n"
"v_mov_b32 v29, v185\n"
"v_mov_b32 v30, v186\n"
"v_mov_b32 v31, v187\n"
"s_movk_i32 s78, 0x13c0\n"
"s_call_b64 s[42:43], LEXC\n"
"v_cndmask_b32 v145, v145, v58, vcc\n"
"v_cndmask_b32 v146, v146, v58, vcc\n"
"v_cndmask_b32 v147, v147, v58, vcc\n"
"s_branch Lb_407\n"
"Lx_408:\n"
"v_mov_b32 v28, v188\n"
"v_mov_b32 v29, v189\n"
"v_mov_b32 v30, v190\n"
"v_mov_b32 v31, v191\n"
"s_movk_i32 s78, 0x1480\n"
"s_call_b64 s[42:43], LEXC\n"
"v_cndmask_b32 v124, v124, v58, vcc\n"
"v_cndmask_b32 v125, v125, v58, vcc\n"
"v_cndmask_b32 v126, v126, v58, vcc\n"
"s_branch Lb_409\n"
"Lx_410:\n"
"v_mov_b32 v28, v192\n"
"v_mov_b32 v29, v193\n"
"v_mov_b32 v30, v194\n"
"v_mov_b32 v31, v195\n"
"s_movk_i32 s78, 0x1580\n"
"s_call_b64 s[42:43], LEXC\n"
"v_cndmask_b32 v127, v127, v58, vcc\n"
"v_cndmask_b32 v128, v128, v58, vcc\n"
"v_cndmask_b32 v129, v129, v58, vcc\n"
"s_branch Lb_411\n"
"Lx_414:\n"
"v_mov_b32 v28, v212\n"
"v_mov_b32 v29, v213\n"
"v_mov_b32 v30, v214\n"
"v_mov_b32 v31, v215\n"
"s_movk_i32 s78, 0x1680\n"
"s_call_b64 s[42:43], LEXC\n"
"v_cndmask_b32 v130, v130, v58, vcc\n"
"v_cndmask_b32 v131, v131, v58, vcc\n"
"v_cndmask_b32 v132, v132, v58, vcc\n"
"s_branch Lb_415\n"
"Lx_416:\n"
"v_mov_b32 v28, v216\n"
"v_mov_b32 v29, v217\n"
"v_mov_b32 v30, v218\n"
"v_mov_b32 v31, v219\n"
"s_movk_i32 s78, 0x1780\n"
"s_call_b64 s[42:43], LEXC\n"
"v_cndmask_b32 v133, v133, v58, vcc\n"
"v_cndmask_b32 v134, v134, v58, vcc\n"
"v_cndmask_b32 v135, v135, v58, vcc\n"
"s_branch Lb_417\n"
"Lx_418:\n"
"v_mov_b32 v28, v232\n"
"v_mov_b32 v29, v233\n"
"v_mov_b32 v30, v234\n"
"v_mov_b32 v31, v235\n"
"s_movk_i32 s78, 0x14c0\n"
"s_call_b64 s[42:43], LEXC\n"
"v_cndmask_b32 v148, v148, v58, vcc\n"
"v_cndmask_b32 v149, v149, v58, vcc\n"
"v_cndmask_b32 v150, v150, v58, vcc\n"
"s_branch Lb_419\n"
"Lx_420:\n"
"v_mov_b32 v28, v208\n"
"v_mov_b32 v29, v209\n"
"v_mov_b32 v30, v210\n"
"v_mov_b32 v31, v211\n"
"s_movk_i32 s78, 0x15c0\n"
"s_call_b64 s[42:43], LEXC\n"
"v_cndmask_b32 v151, v151, v58, vcc\n"
"v_cndmask_b32 v152, v152, v58, vcc\n"
"v_cndmask_b32 v153, v153, v58, vcc\n"
"s_branch Lb_421\n"
"Lx_424:\n"
"v_mov_b32 v28, v236\n"
"v_mov_b32 v29, v237\n"
"v_mov_b32 v30, v238\n"
"v_mov_b32 v31, v239\n"
"s_movk_i32 s78, 0x16c0\n"
"s_call_b64 s[42:43], LEXC\n"
"v_cndmask_b32 v154, v154, v58, vcc\n"
"v_cndmask_b32 v155, v155, v58, vcc\n"
"v_cndmask_b32 v156, v156, v58, vcc\n"
"s_branch Lb_425\n"
"Lx_426:\n"
"v_mov_b32 v28, v240\n"
"v_mov_b32 v29, v241\n"
"v_mov_b32 v30, v242\n"
"v_mov_b32 v31, v243\n"
"s_movk_i32 s78, 0x17c0\n"
"s_call_b64 s[42:43], LEXC\n"
"v_cndmask_b32 v157, v157, v58, vcc\n"
"v_cndmask_b32 v158, v158, v58, vcc\n"
"v_cndmask_b32 v159, v159, v58, vcc\n"
"s_branch Lb_427\n"
"Lx_428:\n"
"v_mov_b32 v28, v172\n"
"v_mov_b32 v29, v173\n"
"v_mov_b32 v30, v174\n"
"v_mov_b32 v31, v175\n"
"s_movk_i32 s78, 0x1800\n"
"s_call_b64 s[42:43], LEXC\n"
"v_cndmask_b32 v160, v160, v58, vcc\n"
"v_cndmask_b32 v161, v161, v58, vcc\n"
"v_cndmask_b32 v162, v162, v58, vcc\n"
"s_branch Lb_429\n"
"Lx_430:\n"
"v_mov_b32 v28, v176\n"
"v_mov_b32 v29, v177\n"
"v_mov_b32 v30, v178\n"
"v_mov_b32 v31, v179\n"
"s_movk_i32 s78, 0x1900\n"
"s_call_b64 s[42:43], LEXC\n"
"v_cndmask_b32 v163, v163, v58, vcc\n"
"v_cndmask_b32 v164, v164, v58, vcc\n"
"v_cndmask_b32 v165, v165, v58, vcc\n"
"s_branch Lb_431\n"
"Lx_432:\n"
"v_mov_b32 v28, v180\n"
"v_mov_b32 v29, v181\n"
"v_mov_b32 v30, v182\n"
"v_mov_b32 v31, v183\n"
"s_movk_i32 s78, 0x1a00\n"
"s_call_b64 s[42:43], LEXC\n"
"v_cndmask_b32 v166, v166, v58, vcc\n"
"v_cndmask_b32 v167, v167, v58, vcc\n"
"v_cndmask_b32 v168, v168, v58, vcc\n"
"s_branch Lb_433\n"
"Lx_434:\n"
"v_mov_b32 v28, v196\n"
"v_mov_b32 v29, v197\n"
"v_mov_b32 v30, v198\n"
"v_mov_b32 v31, v199\n"
"s_movk_i32 s78, 0x1b00\n"
"s_call_b64 s[42:43], LEXC\n"
"v_cndmask_b32 v169, v169, v58, vcc\n"
"v_cndmask_b32 v170, v170, v58, vcc\n"
"v_cndmask_b32 v171, v171, v58, vcc\n"
"s_branch Lb_435\n"
"Lx_436:\n"
"v_mov_b32 v28, v200\n"
"v_mov_b32 v29, v201\n"
"v_mov_b32 v30, v202\n"
"v_mov_b32 v31, v203\n"
"s_movk_i32 s78, 0x1840\n"
"s_call_b64 s[42:43], LEXC\n"
"v_cndmask_b32 v184, v184, v58, vcc\n"
"v_cndmask_b32 v185, v185, v58, vcc\n"
"v_cndmask_b32 v186, v186, v58, vcc\n"
"s_branch Lb_437\n"
"Lx_438:\n"
"v_mov_b32 v28, v204\n"
"v_mov_b32 v29, v205\n"
"v_mov_b32 v30, v206\n"
"v_mov_b32 v31, v207\n"
"s_movk_i32 s78, 0x1940\n"
"s_call_b64 s[42:43], LEXC\n"
"v_cndmask_b32 v187, v187, v58, vcc\n"
"v_cndmask_b32 v188, v188, v58, vcc\n"
"v_cndmask_b32 v189, v189, v58, vcc\n"
"s_branch Lb_439\n"
"Lx_440:\n"
"v_mov_b32 v28, v220\n"
"v_mov_b32 v29, v221\n"
"v_mov_b32 v30, v222\n"
"v_mov_b32 v31, v223\n"
"s_movk_i32 s78, 0x1a40\n"
"s_call_b64 s[42:43], LEXC\n"
"v_cndmask_b32 v190, v190, v58, vcc\n"
"v_cndmask_b32 v191, v191, v58, vcc\n"
"v_cndmask_b32 v192, v192, v58, vcc\n"
"s_branch Lb_441\n"
"Lx_442:\n"
"v_mov_b32 v28, v224\n"
"v_mov_b32 v29, v225\n"
"v_mov_b32 v30, v226\n"
"v_mov_b32 v31, v227\n"
"s_movk_i32 s78, 0x1b40\n"
"s_call_b64 s[42:43], LEXC\n"
"v_cndmask_b32 v193, v193, v58, vcc\n"
"v_cndmask_b32 v194, v194, v58, vcc\n"
"v_cndmask_b32 v195, v195, v58, vcc\n"
"s_branch Lb_443\n"
"Lx_444:\n"
"v_mov_b32 v28, v228\n"
"v_mov_b32 v29, v229\n"
"v_mov_b32 v30, v230\n"
"v_mov_b32 v31, v231\n"
"s_movk_i32 s78, 0x1880\n"
"s_call_b64 s[42:43], LEXC\n"
"v_cndmask_b32 v208, v208, v58, vcc\n"
"v_cndmask_b32 v209, v209, v58, vcc\n"
"v_cndmask_b32 v210, v210, v58, vcc\n"
"s_branch Lb_445\n"
"Lx_446:\n"
"v_mov_b32 v28, v244\n"
"v_mov_b32 v29, v245\n"
"v_mov_b32 v30, v246\n"
"v_mov_b32 v31, v247\n"
"s_movk_i32 s78, 0x1980\n"
"s_call_b64 s[42:43], LEXC\n"
"v_cndmask_b32 v211, v211, v58, vcc\n"
"v_cndmask_b32 v212, v212, v58, vcc\n"
"v_cndmask_b32 v213, v213, v58, vcc\n"
"s_branch Lb_447\n"
"Lx_450:\n"
"v_mov_b32 v28, v248\n"
"v_mov_b32 v29, v249\n"
"v_mov_b32 v30, v250\n"
"v_mov_b32 v31, v251\n"
"s_movk_i32 s78, 0x1a80\n"
"s_call_b64 s[42:43], LEXC\n"
"v_cndmask_b32 v214, v214, v58, vcc\n"
"v_cndmask_b32 v215, v215, v58, vcc\n"
"v_cndmask_b32 v216, v216, v58, vcc\n"
"s_branch Lb_451\n"
"Lx_452:\n"
"v_mov_b32 v28, v252\n"
"v_mov_b32 v29, v253\n"
"v_mov_b32 v30, v254\n"
"v_mov_b32 v31, v255\n"
"s_movk_i32 s78, 0x1b80\n"
"s_call_b64 s[42:43], LEXC\n"
"v_cndmask_b32 v217, v217, v58, vcc\n"
"v_cndmask_b32 v218, v218, v58, vcc\n"
"v_cndmask_b32 v219, v219, v58, vcc\n"
"s_branch Lb_453\n"
"Lx_454:\n"
"s_movk_i32 s78, 0x18c0\n"
"s_call_b64 s[42:43], LEXC\n"
"v_cndmask_b32 v232, v232, v58, vcc\n"
"v_cndmask_b32 v233, v233, v58, vcc\n"
"v_cndmask_b32 v234, v234, v58, vcc\n"
"s_branch Lb_455\n"
"Lx_456:\n"
"s_movk_i32 s78, 0x19c0\n"
"s_call_b64 s[42:43], LEXC\n"
"v_cndmask_b32 v235, v235, v58, vcc\n"
"v_cndmask_b32 v236, v236, v58, vcc\n"
"v_cndmask_b32 v237, v237, v58, vcc\n"
"s_branch Lb_457\n"
"Lx_460:\n"
"s_movk_i32 s78, 0x1ac0\n"
"s_call_b64 s[42:43], LEXC\n"
"v_cndmask_b32 v238, v238, v58, vcc\n"
"v_cndmask_b32 v239, v239, v58, vcc\n"
"v_cndmask_b32 v240, v240, v58, vcc\n"
"s_branch Lb_461\n"
"Lx_462:\n"
"s_movk_i32 s78, 0x1bc0\n"
"s_call_b64 s[42:43], LEXC\n"
"v_cndmask_b32 v241, v241, v58, vcc\n"
"v_cndmask_b32 v242, v242, v58, vcc\n"
"v_cndmask_b32 v243, v243, v58, vcc\n"
"s_branch Lb_463\n"
"Lx_466:\n"
"s_movk_i32 s78, 0x1c00\n"
"s_call_b64 s[42:43], LEXC\n"
"v_cndmask_b32 v172, v172, v58, vcc\n"
"v_cndmask_b32 v173, v173, v58, vcc\n"
"v_cndmask_b32 v174, v174, v58, vcc\n"
"s_branch Lb_467\n"
"Lx_468:\n"
"s_movk_i32 s78, 0x1d00\n"
"s_call_b64 s[42:43], LEXC\n"
"v_cndmask_b32 v175, v175, v58, vcc\n"
"v_cndmask_b32 v176, v176, v58, vcc\n"
"v_cndmask_b32 v177, v177, v58, vcc\n"
"s_branch Lb_469\n"
"Lx_472:\n"
"s_movk_i32 s78, 0x1e00\n"
"s_call_b64 s[42:43], LEXC\n"
"v_cndmask_b32 v178, v178, v58, vcc\n"
"v_cndmask_b32 v179, v179, v58, vcc\n"
"v_cndmask_b32 v180, v180, v58, vcc\n"
"s_branch Lb_473\n"
"Lx_474:\n"
"s_movk_i32 s78, 0x1f00\n"
"s_call_b64 s[42:43], LEXC\n"
"v_cndmask_b32 v181, v181, v58, vcc\n"
"v_cndmask_b32 v182, v182, v58, vcc\n"
"v_cndmask_b32 v183, v183, v58, vcc\n"
"s_branch Lb_475\n"
"Lx_476:\n"
"s_movk_i32 s78, 0x1c40\n"
"s_call_b64 s[42:43], LEXC\n"
"v_cndmask_b32 v196, v196, v58, vcc\n"
"v_cndmask_b32 v197, v197, v58, vcc\n"
"v_cndmask_b32 v198, v198, v58, vcc\n"
"s_branch Lb_477\n"
"Lx_478:\n"
"s_movk_i32 s78, 0x1d40\n"
"s_call_b64 s[42:43], LEXC\n"
"v_cndmask_b32 v199, v199, v58, vcc\n"
"v_cndmask_b32 v200, v200, v58, vcc\n"
"v_cndmask_b32 v201, v201, v58, vcc\n"
"s_branch Lb_479\n"
"Lx_482:\n"
"v_mov_b32 v28, v220\n"
"v_mov_b32 v29, v221\n"
"v_mov_b32 v30, v222\n"
"v_mov_b32 v31, v223\n"
"s_movk_i32 s78, 0x1e40\n"
"s_call_b64 s[42:43], LEXC\n"
"v_cndmask_b32 v202, v202, v58, vcc\n"
"v_cndmask_b32 v203, v203, v58, vcc\n"
"v_cndmask_b32 v204, v204, v58, vcc\n"
"s_branch Lb_483\n"
"Lx_484:\n"
"v_mov_b32 v28, v224\n"
"v_mov_b32 v29, v225\n"
"v_mov_b32 v30, v226\n"
"v_mov_b32 v31, v227\n"
"s_movk_i32 s78, 0x1f40\n"
"s_call_b64 s[42:43], LEXC\n"
"v_cndmask_b32 v205, v205, v58, vcc\n"
"v_cndmask_b32 v206, v206, v58, vcc\n"
"v_cndmask_b32 v207, v207, v58, vcc\n"
"s_branch Lb_485\n"
"Lx_486:\n"
"v_mov_b32 v28, v228\n"
"v_mov_b32 v29, v229\n"
"v_mov_b32 v30, v230\n"
"v_mov_b32 v31, v231\n"
"s_movk_i32 s78, 0x1c80\n"
"s_call_b64 s[42:43], LEXC\n"
"v_cndmask_b32 v220, v220, v58, vcc\n"
"v_cndmask_b32 v221, v221, v58, vcc\n"
"v_cndmask_b32 v222, v222, v58, vcc\n"
"s_branch Lb_487\n"
"Lx_488:\n"
"v_mov_b32 v28, v244\n"
"v_mov_b32 v29, v245\n"
"v_mov_b32 v30, v246\n"
"v_mov_b32 v31, v247\n"
"s_movk_i32 s78, 0x1d80\n"
"s_call_b64 s[42:43], LEXC\n"
"v_cndmask_b32 v223, v223, v58, vcc\n"
"v_cndmask_b32 v224, v224, v58, vcc\n"
"v_cndmask_b32 v225, v225, v58, vcc\n"
"s_branch Lb_489\n"
"Lx_490:\n"
"v_mov_b32 v28, v248\n"
"v_mov_b32 v29, v249\n"
"v_mov_b32 v30, v250\n"
"v_mov_b32 v31, v251\n"
"s_movk_i32 s78, 0x1e80\n"
"s_call_b64 s[42:43], LEXC\n"
"v_cndmask_b32 v226, v226, v58, vcc\n"
"v_cndmask_b32 v227, v227, v58, vcc\n"
"v_cndmask_b32 v228, v228, v58, vcc\n"
"s_branch Lb_491\n"
"Lx_492:\n"
"v_mov_b32 v28, v252\n"
"v_mov_b32 v29, v253\n"
"v_mov_b32 v30, v254\n"
"v_mov_b32 v31, v255\n"
"s_movk_i32 s78, 0x1f80\n"
"s_call_b64 s[42:43], LEXC\n"
"v_cndmask_b32 v229, v229, v58, vcc\n"
"v_cndmask_b32 v230, v230, v58, vcc\n"
"v_cndmask_b32 v231, v231, v58, vcc\n"
"s_branch Lb_493\n"
"Lx_494:\n"
"s_movk_i32 s78, 0x1cc0\n"
"s_call_b64 s[42:43], LEXC\n"
"v_cndmask_b32 v244, v244, v58, vcc\n"
"v_cndmask_b32 v245, v245, v58, vcc\n"
"v_cndmask_b32 v246, v246, v58, vcc\n"
"s_branch Lb_495\n"
"Lx_496:\n"
"s_movk_i32 s78, 0x1dc0\n"
"s_call_b64 s[42:43], LEXC\n"
"v_cndmask_b32 v247, v247, v58, vcc\n"
"v_cndmask_b32 v248, v248, v58, vcc\n"
"v_cndmask_b32 v249, v249, v58, vcc\n"
"s_branch Lb_497\n"
"Lx_498:\n"
"s_movk_i32 s78, 0x1ec0\n"
"s_call_b64 s[42:43], LEXC\n"
"v_cndmask_b32 v250, v250, v58, vcc\n"
"v_cndmask_b32 v251, v251, v58, vcc\n"
"v_cndmask_b32 v252, v252, v58, vcc\n"
"s_branch Lb_499\n"
"Lx_500:\n"
"s_movk_i32 s78, 0x1fc0\n"
"s_call_b64 s[42:43], LEXC\n"
"v_cndmask_b32 v253, v253, v58, vcc\n"
"v_cndmask_b32 v254, v254, v58, vcc\n"
"v_cndmask_b32 v255, v255, v58, vcc\n"
"s_branch Lb_501\n"
"LEXC:\n"
"s_mov_b64 s[72:73], vcc\n"
"LEXC1:\n"
"s_cmp_ge_u32 s19, 64\n"
"s_cbranch_scc1 LEXC2\n"
"s_ff1_i32_b64 s79, s[72:73]\n"
"s_bitset0_b64 s[72:73], s79\n"
"v_readlane_b32 s74, v28, s79\n"
"v_readlane_b32 s75, v29, s79\n"
"v_readlane_b32 s76, v30, s79\n"
"v_readlane_b32 s77, v31, s79\n"
"s_or_b32 s79, s79, s78\n"
"s_lshl_b64 exec, 1, s19\n"
"s_nop 3\n"
"v_mov_b32 v53, s74\n"
"v_mov_b32 v54, s75\n"
"v_mov_b32 v55, s76\n"
"v_mov_b32 v56, s77\n"
"v_mov_b32 v57, s79\n"
"s_add_u32 s19, s19, 1\n"
"s_cmp_lg_u64 s[72:73], 0\n"
"s_cbranch_scc1 LEXC1\n"
"s_branch LEXC3\n"
"LEXC2:\n"
"s_mov_b32 s21, 1\n"
"LEXC3:\n"
"s_mov_b64 exec, vcc\n"
"v_mov_b32 v58, 0\n"
"v_mov_b32 v59, 0\n"
"v_mov_b32 v60, 0\n"
"s_mov_b64 exec, -1\n"
"s_setpc_b64 s[42:43]\n"
"LKFIX:\n"
"s_cmp_eq_u32 s20, 0\n"
"s_cbranch_scc1 LKFIX2\n"
"v_and_b32 v28, 63, v57\n"
"v_lshlrev_b32 v28, 2, v28\n"
"ds_bpermute_b32 v24, v28, v40\n"
"ds_bpermute_b32 v25, v28, v41\n"
"ds_bpermute_b32 v26, v28, v42\n"
"ds_bpermute_b32 v27, v28, v43\n"
"v_bfe_u32 v29, v57, 2, 2\n"
"v_bfe_u32 v30, v57, 6, 4\n"
"v_lshl_add_u32 v29, v29, 4, v30\n"
"v_and_b32 v30, 0x3c00, v57\n"
"v_or_b32 v29, v29, v30\n"
"v_mov_b32 v28, 0\n"
"s_waitcnt lgkmcnt(0)\n"
"v_lshlrev_b32 v30, 16, v53\n"
"v_lshlrev_b32 v31, 16, v24\n"
"v_fmac_f32 v28, v30, v31\n"
"v_and_b32 v30, 0xffff0000, v53\n"
"v_and_b32 v31, 0xffff0000, v24\n"
"v_fmac_f32 v28, v30, v31\n"
"v_lshlrev_b32 v30, 16, v54\n"
"v_lshlrev_b32 v31, 16, v25\n"
"v_fmac_f32 v28, v30, v31\n"
"v_and_b32 v30, 0xffff0000, v54\n"
"v_and_b32 v31, 0xffff0000, v25\n"
"v_fmac_f32 v28, v30, v31\n"
"v_lshlrev_b32 v30, 16, v55\n"
"v_lshlrev_b32 v31, 16, v26\n"
"v_fmac_f32 v28, v30, v31\n"
"v_and_b32 v30, 0xffff0000, v55\n"
"v_and_b32 v31, 0xffff0000, v26\n"
"v_fmac_f32 v28, v30, v31\n"
"v_lshlrev_b32 v30, 16, v56\n"
"v_lshlrev_b32 v31, 16, v27\n"
"v_fmac_f32 v28, v30, v31\n"
"v_and_b32 v30, 0xffff0000, v56\n"
"v_and_b32 v31, 0xffff0000, v27\n"
"v_fmac_f32 v28, v30, v31\n"
"s_mov_b32 s72, 0\n"
"LKFIX1:\n"
"v_readlane_b32 s74, v29, s72\n"
"v_readlane_b32 s73, v28, s72\n"
"s_add_u32 s72, s72, 1\n"
"s_lshl_b64 s[76:77], 1, s74\n"
"s_bfe_u32 s74, s74, 0x4000a\n"
"s_and_b64 exec, s[76:77], s[46:47]\n"
"s_set_gpr_idx_on s74, gpr_idx(SRC1,DST)\n"
"s_cmp_lt_u32 s72, s20\n"
"v_add_f32 v44, s73, v44\n"
"s_set_gpr_idx_off\n"
"s_cbranch_scc1 LKFIX1\n"
"s_mov_b64 exec, s[46:47]\n"
"LKFIX2:\n"
"s_setpc_b64 s[44:45]\n"
"LA_P2:\n"
"s_nop 7\n"
"s_mov_b32 s56, 0x1000\n"
"buffer_load_dwordx4 v[24:27], v2, s[36:39], s56 offen sc1\n"
"s_mov_b64 s[46:47], 0\n"
"s_mov_b64 exec, 0\n"
"s_cmp_eq_u32 s50, 0\n"
"s_cbranch_scc1 LDRY0\n"
"s_cmp_eq_u32 s50, 1\n"
"s_cbranch_scc1 LDRY1\n"
"s_cmp_eq_u32 s50, 2\n"
"s_cbranch_scc1 LDRY2\n"
"s_cmp_eq_u32 s50, 3\n"
"s_cbranch_scc1 LDRY3\n"
"LDRYR:\n"
"s_waitcnt vmcnt(0) lgkmcnt(0)\n"
"s_mov_b64 s[46:47], -1\n"
"s_mov_b64 exec, -1\n"
"s_nop 7\n"
"v_cmp_eq_u32 vcc, s59, v25\n"
"s_nop 1\n"
"v_cndmask_b32 v24, 0, v24, vcc\n"
"v_cmp_eq_u32 vcc, s59, v27\n"
"s_nop 1\n"
"v_cndmask_b32 v26, 0, v26, vcc\n"
"v_add_u32 v24, v24, v26\n"
"v_cvt_f32_i32 v24, v24\n"
"s_nop 1\n"
"v_add_f32_dpp v24, v24, v24 quad_perm:[1,0,3,2] row_mask:0xf bank_mask:0xf\n"
"s_nop 1\n"
"v_add_f32_dpp v24, v24, v24 quad_perm:[2,3,0,1] row_mask:0xf bank_mask:0xf\n"
"s_nop 1\n"
"v_add_f32_dpp v24, v24, v24 row_half_mirror row_mask:0xf bank_mask:0xf\n"
"s_nop 1\n"
"v_add_f32_dpp v24, v24, v24 row_mirror row_mask:0xf bank_mask:0xf\n"
"s_nop 1\n"
"v_add_f32_dpp v24, v24, v24 row_bcast:15 row_mask:0xa bank_mask:0xf\n"
"s_nop 1\n"
"v_add_f32_dpp v24, v24, v24 row_bcast:31 row_mask:0xc bank_mask:0xf\n"
"s_nop 0\n"
"v_readlane_b32 s70, v24, 63\n"
"s_getreg_b32 s71, hwreg(HW_REG_XCC_ID, 2, 1)\n"
"s_nop 3\n"
"s_lshr_b32 s70, s70, 31\n"
"s_xor_b32 s70, s70, s71\n"
"s_lshl_b32 s99, s70, 13\n"
"LA_P2B:\n"
"v_lshrrev_b32 v32, 4, v0\n"
"v_lshlrev_b32 v32, 7, v32\n"
"v_and_b32 v58, 3, v0\n"
"v_lshl_or_b32 v32, v58, 5, v32\n"
"v_cmp_gt_u32_e64 s[40:41], s20, v0\n"
"v_cmp_gt_u32_e64 s[42:43], s19, v0\n"
"v_bfe_u32 v58, v57, 2, 2\n"
"v_bfe_u32 v59, v57, 4, 2\n"
"v_cndmask_b32_e64 v58, v59, v58, s[40:41]\n"
"s_andn2_b64 s[42:43], s[42:43], s[40:41]\n"
"v_bfe_u32 v59, v57, 6, 4\n"
"v_lshl_add_u32 v58, v58, 4, v59\n"
"v_lshl_add_u32 v1, v58, 4, s57\n"
"v_bfe_u32 v58, v57, 12, 2\n"
"v_lshl_add_u32 v1, v58, 10, v1\n"
"v_bfe_u32 v58, v57, 10, 2\n"
"v_lshl_add_u32 v1, v58, 2, v1\n"
"v_and_b32 v58, 63, v57\n"
"v_lshlrev_b32 v58, 2, v58\n"
"v_and_b32 v59, 15, v57\n"
"v_lshl_add_u32 v59, v59, 4, s57\n"
"v_cndmask_b32_e64 v57, v59, v58, s[40:41]\n"
"v_mov_b32 v58, v4\n"
"v_mov_b32 v59, v5\n"
"v_mov_b32 v60, v6\n"
"v_mov_b32 v48, v7\n"
"v_mov_b32 v44, v8\n"
"v_mov_b32 v45, v9\n"
"v_mov_b32 v46, v10\n"
"v_mov_b32 v47, v11\n"
"v_mov_b32 v49, v12\n"
"v_mov_b32 v50, v13\n"
"v_mov_b32 v51, v14\n"
"v_mov_b32 v52, v15\n"
"v_mov_b32 v20, v16\n"
"v_mov_b32 v21, v17\n"
"v_mov_b32 v22, v18\n"
"v_mov_b32 v23, v19\n"
"v_mov_b32 v4, 0\n"
"v_mov_b32 v5, 0\n"
"v_mov_b32 v6, 0\n"
"v_mov_b32 v7, 0\n"
"v_mov_b32 v8, 0\n"
"v_mov_b32 v9, 0\n"
"v_mov_b32 v10, 0\n"
"v_mov_b32 v11, 0\n"
"v_mov_b32 v12, 0\n"
"v_mov_b32 v13, 0\n"
"v_mov_b32 v14, 0\n"
"v_mov_b32 v15, 0\n"
"v_mov_b32 v16, 0\n"
"v_mov_b32 v17, 0\n"
"v_mov_b32 v18, 0\n"
"v_mov_b32 v19, 0\n"
"s_mov_b32 s51, 1\n"
"s_lshr_b32 s56, s48, 4\n"
"s_lshl_b32 s56, s56, 4\n"
"s_add_u32 s56, s56, s48\n"
"s_lshl_b32 s56, s56, 9\n"
"s_add_u32 s56, s56, 0x10000\n"
"s_add_u32 s56, s56, s99\n"
"v_add_u32 v33, 162304, v32\n"
"s_cmp_lg_u32 s50, 0\n"
"s_cbranch_scc1 LQW\n"
"s_mov_b32 s55, 4194304\n"
"Ltpoll_504:\n"
"buffer_load_dwordx4 v[24:27], v32, s[36:39], s56 offen sc1\n"
"buffer_load_dwordx4 v[28:31], v32, s[36:39], s56 offen offset:16 sc1\n"
"s_waitcnt vmcnt(0)\n"
"v_cmp_ne_u32_e64 vcc, s59, v25\n"
"v_cmp_ne_u32_e64 s[72:73], s59, v27\n"
"v_cmp_ne_u32_e64 s[74:75], s59, v29\n"
"v_cmp_ne_u32_e64 s[76:77], s59, v31\n"
"s_or_b64 vcc, vcc, s[72:73]\n"
"s_or_b64 vcc, vcc, s[74:75]\n"
"s_or_b64 vcc, vcc, s[76:77]\n"
"s_cbranch_scc0 Ltdone_505\n"
"s_sleep 4\n"
"s_sub_u32 s55, s55, 1\n"
"s_cbranch_scc0 Ltpoll_504\n"
"Ltdone_505:\n"
"ds_write_b128 v33, v[24:27]\n"
"ds_write_b128 v33, v[28:31] offset:16\n"
"s_waitcnt lgkmcnt(0)\n"
"s_barrier\n"
"s_branch LQG\n"
"LQW:\n"
"s_barrier\n"
"ds_read_b128 v[24:27], v33\n"
"ds_read_b128 v[28:31], v33 offset:16\n"
"s_waitcnt lgkmcnt(0)\n"
"LQG:\n"
"s_cmp_lg_u32 s21, 0\n"
"s_cbranch_scc0 LDRY0\n"
"v_mov_b32 v40, v24\n"
"v_mov_b32 v41, v26\n"
"v_mov_b32 v42, v28\n"
"v_mov_b32 v43, v30\n"
"s_branch LSLOW\n"
"LDRY0:\n"
"v_lshlrev_b32 v61, 16, v24\n"
"v_and_b32 v62, 0xffff0000, v24\n"
"v_mov_b32 v40, v24\n"
"v_mul_f32 v61, s17, v61\n"
"v_mul_f32 v62, s17, v62\n"
"v_perm_b32 v24, v62, v61, s69\n"
"v_max_f32 v63, |v61|, |v62|\n"
"v_lshlrev_b32 v61, 16, v26\n"
"v_and_b32 v62, 0xffff0000, v26\n"
"v_mov_b32 v41, v26\n"
"v_mul_f32 v61, s17, v61\n"
"v_mul_f32 v62, s17, v62\n"
"v_perm_b32 v26, v62, v61, s69\n"
"v_max3_f32 v63, |v61|, |v62|, v63\n"
"v_lshlrev_b32 v61, 16, v28\n"
"v_and_b32 v62, 0xffff0000, v28\n"
"v_mov_b32 v42, v28\n"
"v_mul_f32 v61, s17, v61\n"
"v_mul_f32 v62, s17, v62\n"
"v_perm_b32 v28, v62, v61, s69\n"
"v_max3_f32 v63, |v61|, |v62|, v63\n"
"v_lshlrev_b32 v61, 16, v30\n"
"v_and_b32 v62, 0xffff0000, v30\n"
"v_mov_b32 v43, v30\n"
"v_mul_f32 v61, s17, v61\n"
"v_mul_f32 v62, s17, v62\n"
"v_perm_b32 v30, v62, v61, s69\n"
"v_max3_f32 v63, |v61|, |v62|, v63\n"
"v_cmp_eq_u32 vcc, 0x7f800000, v63\n"
"v_mov_b32_dpp v4, v24 quad_perm:[0,1,2,3] row_mask:0xf bank_mask:0x1\n"
"v_mov_b32_dpp v5, v26 quad_perm:[0,1,2,3] row_mask:0xf bank_mask:0x1\n"
"v_mov_b32_dpp v6, v28 quad_perm:[0,1,2,3] row_mask:0xf bank_mask:0x1\n"
"v_mov_b32_dpp v7, v30 quad_perm:[0,1,2,3] row_mask:0xf bank_mask:0x1\n"
"v_mov_b32_dpp v8, v24 quad_perm:[0,1,2,3] row_mask:0xf bank_mask:0x2\n"
"v_mov_b32_dpp v9, v26 quad_perm:[0,1,2,3] row_mask:0xf bank_mask:0x2\n"
"v_mov_b32_dpp v10, v28 quad_perm:[0,1,2,3] row_mask:0xf bank_mask:0x2\n"
"v_mov_b32_dpp v11, v30 quad_perm:[0,1,2,3] row_mask:0xf bank_mask:0x2\n"
"v_mov_b32_dpp v12, v24 quad_perm:[0,1,2,3] row_mask:0xf bank_mask:0x4\n"
"v_mov_b32_dpp v13, v26 quad_perm:[0,1,2,3] row_mask:0xf bank_mask:0x4\n"
"v_mov_b32_dpp v14, v28 quad_perm:[0,1,2,3] row_mask:0xf bank_mask:0x4\n"
"v_mov_b32_dpp v15, v30 quad_perm:[0,1,2,3] row_mask:0xf bank_mask:0x4\n"
"v_mov_b32_dpp v16, v24 quad_perm:[0,1,2,3] row_mask:0xf bank_mask:0x8\n"
"v_mov_b32_dpp v17, v26 quad_perm:[0,1,2,3] row_mask:0xf bank_mask:0x8\n"
"v_mov_b32_dpp v18, v28 quad_perm:[0,1,2,3] row_mask:0xf bank_mask:0x8\n"
"v_mov_b32_dpp v19, v30 quad_perm:[0,1,2,3] row_mask:0xf bank_mask:0x8\n"
"s_cbranch_vccnz LSLOW\n"
"v_and_b32 v61, s8, v66\n"
"v_alignbit_b32 v62, v66, v66, 4\n"
"v_perm_b32 v24, v61, v64, s11\n"
"v_and_b32 v62, s8, v62\n"
"v_perm_b32 v25, v61, v64, s12\n"
"v_perm_b32 v26, v62, v65, s11\n"
"v_perm_b32 v27, v62, v65, s12\n"
"v_and_b32 v61, s8, v69\n"
"v_alignbit_b32 v62, v69, v69, 4\n"
"v_mfma_f32_16x16x32_bf16 v[32:35], v[24:27], v[4:7], 0\n"
"v_perm_b32 v28, v61, v67, s11\n"
"v_and_b32 v62, s8, v62\n"
"v_perm_b32 v29, v61, v67, s12\n"
"v_perm_b32 v30, v62, v68, s11\n"
"v_perm_b32 v31, v62, v68, s12\n"
"v_and_b32 v61, s8, v72\n"
"v_alignbit_b32 v62, v72, v72, 4\n"
"v_mfma_f32_16x16x32_bf16 v[32:35], v[28:31], v[8:11], v[32:35]\n"
"v_perm_b32 v24, v61, v70, s11\n"
"v_and_b32 v62, s8, v62\n"
"v_perm_b32 v25, v61, v70, s12\n"
"v_perm_b32 v26, v62, v71, s11\n"
"v_perm_b32 v27, v62, v71, s12\n"
"s_add_u32 s52, s53, 0x0\n"
"s_and_b32 s52, s52, s54\n"
"buffer_load_dwordx4 v[64:67], v2, s[32:35], s52 offen offset:0 nt\n"
"v_and_b32 v61, s8, v75\n"
"v_alignbit_b32 v62, v75, v75, 4\n"
"v_mfma_f32_16x16x32_bf16 v[32:35], v[24:27], v[12:15], v[32:35]\n"
"v_perm_b32 v28, v61, v73, s11\n"
"v_and_b32 v62, s8, v62\n"
"v_perm_b32 v29, v61, v73, s12\n"
"v_perm_b32 v30, v62, v74, s11\n"
"v_perm_b32 v31, v62, v74, s12\n"
"s_add_u32 s52, s53, 0x70000\n"
"s_and_b32 s52, s52, s54\n"
"buffer_load_dwordx4 v[68:71], v2, s[32:35], s52 offen offset:0 nt\n"
"v_and_b32 v61, s8, v78\n"
"v_alignbit_b32 v62, v78, v78, 4\n"
"v_mfma_f32_16x16x32_bf16 v[32:35], v[28:31], v[16:19], v[32:35]\n"
"v_perm_b32 v24, v61, v76, s11\n"
"v_and_b32 v62, s8, v62\n"
"v_perm_b32 v25, v61, v76, s12\n"
"v_perm_b32 v26, v62, v77, s11\n"
"v_perm_b32 v27, v62, v77, s12\n"
"v_and_b32 v61, s8, v81\n"
"v_alignbit_b32 v62, v81, v81, 4\n"
"v_mfma_f32_16x16x32_bf16 v[36:39], v[24:27], v[4:7], 0\n"
"v_perm_b32 v28, v61, v79, s11\n"
"v_and_b32 v62, s8, v62\n"
"v_perm_b32 v29, v61, v79, s12\n"
"v_perm_b32 v30, v62, v80, s11\n"
"v_perm_b32 v31, v62, v80, s12\n"
"v_mov_b32 v72, v58\n"
"v_mov_b32 v73, v59\n"
"v_mov_b32 v74, v60\n"
"v_mov_b32 v75, v48\n"
"v_mov_b32_dpp v63, v32 quad_perm:[0,0,0,0] row_mask:0xf bank_mask:0xf\n"
"v_add_f32_dpp v63, v33, v63 quad_perm:[1,1,1,1] row_mask:0xf bank_mask:0xf\n"
"v_add_f32_dpp v63, v34, v63 quad_perm:[2,2,2,2] row_mask:0xf bank_mask:0xf\n"
"v_add_f32_dpp v63, v35, v63 quad_perm:[3,3,3,3] row_mask:0xf bank_mask:0xf\n"
"v_cndmask_b32_e64 v48, v48, v63, s[64:65]\n"
"v_and_b32 v61, s8, v84\n"
"v_alignbit_b32 v62, v84, v84, 4\n"
"v_mfma_f32_16x16x32_bf16 v[36:39], v[28:31], v[8:11], v[36:39]\n"
"v_perm_b32 v24, v61, v82, s11\n"
"v_and_b32 v62, s8, v62\n"
"v_perm_b32 v25, v61, v82, s12\n"
"v_perm_b32 v26, v62, v83, s11\n"
"v_perm_b32 v27, v62, v83, s12\n"
"v_and_b32 v61, s8, v87\n"
"v_alignbit_b32 v62, v87, v87, 4\n"
"v_mfma_f32_16x16x32_bf16 v[36:39], v[24:27], v[12:15], v[36:39]\n"
"v_perm_b32 v28, v61, v85, s11\n"
"v_and_b32 v62, s8, v62\n"
"v_perm_b32 v29, v61, v85, s12\n"
"v_perm_b32 v30, v62, v86, s11\n"
"v_perm_b32 v31, v62, v86, s12\n"
"s_add_u32 s52, s53, 0xe000\n"
"s_and_b32 s52, s52, s54\n"
"buffer_load_dwordx4 v[80:83], v2, s[32:35], s52 offen offset:0 nt\n"
"v_accvgpr_read_b32 v60, a2\n"
"v_accvgpr_read_b32 v58, a0\n"
"v_mfma_f32_16x16x32_bf16 v[36:39], v[28:31], v[16:19], v[36:39]\n"
"v_accvgpr_read_b32 v59, a1\n"
"v_and_b32 v61, s8, v60\n"
"v_alignbit_b32 v62, v60, v60, 4\n"
"v_perm_b32 v24, v61, v58, s11\n"
"v_and_b32 v62, s8, v62\n"
"v_perm_b32 v25, v61, v58, s12\n"
"v_perm_b32 v26, v62, v59, s11\n"
"v_perm_b32 v27, v62, v59, s12\n"
"s_add_u32 s52, s53, 0x7e000\n"
"s_and_b32 s52, s52, s54\n"
"buffer_load_dwordx4 v[84:87], v2, s[32:35], s52 offen offset:0 nt\n"
"v_accvgpr_read_b32 v60, a5\n"
"v_accvgpr_read_b32 v58, a3\n"
"v_mfma_f32_16x16x32_bf16 v[32:35], v[24:27], v[4:7], 0\n"
"v_accvgpr_read_b32 v59, a4\n"
"v_and_b32 v61, s8, v60\n"
"v_alignbit_b32 v62, v60, v60, 4\n"
"v_perm_b32 v28, v61, v58, s11\n"
"v_and_b32 v62, s8, v62\n"
"v_perm_b32 v29, v61, v58, s12\n"
"v_perm_b32 v30, v62, v59, s11\n"
"v_perm_b32 v31, v62, v59, s12\n"
"v_mov_b32_dpp v63, v36 quad_perm:[0,0,0,0] row_mask:0xf bank_mask:0xf\n"
"v_add_f32_dpp v63, v37, v63 quad_perm:[1,1,1,1] row_mask:0xf bank_mask:0xf\n"
"v_add_f32_dpp v63, v38, v63 quad_perm:[2,2,2,2] row_mask:0xf bank_mask:0xf\n"
"v_add_f32_dpp v63, v39, v63 quad_perm:[3,3,3,3] row_mask:0xf bank_mask:0xf\n"
"v_cndmask_b32_e64 v48, v48, v63, s[66:67]\n"
"v_accvgpr_read_b32 v60, a8\n"
"v_accvgpr_read_b32 v58, a6\n"
"v_mfma_f32_16x16x32_bf16 v[32:35], v[28:31], v[8:11], v[32:35]\n"
"v_accvgpr_read_b32 v59, a7\n"
"v_and_b32 v61, s8, v60\n"
"v_alignbit_b32 v62, v60, v60, 4\n"
"v_perm_b32 v24, v61, v58, s11\n"
"v_and_b32 v62, s8, v62\n"
"v_perm_b32 v25, v61, v58, s12\n"
"v_perm_b32 v26, v62, v59, s11\n"
"v_perm_b32 v27, v62, v59, s12\n"
"s_add_u32 s52, s53, 0x1e3000\n"
"s_and_b32 s52, s52, s54\n"
"buffer_load_dwordx4 a[0:3], v2, s[32:35], s52 offen offset:0 nt\n"
"v_mov_b32 v76, v44\n"
"v_mov_b32 v77, v45\n"
"v_mov_b32 v78, v46\n"
"v_mov_b32 v79, v47\n"
"v_accvgpr_read_b32 v60, a11\n"
"v_accvgpr_read_b32 v58, a9\n"
"v_mfma_f32_16x16x32_bf16 v[32:35], v[24:27], v[12:15], v[32:35]\n"
"v_accvgpr_read_b32 v59, a10\n"
"v_and_b32 v61, s8, v60\n"
"v_alignbit_b32 v62, v60, v60, 4\n"
"v_perm_b32 v28, v61, v58, s11\n"
"v_and_b32 v62, s8, v62\n"
"v_perm_b32 v29, v61, v58, s12\n"
"v_perm_b32 v30, v62, v59, s11\n"
"v_perm_b32 v31, v62, v59, s12\n"
"s_add_u32 s52, s53, 0x253000\n"
"s_and_b32 s52, s52, s54\n"
"buffer_load_dwordx4 a[4:7], v2, s[32:35], s52 offen offset:0 nt\n"
"v_accvgpr_read_b32 v60, a14\n"
"v_accvgpr_read_b32 v58, a12\n"
"v_mfma_f32_16x16x32_bf16 v[32:35], v[28:31], v[16:19], v[32:35]\n"
"v_accvgpr_read_b32 v59, a13\n"
"v_and_b32 v61, s8, v60\n"
"v_alignbit_b32 v62, v60, v60, 4\n"
"v_perm_b32 v24, v61, v58, s11\n"
"v_and_b32 v62, s8, v62\n"
"v_perm_b32 v25, v61, v58, s12\n"
"v_perm_b32 v26, v62, v59, s11\n"
"v_perm_b32 v27, v62, v59, s12\n"
"v_accvgpr_read_b32 v60, a17\n"
"v_accvgpr_read_b32 v58, a15\n"
"v_mfma_f32_16x16x32_bf16 v[36:39], v[24:27], v[4:7], 0\n"
"v_accvgpr_read_b32 v59, a16\n"
"v_and_b32 v61, s8, v60\n"
"v_alignbit_b32 v62, v60, v60, 4\n"
"v_perm_b32 v28, v61, v58, s11\n"
"v_and_b32 v62, s8, v62\n"
"v_perm_b32 v29, v61, v58, s12\n"
"v_perm_b32 v30, v62, v59, s11\n"
"v_perm_b32 v31, v62, v59, s12\n"
"s_add_u32 s52, s53, 0x7000\n"
"s_and_b32 s52, s52, s54\n"
"buffer_load_dwordx4 a[8:11], v2, s[32:35], s52 offen offset:0 nt\n"
"v_mov_b32_dpp v63, v32 quad_perm:[0,0,0,0] row_mask:0xf bank_mask:0xf\n"
"v_add_f32_dpp v63, v33, v63 quad_perm:[1,1,1,1] row_mask:0xf bank_mask:0xf\n"
"v_add_f32_dpp v63, v34, v63 quad_perm:[2,2,2,2] row_mask:0xf bank_mask:0xf\n"
"v_add_f32_dpp v63, v35, v63 quad_perm:[3,3,3,3] row_mask:0xf bank_mask:0xf\n"
"v_cndmask_b32_e64 v44, v44, v63, s[60:61]\n"
"v_accvgpr_read_b32 v60, a20\n"
"v_accvgpr_read_b32 v58, a18\n"
"v_mfma_f32_16x16x32_bf16 v[36:39], v[28:31], v[8:11], v[36:39]\n"
"v_accvgpr_read_b32 v59, a19\n"
"v_and_b32 v61, s8, v60\n"
"v_alignbit_b32 v62, v60, v60, 4\n"
"v_perm_b32 v24, v61, v58, s11\n"
"v_and_b32 v62, s8, v62\n"
"v_perm_b32 v25, v61, v58, s12\n"
"v_perm_b32 v26, v62, v59, s11\n"
"v_perm_b32 v27, v62, v59, s12\n"
"v_accvgpr_read_b32 v60, a23\n"
"v_accvgpr_read_b32 v58, a21\n"
"v_mfma_f32_16x16x32_bf16 v[36:39], v[24:27], v[12:15], v[36:39]\n"
"v_accvgpr_read_b32 v59, a22\n"
"v_and_b32 v61, s8, v60\n"
"v_alignbit_b32 v62, v60, v60, 4\n"
"v_perm_b32 v28, v61, v58, s11\n"
"v_and_b32 v62, s8, v62\n"
"v_perm_b32 v29, v61, v58, s12\n"
"v_perm_b32 v30, v62, v59, s11\n"
"v_perm_b32 v31, v62, v59, s12\n"
"s_add_u32 s52, s53, 0x1dc000\n"
"s_and_b32 s52, s52, s54\n"
"buffer_load_dwordx4 a[16:19], v2, s[32:35], s52 offen offset:0 nt\n"
"v_accvgpr_read_b32 v60, a26\n"
"v_accvgpr_read_b32 v58, a24\n"
"v_mfma_f32_16x16x32_bf16 v[36:39], v[28:31], v[16:19], v[36:39]\n"
"v_accvgpr_read_b32 v59, a25\n"
"v_and_b32 v61, s8, v60\n"
"v_alignbit_b32 v62, v60, v60, 4\n"
"v_perm_b32 v24, v61, v58, s11\n"
"v_and_b32 v62, s8, v62\n"
"v_perm_b32 v25, v61, v58, s12\n"
"v_perm_b32 v26, v62, v59, s11\n"
"v_perm_b32 v27, v62, v59, s12\n"
"s_add_u32 s52, s53, 0x24c000\n"
"s_and_b32 s52, s52, s54\n"
"buffer_load_dwordx4 a[20:23], v2, s[32:35], s52 offen offset:0 nt\n"
"v_accvgpr_read_b32 v60, a29\n"
"v_accvgpr_read_b32 v58, a27\n"
"v_mfma_f32_16x16x32_bf16 v[32:35], v[24:27], v[4:7], 0\n"
"v_accvgpr_read_b32 v59, a28\n"
"v_and_b32 v61, s8, v60\n"
"v_alignbit_b32 v62, v60, v60, 4\n"
"v_perm_b32 v28, v61, v58, s11\n"
"v_and_b32 v62, s8, v62\n"
"v_perm_b32 v29, v61, v58, s12\n"
"v_perm_b32 v30, v62, v59, s11\n"
"v_perm_b32 v31, v62, v59, s12\n"
"s_add_u32 s52, s53, 0x77000\n"
"s_and_b32 s52, s52, s54\n"
"buffer_load_dwordx4 a[12:15], v2, s[32:35], s52 offen offset:0 nt\n"
"v_mov_b32_dpp v63, v36 quad_perm:[0,0,0,0] row_mask:0xf bank_mask:0xf\n"
"v_add_f32_dpp v63, v37, v63 quad_perm:[1,1,1,1] row_mask:0xf bank_mask:0xf\n"
"v_add_f32_dpp v63, v38, v63 quad_perm:[2,2,2,2] row_mask:0xf bank_mask:0xf\n"
"v_add_f32_dpp v63, v39, v63 quad_perm:[3,3,3,3] row_mask:0xf bank_mask:0xf\n"
"v_cndmask_b32_e64 v44, v44, v63, s[62:63]\n"
"v_accvgpr_read_b32 v60, a32\n"
"v_accvgpr_read_b32 v58, a30\n"
"v_mfma_f32_16x16x32_bf16 v[32:35], v[28:31], v[8:11], v[32:35]\n"
"v_accvgpr_read_b32 v59, a31\n"
"v_and_b32 v61, s8, v60\n"
"v_alignbit_b32 v62, v60, v60, 4\n"
"v_perm_b32 v24, v61, v58, s11\n"
"v_and_b32 v62, s8, v62\n"
"v_perm_b32 v25, v61, v58, s12\n"
"v_perm_b32 v26, v62, v59, s11\n"
"v_perm_b32 v27, v62, v59, s12\n"
"s_add_u32 s52, s53, 0x1ea000\n"
"s_and_b32 s52, s52, s54\n"
"buffer_load_dwordx4 a[24:27], v2, s[32:35], s52 offen offset:0 nt\n"
"v_accvgpr_read_b32 v60, a35\n"
"v_accvgpr_read_b32 v58, a33\n"
"v_mfma_f32_16x16x32_bf16 v[32:35], v[24:27], v[12:15], v[32:35]\n"
"v_accvgpr_read_b32 v59, a34\n"
"v_and_b32 v61, s8, v60\n"
"v_alignbit_b32 v62, v60, v60, 4\n"
"v_perm_b32 v28, v61, v58, s11\n"
"v_and_b32 v62, s8, v62\n"
"v_perm_b32 v29, v61, v58, s12\n"
"v_perm_b32 v30, v62, v59, s11\n"
"v_perm_b32 v31, v62, v59, s12\n"
"s_add_u32 s52, s53, 0x25a000\n"
"s_and_b32 s52, s52, s54\n"
"buffer_load_dwordx4 a[28:31], v2, s[32:35], s52 offen offset:0 nt\n"
"v_accvgpr_read_b32 v60, a38\n"
"v_accvgpr_read_b32 v58, a36\n"
"v_mfma_f32_16x16x32_bf16 v[32:35], v[28:31], v[16:19], v[32:35]\n"
"v_accvgpr_read_b32 v59, a37\n"
"v_and_b32 v61, s8, v60\n"
"v_alignbit_b32 v62, v60, v60, 4\n"
"v_perm_b32 v24, v61, v58, s11\n"
"v_and_b32 v62, s8, v62\n"
"v_perm_b32 v25, v61, v58, s12\n"
"v_perm_b32 v26, v62, v59, s11\n"
"v_perm_b32 v27, v62, v59, s12\n"
"v_accvgpr_read_b32 v60, a41\n"
"v_accvgpr_read_b32 v58, a39\n"
"v_mfma_f32_16x16x32_bf16 v[36:39], v[24:27], v[4:7], 0\n"
"v_accvgpr_read_b32 v59, a40\n"
"v_and_b32 v61, s8, v60\n"
"v_alignbit_b32 v62, v60, v60, 4\n"
"v_perm_b32 v28, v61, v58, s11\n"
"v_and_b32 v62, s8, v62\n"
"v_perm_b32 v29, v61, v58, s12\n"
"v_perm_b32 v30, v62, v59, s11\n"
"v_perm_b32 v31, v62, v59, s12\n"
"s_add_u32 s52, s53, 0x15000\n"
"s_and_b32 s52, s52, s54\n"
"buffer_load_dwordx4 a[32:35], v2, s[32:35], s52 offen offset:0 nt\n"
"v_mov_b32_dpp v63, v32 quad_perm:[0,0,0,0] row_mask:0xf bank_mask:0xf\n"
"v_add_f32_dpp v63, v33, v63 quad_perm:[1,1,1,1] row_mask:0xf bank_mask:0xf\n"
"v_add_f32_dpp v63, v34, v63 quad_perm:[2,2,2,2] row_mask:0xf bank_mask:0xf\n"
"v_add_f32_dpp v63, v35, v63 quad_perm:[3,3,3,3] row_mask:0xf bank_mask:0xf\n"
"v_cndmask_b32_e64 v44, v44, v63, s[64:65]\n"
"v_accvgpr_read_b32 v60, a44\n"
"v_accvgpr_read_b32 v58, a42\n"
"v_mfma_f32_16x16x32_bf16 v[36:39], v[28:31], v[8:11], v[36:39]\n"
"v_accvgpr_read_b32 v59, a43\n"
"v_and_b32 v61, s8, v60\n"
"v_alignbit_b32 v62, v60, v60, 4\n"
"v_perm_b32 v24, v61, v58, s11\n"
"v_and_b32 v62, s8, v62\n"
"v_perm_b32 v25, v61, v58, s12\n"
"v_perm_b32 v26, v62, v59, s11\n"
"v_perm_b32 v27, v62, v59, s12\n"
"v_accvgpr_read_b32 v60, a47\n"
"v_accvgpr_read_b32 v58, a45\n"
"v_mfma_f32_16x16x32_bf16 v[36:39], v[24:27], v[12:15], v[36:39]\n"
"v_accvgpr_read_b32 v59, a46\n"
"v_and_b32 v61, s8, v60\n"
"v_alignbit_b32 v62, v60, v60, 4\n"
"v_perm_b32 v28, v61, v58, s11\n"
"v_and_b32 v62, s8, v62\n"
"v_perm_b32 v29, v61, v58, s12\n"
"v_perm_b32 v30, v62, v59, s11\n"
"v_perm_b32 v31, v62, v59, s12\n"
"s_add_u32 s52, s53, 0x1c7000\n"
"s_and_b32 s52, s52, s54\n"
"buffer_load_dwordx4 a[40:43], v2, s[32:35], s52 offen offset:0 nt\n"
"v_accvgpr_read_b32 v60, a50\n"
"v_accvgpr_read_b32 v58, a48\n"
"v_mfma_f32_16x16x32_bf16 v[36:39], v[28:31], v[16:19], v[36:39]\n"
"v_accvgpr_read_b32 v59, a49\n"
"v_and_b32 v61, s8, v60\n"
"v_alignbit_b32 v62, v60, v60, 4\n"
"v_perm_b32 v24, v61, v58, s11\n"
"v_and_b32 v62, s8, v62\n"
"v_perm_b32 v25, v61, v58, s12\n"
"v_perm_b32 v26, v62, v59, s11\n"
"v_perm_b32 v27, v62, v59, s12\n"
"s_add_u32 s52, s53, 0x237000\n"
"s_and_b32 s52, s52, s54\n"
"buffer_load_dwordx4 a[44:47], v2, s[32:35], s52 offen offset:0 nt\n"
"v_accvgpr_read_b32 v60, a53\n"
"v_accvgpr_read_b32 v58, a51\n"
"v_mfma_f32_16x16x32_bf16 v[32:35], v[24:27], v[4:7], 0\n"
"v_accvgpr_read_b32 v59, a52\n"
"v_and_b32 v61, s8, v60\n"
"v_alignbit_b32 v62, v60, v60, 4\n"
"v_perm_b32 v28, v61, v58, s11\n"
"v_and_b32 v62, s8, v62\n"
"v_perm_b32 v29, v61, v58, s12\n"
"v_perm_b32 v30, v62, v59, s11\n"
"v_perm_b32 v31, v62, v59, s12\n"
"s_add_u32 s52, s53, 0x85000\n"
"s_and_b32 s52, s52, s54\n"
"buffer_load_dwordx4 a[36:39], v2, s[32:35], s52 offen offset:0 nt\n"
"v_mov_b32_dpp v63, v36 quad_perm:[0,0,0,0] row_mask:0xf bank_mask:0xf\n"
"v_add_f32_dpp v63, v37, v63 quad_perm:[1,1,1,1] row_mask:0xf bank_mask:0xf\n"
"v_add_f32_dpp v63, v38, v63 quad_perm:[2,2,2,2] row_mask:0xf bank_mask:0xf\n"
"v_add_f32_dpp v63, v39, v63 quad_perm:[3,3,3,3] row_mask:0xf bank_mask:0xf\n"
"v_cndmask_b32_e64 v44, v44, v63, s[66:67]\n"
"v_accvgpr_read_b32 v60, a56\n"
"v_accvgpr_read_b32 v58, a54\n"
"v_mfma_f32_16x16x32_bf16 v[32:35], v[28:31], v[8:11], v[32:35]\n"
"v_accvgpr_read_b32 v59, a55\n"
"v_and_b32 v61, s8, v60\n"
"v_alignbit_b32 v62, v60, v60, 4\n"
"v_perm_b32 v24, v61, v58, s11\n"
"v_and_b32 v62, s8, v62\n"
"v_perm_b32 v25, v61, v58, s12\n"
"v_perm_b32 v26, v62, v59, s11\n"
"v_perm_b32 v27, v62, v59, s12\n"
"s_add_u32 s52, s53, 0x1d5000\n"
"s_and_b32 s52, s52, s54\n"
"buffer_load_dwordx4 a[48:51], v2, s[32:35], s52 offen offset:0 nt\n"
"v_accvgpr_read_b32 v60, a59\n"
"v_accvgpr_read_b32 v58, a57\n"
"v_mfma_f32_16x16x32_bf16 v[32:35], v[24:27], v[12:15], v[32:35]\n"
"v_accvgpr_read_b32 v59, a58\n"
"v_and_b32 v61, s8, v60\n"
"v_alignbit_b32 v62, v60, v60, 4\n"
"v_perm_b32 v28, v61, v58, s11\n"
"v_and_b32 v62, s8, v62\n"
"v_perm_b32 v29, v61, v58, s12\n"
"v_perm_b32 v30, v62, v59, s11\n"
"v_perm_b32 v31, v62, v59, s12\n"
"s_add_u32 s52, s53, 0x245000\n"
"s_and_b32 s52, s52, s54\n"
"buffer_load_dwordx4 a[52:55], v2, s[32:35], s52 offen offset:0 nt\n"
"v_accvgpr_read_b32 v60, a62\n"
"v_accvgpr_read_b32 v58, a60\n"
"v_mfma_f32_16x16x32_bf16 v[32:35], v[28:31], v[16:19], v[32:35]\n"
"v_accvgpr_read_b32 v59, a61\n"
"v_and_b32 v61, s8, v60\n"
"v_alignbit_b32 v62, v60, v60, 4\n"
"v_perm_b32 v24, v61, v58, s11\n"
"v_and_b32 v62, s8, v62\n"
"v_perm_b32 v25, v61, v58, s12\n"
"v_perm_b32 v26, v62, v59, s11\n"
"v_perm_b32 v27, v62, v59, s12\n"
"v_accvgpr_read_b32 v60, a65\n"
"v_accvgpr_read_b32 v58, a63\n"
"v_mfma_f32_16x16x32_bf16 v[36:39], v[24:27], v[4:7], 0\n"
"v_accvgpr_read_b32 v59, a64\n"
"v_and_b32 v61, s8, v60\n"
"v_alignbit_b32 v62, v60, v60, 4\n"
"v_perm_b32 v28, v61, v58, s11\n"
"v_and_b32 v62, s8, v62\n"
"v_perm_b32 v29, v61, v58, s12\n"
"v_perm_b32 v30, v62, v59, s11\n"
"v_perm_b32 v31, v62, v59, s12\n"
"s_add_u32 s52, s53, 0x1c000\n"
"s_and_b32 s52, s52, s54\n"
"buffer_load_dwordx4 a[56:59], v2, s[32:35], s52 offen offset:0 nt\n"
"v_mov_b32_dpp v63, v32 quad_perm:[0,0,0,0] row_mask:0xf bank_mask:0xf\n"
"v_add_f32_dpp v63, v33, v63 quad_perm:[1,1,1,1] row_mask:0xf bank_mask:0xf\n"
"v_add_f32_dpp v63, v34, v63 quad_perm:[2,2,2,2] row_mask:0xf bank_mask:0xf\n"
"v_add_f32_dpp v63, v35, v63 quad_perm:[3,3,3,3] row_mask:0xf bank_mask:0xf\n"
"v_cndmask_b32_e64 v45, v45, v63, s[60:61]\n"
"v_accvgpr_read_b32 v60, a68\n"
"v_accvgpr_read_b32 v58, a66\n"
"v_mfma_f32_16x16x32_bf16 v[36:39], v[28:31], v[8:11], v[36:39]\n"
"v_accvgpr_read_b32 v59, a67\n"
"v_and_b32 v61, s8, v60\n"
"v_alignbit_b32 v62, v60, v60, 4\n"
"v_perm_b32 v24, v61, v58, s11\n"
"v_and_b32 v62, s8, v62\n"
"v_perm_b32 v25, v61, v58, s12\n"
"v_perm_b32 v26, v62, v59, s11\n"
"v_perm_b32 v27, v62, v59, s12\n"
"v_accvgpr_read_b32 v60, a71\n"
"v_accvgpr_read_b32 v58, a69\n"
"v_mfma_f32_16x16x32_bf16 v[36:39], v[24:27], v[12:15], v[36:39]\n"
"v_accvgpr_read_b32 v59, a70\n"
"v_and_b32 v61, s8, v60\n"
"v_alignbit_b32 v62, v60, v60, 4\n"
"v_perm_b32 v28, v61, v58, s11\n"
"v_and_b32 v62, s8, v62\n"
"v_perm_b32 v29, v61, v58, s12\n"
"v_perm_b32 v30, v62, v59, s11\n"
"v_perm_b32 v31, v62, v59, s12\n"
"s_add_u32 s52, s53, 0x1c0000\n"
"s_and_b32 s52, s52, s54\n"
"buffer_load_dwordx4 a[64:67], v2, s[32:35], s52 offen offset:0 nt\n"
"v_accvgpr_read_b32 v60, a74\n"
"v_accvgpr_read_b32 v58, a72\n"
"v_mfma_f32_16x16x32_bf16 v[36:39], v[28:31], v[16:19], v[36:39]\n"
"v_accvgpr_read_b32 v59, a73\n"
"v_and_b32 v61, s8, v60\n"
"v_alignbit_b32 v62, v60, v60, 4\n"
"v_perm_b32 v24, v61, v58, s11\n"
"v_and_b32 v62, s8, v62\n"
"v_perm_b32 v25, v61, v58, s12\n"
"v_perm_b32 v26, v62, v59, s11\n"
"v_perm_b32 v27, v62, v59, s12\n"
"s_add_u32 s52, s53, 0x230000\n"
"s_and_b32 s52, s52, s54\n"
"buffer_load_dwordx4 a[68:71], v2, s[32:35], s52 offen offset:0 nt\n"
"v_accvgpr_read_b32 v60, a77\n"
"v_accvgpr_read_b32 v58, a75\n"
"v_mfma_f32_16x16x32_bf16 v[32:35], v[24:27], v[4:7], 0\n"
"v_accvgpr_read_b32 v59, a76\n"
"v_and_b32 v61, s8, v60\n"
"v_alignbit_b32 v62, v60, v60, 4\n"
"v_perm_b32 v28, v61, v58, s11\n"
"v_and_b32 v62, s8, v62\n"
"v_perm_b32 v29, v61, v58, s12\n"
"v_perm_b32 v30, v62, v59, s11\n"
"v_perm_b32 v31, v62, v59, s12\n"
"s_add_u32 s52, s53, 0x8c000\n"
"s_and_b32 s52, s52, s54\n"
"buffer_load_dwordx4 a[60:63], v2, s[32:35], s52 offen offset:0 nt\n"
"v_mov_b32_dpp v63, v36 quad_perm:[0,0,0,0] row_mask:0xf bank_mask:0xf\n"
"v_add_f32_dpp v63, v37, v63 quad_perm:[1,1,1,1] row_mask:0xf bank_mask:0xf\n"
"v_add_f32_dpp v63, v38, v63 quad_perm:[2,2,2,2] row_mask:0xf bank_mask:0xf\n"
"v_add_f32_dpp v63, v39, v63 quad_perm:[3,3,3,3] row_mask:0xf bank_mask:0xf\n"
"v_cndmask_b32_e64 v45, v45, v63, s[62:63]\n"
"v_accvgpr_read_b32 v60, a80\n"
"v_accvgpr_read_b32 v58, a78\n"
"v_mfma_f32_16x16x32_bf16 v[32:35], v[28:31], v[8:11], v[32:35]\n"
"v_accvgpr_read_b32 v59, a79\n"
"v_and_b32 v61, s8, v60\n"
"v_alignbit_b32 v62, v60, v60, 4\n"
"v_perm_b32 v24, v61, v58, s11\n"
"v_and_b32 v62, s8, v62\n"
"v_perm_b32 v25, v61, v58, s12\n"
"v_perm_b32 v26, v62, v59, s11\n"
"v_perm_b32 v27, v62, v59, s12\n"
"s_add_u32 s52, s53, 0x1ce000\n"
"s_and_b32 s52, s52, s54\n"
"buffer_load_dwordx4 a[72:75], v2, s[32:35], s52 offen offset:0 nt\n"
"v_accvgpr_read_b32 v60, a83\n"
"v_accvgpr_read_b32 v58, a81\n"
"v_mfma_f32_16x16x32_bf16 v[32:35], v[24:27], v[12:15], v[32:35]\n"
"v_accvgpr_read_b32 v59, a82\n"
"v_and_b32 v61, s8, v60\n"
"v_alignbit_b32 v62, v60, v60, 4\n"
"v_perm_b32 v28, v61, v58, s11\n"
"v_and_b32 v62, s8, v62\n"
"v_perm_b32 v29, v61, v58, s12\n"
"v_perm_b32 v30, v62, v59, s11\n"
"v_perm_b32 v31, v62, v59, s12\n"
"s_add_u32 s52, s53, 0x23e000\n"
"s_and_b32 s52, s52, s54\n"
"buffer_load_dwordx4 a[76:79], v2, s[32:35], s52 offen offset:0 nt\n"
"v_accvgpr_read_b32 v60, a86\n"
"v_accvgpr_read_b32 v58, a84\n"
"v_mfma_f32_16x16x32_bf16 v[32:35], v[28:31], v[16:19], v[32:35]\n"
"v_accvgpr_read_b32 v59, a85\n"
"v_and_b32 v61, s8, v60\n"
"v_alignbit_b32 v62, v60, v60, 4\n"
"v_perm_b32 v24, v61, v58, s11\n"
"v_and_b32 v62, s8, v62\n"
"v_perm_b32 v25, v61, v58, s12\n"
"v_perm_b32 v26, v62, v59, s11\n"
"v_perm_b32 v27, v62, v59, s12\n"
"v_accvgpr_read_b32 v60, a89\n"
"v_accvgpr_read_b32 v58, a87\n"
"v_mfma_f32_16x16x32_bf16 v[36:39], v[24:27], v[4:7], 0\n"
"v_accvgpr_read_b32 v59, a88\n"
"v_and_b32 v61, s8, v60\n"
"v_alignbit_b32 v62, v60, v60, 4\n"
"v_perm_b32 v28, v61, v58, s11\n"
"v_and_b32 v62, s8, v62\n"
"v_perm_b32 v29, v61, v58, s12\n"
"v_perm_b32 v30, v62, v59, s11\n"
"v_perm_b32 v31, v62, v59, s12\n"
"s_add_u32 s52, s53, 0x2a000\n"
"s_and_b32 s52, s52, s54\n"
"buffer_load_dwordx4 a[80:83], v2, s[32:35], s52 offen offset:0 nt\n"
"v_mov_b32_dpp v63, v32 quad_perm:[0,0,0,0] row_mask:0xf bank_mask:0xf\n"
"v_add_f32_dpp v63, v33, v63 quad_perm:[1,1,1,1] row_mask:0xf bank_mask:0xf\n"
"v_add_f32_dpp v63, v34, v63 quad_perm:[2,2,2,2] row_mask:0xf bank_mask:0xf\n"
"v_add_f32_dpp v63, v35, v63 quad_perm:[3,3,3,3] row_mask:0xf bank_mask:0xf\n"
"v_cndmask_b32_e64 v45, v45, v63, s[64:65]\n"
"v_accvgpr_read_b32 v60, a92\n"
"v_accvgpr_read_b32 v58, a90\n"
"v_mfma_f32_16x16x32_bf16 v[36:39], v[28:31], v[8:11], v[36:39]\n"
"v_accvgpr_read_b32 v59, a91\n"
"v_and_b32 v61, s8, v60\n"
"v_alignbit_b32 v62, v60, v60, 4\n"
"v_perm_b32 v24, v61, v58, s11\n"
"v_and_b32 v62, s8, v62\n"
"v_perm_b32 v25, v61, v58, s12\n"
"v_perm_b32 v26, v62, v59, s11\n"
"v_perm_b32 v27, v62, v59, s12\n"
"v_accvgpr_read_b32 v60, a95\n"
"v_accvgpr_read_b32 v58, a93\n"
"v_mfma_f32_16x16x32_bf16 v[36:39], v[24:27], v[12:15], v[36:39]\n"
"v_accvgpr_read_b32 v59, a94\n"
"v_and_b32 v61, s8, v60\n"
"v_alignbit_b32 v62, v60, v60, 4\n"
"v_perm_b32 v28, v61, v58, s11\n"
"v_and_b32 v62, s8, v62\n"
"v_perm_b32 v29, v61, v58, s12\n"
"v_perm_b32 v30, v62, v59, s11\n"
"v_perm_b32 v31, v62, v59, s12\n"
"v_accvgpr_write_b32 a88, v49\n"
"v_accvgpr_write_b32 a89, v50\n"
"v_accvgpr_write_b32 a90, v51\n"
"v_accvgpr_write_b32 a91, v52\n"
"v_mov_b32 v52, 0xff800000\n"
"v_accvgpr_read_b32 v60, a98\n"
"v_accvgpr_read_b32 v58, a96\n"
"v_mfma_f32_16x16x32_bf16 v[36:39], v[28:31], v[16:19], v[36:39]\n"
"v_accvgpr_read_b32 v59, a97\n"
"v_and_b32 v61, s8, v60\n"
"v_alignbit_b32 v62, v60, v60, 4\n"
"v_perm_b32 v24, v61, v58, s11\n"
"v_and_b32 v62, s8, v62\n"
"v_perm_b32 v25, v61, v58, s12\n"
"v_perm_b32 v26, v62, v59, s11\n"
"v_perm_b32 v27, v62, v59, s12\n"
"v_accvgpr_read_b32 v60, a101\n"
"v_accvgpr_read_b32 v58, a99\n"
"v_mfma_f32_16x16x32_bf16 v[32:35], v[24:27], v[4:7], 0\n"
"v_accvgpr_read_b32 v59, a100\n"
"v_and_b32 v61, s8, v60\n"
"v_alignbit_b32 v62, v60, v60, 4\n"
"v_perm_b32 v28, v61, v58, s11\n"
"v_and_b32 v62, s8, v62\n"
"v_perm_b32 v29, v61, v58, s12\n"
"v_perm_b32 v30, v62, v59, s11\n"
"v_perm_b32 v31, v62, v59, s12\n"
"v_mov_b32_dpp v63, v36 quad_perm:[0,0,0,0] row_mask:0xf bank_mask:0xf\n"
"v_add_f32_dpp v63, v37, v63 quad_perm:[1,1,1,1] row_mask:0xf bank_mask:0xf\n"
"v_add_f32_dpp v63, v38, v63 quad_perm:[2,2,2,2] row_mask:0xf bank_mask:0xf\n"
"v_add_f32_dpp v63, v39, v63 quad_perm:[3,3,3,3] row_mask:0xf bank_mask:0xf\n"
"v_cndmask_b32_e64 v45, v45, v63, s[66:67]\n"
"v_accvgpr_read_b32 v60, a104\n"
"v_accvgpr_read_b32 v58, a102\n"
"v_mfma_f32_16x16x32_bf16 v[32:35], v[28:31], v[8:11], v[32:35]\n"
"v_accvgpr_read_b32 v59, a103\n"
"v_and_b32 v61, s8, v60\n"
"v_alignbit_b32 v62, v60, v60, 4\n"
"v_perm_b32 v24, v61, v58, s11\n"
"v_and_b32 v62, s8, v62\n"
"v_perm_b32 v25, v61, v58, s12\n"
"v_perm_b32 v26, v62, v59, s11\n"
"v_perm_b32 v27, v62, v59, s12\n"
"s_add_u32 s52, s53, 0x13b000\n"
"s_and_b32 s52, s52, s54\n"
"buffer_load_dwordx4 a[96:99], v2, s[32:35], s52 offen offset:0 nt\n"
"v_accvgpr_write_b32 a92, v20\n"
"v_accvgpr_write_b32 a93, v21\n"
"v_accvgpr_write_b32 a94, v22\n"
"v_accvgpr_write_b32 a95, v23\n"
"ds_bpermute_b32 v20, v57, v40\n"
"ds_bpermute_b32 v21, v57, v41\n"
"ds_bpermute_b32 v22, v57, v42\n"
"ds_bpermute_b32 v23, v57, v43\n"
"v_accvgpr_read_b32 v60, a107\n"
"v_accvgpr_read_b32 v58, a105\n"
"v_mfma_f32_16x16x32_bf16 v[32:35], v[24:27], v[12:15], v[32:35]\n"
"v_accvgpr_read_b32 v59, a106\n"
"v_and_b32 v61, s8, v60\n"
"v_alignbit_b32 v62, v60, v60, 4\n"
"v_perm_b32 v28, v61, v58, s11\n"
"v_and_b32 v62, s8, v62\n"
"v_perm_b32 v29, v61, v58, s12\n"
"v_perm_b32 v30, v62, v59, s11\n"
"v_perm_b32 v31, v62, v59, s12\n"
"s_add_u32 s52, s53, 0x1ab000\n"
"s_and_b32 s52, s52, s54\n"
"buffer_load_dwordx4 a[100:103], v2, s[32:35], s52 offen offset:0 nt\n"
"v_accvgpr_read_b32 v60, a110\n"
"v_accvgpr_read_b32 v58, a108\n"
"v_mfma_f32_16x16x32_bf16 v[32:35], v[28:31], v[16:19], v[32:35]\n"
"v_accvgpr_read_b32 v59, a109\n"
"v_and_b32 v61, s8, v60\n"
"v_alignbit_b32 v62, v60, v60, 4\n"
"v_perm_b32 v24, v61, v58, s11\n"
"v_and_b32 v62, s8, v62\n"
"v_perm_b32 v25, v61, v58, s12\n"
"v_perm_b32 v26, v62, v59, s11\n"
"v_perm_b32 v27, v62, v59, s12\n"
"s_add_u32 s52, s53, 0x9a000\n"
"s_and_b32 s52, s52, s54\n"
"buffer_load_dwordx4 a[84:87], v2, s[32:35], s52 offen offset:0 nt\n"
"v_accvgpr_read_b32 v60, a113\n"
"v_accvgpr_read_b32 v58, a111\n"
"v_mfma_f32_16x16x32_bf16 v[36:39], v[24:27], v[4:7], 0\n"
"v_accvgpr_read_b32 v59, a112\n"
"v_and_b32 v61, s8, v60\n"
"v_alignbit_b32 v62, v60, v60, 4\n"
"v_perm_b32 v28, v61, v58, s11\n"
"v_and_b32 v62, s8, v62\n"
"v_perm_b32 v29, v61, v58, s12\n"
"v_perm_b32 v30, v62, v59, s11\n"
"v_perm_b32 v31, v62, v59, s12\n"
"s_add_u32 s52, s53, 0x149000\n"
"s_and_b32 s52, s52, s54\n"
"buffer_load_dwordx4 a[104:107], v2, s[32:35], s52 offen offset:0 nt\n"
"v_mov_b32_dpp v63, v32 quad_perm:[0,0,0,0] row_mask:0xf bank_mask:0xf\n"
"v_add_f32_dpp v63, v33, v63 quad_perm:[1,1,1,1] row_mask:0xf bank_mask:0xf\n"
"v_add_f32_dpp v63, v34, v63 quad_perm:[2,2,2,2] row_mask:0xf bank_mask:0xf\n"
"v_add_f32_dpp v63, v35, v63 quad_perm:[3,3,3,3] row_mask:0xf bank_mask:0xf\n"
"v_cndmask_b32_e64 v46, v46, v63, s[60:61]\n"
"v_accvgpr_read_b32 v60, a116\n"
"v_accvgpr_read_b32 v58, a114\n"
"v_mfma_f32_16x16x32_bf16 v[36:39], v[28:31], v[8:11], v[36:39]\n"
"s_cbranch_execz LDRYR\n"
"LDRY1:\n"
"v_accvgpr_read_b32 v59, a115\n"
"v_and_b32 v61, s8, v60\n"
"v_alignbit_b32 v62, v60, v60, 4\n"
"v_perm_b32 v24, v61, v58, s11\n"
"v_and_b32 v62, s8, v62\n"
"v_perm_b32 v25, v61, v58, s12\n"
"v_perm_b32 v26, v62, v59, s11\n"
"v_perm_b32 v27, v62, v59, s12\n"
"s_add_u32 s52, s53, 0x1b9000\n"
"s_and_b32 s52, s52, s54\n"
"buffer_load_dwordx4 a[108:111], v2, s[32:35], s52 offen offset:0 nt\n"
"v_accvgpr_read_b32 v60, a119\n"
"v_accvgpr_read_b32 v58, a117\n"
"v_mfma_f32_16x16x32_bf16 v[36:39], v[24:27], v[12:15], v[36:39]\n"
"v_accvgpr_read_b32 v59, a118\n"
"v_and_b32 v61, s8, v60\n"
"v_alignbit_b32 v62, v60, v60, 4\n"
"v_perm_b32 v28, v61, v58, s11\n"
"v_and_b32 v62, s8, v62\n"
"v_perm_b32 v29, v61, v58, s12\n"
"v_perm_b32 v30, v62, v59, s11\n"
"v_perm_b32 v31, v62, v59, s12\n"
"s_add_u32 s52, s53, 0x23000\n"
"s_and_b32 s52, s52, s54\n"
"buffer_load_dwordx4 a[112:115], v2, s[32:35], s52 offen offset:0 nt\n"
"v_accvgpr_read_b32 v60, a122\n"
"v_accvgpr_read_b32 v58, a120\n"
"v_mfma_f32_16x16x32_bf16 v[36:39], v[28:31], v[16:19], v[36:39]\n"
"v_accvgpr_read_b32 v59, a121\n"
"v_and_b32 v61, s8, v60\n"
"v_alignbit_b32 v62, v60, v60, 4\n"
"v_perm_b32 v24, v61, v58, s11\n"
"v_and_b32 v62, s8, v62\n"
"v_perm_b32 v25, v61, v58, s12\n"
"v_perm_b32 v26, v62, v59, s11\n"
"v_perm_b32 v27, v62, v59, s12\n"
"v_accvgpr_read_b32 v60, a125\n"
"v_accvgpr_read_b32 v58, a123\n"
"v_mfma_f32_16x16x32_bf16 v[32:35], v[24:27], v[4:7], 0\n"
"v_accvgpr_read_b32 v59, a124\n"
"v_and_b32 v61, s8, v60\n"
"v_alignbit_b32 v62, v60, v60, 4\n"
"v_perm_b32 v28, v61, v58, s11\n"
"v_and_b32 v62, s8, v62\n"
"v_perm_b32 v29, v61, v58, s12\n"
"v_perm_b32 v30, v62, v59, s11\n"
"v_perm_b32 v31, v62, v59, s12\n"
"v_mov_b32_dpp v63, v36 quad_perm:[0,0,0,0] row_mask:0xf bank_mask:0xf\n"
"v_add_f32_dpp v63, v37, v63 quad_perm:[1,1,1,1] row_mask:0xf bank_mask:0xf\n"
"v_add_f32_dpp v63, v38, v63 quad_perm:[2,2,2,2] row_mask:0xf bank_mask:0xf\n"
"v_add_f32_dpp v63, v39, v63 quad_perm:[3,3,3,3] row_mask:0xf bank_mask:0xf\n"
"v_cndmask_b32_e64 v46, v46, v63, s[62:63]\n"
"v_accvgpr_read_b32 v60, a128\n"
"v_accvgpr_read_b32 v58, a126\n"
"v_mfma_f32_16x16x32_bf16 v[32:35], v[28:31], v[8:11], v[32:35]\n"
"v_accvgpr_read_b32 v59, a127\n"
"v_and_b32 v61, s8, v60\n"
"v_alignbit_b32 v62, v60, v60, 4\n"
"v_perm_b32 v24, v61, v58, s11\n"
"v_and_b32 v62, s8, v62\n"
"v_perm_b32 v25, v61, v58, s12\n"
"v_perm_b32 v26, v62, v59, s11\n"
"v_perm_b32 v27, v62, v59, s12\n"
"s_add_u32 s52, s53, 0x134000\n"
"s_and_b32 s52, s52, s54\n"
"buffer_load_dwordx4 a[120:123], v2, s[32:35], s52 offen offset:0 nt\n"
"s_waitcnt lgkmcnt(0)\n"
"v_dot2_f32_bf16 v40, v53, v20, 0\n"
"v_dot2_f32_bf16 v40, v54, v21, v40\n"
"v_dot2_f32_bf16 v40, v55, v22, v40\n"
"v_dot2_f32_bf16 v40, v56, v23, v40\n"
"v_mov_b32 v20, 0\n"
"v_mov_b32 v21, 0\n"
"v_mov_b32 v22, 0\n"
"v_mov_b32 v23, 0\n"
"v_accvgpr_read_b32 v60, a131\n"
"v_accvgpr_read_b32 v58, a129\n"
"v_mfma_f32_16x16x32_bf16 v[32:35], v[24:27], v[12:15], v[32:35]\n"
"v_accvgpr_read_b32 v59, a130\n"
"v_and_b32 v61, s8, v60\n"
"v_alignbit_b32 v62, v60, v60, 4\n"
"v_perm_b32 v28, v61, v58, s11\n"
"v_and_b32 v62, s8, v62\n"
"v_perm_b32 v29, v61, v58, s12\n"
"v_perm_b32 v30, v62, v59, s11\n"
"v_perm_b32 v31, v62, v59, s12\n"
"s_add_u32 s52, s53, 0x1a4000\n"
"s_and_b32 s52, s52, s54\n"
"buffer_load_dwordx4 a[124:127], v2, s[32:35], s52 offen offset:0 nt\n"
"v_accvgpr_read_b32 v60, a134\n"
"v_accvgpr_read_b32 v58, a132\n"
"v_mfma_f32_16x16x32_bf16 v[32:35], v[28:31], v[16:19], v[32:35]\n"
"v_accvgpr_read_b32 v59, a133\n"
"v_and_b32 v61, s8, v60\n"
"v_alignbit_b32 v62, v60, v60, 4\n"
"v_perm_b32 v24, v61, v58, s11\n"
"v_and_b32 v62, s8, v62\n"
"v_perm_b32 v25, v61, v58, s12\n"
"v_perm_b32 v26, v62, v59, s11\n"
"v_perm_b32 v27, v62, v59, s12\n"
"s_add_u32 s52, s53, 0x93000\n"
"s_and_b32 s52, s52, s54\n"
"buffer_load_dwordx4 a[116:119], v2, s[32:35], s52 offen offset:0 nt\n"
"v_accvgpr_read_b32 v60, a137\n"
"v_accvgpr_read_b32 v58, a135\n"
"v_mfma_f32_16x16x32_bf16 v[36:39], v[24:27], v[4:7], 0\n"
"v_accvgpr_read_b32 v59, a136\n"
"v_and_b32 v61, s8, v60\n"
"v_alignbit_b32 v62, v60, v60, 4\n"
"v_perm_b32 v28, v61, v58, s11\n"
"v_and_b32 v62, s8, v62\n"
"v_perm_b32 v29, v61, v58, s12\n"
"v_perm_b32 v30, v62, v59, s11\n"
"v_perm_b32 v31, v62, v59, s12\n"
"s_add_u32 s52, s53, 0x142000\n"
"s_and_b32 s52, s52, s54\n"
"buffer_load_dwordx4 a[128:131], v2, s[32:35], s52 offen offset:0 nt\n"
"v_mov_b32_dpp v63, v32 quad_perm:[0,0,0,0] row_mask:0xf bank_mask:0xf\n"
"v_add_f32_dpp v63, v33, v63 quad_perm:[1,1,1,1] row_mask:0xf bank_mask:0xf\n"
"v_add_f32_dpp v63, v34, v63 quad_perm:[2,2,2,2] row_mask:0xf bank_mask:0xf\n"
"v_add_f32_dpp v63, v35, v63 quad_perm:[3,3,3,3] row_mask:0xf bank_mask:0xf\n"
"v_cndmask_b32_e64 v46, v46, v63, s[64:65]\n"
"v_accvgpr_read_b32 v60, a140\n"
"v_accvgpr_read_b32 v58, a138\n"
"v_mfma_f32_16x16x32_bf16 v[36:39], v[28:31], v[8:11], v[36:39]\n"
"v_accvgpr_read_b32 v59, a139\n"
"v_and_b32 v61, s8, v60\n"
"v_alignbit_b32 v62, v60, v60, 4\n"
"v_perm_b32 v24, v61, v58, s11\n"
"v_and_b32 v62, s8, v62\n"
"v_perm_b32 v25, v61, v58, s12\n"
"v_perm_b32 v26, v62, v59, s11\n"
"v_perm_b32 v27, v62, v59, s12\n"
"s_add_u32 s52, s53, 0x1b2000\n"
"s_and_b32 s52, s52, s54\n"
"buffer_load_dwordx4 a[132:135], v2, s[32:35], s52 offen offset:0 nt\n"
"v_accvgpr_read_b32 v60, a143\n"
"v_accvgpr_read_b32 v58, a141\n"
"v_mfma_f32_16x16x32_bf16 v[36:39], v[24:27], v[12:15], v[36:39]\n"
"v_accvgpr_read_b32 v59, a142\n"
"v_and_b32 v61, s8, v60\n"
"v_alignbit_b32 v62, v60, v60, 4\n"
"v_perm_b32 v28, v61, v58, s11\n"
"v_and_b32 v62, s8, v62\n"
"v_perm_b32 v29, v61, v58, s12\n"
"v_perm_b32 v30, v62, v59, s11\n"
"v_perm_b32 v31, v62, v59, s12\n"
"s_add_u32 s52, s53, 0x31000\n"
"s_and_b32 s52, s52, s54\n"
"buffer_load_dwordx4 a[136:139], v2, s[32:35], s52 offen offset:0 nt\n"
"v_accvgpr_read_b32 v60, a146\n"
"v_accvgpr_read_b32 v58, a144\n"
"v_mfma_f32_16x16x32_bf16 v[36:39], v[28:31], v[16:19], v[36:39]\n"
"v_accvgpr_read_b32 v59, a145\n"
"v_and_b32 v61, s8, v60\n"
"v_alignbit_b32 v62, v60, v60, 4\n"
"v_perm_b32 v24, v61, v58, s11\n"
"v_and_b32 v62, s8, v62\n"
"v_perm_b32 v25, v61, v58, s12\n"
"v_perm_b32 v26, v62, v59, s11\n"
"v_perm_b32 v27, v62, v59, s12\n"
"v_accvgpr_read_b32 v60, a149\n"
"v_accvgpr_read_b32 v58, a147\n"
"v_mfma_f32_16x16x32_bf16 v[32:35], v[24:27], v[4:7], 0\n"
"v_accvgpr_read_b32 v59, a148\n"
"v_and_b32 v61, s8, v60\n"
"v_alignbit_b32 v62, v60, v60, 4\n"
"v_perm_b32 v28, v61, v58, s11\n"
"v_and_b32 v62, s8, v62\n"
"v_perm_b32 v29, v61, v58, s12\n"
"v_perm_b32 v30, v62, v59, s11\n"
"v_perm_b32 v31, v62, v59, s12\n"
"v_mov_b32_dpp v63, v36 quad_perm:[0,0,0,0] row_mask:0xf bank_mask:0xf\n"
"v_add_f32_dpp v63, v37, v63 quad_perm:[1,1,1,1] row_mask:0xf bank_mask:0xf\n"
"v_add_f32_dpp v63, v38, v63 quad_perm:[2,2,2,2] row_mask:0xf bank_mask:0xf\n"
"v_add_f32_dpp v63, v39, v63 quad_perm:[3,3,3,3] row_mask:0xf bank_mask:0xf\n"
"v_cndmask_b32_e64 v46, v46, v63, s[66:67]\n"
"v_accvgpr_read_b32 v60, a152\n"
"v_accvgpr_read_b32 v58, a150\n"
"v_mfma_f32_16x16x32_bf16 v[32:35], v[28:31], v[8:11], v[32:35]\n"
"v_accvgpr_read_b32 v59, a151\n"
"v_and_b32 v61, s8, v60\n"
"v_alignbit_b32 v62, v60, v60, 4\n"
"v_perm_b32 v24, v61, v58, s11\n"
"v_and_b32 v62, s8, v62\n"
"v_perm_b32 v25, v61, v58, s12\n"
"v_perm_b32 v26, v62, v59, s11\n"
"v_perm_b32 v27, v62, v59, s12\n"
"s_add_u32 s52, s53, 0x11f000\n"
"s_and_b32 s52, s52, s54\n"
"buffer_load_dwordx4 a[144:147], v2, s[32:35], s52 offen offset:0 nt\n"
"v_accvgpr_read_b32 v60, a155\n"
"v_accvgpr_read_b32 v58, a153\n"
"v_mfma_f32_16x16x32_bf16 v[32:35], v[24:27], v[12:15], v[32:35]\n"
"v_accvgpr_read_b32 v59, a154\n"
"v_and_b32 v61, s8, v60\n"
"v_alignbit_b32 v62, v60, v60, 4\n"
"v_perm_b32 v28, v61, v58, s11\n"
"v_and_b32 v62, s8, v62\n"
"v_perm_b32 v29, v61, v58, s12\n"
"v_perm_b32 v30, v62, v59, s11\n"
"v_perm_b32 v31, v62, v59, s12\n"
"s_add_u32 s52, s53, 0x18f000\n"
"s_and_b32 s52, s52, s54\n"
"buffer_load_dwordx4 a[148:151], v2, s[32:35], s52 offen offset:0 nt\n"
"v_accvgpr_read_b32 v60, a158\n"
"v_accvgpr_read_b32 v58, a156\n"
"v_mfma_f32_16x16x32_bf16 v[32:35], v[28:31], v[16:19], v[32:35]\n"
"v_accvgpr_read_b32 v59, a157\n"
"v_and_b32 v61, s8, v60\n"
"v_alignbit_b32 v62, v60, v60, 4\n"
"v_perm_b32 v24, v61, v58, s11\n"
"v_and_b32 v62, s8, v62\n"
"v_perm_b32 v25, v61, v58, s12\n"
"v_perm_b32 v26, v62, v59, s11\n"
"v_perm_b32 v27, v62, v59, s12\n"
"s_add_u32 s52, s53, 0xa1000\n"
"s_and_b32 s52, s52, s54\n"
"buffer_load_dwordx4 a[140:143], v2, s[32:35], s52 offen offset:0 nt\n"
"v_accvgpr_read_b32 v60, a161\n"
"v_accvgpr_read_b32 v58, a159\n"
"v_mfma_f32_16x16x32_bf16 v[36:39], v[24:27], v[4:7], 0\n"
"v_accvgpr_read_b32 v59, a160\n"
"v_and_b32 v61, s8, v60\n"
"v_alignbit_b32 v62, v60, v60, 4\n"
"v_perm_b32 v28, v61, v58, s11\n"
"v_and_b32 v62, s8, v62\n"
"v_perm_b32 v29, v61, v58, s12\n"
"v_perm_b32 v30, v62, v59, s11\n"
"v_perm_b32 v31, v62, v59, s12\n"
"v_mov_b32_dpp v63, v32 quad_perm:[0,0,0,0] row_mask:0xf bank_mask:0xf\n"
"v_add_f32_dpp v63, v33, v63 quad_perm:[1,1,1,1] row_mask:0xf bank_mask:0xf\n"
"v_add_f32_dpp v63, v34, v63 quad_perm:[2,2,2,2] row_mask:0xf bank_mask:0xf\n"
"v_add_f32_dpp v63, v35, v63 quad_perm:[3,3,3,3] row_mask:0xf bank_mask:0xf\n"
"v_cndmask_b32_e64 v47, v47, v63, s[60:61]\n"
"v_accvgpr_read_b32 v60, a164\n"
"v_accvgpr_read_b32 v58, a162\n"
"v_mfma_f32_16x16x32_bf16 v[36:39], v[28:31], v[8:11], v[36:39]\n"
"v_accvgpr_read_b32 v59, a163\n"
"v_and_b32 v61, s8, v60\n"
"v_alignbit_b32 v62, v60, v60, 4\n"
"v_perm_b32 v24, v61, v58, s11\n"
"v_and_b32 v62, s8, v62\n"
"v_perm_b32 v25, v61, v58, s12\n"
"v_perm_b32 v26, v62, v59, s11\n"
"v_perm_b32 v27, v62, v59, s12\n"
"s_add_u32 s52, s53, 0x12d000\n"
"s_and_b32 s52, s52, s54\n"
"buffer_load_dwordx4 a[152:155], v2, s[32:35], s52 offen offset:0 nt\n"
"v_accvgpr_read_b32 v60, a167\n"
"v_accvgpr_read_b32 v58, a165\n"
"v_mfma_f32_16x16x32_bf16 v[36:39], v[24:27], v[12:15], v[36:39]\n"
"v_accvgpr_read_b32 v59, a166\n"
"v_and_b32 v61, s8, v60\n"
"v_alignbit_b32 v62, v60, v60, 4\n"
"v_perm_b32 v28, v61, v58, s11\n"
"v_and_b32 v62, s8, v62\n"
"v_perm_b32 v29, v61, v58, s12\n"
"v_perm_b32 v30, v62, v59, s11\n"
"v_perm_b32 v31, v62, v59, s12\n"
"s_add_u32 s52, s53, 0x19d000\n"
"s_and_b32 s52, s52, s54\n"
"buffer_load_dwordx4 a[156:159], v2, s[32:35], s52 offen offset:0 nt\n"
"v_accvgpr_read_b32 v60, a170\n"
"v_accvgpr_read_b32 v58, a168\n"
"v_mfma_f32_16x16x32_bf16 v[36:39], v[28:31], v[16:19], v[36:39]\n"
"v_accvgpr_read_b32 v59, a169\n"
"v_and_b32 v61, s8, v60\n"
"v_alignbit_b32 v62, v60, v60, 4\n"
"v_perm_b32 v24, v61, v58, s11\n"
"v_and_b32 v62, s8, v62\n"
"v_perm_b32 v25, v61, v58, s12\n"
"v_perm_b32 v26, v62, v59, s11\n"
"v_perm_b32 v27, v62, v59, s12\n"
"s_add_u32 s52, s53, 0x38000\n"
"s_and_b32 s52, s52, s54\n"
"buffer_load_dwordx4 a[160:163], v2, s[32:35], s52 offen offset:0 nt\n"
"v_accvgpr_read_b32 v60, a173\n"
"v_accvgpr_read_b32 v58, a171\n"
"v_mfma_f32_16x16x32_bf16 v[32:35], v[24:27], v[4:7], 0\n"
"v_accvgpr_read_b32 v59, a172\n"
"v_and_b32 v61, s8, v60\n"
"v_alignbit_b32 v62, v60, v60, 4\n"
"v_perm_b32 v28, v61, v58, s11\n"
"v_and_b32 v62, s8, v62\n"
"v_perm_b32 v29, v61, v58, s12\n"
"v_perm_b32 v30, v62, v59, s11\n"
"v_perm_b32 v31, v62, v59, s12\n"
"v_mov_b32_dpp v63, v36 quad_perm:[0,0,0,0] row_mask:0xf bank_mask:0xf\n"
"v_add_f32_dpp v63, v37, v63 quad_perm:[1,1,1,1] row_mask:0xf bank_mask:0xf\n"
"v_add_f32_dpp v63, v38, v63 quad_perm:[2,2,2,2] row_mask:0xf bank_mask:0xf\n"
"v_add_f32_dpp v63, v39, v63 quad_perm:[3,3,3,3] row_mask:0xf bank_mask:0xf\n"
"v_cndmask_b32_e64 v47, v47, v63, s[62:63]\n"
"v_accvgpr_read_b32 v60, a176\n"
"v_accvgpr_read_b32 v58, a174\n"
"v_mfma_f32_16x16x32_bf16 v[32:35], v[28:31], v[8:11], v[32:35]\n"
"v_accvgpr_read_b32 v59, a175\n"
"v_and_b32 v61, s8, v60\n"
"v_alignbit_b32 v62, v60, v60, 4\n"
"v_perm_b32 v24, v61, v58, s11\n"
"v_and_b32 v62, s8, v62\n"
"v_perm_b32 v25, v61, v58, s12\n"
"v_perm_b32 v26, v62, v59, s11\n"
"v_perm_b32 v27, v62, v59, s12\n"
"v_accvgpr_read_b32 v60, a179\n"
"v_accvgpr_read_b32 v58, a177\n"
"v_mfma_f32_16x16x32_bf16 v[32:35], v[24:27], v[12:15], v[32:35]\n"
"v_accvgpr_read_b32 v59, a178\n"
"v_and_b32 v61, s8, v60\n"
"v_alignbit_b32 v62, v60, v60, 4\n"
"v_perm_b32 v28, v61, v58, s11\n"
"v_and_b32 v62, s8, v62\n"
"v_perm_b32 v29, v61, v58, s12\n"
"v_perm_b32 v30, v62, v59, s11\n"
"v_perm_b32 v31, v62, v59, s12\n"
"s_add_u32 s52, s53, 0x126000\n"
"s_and_b32 s52, s52, s54\n"
"buffer_load_dwordx4 a[168:171], v2, s[32:35], s52 offen offset:0 nt\n"
"v_accvgpr_read_b32 v60, a182\n"
"v_accvgpr_read_b32 v58, a180\n"
"v_mfma_f32_16x16x32_bf16 v[32:35], v[28:31], v[16:19], v[32:35]\n"
"v_accvgpr_read_b32 v59, a181\n"
"v_and_b32 v61, s8, v60\n"
"v_alignbit_b32 v62, v60, v60, 4\n"
"v_perm_b32 v24, v61, v58, s11\n"
"v_and_b32 v62, s8, v62\n"
"v_perm_b32 v25, v61, v58, s12\n"
"v_perm_b32 v26, v62, v59, s11\n"
"v_perm_b32 v27, v62, v59, s12\n"
"s_add_u32 s52, s53, 0x196000\n"
"s_and_b32 s52, s52, s54\n"
"buffer_load_dwordx4 a[172:175], v2, s[32:35], s52 offen offset:0 nt\n"
"v_accvgpr_read_b32 v60, a185\n"
"v_accvgpr_read_b32 v58, a183\n"
"v_mfma_f32_16x16x32_bf16 v[36:39], v[24:27], v[4:7], 0\n"
"v_accvgpr_read_b32 v59, a184\n"
"v_and_b32 v61, s8, v60\n"
"v_alignbit_b32 v62, v60, v60, 4\n"
"v_perm_b32 v28, v61, v58, s11\n"
"v_and_b32 v62, s8, v62\n"
"v_perm_b32 v29, v61, v58, s12\n"
"v_perm_b32 v30, v62, v59, s11\n"
"v_perm_b32 v31, v62, v59, s12\n"
"s_add_u32 s52, s53, 0xa8000\n"
"s_and_b32 s52, s52, s54\n"
"buffer_load_dwordx4 a[164:167], v2, s[32:35], s52 offen offset:0 nt\n"
"v_mov_b32_dpp v63, v32 quad_perm:[0,0,0,0] row_mask:0xf bank_mask:0xf\n"
"v_add_f32_dpp v63, v33, v63 quad_perm:[1,1,1,1] row_mask:0xf bank_mask:0xf\n"
"v_add_f32_dpp v63, v34, v63 quad_perm:[2,2,2,2] row_mask:0xf bank_mask:0xf\n"
"v_add_f32_dpp v63, v35, v63 quad_perm:[3,3,3,3] row_mask:0xf bank_mask:0xf\n"
"v_cndmask_b32_e64 v47, v47, v63, s[64:65]\n"
"v_accvgpr_read_b32 v60, a188\n"
"v_accvgpr_read_b32 v58, a186\n"
"v_mfma_f32_16x16x32_bf16 v[36:39], v[28:31], v[8:11], v[36:39]\n"
"v_accvgpr_read_b32 v59, a187\n"
"v_and_b32 v61, s8, v60\n"
"v_alignbit_b32 v62, v60, v60, 4\n"
"v_perm_b32 v24, v61, v58, s11\n"
"v_and_b32 v62, s8, v62\n"
"v_perm_b32 v25, v61, v58, s12\n"
"v_perm_b32 v26, v62, v59, s11\n"
"v_perm_b32 v27, v62, v59, s12\n"
"v_accvgpr_read_b32 v60, a191\n"
"v_accvgpr_read_b32 v58, a189\n"
"v_mfma_f32_16x16x32_bf16 v[36:39], v[24:27], v[12:15], v[36:39]\n"
"v_accvgpr_read_b32 v59, a190\n"
"v_and_b32 v61, s8, v60\n"
"v_alignbit_b32 v62, v60, v60, 4\n"
"v_perm_b32 v28, v61, v58, s11\n"
"v_and_b32 v62, s8, v62\n"
"v_perm_b32 v29, v61, v58, s12\n"
"v_perm_b32 v30, v62, v59, s11\n"
"v_perm_b32 v31, v62, v59, s12\n"
"v_accvgpr_read_b32 v60, a194\n"
"v_accvgpr_read_b32 v58, a192\n"
"v_mfma_f32_16x16x32_bf16 v[36:39], v[28:31], v[16:19], v[36:39]\n"
"v_accvgpr_read_b32 v59, a193\n"
"v_and_b32 v61, s8, v60\n"
"v_alignbit_b32 v62, v60, v60, 4\n"
"v_perm_b32 v24, v61, v58, s11\n"
"v_and_b32 v62, s8, v62\n"
"v_perm_b32 v25, v61, v58, s12\n"
"v_perm_b32 v26, v62, v59, s11\n"
"v_perm_b32 v27, v62, v59, s12\n"
"v_accvgpr_read_b32 v60, a197\n"
"v_accvgpr_read_b32 v58, a195\n"
"v_mfma_f32_16x16x32_bf16 v[32:35], v[24:27], v[4:7], 0\n"
"v_accvgpr_read_b32 v59, a196\n"
"v_and_b32 v61, s8, v60\n"
"v_alignbit_b32 v62, v60, v60, 4\n"
"v_perm_b32 v28, v61, v58, s11\n"
"v_and_b32 v62, s8, v62\n"
"v_perm_b32 v29, v61, v58, s12\n"
"v_perm_b32 v30, v62, v59, s11\n"
"v_perm_b32 v31, v62, v59, s12\n"
"s_add_u32 s52, s53, 0x46000\n"
"s_and_b32 s52, s52, s54\n"
"buffer_load_dwordx4 a[176:179], v2, s[32:35], s52 offen offset:0 nt\n"
"v_mov_b32_dpp v63, v36 quad_perm:[0,0,0,0] row_mask:0xf bank_mask:0xf\n"
"v_add_f32_dpp v63, v37, v63 quad_perm:[1,1,1,1] row_mask:0xf bank_mask:0xf\n"
"v_add_f32_dpp v63, v38, v63 quad_perm:[2,2,2,2] row_mask:0xf bank_mask:0xf\n"
"v_add_f32_dpp v63, v39, v63 quad_perm:[3,3,3,3] row_mask:0xf bank_mask:0xf\n"
"v_cndmask_b32_e64 v47, v47, v63, s[66:67]\n"
"v_accvgpr_read_b32 v60, a200\n"
"v_accvgpr_read_b32 v58, a198\n"
"v_mfma_f32_16x16x32_bf16 v[32:35], v[28:31], v[8:11], v[32:35]\n"
"v_accvgpr_read_b32 v59, a199\n"
"v_and_b32 v61, s8, v60\n"
"v_alignbit_b32 v62, v60, v60, 4\n"
"v_perm_b32 v24, v61, v58, s11\n"
"v_and_b32 v62, s8, v62\n"
"v_perm_b32 v25, v61, v58, s12\n"
"v_perm_b32 v26, v62, v59, s11\n"
"v_perm_b32 v27, v62, v59, s12\n"
"v_accvgpr_read_b32 v60, a203\n"
"v_accvgpr_read_b32 v58, a201\n"
"v_mfma_f32_16x16x32_bf16 v[32:35], v[24:27], v[12:15], v[32:35]\n"
"v_accvgpr_read_b32 v59, a202\n"
"v_and_b32 v61, s8, v60\n"
"v_alignbit_b32 v62, v60, v60, 4\n"
"v_perm_b32 v28, v61, v58, s11\n"
"v_and_b32 v62, s8, v62\n"
"v_perm_b32 v29, v61, v58, s12\n"
"v_perm_b32 v30, v62, v59, s11\n"
"v_perm_b32 v31, v62, v59, s12\n"
"v_accvgpr_read_b32 v60, a206\n"
"v_accvgpr_read_b32 v58, a204\n"
"v_mfma_f32_16x16x32_bf16 v[32:35], v[28:31], v[16:19], v[32:35]\n"
"v_accvgpr_read_b32 v59, a205\n"
"v_and_b32 v61, s8, v60\n"
"v_alignbit_b32 v62, v60, v60, 4\n"
"v_perm_b32 v24, v61, v58, s11\n"
"v_and_b32 v62, s8, v62\n"
"v_perm_b32 v25, v61, v58, s12\n"
"v_perm_b32 v26, v62, v59, s11\n"
"v_perm_b32 v27, v62, v59, s12\n"
"v_accvgpr_read_b32 v60, a209\n"
"v_accvgpr_read_b32 v58, a207\n"
"v_mfma_f32_16x16x32_bf16 v[36:39], v[24:27], v[4:7], 0\n"
"v_accvgpr_read_b32 v59, a208\n"
"v_and_b32 v61, s8, v60\n"
"v_alignbit_b32 v62, v60, v60, 4\n"
"v_perm_b32 v28, v61, v58, s11\n"
"v_and_b32 v62, s8, v62\n"
"v_perm_b32 v29, v61, v58, s12\n"
"v_perm_b32 v30, v62, v59, s11\n"
"v_perm_b32 v31, v62, v59, s12\n"
"v_mov_b32_dpp v63, v32 quad_perm:[0,0,0,0] row_mask:0xf bank_mask:0xf\n"
"v_add_f32_dpp v63, v33, v63 quad_perm:[1,1,1,1] row_mask:0xf bank_mask:0xf\n"
"v_add_f32_dpp v63, v34, v63 quad_perm:[2,2,2,2] row_mask:0xf bank_mask:0xf\n"
"v_add_f32_dpp v63, v35, v63 quad_perm:[3,3,3,3] row_mask:0xf bank_mask:0xf\n"
"v_cndmask_b32_e64 v48, v48, v63, s[60:61]\n"
"v_accvgpr_read_b32 v60, a212\n"
"v_accvgpr_read_b32 v58, a210\n"
"v_mfma_f32_16x16x32_bf16 v[36:39], v[28:31], v[8:11], v[36:39]\n"
"v_accvgpr_read_b32 v59, a211\n"
"v_and_b32 v61, s8, v60\n"
"v_alignbit_b32 v62, v60, v60, 4\n"
"v_perm_b32 v24, v61, v58, s11\n"
"v_and_b32 v62, s8, v62\n"
"v_perm_b32 v25, v61, v58, s12\n"
"v_perm_b32 v26, v62, v59, s11\n"
"v_perm_b32 v27, v62, v59, s12\n"
"s_add_u32 s52, s53, 0xb6000\n"
"s_and_b32 s52, s52, s54\n"
"buffer_load_dwordx4 a[180:183], v2, s[32:35], s52 offen offset:0 nt\n"
"v_accvgpr_read_b32 v60, a215\n"
"v_accvgpr_read_b32 v58, a213\n"
"v_mfma_f32_16x16x32_bf16 v[36:39], v[24:27], v[12:15], v[36:39]\n"
"v_accvgpr_read_b32 v59, a214\n"
"v_and_b32 v61, s8, v60\n"
"v_alignbit_b32 v62, v60, v60, 4\n"
"v_perm_b32 v28, v61, v58, s11\n"
"v_and_b32 v62, s8, v62\n"
"v_perm_b32 v29, v61, v58, s12\n"
"v_perm_b32 v30, v62, v59, s11\n"
"v_perm_b32 v31, v62, v59, s12\n"
"v_and_b32 v61, s8, v90\n"
"v_alignbit_b32 v62, v90, v90, 4\n"
"v_mfma_f32_16x16x32_bf16 v[36:39], v[28:31], v[16:19], v[36:39]\n"
"v_perm_b32 v24, v61, v88, s11\n"
"v_and_b32 v62, s8, v62\n"
"v_perm_b32 v25, v61, v88, s12\n"
"v_perm_b32 v26, v62, v89, s11\n"
"v_perm_b32 v27, v62, v89, s12\n"
"v_and_b32 v61, s8, v93\n"
"v_alignbit_b32 v62, v93, v93, 4\n"
"v_mfma_f32_16x16x32_bf16 v[32:35], v[24:27], v[4:7], 0\n"
"v_perm_b32 v28, v61, v91, s11\n"
"v_and_b32 v62, s8, v62\n"
"v_perm_b32 v29, v61, v91, s12\n"
"v_perm_b32 v30, v62, v92, s11\n"
"v_perm_b32 v31, v62, v92, s12\n"
"v_mov_b32_dpp v63, v36 quad_perm:[0,0,0,0] row_mask:0xf bank_mask:0xf\n"
"v_add_f32_dpp v63, v37, v63 quad_perm:[1,1,1,1] row_mask:0xf bank_mask:0xf\n"
"v_add_f32_dpp v63, v38, v63 quad_perm:[2,2,2,2] row_mask:0xf bank_mask:0xf\n"
"v_add_f32_dpp v63, v39, v63 quad_perm:[3,3,3,3] row_mask:0xf bank_mask:0xf\n"
"v_cndmask_b32_e64 v48, v48, v63, s[62:63]\n"
"v_and_b32 v61, s8, v96\n"
"v_alignbit_b32 v62, v96, v96, 4\n"
"v_mfma_f32_16x16x32_bf16 v[32:35], v[28:31], v[8:11], v[32:35]\n"
"v_perm_b32 v24, v61, v94, s11\n"
"v_and_b32 v62, s8, v62\n"
"v_perm_b32 v25, v61, v94, s12\n"
"v_perm_b32 v26, v62, v95, s11\n"
"v_perm_b32 v27, v62, v95, s12\n"
"s_add_u32 s52, s53, 0x3f000\n"
"s_and_b32 s52, s52, s54\n"
"buffer_load_dwordx4 a[184:187], v2, s[32:35], s52 offen offset:0 nt\n"
"v_and_b32 v61, s8, v99\n"
"v_alignbit_b32 v62, v99, v99, 4\n"
"v_mfma_f32_16x16x32_bf16 v[32:35], v[24:27], v[12:15], v[32:35]\n"
"v_perm_b32 v28, v61, v97, s11\n"
"v_and_b32 v62, s8, v62\n"
"v_perm_b32 v29, v61, v97, s12\n"
"v_perm_b32 v30, v62, v98, s11\n"
"v_perm_b32 v31, v62, v98, s12\n"
"ds_read_b128 v[88:91], v3 offset:0\n"
"ds_read_b128 v[92:95], v3 offset:1024\n"
"ds_read_b128 v[96:99], v3 offset:2048\n"
"v_and_b32 v61, s8, v102\n"
"v_alignbit_b32 v62, v102, v102, 4\n"
"v_mfma_f32_16x16x32_bf16 v[32:35], v[28:31], v[16:19], v[32:35]\n"
"v_perm_b32 v24, v61, v100, s11\n"
"v_and_b32 v62, s8, v62\n"
"v_perm_b32 v25, v61, v100, s12\n"
"v_perm_b32 v26, v62, v101, s11\n"
"v_perm_b32 v27, v62, v101, s12\n"
"v_and_b32 v61, s8, v105\n"
"v_alignbit_b32 v62, v105, v105, 4\n"
"v_mfma_f32_16x16x32_bf16 v[36:39], v[24:27], v[4:7], 0\n"
"v_perm_b32 v28, v61, v103, s11\n"
"v_and_b32 v62, s8, v62\n"
"v_perm_b32 v29, v61, v103, s12\n"
"v_perm_b32 v30, v62, v104, s11\n"
"v_perm_b32 v31, v62, v104, s12\n"
"v_mov_b32_dpp v63, v32 quad_perm:[0,0,0,0] row_mask:0xf bank_mask:0xf\n"
"v_add_f32_dpp v63, v33, v63 quad_perm:[1,1,1,1] row_mask:0xf bank_mask:0xf\n"
"v_add_f32_dpp v63, v34, v63 quad_perm:[2,2,2,2] row_mask:0xf bank_mask:0xf\n"
"v_add_f32_dpp v63, v35, v63 quad_perm:[3,3,3,3] row_mask:0xf bank_mask:0xf\n"
"v_cndmask_b32_e64 v49, v49, v63, s[60:61]\n"
"v_and_b32 v61, s8, v108\n"
"v_alignbit_b32 v62, v108, v108, 4\n"
"v_mfma_f32_16x16x32_bf16 v[36:39], v[28:31], v[8:11], v[36:39]\n"
"v_perm_b32 v24, v61, v106, s11\n"
"v_and_b32 v62, s8, v62\n"
"v_perm_b32 v25, v61, v106, s12\n"
"v_perm_b32 v26, v62, v107, s11\n"
"v_perm_b32 v27, v62, v107, s12\n"
"v_and_b32 v61, s8, v111\n"
"v_alignbit_b32 v62, v111, v111, 4\n"
"v_mfma_f32_16x16x32_bf16 v[36:39], v[24:27], v[12:15], v[36:39]\n"
"v_perm_b32 v28, v61, v109, s11\n"
"v_and_b32 v62, s8, v62\n"
"v_perm_b32 v29, v61, v109, s12\n"
"v_perm_b32 v30, v62, v110, s11\n"
"v_perm_b32 v31, v62, v110, s12\n"
"ds_read_b128 v[100:103], v3 offset:3072\n"
"ds_read_b128 v[104:107], v3 offset:4096\n"
"ds_read_b128 v[108:111], v3 offset:5120\n"
"s_waitcnt lgkmcnt(3)\n"
"v_and_b32 v61, s8, v88\n"
"v_alignbit_b32 v62, v88, v88, 4\n"
"v_mfma_f32_16x16x32_bf16 v[36:39], v[28:31], v[16:19], v[36:39]\n"
"v_perm_b32 v24, v61, v92, s11\n"
"v_and_b32 v62, s8, v62\n"
"v_perm_b32 v25, v61, v92, s12\n"
"v_perm_b32 v26, v62, v93, s11\n"
"v_perm_b32 v27, v62, v93, s12\n"
"s_add_u32 s52, s53, 0xaf000\n"
"s_and_b32 s52, s52, s54\n"
"buffer_load_dwordx4 a[188:191], v2, s[32:35], s52 offen offset:0 nt\n"
"v_and_b32 v61, s8, v89\n"
"v_alignbit_b32 v62, v89, v89, 4\n"
"v_mfma_f32_16x16x32_bf16 v[32:35], v[24:27], v[4:7], 0\n"
"v_perm_b32 v28, v61, v94, s11\n"
"v_and_b32 v62, s8, v62\n"
"v_perm_b32 v29, v61, v94, s12\n"
"v_perm_b32 v30, v62, v95, s11\n"
"v_perm_b32 v31, v62, v95, s12\n"
"v_mov_b32_dpp v63, v36 quad_perm:[0,0,0,0] row_mask:0xf bank_mask:0xf\n"
"v_add_f32_dpp v63, v37, v63 quad_perm:[1,1,1,1] row_mask:0xf bank_mask:0xf\n"
"v_add_f32_dpp v63, v38, v63 quad_perm:[2,2,2,2] row_mask:0xf bank_mask:0xf\n"
"v_add_f32_dpp v63, v39, v63 quad_perm:[3,3,3,3] row_mask:0xf bank_mask:0xf\n"
"v_cndmask_b32_e64 v49, v49, v63, s[62:63]\n"
"v_and_b32 v61, s8, v90\n"
"v_alignbit_b32 v62, v90, v90, 4\n"
"v_mfma_f32_16x16x32_bf16 v[32:35], v[28:31], v[8:11], v[32:35]\n"
"v_perm_b32 v24, v61, v96, s11\n"
"v_and_b32 v62, s8, v62\n"
"v_perm_b32 v25, v61, v96, s12\n"
"v_perm_b32 v26, v62, v97, s11\n"
"v_perm_b32 v27, v62, v97, s12\n"
"v_and_b32 v61, s8, v91\n"
"v_alignbit_b32 v62, v91, v91, 4\n"
"v_mfma_f32_16x16x32_bf16 v[32:35], v[24:27], v[12:15], v[32:35]\n"
"v_perm_b32 v28, v61, v98, s11\n"
"v_and_b32 v62, s8, v62\n"
"v_perm_b32 v29, v61, v98, s12\n"
"v_perm_b32 v30, v62, v99, s11\n"
"v_perm_b32 v31, v62, v99, s12\n"
"ds_read_b128 v[88:91], v3 offset:6144\n"
"ds_read_b128 v[92:95], v3 offset:7168\n"
"ds_read_b128 v[96:99], v3 offset:8192\n"
"s_waitcnt lgkmcnt(3)\n"
"v_and_b32 v61, s8, v100\n"
"v_alignbit_b32 v62, v100, v100, 4\n"
"v_mfma_f32_16x16x32_bf16 v[32:35], v[28:31], v[16:19], v[32:35]\n"
"v_perm_b32 v24, v61, v104, s11\n"
"v_and_b32 v62, s8, v62\n"
"v_perm_b32 v25, v61, v104, s12\n"
"v_perm_b32 v26, v62, v105, s11\n"
"v_perm_b32 v27, v62, v105, s12\n"
"s_add_u32 s52, s53, 0x4d000\n"
"s_and_b32 s52, s52, s54\n"
"buffer_load_dwordx4 a[192:195], v2, s[32:35], s52 offen offset:0 nt\n"
"v_and_b32 v61, s8, v101\n"
"v_alignbit_b32 v62, v101, v101, 4\n"
"v_mfma_f32_16x16x32_bf16 v[36:39], v[24:27], v[4:7], 0\n"
"v_perm_b32 v28, v61, v106, s11\n"
"v_and_b32 v62, s8, v62\n"
"v_perm_b32 v29, v61, v106, s12\n"
"v_perm_b32 v30, v62, v107, s11\n"
"v_perm_b32 v31, v62, v107, s12\n"
"v_mov_b32_dpp v63, v32 quad_perm:[0,0,0,0] row_mask:0xf bank_mask:0xf\n"
"v_add_f32_dpp v63, v33, v63 quad_perm:[1,1,1,1] row_mask:0xf bank_mask:0xf\n"
"v_add_f32_dpp v63, v34, v63 quad_perm:[2,2,2,2] row_mask:0xf bank_mask:0xf\n"
"v_add_f32_dpp v63, v35, v63 quad_perm:[3,3,3,3] row_mask:0xf bank_mask:0xf\n"
"v_cndmask_b32_e64 v49, v49, v63, s[64:65]\n"
"v_and_b32 v61, s8, v102\n"
"v_alignbit_b32 v62, v102, v102, 4\n"
"v_mfma_f32_16x16x32_bf16 v[36:39], v[28:31], v[8:11], v[36:39]\n"
"v_perm_b32 v24, v61, v108, s11\n"
"v_and_b32 v62, s8, v62\n"
"v_perm_b32 v25, v61, v108, s12\n"
"v_perm_b32 v26, v62, v109, s11\n"
"v_perm_b32 v27, v62, v109, s12\n"
"v_and_b32 v61, s8, v103\n"
"v_alignbit_b32 v62, v103, v103, 4\n"
"v_mfma_f32_16x16x32_bf16 v[36:39], v[24:27], v[12:15], v[36:39]\n"
"v_perm_b32 v28, v61, v110, s11\n"
"v_and_b32 v62, s8, v62\n"
"v_perm_b32 v29, v61, v110, s12\n"
"v_perm_b32 v30, v62, v111, s11\n"
"v_perm_b32 v31, v62, v111, s12\n"
"ds_read_b128 v[100:103], v3 offset:9216\n"
"ds_read_b128 v[104:107], v3 offset:10240\n"
"ds_read_b128 v[108:111], v3 offset:11264\n"
"s_waitcnt lgkmcnt(3)\n"
"v_and_b32 v61, s8, v88\n"
"v_alignbit_b32 v62, v88, v88, 4\n"
"v_mfma_f32_16x16x32_bf16 v[36:39], v[28:31], v[16:19], v[36:39]\n"
"v_perm_b32 v24, v61, v92, s11\n"
"v_and_b32 v62, s8, v62\n"
"v_perm_b32 v25, v61, v92, s12\n"
"v_perm_b32 v26, v62, v93, s11\n"
"v_perm_b32 v27, v62, v93, s12\n"
"v_and_b32 v61, s8, v89\n"
"v_alignbit_b32 v62, v89, v89, 4\n"
"v_mfma_f32_16x16x32_bf16 v[32:35], v[24:27], v[4:7], 0\n"
"v_perm_b32 v28, v61, v94, s11\n"
"v_and_b32 v62, s8, v62\n"
"v_perm_b32 v29, v61, v94, s12\n"
"v_perm_b32 v30, v62, v95, s11\n"
"v_perm_b32 v31, v62, v95, s12\n"
"v_mov_b32_dpp v63, v36 quad_perm:[0,0,0,0] row_mask:0xf bank_mask:0xf\n"
"v_add_f32_dpp v63, v37, v63 quad_perm:[1,1,1,1] row_mask:0xf bank_mask:0xf\n"
"v_add_f32_dpp v63, v38, v63 quad_perm:[2,2,2,2] row_mask:0xf bank_mask:0xf\n"
"v_add_f32_dpp v63, v39, v63 quad_perm:[3,3,3,3] row_mask:0xf bank_mask:0xf\n"
"v_cndmask_b32_e64 v49, v49, v63, s[66:67]\n"
"v_and_b32 v61, s8, v90\n"
"v_alignbit_b32 v62, v90, v90, 4\n"
"v_mfma_f32_16x16x32_bf16 v[32:35], v[28:31], v[8:11], v[32:35]\n"
"v_perm_b32 v24, v61, v96, s11\n"
"v_and_b32 v62, s8, v62\n"
"v_perm_b32 v25, v61, v96, s12\n"
"v_perm_b32 v26, v62, v97, s11\n"
"v_perm_b32 v27, v62, v97, s12\n"
"s_add_u32 s52, s53, 0xbd000\n"
"s_and_b32 s52, s52, s54\n"
"buffer_load_dwordx4 a[196:199], v2, s[32:35], s52 offen offset:0 nt\n"
"v_and_b32 v61, s8, v91\n"
"v_alignbit_b32 v62, v91, v91, 4\n"
"v_mfma_f32_16x16x32_bf16 v[32:35], v[24:27], v[12:15], v[32:35]\n"
"s_cbranch_execz LDRYR\n"
"LDRY2:\n"
"v_perm_b32 v28, v61, v98, s11\n"
"v_and_b32 v62, s8, v62\n"
"v_perm_b32 v29, v61, v98, s12\n"
"v_perm_b32 v30, v62, v99, s11\n"
"v_perm_b32 v31, v62, v99, s12\n"
"ds_read_b128 v[88:91], v3 offset:12288\n"
"ds_read_b128 v[92:95], v3 offset:13312\n"
"ds_read_b128 v[96:99], v3 offset:14336\n"
"s_waitcnt lgkmcnt(3)\n"
"v_and_b32 v61, s8, v100\n"
"v_alignbit_b32 v62, v100, v100, 4\n"
"v_mfma_f32_16x16x32_bf16 v[32:35], v[28:31], v[16:19], v[32:35]\n"
"v_perm_b32 v24, v61, v104, s11\n"
"v_and_b32 v62, s8, v62\n"
"v_perm_b32 v25, v61, v104, s12\n"
"v_perm_b32 v26, v62, v105, s11\n"
"v_perm_b32 v27, v62, v105, s12\n"
"v_and_b32 v61, s8, v101\n"
"v_alignbit_b32 v62, v101, v101, 4\n"
"v_mfma_f32_16x16x32_bf16 v[36:39], v[24:27], v[4:7], 0\n"
"v_perm_b32 v28, v61, v106, s11\n"
"v_and_b32 v62, s8, v62\n"
"v_perm_b32 v29, v61, v106, s12\n"
"v_perm_b32 v30, v62, v107, s11\n"
"v_perm_b32 v31, v62, v107, s12\n"
"v_mov_b32_dpp v63, v32 quad_perm:[0,0,0,0] row_mask:0xf bank_mask:0xf\n"
"v_add_f32_dpp v63, v33, v63 quad_perm:[1,1,1,1] row_mask:0xf bank_mask:0xf\n"
"v_add_f32_dpp v63, v34, v63 quad_perm:[2,2,2,2] row_mask:0xf bank_mask:0xf\n"
"v_add_f32_dpp v63, v35, v63 quad_perm:[3,3,3,3] row_mask:0xf bank_mask:0xf\n"
"v_cndmask_b32_e64 v50, v50, v63, s[60:61]\n"
"v_and_b32 v61, s8, v102\n"
"v_alignbit_b32 v62, v102, v102, 4\n"
"v_mfma_f32_16x16x32_bf16 v[36:39], v[28:31], v[8:11], v[36:39]\n"
"v_perm_b32 v24, v61, v108, s11\n"
"v_and_b32 v62, s8, v62\n"
"v_perm_b32 v25, v61, v108, s12\n"
"v_perm_b32 v26, v62, v109, s11\n"
"v_perm_b32 v27, v62, v109, s12\n"
"v_and_b32 v61, s8, v103\n"
"v_alignbit_b32 v62, v103, v103, 4\n"
"v_mfma_f32_16x16x32_bf16 v[36:39], v[24:27], v[12:15], v[36:39]\n"
"v_perm_b32 v28, v61, v110, s11\n"
"v_and_b32 v62, s8, v62\n"
"v_perm_b32 v29, v61, v110, s12\n"
"v_perm_b32 v30, v62, v111, s11\n"
"v_perm_b32 v31, v62, v111, s12\n"
"s_add_u32 s52, s53, 0x54000\n"
"s_and_b32 s52, s52, s54\n"
"buffer_load_dwordx4 a[200:203], v2, s[32:35], s52 offen offset:0 nt\n"
"ds_read_b128 v[100:103], v3 offset:15360\n"
"ds_read_b128 v[104:107], v3 offset:16384\n"
"ds_read_b128 v[108:111], v3 offset:17408\n"
"s_waitcnt lgkmcnt(3)\n"
"v_and_b32 v61, s8, v88\n"
"v_alignbit_b32 v62, v88, v88, 4\n"
"v_mfma_f32_16x16x32_bf16 v[36:39], v[28:31], v[16:19], v[36:39]\n"
"v_perm_b32 v24, v61, v92, s11\n"
"v_and_b32 v62, s8, v62\n"
"v_perm_b32 v25, v61, v92, s12\n"
"v_perm_b32 v26, v62, v93, s11\n"
"v_perm_b32 v27, v62, v93, s12\n"
"v_and_b32 v61, s8, v89\n"
"v_alignbit_b32 v62, v89, v89, 4\n"
"v_mfma_f32_16x16x32_bf16 v[32:35], v[24:27], v[4:7], 0\n"
"v_perm_b32 v28, v61, v94, s11\n"
"v_and_b32 v62, s8, v62\n"
"v_perm_b32 v29, v61, v94, s12\n"
"v_perm_b32 v30, v62, v95, s11\n"
"v_perm_b32 v31, v62, v95, s12\n"
"v_mov_b32_dpp v63, v36 quad_perm:[0,0,0,0] row_mask:0xf bank_mask:0xf\n"
"v_add_f32_dpp v63, v37, v63 quad_perm:[1,1,1,1] row_mask:0xf bank_mask:0xf\n"
"v_add_f32_dpp v63, v38, v63 quad_perm:[2,2,2,2] row_mask:0xf bank_mask:0xf\n"
"v_add_f32_dpp v63, v39, v63 quad_perm:[3,3,3,3] row_mask:0xf bank_mask:0xf\n"
"v_cndmask_b32_e64 v50, v50, v63, s[62:63]\n"
"v_and_b32 v61, s8, v90\n"
"v_alignbit_b32 v62, v90, v90, 4\n"
"v_mfma_f32_16x16x32_bf16 v[32:35], v[28:31], v[8:11], v[32:35]\n"
"v_perm_b32 v24, v61, v96, s11\n"
"v_and_b32 v62, s8, v62\n"
"v_perm_b32 v25, v61, v96, s12\n"
"v_perm_b32 v26, v62, v97, s11\n"
"v_perm_b32 v27, v62, v97, s12\n"
"v_and_b32 v61, s8, v91\n"
"v_alignbit_b32 v62, v91, v91, 4\n"
"v_mfma_f32_16x16x32_bf16 v[32:35], v[24:27], v[12:15], v[32:35]\n"
"v_perm_b32 v28, v61, v98, s11\n"
"v_and_b32 v62, s8, v62\n"
"v_perm_b32 v29, v61, v98, s12\n"
"v_perm_b32 v30, v62, v99, s11\n"
"v_perm_b32 v31, v62, v99, s12\n"
"ds_read_b128 v[88:91], v3 offset:18432\n"
"ds_read_b128 v[92:95], v3 offset:19456\n"
"ds_read_b128 v[96:99], v3 offset:20480\n"
"s_waitcnt lgkmcnt(3)\n"
"v_and_b32 v61, s8, v100\n"
"v_alignbit_b32 v62, v100, v100, 4\n"
"v_mfma_f32_16x16x32_bf16 v[32:35], v[28:31], v[16:19], v[32:35]\n"
"v_perm_b32 v24, v61, v104, s11\n"
"v_and_b32 v62, s8, v62\n"
"v_perm_b32 v25, v61, v104, s12\n"
"v_perm_b32 v26, v62, v105, s11\n"
"v_perm_b32 v27, v62, v105, s12\n"
"s_add_u32 s52, s53, 0xc4000\n"
"s_and_b32 s52, s52, s54\n"
"buffer_load_dwordx4 a[204:207], v2, s[32:35], s52 offen offset:0 nt\n"
"v_and_b32 v61, s8, v101\n"
"v_alignbit_b32 v62, v101, v101, 4\n"
"v_mfma_f32_16x16x32_bf16 v[36:39], v[24:27], v[4:7], 0\n"
"v_perm_b32 v28, v61, v106, s11\n"
"v_and_b32 v62, s8, v62\n"
"v_perm_b32 v29, v61, v106, s12\n"
"v_perm_b32 v30, v62, v107, s11\n"
"v_perm_b32 v31, v62, v107, s12\n"
"v_mov_b32_dpp v63, v32 quad_perm:[0,0,0,0] row_mask:0xf bank_mask:0xf\n"
"v_add_f32_dpp v63, v33, v63 quad_perm:[1,1,1,1] row_mask:0xf bank_mask:0xf\n"
"v_add_f32_dpp v63, v34, v63 quad_perm:[2,2,2,2] row_mask:0xf bank_mask:0xf\n"
"v_add_f32_dpp v63, v35, v63 quad_perm:[3,3,3,3] row_mask:0xf bank_mask:0xf\n"
"v_cndmask_b32_e64 v50, v50, v63, s[64:65]\n"
"v_and_b32 v61, s8, v102\n"
"v_alignbit_b32 v62, v102, v102, 4\n"
"v_mfma_f32_16x16x32_bf16 v[36:39], v[28:31], v[8:11], v[36:39]\n"
"v_perm_b32 v24, v61, v108, s11\n"
"v_and_b32 v62, s8, v62\n"
"v_perm_b32 v25, v61, v108, s12\n"
"v_perm_b32 v26, v62, v109, s11\n"
"v_perm_b32 v27, v62, v109, s12\n"
"v_and_b32 v61, s8, v103\n"
"v_alignbit_b32 v62, v103, v103, 4\n"
"v_mfma_f32_16x16x32_bf16 v[36:39], v[24:27], v[12:15], v[36:39]\n"
"v_perm_b32 v28, v61, v110, s11\n"
"v_and_b32 v62, s8, v62\n"
"v_perm_b32 v29, v61, v110, s12\n"
"v_perm_b32 v30, v62, v111, s11\n"
"v_perm_b32 v31, v62, v111, s12\n"
"ds_read_b128 v[100:103], v3 offset:21504\n"
"ds_read_b128 v[104:107], v3 offset:22528\n"
"ds_read_b128 v[108:111], v3 offset:23552\n"
"s_waitcnt lgkmcnt(3)\n"
"v_and_b32 v61, s8, v88\n"
"v_alignbit_b32 v62, v88, v88, 4\n"
"v_mfma_f32_16x16x32_bf16 v[36:39], v[28:31], v[16:19], v[36:39]\n"
"v_perm_b32 v24, v61, v92, s11\n"
"v_and_b32 v62, s8, v62\n"
"v_perm_b32 v25, v61, v92, s12\n"
"v_perm_b32 v26, v62, v93, s11\n"
"v_perm_b32 v27, v62, v93, s12\n"
"s_add_u32 s52, s53, 0x62000\n"
"s_and_b32 s52, s52, s54\n"
"buffer_load_dwordx4 a[208:211], v2, s[32:35], s52 offen offset:0 nt\n"
"v_and_b32 v61, s8, v89\n"
"v_alignbit_b32 v62, v89, v89, 4\n"
"v_mfma_f32_16x16x32_bf16 v[32:35], v[24:27], v[4:7], 0\n"
"v_perm_b32 v28, v61, v94, s11\n"
"v_and_b32 v62, s8, v62\n"
"v_perm_b32 v29, v61, v94, s12\n"
"v_perm_b32 v30, v62, v95, s11\n"
"v_perm_b32 v31, v62, v95, s12\n"
"v_mov_b32_dpp v63, v36 quad_perm:[0,0,0,0] row_mask:0xf bank_mask:0xf\n"
"v_add_f32_dpp v63, v37, v63 quad_perm:[1,1,1,1] row_mask:0xf bank_mask:0xf\n"
"v_add_f32_dpp v63, v38, v63 quad_perm:[2,2,2,2] row_mask:0xf bank_mask:0xf\n"
"v_add_f32_dpp v63, v39, v63 quad_perm:[3,3,3,3] row_mask:0xf bank_mask:0xf\n"
"v_cndmask_b32_e64 v50, v50, v63, s[66:67]\n"
"v_and_b32 v61, s8, v90\n"
"v_alignbit_b32 v62, v90, v90, 4\n"
"v_mfma_f32_16x16x32_bf16 v[32:35], v[28:31], v[8:11], v[32:35]\n"
"v_perm_b32 v24, v61, v96, s11\n"
"v_and_b32 v62, s8, v62\n"
"v_perm_b32 v25, v61, v96, s12\n"
"v_perm_b32 v26, v62, v97, s11\n"
"v_perm_b32 v27, v62, v97, s12\n"
"v_and_b32 v61, s8, v91\n"
"v_alignbit_b32 v62, v91, v91, 4\n"
"v_mfma_f32_16x16x32_bf16 v[32:35], v[24:27], v[12:15], v[32:35]\n"
"v_perm_b32 v28, v61, v98, s11\n"
"v_and_b32 v62, s8, v62\n"
"v_perm_b32 v29, v61, v98, s12\n"
"v_perm_b32 v30, v62, v99, s11\n"
"v_perm_b32 v31, v62, v99, s12\n"
"ds_read_b128 v[88:91], v3 offset:24576\n"
"ds_read_b128 v[92:95], v3 offset:25600\n"
"ds_read_b128 v[96:99], v3 offset:26624\n"
"s_waitcnt lgkmcnt(3)\n"
"v_and_b32 v61, s8, v100\n"
"v_alignbit_b32 v62, v100, v100, 4\n"
"v_mfma_f32_16x16x32_bf16 v[32:35], v[28:31], v[16:19], v[32:35]\n"
"v_perm_b32 v24, v61, v104, s11\n"
"v_and_b32 v62, s8, v62\n"
"v_perm_b32 v25, v61, v104, s12\n"
"v_perm_b32 v26, v62, v105, s11\n"
"v_perm_b32 v27, v62, v105, s12\n"
"v_and_b32 v61, s8, v101\n"
"v_alignbit_b32 v62, v101, v101, 4\n"
"v_mfma_f32_16x16x32_bf16 v[36:39], v[24:27], v[4:7], 0\n"
"v_perm_b32 v28, v61, v106, s11\n"
"v_and_b32 v62, s8, v62\n"
"v_perm_b32 v29, v61, v106, s12\n"
"v_perm_b32 v30, v62, v107, s11\n"
"v_perm_b32 v31, v62, v107, s12\n"
"v_mov_b32_dpp v63, v32 quad_perm:[0,0,0,0] row_mask:0xf bank_mask:0xf\n"
"v_add_f32_dpp v63, v33, v63 quad_perm:[1,1,1,1] row_mask:0xf bank_mask:0xf\n"
"v_add_f32_dpp v63, v34, v63 quad_perm:[2,2,2,2] row_mask:0xf bank_mask:0xf\n"
"v_add_f32_dpp v63, v35, v63 quad_perm:[3,3,3,3] row_mask:0xf bank_mask:0xf\n"
"v_cndmask_b32_e64 v51, v51, v63, s[60:61]\n"
"v_and_b32 v61, s8, v102\n"
"v_alignbit_b32 v62, v102, v102, 4\n"
"v_mfma_f32_16x16x32_bf16 v[36:39], v[28:31], v[8:11], v[36:39]\n"
"v_perm_b32 v24, v61, v108, s11\n"
"v_and_b32 v62, s8, v62\n"
"v_perm_b32 v25, v61, v108, s12\n"
"v_perm_b32 v26, v62, v109, s11\n"
"v_perm_b32 v27, v62, v109, s12\n"
"s_add_u32 s52, s53, 0xd2000\n"
"s_and_b32 s52, s52, s54\n"
"buffer_load_dwordx4 a[212:215], v2, s[32:35], s52 offen offset:0 nt\n"
"v_and_b32 v61, s8, v103\n"
"v_alignbit_b32 v62, v103, v103, 4\n"
"v_mfma_f32_16x16x32_bf16 v[36:39], v[24:27], v[12:15], v[36:39]\n"
"v_perm_b32 v28, v61, v110, s11\n"
"v_and_b32 v62, s8, v62\n"
"v_perm_b32 v29, v61, v110, s12\n"
"v_perm_b32 v30, v62, v111, s11\n"
"v_perm_b32 v31, v62, v111, s12\n"
"ds_read_b128 v[100:103], v3 offset:27648\n"
"ds_read_b128 v[104:107], v3 offset:28672\n"
"ds_read_b128 v[108:111], v3 offset:29696\n"
"s_waitcnt lgkmcnt(3)\n"
"v_and_b32 v61, s8, v88\n"
"v_alignbit_b32 v62, v88, v88, 4\n"
"v_mfma_f32_16x16x32_bf16 v[36:39], v[28:31], v[16:19], v[36:39]\n"
"v_perm_b32 v24, v61, v92, s11\n"
"v_and_b32 v62, s8, v62\n"
"v_perm_b32 v25, v61, v92, s12\n"
"v_perm_b32 v26, v62, v93, s11\n"
"v_perm_b32 v27, v62, v93, s12\n"
"v_and_b32 v61, s8, v89\n"
"v_alignbit_b32 v62, v89, v89, 4\n"
"v_mfma_f32_16x16x32_bf16 v[32:35], v[24:27], v[4:7], 0\n"
"v_perm_b32 v28, v61, v94, s11\n"
"v_and_b32 v62, s8, v62\n"
"v_perm_b32 v29, v61, v94, s12\n"
"v_perm_b32 v30, v62, v95, s11\n"
"v_perm_b32 v31, v62, v95, s12\n"
"v_mov_b32_dpp v63, v36 quad_perm:[0,0,0,0] row_mask:0xf bank_mask:0xf\n"
"v_add_f32_dpp v63, v37, v63 quad_perm:[1,1,1,1] row_mask:0xf bank_mask:0xf\n"
"v_add_f32_dpp v63, v38, v63 quad_perm:[2,2,2,2] row_mask:0xf bank_mask:0xf\n"
"v_add_f32_dpp v63, v39, v63 quad_perm:[3,3,3,3] row_mask:0xf bank_mask:0xf\n"
"v_cndmask_b32_e64 v51, v51, v63, s[62:63]\n"
"v_and_b32 v61, s8, v90\n"
"v_alignbit_b32 v62, v90, v90, 4\n"
"v_mfma_f32_16x16x32_bf16 v[32:35], v[28:31], v[8:11], v[32:35]\n"
"v_perm_b32 v24, v61, v96, s11\n"
"v_and_b32 v62, s8, v62\n"
"v_perm_b32 v25, v61, v96, s12\n"
"v_perm_b32 v26, v62, v97, s11\n"
"v_perm_b32 v27, v62, v97, s12\n"
"v_and_b32 v61, s8, v91\n"
"v_alignbit_b32 v62, v91, v91, 4\n"
"v_mfma_f32_16x16x32_bf16 v[32:35], v[24:27], v[12:15], v[32:35]\n"
"v_perm_b32 v28, v61, v98, s11\n"
"v_and_b32 v62, s8, v62\n"
"v_perm_b32 v29, v61, v98, s12\n"
"v_perm_b32 v30, v62, v99, s11\n"
"v_perm_b32 v31, v62, v99, s12\n"
"ds_read_b128 v[88:91], v3 offset:30720\n"
"ds_read_b128 v[92:95], v3 offset:31744\n"
"ds_read_b128 v[96:99], v3 offset:32768\n"
"s_waitcnt lgkmcnt(3)\n"
"v_and_b32 v61, s8, v100\n"
"v_alignbit_b32 v62, v100, v100, 4\n"
"v_mfma_f32_16x16x32_bf16 v[32:35], v[28:31], v[16:19], v[32:35]\n"
"v_perm_b32 v24, v61, v104, s11\n"
"v_and_b32 v62, s8, v62\n"
"v_perm_b32 v25, v61, v104, s12\n"
"v_perm_b32 v26, v62, v105, s11\n"
"v_perm_b32 v27, v62, v105, s12\n"
"v_and_b32 v61, s8, v101\n"
"v_alignbit_b32 v62, v101, v101, 4\n"
"v_mfma_f32_16x16x32_bf16 v[36:39], v[24:27], v[4:7], 0\n"
"v_perm_b32 v28, v61, v106, s11\n"
"v_and_b32 v62, s8, v62\n"
"v_perm_b32 v29, v61, v106, s12\n"
"v_perm_b32 v30, v62, v107, s11\n"
"v_perm_b32 v31, v62, v107, s12\n"
"v_mov_b32_dpp v63, v32 quad_perm:[0,0,0,0] row_mask:0xf bank_mask:0xf\n"
"v_add_f32_dpp v63, v33, v63 quad_perm:[1,1,1,1] row_mask:0xf bank_mask:0xf\n"
"v_add_f32_dpp v63, v34, v63 quad_perm:[2,2,2,2] row_mask:0xf bank_mask:0xf\n"
"v_add_f32_dpp v63, v35, v63 quad_perm:[3,3,3,3] row_mask:0xf bank_mask:0xf\n"
"v_cndmask_b32_e64 v51, v51, v63, s[64:65]\n"
"v_and_b32 v61, s8, v102\n"
"v_alignbit_b32 v62, v102, v102, 4\n"
"v_mfma_f32_16x16x32_bf16 v[36:39], v[28:31], v[8:11], v[36:39]\n"
"v_perm_b32 v24, v61, v108, s11\n"
"v_and_b32 v62, s8, v62\n"
"v_perm_b32 v25, v61, v108, s12\n"
"v_perm_b32 v26, v62, v109, s11\n"
"v_perm_b32 v27, v62, v109, s12\n"
"v_and_b32 v61, s8, v103\n"
"v_alignbit_b32 v62, v103, v103, 4\n"
"v_mfma_f32_16x16x32_bf16 v[36:39], v[24:27], v[12:15], v[36:39]\n"
"v_perm_b32 v28, v61, v110, s11\n"
"v_and_b32 v62, s8, v62\n"
"v_perm_b32 v29, v61, v110, s12\n"
"v_perm_b32 v30, v62, v111, s11\n"
"v_perm_b32 v31, v62, v111, s12\n"
"ds_read_b128 v[100:103], v3 offset:33792\n"
"ds_read_b128 v[104:107], v3 offset:34816\n"
"ds_read_b128 v[108:111], v3 offset:35840\n"
"s_waitcnt lgkmcnt(3)\n"
"v_and_b32 v61, s8, v88\n"
"v_alignbit_b32 v62, v88, v88, 4\n"
"v_mfma_f32_16x16x32_bf16 v[36:39], v[28:31], v[16:19], v[36:39]\n"
"v_perm_b32 v24, v61, v92, s11\n"
"v_and_b32 v62, s8, v62\n"
"v_perm_b32 v25, v61, v92, s12\n"
"v_perm_b32 v26, v62, v93, s11\n"
"v_perm_b32 v27, v62, v93, s12\n"
"v_and_b32 v61, s8, v89\n"
"v_alignbit_b32 v62, v89, v89, 4\n"
"v_mfma_f32_16x16x32_bf16 v[32:35], v[24:27], v[4:7], 0\n"
"v_perm_b32 v28, v61, v94, s11\n"
"v_and_b32 v62, s8, v62\n"
"v_perm_b32 v29, v61, v94, s12\n"
"v_perm_b32 v30, v62, v95, s11\n"
"v_perm_b32 v31, v62, v95, s12\n"
"v_mov_b32_dpp v63, v36 quad_perm:[0,0,0,0] row_mask:0xf bank_mask:0xf\n"
"v_add_f32_dpp v63, v37, v63 quad_perm:[1,1,1,1] row_mask:0xf bank_mask:0xf\n"
"v_add_f32_dpp v63, v38, v63 quad_perm:[2,2,2,2] row_mask:0xf bank_mask:0xf\n"
"v_add_f32_dpp v63, v39, v63 quad_perm:[3,3,3,3] row_mask:0xf bank_mask:0xf\n"
"v_cndmask_b32_e64 v51, v51, v63, s[66:67]\n"
"v_and_b32 v61, s8, v90\n"
"v_alignbit_b32 v62, v90, v90, 4\n"
"v_mfma_f32_16x16x32_bf16 v[32:35], v[28:31], v[8:11], v[32:35]\n"
"v_perm_b32 v24, v61, v96, s11\n"
"v_and_b32 v62, s8, v62\n"
"v_perm_b32 v25, v61, v96, s12\n"
"v_perm_b32 v26, v62, v97, s11\n"
"v_perm_b32 v27, v62, v97, s12\n"
"ds_write_b128 v3, v[20:23] offset:3072\n"
"ds_write_b128 v3, v[44:47]\n"
"ds_write_b128 v3, v[48:51] offset:1024\n"
"v_and_b32 v61, s8, v91\n"
"v_alignbit_b32 v62, v91, v91, 4\n"
"v_mfma_f32_16x16x32_bf16 v[32:35], v[24:27], v[12:15], v[32:35]\n"
"v_perm_b32 v28, v61, v98, s11\n"
"v_and_b32 v62, s8, v62\n"
"v_perm_b32 v29, v61, v98, s12\n"
"v_perm_b32 v30, v62, v99, s11\n"
"v_perm_b32 v31, v62, v99, s12\n"
"ds_read_b128 v[88:91], v3 offset:36864\n"
"ds_read_b128 v[92:95], v3 offset:37888\n"
"ds_read_b128 v[96:99], v3 offset:38912\n"
"s_waitcnt lgkmcnt(3)\n"
"v_and_b32 v61, s8, v100\n"
"v_alignbit_b32 v62, v100, v100, 4\n"
"v_mfma_f32_16x16x32_bf16 v[32:35], v[28:31], v[16:19], v[32:35]\n"
"v_perm_b32 v24, v61, v104, s11\n"
"v_and_b32 v62, s8, v62\n"
"v_perm_b32 v25, v61, v104, s12\n"
"v_perm_b32 v26, v62, v105, s11\n"
"v_perm_b32 v27, v62, v105, s12\n"
"v_and_b32 v61, s8, v101\n"
"v_alignbit_b32 v62, v101, v101, 4\n"
"v_mfma_f32_16x16x32_bf16 v[36:39], v[24:27], v[4:7], 0\n"
"v_perm_b32 v28, v61, v106, s11\n"
"v_and_b32 v62, s8, v62\n"
"v_perm_b32 v29, v61, v106, s12\n"
"v_perm_b32 v30, v62, v107, s11\n"
"v_perm_b32 v31, v62, v107, s12\n"
"v_mov_b32_dpp v63, v32 quad_perm:[0,0,0,0] row_mask:0xf bank_mask:0xf\n"
"v_add_f32_dpp v63, v33, v63 quad_perm:[1,1,1,1] row_mask:0xf bank_mask:0xf\n"
"v_add_f32_dpp v63, v34, v63 quad_perm:[2,2,2,2] row_mask:0xf bank_mask:0xf\n"
"v_add_f32_dpp v63, v35, v63 quad_perm:[3,3,3,3] row_mask:0xf bank_mask:0xf\n"
"v_cndmask_b32_e64 v52, v52, v63, s[60:61]\n"
"v_and_b32 v61, s8, v102\n"
"v_alignbit_b32 v62, v102, v102, 4\n"
"v_mfma_f32_16x16x32_bf16 v[36:39], v[28:31], v[8:11], v[36:39]\n"
"v_perm_b32 v24, v61, v108, s11\n"
"v_and_b32 v62, s8, v62\n"
"v_perm_b32 v25, v61, v108, s12\n"
"v_perm_b32 v26, v62, v109, s11\n"
"v_perm_b32 v27, v62, v109, s12\n"
"v_and_b32 v61, s8, v103\n"
"v_alignbit_b32 v62, v103, v103, 4\n"
"v_mfma_f32_16x16x32_bf16 v[36:39], v[24:27], v[12:15], v[36:39]\n"
"v_perm_b32 v28, v61, v110, s11\n"
"v_and_b32 v62, s8, v62\n"
"v_perm_b32 v29, v61, v110, s12\n"
"v_perm_b32 v30, v62, v111, s11\n"
"v_perm_b32 v31, v62, v111, s12\n"
"s_waitcnt lgkmcnt(0)\n"
"v_and_b32 v61, s8, v88\n"
"v_alignbit_b32 v62, v88, v88, 4\n"
"v_mfma_f32_16x16x32_bf16 v[36:39], v[28:31], v[16:19], v[36:39]\n"
"v_perm_b32 v24, v61, v92, s11\n"
"v_and_b32 v62, s8, v62\n"
"v_perm_b32 v25, v61, v92, s12\n"
"v_perm_b32 v26, v62, v93, s11\n"
"v_perm_b32 v27, v62, v93, s12\n"
"s_add_u32 s52, s53, 0x5b000\n"
"s_and_b32 s52, s52, s54\n"
"buffer_load_dwordx4 v[104:107], v2, s[32:35], s52 offen offset:0 nt\n"
"v_and_b32 v61, s8, v89\n"
"v_alignbit_b32 v62, v89, v89, 4\n"
"v_mfma_f32_16x16x32_bf16 v[32:35], v[24:27], v[4:7], 0\n"
"v_perm_b32 v28, v61, v94, s11\n"
"v_and_b32 v62, s8, v62\n"
"v_perm_b32 v29, v61, v94, s12\n"
"v_perm_b32 v30, v62, v95, s11\n"
"v_perm_b32 v31, v62, v95, s12\n"
"s_add_u32 s52, s53, 0xcb000\n"
"s_and_b32 s52, s52, s54\n"
"buffer_load_dwordx4 v[108:111], v2, s[32:35], s52 offen offset:0 nt\n"
"v_mov_b32_dpp v63, v36 quad_perm:[0,0,0,0] row_mask:0xf bank_mask:0xf\n"
"v_add_f32_dpp v63, v37, v63 quad_perm:[1,1,1,1] row_mask:0xf bank_mask:0xf\n"
"v_add_f32_dpp v63, v38, v63 quad_perm:[2,2,2,2] row_mask:0xf bank_mask:0xf\n"
"v_add_f32_dpp v63, v39, v63 quad_perm:[3,3,3,3] row_mask:0xf bank_mask:0xf\n"
"v_cndmask_b32_e64 v52, v52, v63, s[62:63]\n"
"v_and_b32 v61, s8, v90\n"
"v_alignbit_b32 v62, v90, v90, 4\n"
"v_mfma_f32_16x16x32_bf16 v[32:35], v[28:31], v[8:11], v[32:35]\n"
"v_perm_b32 v24, v61, v96, s11\n"
"v_and_b32 v62, s8, v62\n"
"v_perm_b32 v25, v61, v96, s12\n"
"v_perm_b32 v26, v62, v97, s11\n"
"v_perm_b32 v27, v62, v97, s12\n"
"v_and_b32 v61, s8, v91\n"
"v_alignbit_b32 v62, v91, v91, 4\n"
"v_mfma_f32_16x16x32_bf16 v[32:35], v[24:27], v[12:15], v[32:35]\n"
"v_perm_b32 v28, v61, v98, s11\n"
"v_and_b32 v62, s8, v62\n"
"v_perm_b32 v29, v61, v98, s12\n"
"v_perm_b32 v30, v62, v99, s11\n"
"v_perm_b32 v31, v62, v99, s12\n"
"s_nop 1\n"
"v_mfma_f32_16x16x32_bf16 v[32:35], v[28:31], v[16:19], v[32:35]\n"
"s_add_u32 s52, s53, 0x69000\n"
"s_and_b32 s52, s52, s54\n"
"buffer_load_dwordx4 v[96:99], v2, s[32:35], s52 offen offset:0 nt\n"
"s_add_u32 s52, s53, 0xd9000\n"
"s_and_b32 s52, s52, s54\n"
"buffer_load_dwordx4 v[100:103], v2, s[32:35], s52 offen offset:0 nt\n"
"v_and_b32 v14, 3, v0\n"
"v_mul_u32_u24 v14, 0x5555, v14\n"
"v_mov_b32 v24, 0\n"
"v_mov_b32 v25, 0\n"
"v_mov_b32 v26, 0\n"
"v_mov_b32 v27, 0\n"
"v_mov_b32 v28, 0\n"
"v_mov_b32 v29, 0\n"
"v_mov_b32 v30, 0\n"
"v_mov_b32 v31, 0\n"
"v_mov_b32_dpp v63, v32 quad_perm:[0,0,0,0] row_mask:0xf bank_mask:0xf\n"
"v_add_f32_dpp v63, v33, v63 quad_perm:[1,1,1,1] row_mask:0xf bank_mask:0xf\n"
"v_add_f32_dpp v63, v34, v63 quad_perm:[2,2,2,2] row_mask:0xf bank_mask:0xf\n"
"v_add_f32_dpp v63, v35, v63 quad_perm:[3,3,3,3] row_mask:0xf bank_mask:0xf\n"
"v_cndmask_b32_e64 v52, v52, v63, s[64:65]\n"
"ds_write_b32 v3, v52 offset:2048\n"
"s_and_b64 exec, s[40:41], s[46:47]\n"
"ds_add_f32 v1, v40\n"
"s_mov_b64 exec, s[46:47]\n"
"ds_read_b128 v[44:47], v3\n"
"ds_read_b128 v[48:51], v3 offset:1024\n"
"ds_read_b32 v52, v3 offset:2048\n"
"ds_read_b32 v16, v1\n"
"v_lshlrev_b32 v32, 16, v53\n"
"v_and_b32 v33, 0xffff0000, v53\n"
"v_lshlrev_b32 v34, 16, v54\n"
"v_and_b32 v35, 0xffff0000, v54\n"
"v_lshlrev_b32 v36, 16, v55\n"
"v_and_b32 v37, 0xffff0000, v55\n"
"v_lshlrev_b32 v38, 16, v56\n"
"v_and_b32 v39, 0xffff0000, v56\n"
"s_waitcnt lgkmcnt(0)\n"
"s_waitcnt vmcnt(52)\n"
"s_add_u32 s52, s53, 0xe0000\n"
"s_and_b32 s52, s52, s54\n"
"buffer_load_dwordx4 v[88:91], v2, s[32:35], s52 offen offset:0 nt\n"
"v_cvt_pk_bf16_f32 v58, v44, v45\n"
"v_lshlrev_b32 v44, 16, v58\n"
"v_and_b32 v45, 0xffff0000, v58\n"
"v_mul_f32 v44, 0x3db504f3, v44\n"
"v_mul_f32 v45, 0x3db504f3, v45\n"
"v_cvt_pk_bf16_f32 v58, v44, v45\n"
"v_lshlrev_b32 v44, 16, v58\n"
"v_and_b32 v45, 0xffff0000, v58\n"
"v_cvt_pk_bf16_f32 v58, v46, v47\n"
"v_lshlrev_b32 v46, 16, v58\n"
"v_and_b32 v47, 0xffff0000, v58\n"
"v_mul_f32 v46, 0x3db504f3, v46\n"
"v_mul_f32 v47, 0x3db504f3, v47\n"
"v_cvt_pk_bf16_f32 v58, v46, v47\n"
"v_lshlrev_b32 v46, 16, v58\n"
"v_and_b32 v47, 0xffff0000, v58\n"
"v_cvt_pk_bf16_f32 v58, v48, v49\n"
"v_lshlrev_b32 v48, 16, v58\n"
"v_and_b32 v49, 0xffff0000, v58\n"
"v_mul_f32 v48, 0x3db504f3, v48\n"
"v_mul_f32 v49, 0x3db504f3, v49\n"
"v_cvt_pk_bf16_f32 v58, v48, v49\n"
"v_lshlrev_b32 v48, 16, v58\n"
"v_and_b32 v49, 0xffff0000, v58\n"
"v_cvt_pk_bf16_f32 v58, v50, v51\n"
"v_lshlrev_b32 v50, 16, v58\n"
"v_and_b32 v51, 0xffff0000, v58\n"
"v_mul_f32 v50, 0x3db504f3, v50\n"
"v_mul_f32 v51, 0x3db504f3, v51\n"
"v_cvt_pk_bf16_f32 v58, v50, v51\n"
"v_lshlrev_b32 v50, 16, v58\n"
"v_and_b32 v51, 0xffff0000, v58\n"
"s_add_u32 s52, s53, 0x150000\n"
"s_and_b32 s52, s52, s54\n"
"buffer_load_dwordx4 v[92:95], v2, s[32:35], s52 offen offset:0 nt\n"
"v_cvt_pk_bf16_f32 v58, v52, v16\n"
"v_lshlrev_b32 v52, 16, v58\n"
"v_and_b32 v16, 0xffff0000, v58\n"
"v_mul_f32 v52, 0x3db504f3, v52\n"
"v_mul_f32 v16, 0x3db504f3, v16\n"
"v_cvt_pk_bf16_f32 v58, v52, v16\n"
"v_lshlrev_b32 v52, 16, v58\n"
"v_and_b32 v16, 0xffff0000, v58\n"
"v_max_f32 v58, v44, v45\n"
"v_max3_f32 v58, v58, v46, v47\n"
"v_max3_f32 v58, v58, v48, v49\n"
"v_max3_f32 v58, v58, v50, v51\n"
"v_max_f32 v58, v58, v52\n"
"s_nop 1\n"
"v_max_f32_dpp v58, v58, v58 quad_perm:[1,0,3,2] row_mask:0xf bank_mask:0xf\n"
"s_nop 1\n"
"v_max_f32_dpp v58, v58, v58 quad_perm:[2,3,0,1] row_mask:0xf bank_mask:0xf\n"
"s_nop 1\n"
"v_max_f32_dpp v58, v58, v58 row_half_mirror row_mask:0xf bank_mask:0xf\n"
"s_nop 1\n"
"v_max_f32_dpp v58, v58, v58 row_mirror row_mask:0xf bank_mask:0xf\n"
"s_nop 1\n"
"v_max_f32_dpp v58, v58, v58 row_bcast:15 row_mask:0xa bank_mask:0xf\n"
"s_nop 1\n"
"v_max_f32_dpp v58, v58, v58 row_bcast:31 row_mask:0xc bank_mask:0xf\n"
"v_mul_f32 v58, 0x3fb8aa3b, v58\n"
"s_nop 0\n"
"v_readlane_b32 s58, v58, 63\n"
"v_mov_b32 v59, 0x3fb8aa3b\n"
"s_nop 0\n"
"v_fma_f32 v16, v16, v59, -s58\n"
"v_fma_f32 v44, v44, v59, -s58\n"
"v_fma_f32 v45, v45, v59, -s58\n"
"v_fma_f32 v46, v46, v59, -s58\n"
"v_fma_f32 v47, v47, v59, -s58\n"
"v_fma_f32 v48, v48, v59, -s58\n"
"v_fma_f32 v49, v49, v59, -s58\n"
"v_fma_f32 v50, v50, v59, -s58\n"
"v_fma_f32 v51, v51, v59, -s58\n"
"v_fma_f32 v52, v52, v59, -s58\n"
"v_exp_f32 v16, v16\n"
"v_exp_f32 v44, v44\n"
"v_exp_f32 v45, v45\n"
"v_exp_f32 v46, v46\n"
"v_exp_f32 v47, v47\n"
"v_exp_f32 v48, v48\n"
"v_exp_f32 v49, v49\n"
"v_exp_f32 v50, v50\n"
"v_exp_f32 v51, v51\n"
"v_exp_f32 v52, v52\n"
"v_cvt_pk_bf16_f32 v16, 0, v16\n"
"v_pk_mul_f32 v[32:33], v[32:33], v[16:17] op_sel_hi:[1,0]\n"
"v_pk_mul_f32 v[34:35], v[34:35], v[16:17] op_sel_hi:[1,0]\n"
"v_pk_mul_f32 v[36:37], v[36:37], v[16:17] op_sel_hi:[1,0]\n"
"v_pk_mul_f32 v[38:39], v[38:39], v[16:17] op_sel_hi:[1,0]\n"
"s_and_b64 exec, s[42:43], s[46:47]\n"
"ds_add_f32 v57, v32 offset:3072\n"
"ds_add_f32 v57, v33 offset:3076\n"
"ds_add_f32 v57, v34 offset:3080\n"
"ds_add_f32 v57, v35 offset:3084\n"
"ds_add_f32 v57, v36 offset:3328\n"
"ds_add_f32 v57, v37 offset:3332\n"
"ds_add_f32 v57, v38 offset:3336\n"
"ds_add_f32 v57, v39 offset:3340\n"
"s_mov_b64 exec, s[46:47]\n"
"ds_read_b128 v[40:43], v3 offset:3072\n"
"v_add_f32 v58, v44, v45\n"
"v_add_f32 v58, v58, v46\n"
"v_add_f32 v58, v58, v47\n"
"v_add_f32 v58, v58, v48\n"
"v_add_f32 v58, v58, v49\n"
"v_add_f32 v58, v58, v50\n"
"v_add_f32 v58, v58, v51\n"
"v_add_f32 v58, v58, v52\n"
"s_and_b32 exec_lo, exec_lo, 0xcccccccc\n"
"s_and_b32 exec_hi, exec_hi, 0xcccccccc\n"
"v_mul_f32 v48, s18, v48\n"
"s_mov_b64 exec, s[46:47]\n"
"s_and_b32 exec_lo, exec_lo, 0xcccccccc\n"
"s_and_b32 exec_hi, exec_hi, 0xcccccccc\n"
"v_mul_f32 v49, s18, v49\n"
"s_mov_b64 exec, s[46:47]\n"
"v_mul_f32 v50, s18, v50\n"
"v_mul_f32 v51, s18, v51\n"
"v_cvt_pk_bf16_f32 v44, v44, 0\n"
"v_cvt_pk_bf16_f32 v45, v45, 0\n"
"v_cvt_pk_bf16_f32 v46, v46, 0\n"
"v_cvt_pk_bf16_f32 v47, v47, 0\n"
"v_cvt_pk_bf16_f32 v48, v48, 0\n"
"v_cvt_pk_bf16_f32 v49, v49, 0\n"
"v_cvt_pk_bf16_f32 v50, v50, 0\n"
"v_cvt_pk_bf16_f32 v51, v51, 0\n"
"v_cvt_pk_bf16_f32 v52, v52, 0\n"
"v_add_f32_dpp v58, v58, v58 quad_perm:[1,0,3,2] row_mask:0xf bank_mask:0xf\n"
"s_nop 1\n"
"v_add_f32_dpp v58, v58, v58 quad_perm:[2,3,0,1] row_mask:0xf bank_mask:0xf\n"
"s_nop 1\n"
"v_add_f32_dpp v58, v58, v58 row_half_mirror row_mask:0xf bank_mask:0xf\n"
"s_nop 1\n"
"v_add_f32_dpp v58, v58, v58 row_mirror row_mask:0xf bank_mask:0xf\n"
"s_nop 1\n"
"v_add_f32_dpp v58, v58, v58 row_bcast:15 row_mask:0xa bank_mask:0xf\n"
"s_nop 1\n"
"v_add_f32_dpp v58, v58, v58 row_bcast:31 row_mask:0xc bank_mask:0xf\n"
"v_mov_b32 v61, v58\n"
"v_mov_b32 v60, s58\n"
"s_mul_i32 s56, s50, 576\n"
"s_add_u32 s56, s56, 160256\n"
"v_mov_b32 v59, s56\n"
"s_mov_b32 exec_lo, 0\n"
"s_and_b32 exec_hi, exec_hi, 0x80000000\n"
"ds_write_b64 v59, v[60:61]\n"
"s_mov_b64 exec, s[46:47]\n"
"v_mov_b32_dpp v15, v52 row_shl:4 row_mask:0xf bank_mask:0xf bound_ctrl:0\n"
"v_permlane32_swap_b32 v50, v51\n"
"v_permlane32_swap_b32 v48, v49\n"
"v_permlane32_swap_b32 v46, v47\n"
"v_permlane32_swap_b32 v44, v45\n"
"v_permlane32_swap_b32 v52, v15\n"
"v_permlane16_swap_b32 v50, v51\n"
"v_permlane16_swap_b32 v48, v49\n"
"v_permlane16_swap_b32 v46, v47\n"
"v_permlane16_swap_b32 v44, v45\n"
"v_permlane16_swap_b32 v52, v15\n"
"s_waitcnt lgkmcnt(0)\n"
"v_mov_b32_dpp v11, v51 row_half_mirror row_mask:0xf bank_mask:0xf\n"
"v_mov_b32_dpp v10, v50 row_half_mirror row_mask:0xf bank_mask:0xf\n"
"v_mov_b32_dpp v13, v15 row_half_mirror row_mask:0xf bank_mask:0xf\n"
"v_mov_b32_dpp v12, v52 row_half_mirror row_mask:0xf bank_mask:0xf\n"
"v_mov_b32_dpp v9, v49 row_half_mirror row_mask:0xf bank_mask:0xf\n"
"v_mov_b32_dpp v8, v48 row_half_mirror row_mask:0xf bank_mask:0xf\n"
"v_mov_b32_dpp v7, v47 row_half_mirror row_mask:0xf bank_mask:0xf\n"
"v_mov_b32_dpp v6, v46 row_half_mirror row_mask:0xf bank_mask:0xf\n"
"v_mov_b32_dpp v5, v45 row_half_mirror row_mask:0xf bank_mask:0xf\n"
"v_mov_b32_dpp v4, v44 row_half_mirror row_mask:0xf bank_mask:0xf\n"
"v_mov_b32_dpp v24, v8 quad_perm:[2,2,2,2] row_mask:0xf bank_mask:0x5\n"
"v_mov_b32_dpp v25, v48 quad_perm:[1,1,1,1] row_mask:0xf bank_mask:0xa\n"
"v_mov_b32_dpp v26, v9 quad_perm:[2,2,2,2] row_mask:0xf bank_mask:0x5\n"
"v_mov_b32_dpp v27, v49 quad_perm:[1,1,1,1] row_mask:0xf bank_mask:0xa\n"
"v_mov_b32_dpp v28, v52 quad_perm:[0,0,0,0] row_mask:0xf bank_mask:0x5\n"
"v_mov_b32_dpp v29, v12 quad_perm:[3,3,3,3] row_mask:0xf bank_mask:0xa\n"
"v_smfmac_f32_16x16x64_bf16 v[20:23], v[24:27], a[216:223], v14\n"
"s_add_u32 s52, s53, 0xee000\n"
"s_and_b32 s52, s52, s54\n"
"buffer_load_dwordx4 a[216:219], v2, s[32:35], s52 offen offset:0 nt\n"
"v_mov_b32_dpp v30, v15 quad_perm:[0,0,0,0] row_mask:0xf bank_mask:0x5\n"
"v_mov_b32_dpp v31, v13 quad_perm:[3,3,3,3] row_mask:0xf bank_mask:0xa\n"
"v_mov_b32_dpp v24, v52 quad_perm:[1,1,1,1] row_mask:0xf bank_mask:0x5\n"
"v_mov_b32_dpp v25, v12 quad_perm:[2,2,2,2] row_mask:0xf bank_mask:0xa\n"
"v_smfmac_f32_16x16x64_bf16 v[40:43], v[28:31], a[224:231], v14\n"
"s_add_u32 s52, s53, 0x15e000\n"
"s_and_b32 s52, s52, s54\n"
"buffer_load_dwordx4 a[220:223], v2, s[32:35], s52 offen offset:0 nt\n"
"v_mov_b32_dpp v26, v15 quad_perm:[1,1,1,1] row_mask:0xf bank_mask:0x5\n"
"v_mov_b32_dpp v27, v13 quad_perm:[2,2,2,2] row_mask:0xf bank_mask:0xa\n"
"v_smfmac_f32_16x16x64_bf16 v[20:23], v[28:31], a[232:239], v14\n"
"v_mov_b32_dpp v28, v52 quad_perm:[2,2,2,2] row_mask:0xf bank_mask:0x5\n"
"v_mov_b32_dpp v29, v12 quad_perm:[1,1,1,1] row_mask:0xf bank_mask:0xa\n"
"v_smfmac_f32_16x16x64_bf16 v[40:43], v[24:27], a[240:247], v14\n"
"v_mov_b32_dpp v30, v15 quad_perm:[2,2,2,2] row_mask:0xf bank_mask:0x5\n"
"v_mov_b32_dpp v31, v13 quad_perm:[1,1,1,1] row_mask:0xf bank_mask:0xa\n"
"v_smfmac_f32_16x16x64_bf16 v[20:23], v[24:27], a[248:255], v14\n"
"s_add_u32 s52, s53, 0xe7000\n"
"s_and_b32 s52, s52, s54\n"
"buffer_load_dwordx4 a[224:227], v2, s[32:35], s52 offen offset:0 nt\n"
"v_and_b32 v61, s8, v114\n"
"v_alignbit_b32 v62, v114, v114, 4\n"
"v_smfmac_f32_16x16x64_bf16 v[40:43], v[28:31], v[72:79], v14\n"
"v_perm_b32 v32, v61, v112, s11\n"
"v_and_b32 v62, s8, v62\n"
"v_smfmac_f32_16x16x64_bf16 v[20:23], v[28:31], a[88:95], v14\n"
"v_perm_b32 v33, v61, v112, s12\n"
"v_perm_b32 v34, v62, v113, s11\n"
"v_perm_b32 v35, v62, v113, s12\n"
"v_mov_b32_dpp v24, v48 quad_perm:[2,2,2,2] row_mask:0xf bank_mask:0x5\n"
"v_mov_b32_dpp v25, v8 quad_perm:[1,1,1,1] row_mask:0xf bank_mask:0xa\n"
"v_mov_b32_dpp v26, v49 quad_perm:[2,2,2,2] row_mask:0xf bank_mask:0x5\n"
"v_mov_b32_dpp v27, v9 quad_perm:[1,1,1,1] row_mask:0xf bank_mask:0xa\n"
"v_and_b32 v61, s8, v126\n"
"v_alignbit_b32 v62, v126, v126, 4\n"
"v_perm_b32 v36, v61, v124, s11\n"
"v_and_b32 v62, s8, v62\n"
"v_perm_b32 v37, v61, v124, s12\n"
"v_perm_b32 v38, v62, v125, s11\n"
"v_perm_b32 v39, v62, v125, s12\n"
"v_and_b32 v61, s8, v120\n"
"v_alignbit_b32 v62, v120, v120, 4\n"
"v_smfmac_f32_16x16x64_bf16 v[40:43], v[24:27], v[32:39], v14\n"
"s_cbranch_execz LDRYR\n"
"LDRY3:\n"
"v_perm_b32 v32, v61, v118, s11\n"
"v_and_b32 v62, s8, v62\n"
"v_perm_b32 v33, v61, v118, s12\n"
"v_perm_b32 v34, v62, v119, s11\n"
"v_perm_b32 v35, v62, v119, s12\n"
"v_and_b32 v61, s8, v132\n"
"v_alignbit_b32 v62, v132, v132, 4\n"
"v_perm_b32 v36, v61, v130, s11\n"
"v_and_b32 v62, s8, v62\n"
"v_perm_b32 v37, v61, v130, s12\n"
"v_perm_b32 v38, v62, v131, s11\n"
"v_perm_b32 v39, v62, v131, s12\n"
"v_and_b32 v61, s8, v117\n"
"v_alignbit_b32 v62, v117, v117, 4\n"
"v_smfmac_f32_16x16x64_bf16 v[20:23], v[24:27], v[32:39], v14\n"
"v_perm_b32 v32, v61, v115, s11\n"
"v_and_b32 v62, s8, v62\n"
"v_perm_b32 v33, v61, v115, s12\n"
"v_perm_b32 v34, v62, v116, s11\n"
"v_perm_b32 v35, v62, v116, s12\n"
"v_mov_b32_dpp v28, v8 quad_perm:[1,1,1,1] row_mask:0xf bank_mask:0x5\n"
"v_mov_b32_dpp v29, v48 quad_perm:[2,2,2,2] row_mask:0xf bank_mask:0xa\n"
"v_mov_b32_dpp v30, v9 quad_perm:[1,1,1,1] row_mask:0xf bank_mask:0x5\n"
"v_mov_b32_dpp v31, v49 quad_perm:[2,2,2,2] row_mask:0xf bank_mask:0xa\n"
"v_and_b32 v61, s8, v129\n"
"v_alignbit_b32 v62, v129, v129, 4\n"
"v_perm_b32 v36, v61, v127, s11\n"
"v_and_b32 v62, s8, v62\n"
"v_perm_b32 v37, v61, v127, s12\n"
"v_perm_b32 v38, v62, v128, s11\n"
"v_perm_b32 v39, v62, v128, s12\n"
"v_and_b32 v61, s8, v123\n"
"v_alignbit_b32 v62, v123, v123, 4\n"
"v_smfmac_f32_16x16x64_bf16 v[40:43], v[28:31], v[32:39], v14\n"
"s_add_u32 s52, s53, 0x157000\n"
"s_and_b32 s52, s52, s54\n"
"buffer_load_dwordx4 a[228:231], v2, s[32:35], s52 offen offset:0 nt\n"
"v_perm_b32 v32, v61, v121, s11\n"
"v_and_b32 v62, s8, v62\n"
"v_perm_b32 v33, v61, v121, s12\n"
"v_perm_b32 v34, v62, v122, s11\n"
"v_perm_b32 v35, v62, v122, s12\n"
"v_and_b32 v61, s8, v135\n"
"v_alignbit_b32 v62, v135, v135, 4\n"
"v_perm_b32 v36, v61, v133, s11\n"
"v_and_b32 v62, s8, v62\n"
"v_perm_b32 v37, v61, v133, s12\n"
"v_perm_b32 v38, v62, v134, s11\n"
"v_perm_b32 v39, v62, v134, s12\n"
"v_and_b32 v61, s8, v138\n"
"v_alignbit_b32 v62, v138, v138, 4\n"
"v_smfmac_f32_16x16x64_bf16 v[20:23], v[28:31], v[32:39], v14\n"
"v_perm_b32 v32, v61, v136, s11\n"
"v_and_b32 v62, s8, v62\n"
"v_perm_b32 v33, v61, v136, s12\n"
"v_perm_b32 v34, v62, v137, s11\n"
"v_perm_b32 v35, v62, v137, s12\n"
"v_mov_b32_dpp v24, v48 quad_perm:[3,3,3,3] row_mask:0xf bank_mask:0x5\n"
"v_mov_b32_dpp v25, v8 quad_perm:[0,0,0,0] row_mask:0xf bank_mask:0xa\n"
"v_mov_b32_dpp v26, v49 quad_perm:[3,3,3,3] row_mask:0xf bank_mask:0x5\n"
"v_mov_b32_dpp v27, v9 quad_perm:[0,0,0,0] row_mask:0xf bank_mask:0xa\n"
"v_and_b32 v61, s8, v150\n"
"v_alignbit_b32 v62, v150, v150, 4\n"
"v_perm_b32 v36, v61, v148, s11\n"
"v_and_b32 v62, s8, v62\n"
"v_perm_b32 v37, v61, v148, s12\n"
"v_perm_b32 v38, v62, v149, s11\n"
"v_perm_b32 v39, v62, v149, s12\n"
"v_and_b32 v61, s8, v144\n"
"v_alignbit_b32 v62, v144, v144, 4\n"
"v_smfmac_f32_16x16x64_bf16 v[40:43], v[24:27], v[32:39], v14\n"
"v_perm_b32 v32, v61, v142, s11\n"
"v_and_b32 v62, s8, v62\n"
"v_perm_b32 v33, v61, v142, s12\n"
"v_perm_b32 v34, v62, v143, s11\n"
"v_perm_b32 v35, v62, v143, s12\n"
"v_and_b32 v61, s8, v156\n"
"v_alignbit_b32 v62, v156, v156, 4\n"
"v_perm_b32 v36, v61, v154, s11\n"
"v_and_b32 v62, s8, v62\n"
"v_perm_b32 v37, v61, v154, s12\n"
"v_perm_b32 v38, v62, v155, s11\n"
"v_perm_b32 v39, v62, v155, s12\n"
"v_and_b32 v61, s8, v141\n"
"v_alignbit_b32 v62, v141, v141, 4\n"
"v_smfmac_f32_16x16x64_bf16 v[20:23], v[24:27], v[32:39], v14\n"
"s_add_u32 s52, s53, 0xf5000\n"
"s_and_b32 s52, s52, s54\n"
"buffer_load_dwordx4 a[232:235], v2, s[32:35], s52 offen offset:0 nt\n"
"v_perm_b32 v32, v61, v139, s11\n"
"v_and_b32 v62, s8, v62\n"
"v_perm_b32 v33, v61, v139, s12\n"
"v_perm_b32 v34, v62, v140, s11\n"
"v_perm_b32 v35, v62, v140, s12\n"
"v_mov_b32_dpp v28, v8 quad_perm:[0,0,0,0] row_mask:0xf bank_mask:0x5\n"
"v_mov_b32_dpp v29, v48 quad_perm:[3,3,3,3] row_mask:0xf bank_mask:0xa\n"
"v_mov_b32_dpp v30, v9 quad_perm:[0,0,0,0] row_mask:0xf bank_mask:0x5\n"
"v_mov_b32_dpp v31, v49 quad_perm:[3,3,3,3] row_mask:0xf bank_mask:0xa\n"
"v_and_b32 v61, s8, v153\n"
"v_alignbit_b32 v62, v153, v153, 4\n"
"v_perm_b32 v36, v61, v151, s11\n"
"v_and_b32 v62, s8, v62\n"
"v_perm_b32 v37, v61, v151, s12\n"
"v_perm_b32 v38, v62, v152, s11\n"
"v_perm_b32 v39, v62, v152, s12\n"
"v_and_b32 v61, s8, v147\n"
"v_alignbit_b32 v62, v147, v147, 4\n"
"v_smfmac_f32_16x16x64_bf16 v[40:43], v[28:31], v[32:39], v14\n"
"v_perm_b32 v32, v61, v145, s11\n"
"v_and_b32 v62, s8, v62\n"
"v_perm_b32 v33, v61, v145, s12\n"
"v_perm_b32 v34, v62, v146, s11\n"
"v_perm_b32 v35, v62, v146, s12\n"
"v_and_b32 v61, s8, v159\n"
"v_alignbit_b32 v62, v159, v159, 4\n"
"v_perm_b32 v36, v61, v157, s11\n"
"v_and_b32 v62, s8, v62\n"
"v_perm_b32 v37, v61, v157, s12\n"
"v_perm_b32 v38, v62, v158, s11\n"
"v_perm_b32 v39, v62, v158, s12\n"
"v_and_b32 v61, s8, v162\n"
"v_alignbit_b32 v62, v162, v162, 4\n"
"v_smfmac_f32_16x16x64_bf16 v[20:23], v[28:31], v[32:39], v14\n"
"v_perm_b32 v32, v61, v160, s11\n"
"v_and_b32 v62, s8, v62\n"
"v_perm_b32 v33, v61, v160, s12\n"
"v_perm_b32 v34, v62, v161, s11\n"
"v_perm_b32 v35, v62, v161, s12\n"
"v_mov_b32_dpp v24, v50 quad_perm:[0,0,0,0] row_mask:0xf bank_mask:0x5\n"
"v_mov_b32_dpp v25, v10 quad_perm:[3,3,3,3] row_mask:0xf bank_mask:0xa\n"
"v_mov_b32_dpp v26, v51 quad_perm:[0,0,0,0] row_mask:0xf bank_mask:0x5\n"
"v_mov_b32_dpp v27, v11 quad_perm:[3,3,3,3] row_mask:0xf bank_mask:0xa\n"
"v_and_b32 v61, s8, v174\n"
"v_alignbit_b32 v62, v174, v174, 4\n"
"v_perm_b32 v36, v61, v172, s11\n"
"v_and_b32 v62, s8, v62\n"
"v_perm_b32 v37, v61, v172, s12\n"
"v_perm_b32 v38, v62, v173, s11\n"
"v_perm_b32 v39, v62, v173, s12\n"
"v_and_b32 v61, s8, v168\n"
"v_alignbit_b32 v62, v168, v168, 4\n"
"v_smfmac_f32_16x16x64_bf16 v[40:43], v[24:27], v[32:39], v14\n"
"s_add_u32 s52, s53, 0x165000\n"
"s_and_b32 s52, s52, s54\n"
"buffer_load_dwordx4 a[236:239], v2, s[32:35], s52 offen offset:0 nt\n"
"v_perm_b32 v32, v61, v166, s11\n"
"v_and_b32 v62, s8, v62\n"
"v_perm_b32 v33, v61, v166, s12\n"
"v_perm_b32 v34, v62, v167, s11\n"
"v_perm_b32 v35, v62, v167, s12\n"
"v_and_b32 v61, s8, v180\n"
"v_alignbit_b32 v62, v180, v180, 4\n"
"v_perm_b32 v36, v61, v178, s11\n"
"v_and_b32 v62, s8, v62\n"
"v_perm_b32 v37, v61, v178, s12\n"
"v_perm_b32 v38, v62, v179, s11\n"
"v_perm_b32 v39, v62, v179, s12\n"
"v_and_b32 v61, s8, v165\n"
"v_alignbit_b32 v62, v165, v165, 4\n"
"v_smfmac_f32_16x16x64_bf16 v[20:23], v[24:27], v[32:39], v14\n"
"v_perm_b32 v32, v61, v163, s11\n"
"v_and_b32 v62, s8, v62\n"
"v_perm_b32 v33, v61, v163, s12\n"
"v_perm_b32 v34, v62, v164, s11\n"
"v_perm_b32 v35, v62, v164, s12\n"
"v_mov_b32_dpp v28, v10 quad_perm:[3,3,3,3] row_mask:0xf bank_mask:0x5\n"
"v_mov_b32_dpp v29, v50 quad_perm:[0,0,0,0] row_mask:0xf bank_mask:0xa\n"
"v_mov_b32_dpp v30, v11 quad_perm:[3,3,3,3] row_mask:0xf bank_mask:0x5\n"
"v_mov_b32_dpp v31, v51 quad_perm:[0,0,0,0] row_mask:0xf bank_mask:0xa\n"
"v_and_b32 v61, s8, v177\n"
"v_alignbit_b32 v62, v177, v177, 4\n"
"v_perm_b32 v36, v61, v175, s11\n"
"v_and_b32 v62, s8, v62\n"
"v_perm_b32 v37, v61, v175, s12\n"
"v_perm_b32 v38, v62, v176, s11\n"
"v_perm_b32 v39, v62, v176, s12\n"
"v_and_b32 v61, s8, v171\n"
"v_alignbit_b32 v62, v171, v171, 4\n"
"v_smfmac_f32_16x16x64_bf16 v[40:43], v[28:31], v[32:39], v14\n"
"v_perm_b32 v32, v61, v169, s11\n"
"v_and_b32 v62, s8, v62\n"
"v_perm_b32 v33, v61, v169, s12\n"
"v_perm_b32 v34, v62, v170, s11\n"
"v_perm_b32 v35, v62, v170, s12\n"
"v_and_b32 v61, s8, v183\n"
"v_alignbit_b32 v62, v183, v183, 4\n"
"v_perm_b32 v36, v61, v181, s11\n"
"v_and_b32 v62, s8, v62\n"
"v_perm_b32 v37, v61, v181, s12\n"
"v_perm_b32 v38, v62, v182, s11\n"
"v_perm_b32 v39, v62, v182, s12\n"
"v_and_b32 v61, s8, v186\n"
"v_alignbit_b32 v62, v186, v186, 4\n"
"v_smfmac_f32_16x16x64_bf16 v[20:23], v[28:31], v[32:39], v14\n"
"v_perm_b32 v32, v61, v184, s11\n"
"v_and_b32 v62, s8, v62\n"
"v_perm_b32 v33, v61, v184, s12\n"
"v_perm_b32 v34, v62, v185, s11\n"
"v_perm_b32 v35, v62, v185, s12\n"
"v_mov_b32_dpp v24, v50 quad_perm:[1,1,1,1] row_mask:0xf bank_mask:0x5\n"
"v_mov_b32_dpp v25, v10 quad_perm:[2,2,2,2] row_mask:0xf bank_mask:0xa\n"
"v_mov_b32_dpp v26, v51 quad_perm:[1,1,1,1] row_mask:0xf bank_mask:0x5\n"
"s_waitcnt vmcnt(52)\n"
"s_add_u32 s52, s53, 0xfc000\n"
"s_and_b32 s52, s52, s54\n"
"buffer_load_dwordx4 a[240:243], v2, s[32:35], s52 offen offset:0 nt\n"
"v_mov_b32_dpp v27, v11 quad_perm:[2,2,2,2] row_mask:0xf bank_mask:0xa\n"
"v_and_b32 v61, s8, v198\n"
"v_alignbit_b32 v62, v198, v198, 4\n"
"v_perm_b32 v36, v61, v196, s11\n"
"v_and_b32 v62, s8, v62\n"
"v_perm_b32 v37, v61, v196, s12\n"
"v_perm_b32 v38, v62, v197, s11\n"
"v_perm_b32 v39, v62, v197, s12\n"
"v_and_b32 v61, s8, v192\n"
"v_alignbit_b32 v62, v192, v192, 4\n"
"v_smfmac_f32_16x16x64_bf16 v[40:43], v[24:27], v[32:39], v14\n"
"v_perm_b32 v32, v61, v190, s11\n"
"v_and_b32 v62, s8, v62\n"
"v_perm_b32 v33, v61, v190, s12\n"
"v_perm_b32 v34, v62, v191, s11\n"
"v_perm_b32 v35, v62, v191, s12\n"
"v_and_b32 v61, s8, v204\n"
"v_alignbit_b32 v62, v204, v204, 4\n"
"v_perm_b32 v36, v61, v202, s11\n"
"v_and_b32 v62, s8, v62\n"
"v_perm_b32 v37, v61, v202, s12\n"
"v_perm_b32 v38, v62, v203, s11\n"
"v_perm_b32 v39, v62, v203, s12\n"
"v_and_b32 v61, s8, v189\n"
"v_alignbit_b32 v62, v189, v189, 4\n"
"v_smfmac_f32_16x16x64_bf16 v[20:23], v[24:27], v[32:39], v14\n"
"v_perm_b32 v32, v61, v187, s11\n"
"v_and_b32 v62, s8, v62\n"
"v_perm_b32 v33, v61, v187, s12\n"
"v_perm_b32 v34, v62, v188, s11\n"
"v_perm_b32 v35, v62, v188, s12\n"
"v_mov_b32_dpp v28, v10 quad_perm:[2,2,2,2] row_mask:0xf bank_mask:0x5\n"
"v_mov_b32_dpp v29, v50 quad_perm:[1,1,1,1] row_mask:0xf bank_mask:0xa\n"
"v_mov_b32_dpp v30, v11 quad_perm:[2,2,2,2] row_mask:0xf bank_mask:0x5\n"
"v_mov_b32_dpp v31, v51 quad_perm:[1,1,1,1] row_mask:0xf bank_mask:0xa\n"
"v_and_b32 v61, s8, v201\n"
"v_alignbit_b32 v62, v201, v201, 4\n"
"v_perm_b32 v36, v61, v199, s11\n"
"v_and_b32 v62, s8, v62\n"
"v_perm_b32 v37, v61, v199, s12\n"
"v_perm_b32 v38, v62, v200, s11\n"
"v_perm_b32 v39, v62, v200, s12\n"
"v_and_b32 v61, s8, v195\n"
"v_alignbit_b32 v62, v195, v195, 4\n"
"v_smfmac_f32_16x16x64_bf16 v[40:43], v[28:31], v[32:39], v14\n"
"v_perm_b32 v32, v61, v193, s11\n"
"v_and_b32 v62, s8, v62\n"
"v_perm_b32 v33, v61, v193, s12\n"
"v_perm_b32 v34, v62, v194, s11\n"
"s_add_u32 s52, s53, 0x16c000\n"
"s_and_b32 s52, s52, s54\n"
"buffer_load_dwordx4 a[244:247], v2, s[32:35], s52 offen offset:0 nt\n"
"v_perm_b32 v35, v62, v194, s12\n"
"v_and_b32 v61, s8, v207\n"
"v_alignbit_b32 v62, v207, v207, 4\n"
"v_perm_b32 v36, v61, v205, s11\n"
"v_and_b32 v62, s8, v62\n"
"v_perm_b32 v37, v61, v205, s12\n"
"v_perm_b32 v38, v62, v206, s11\n"
"v_perm_b32 v39, v62, v206, s12\n"
"v_and_b32 v61, s8, v210\n"
"v_alignbit_b32 v62, v210, v210, 4\n"
"v_smfmac_f32_16x16x64_bf16 v[20:23], v[28:31], v[32:39], v14\n"
"v_perm_b32 v32, v61, v208, s11\n"
"v_and_b32 v62, s8, v62\n"
"v_perm_b32 v33, v61, v208, s12\n"
"v_perm_b32 v34, v62, v209, s11\n"
"v_perm_b32 v35, v62, v209, s12\n"
"v_mov_b32_dpp v24, v50 quad_perm:[2,2,2,2] row_mask:0xf bank_mask:0x5\n"
"v_mov_b32_dpp v25, v10 quad_perm:[1,1,1,1] row_mask:0xf bank_mask:0xa\n"
"v_mov_b32_dpp v26, v51 quad_perm:[2,2,2,2] row_mask:0xf bank_mask:0x5\n"
"v_mov_b32_dpp v27, v11 quad_perm:[1,1,1,1] row_mask:0xf bank_mask:0xa\n"
"v_and_b32 v61, s8, v222\n"
"v_alignbit_b32 v62, v222, v222, 4\n"
"v_perm_b32 v36, v61, v220, s11\n"
"v_and_b32 v62, s8, v62\n"
"v_perm_b32 v37, v61, v220, s12\n"
"v_perm_b32 v38, v62, v221, s11\n"
"v_perm_b32 v39, v62, v221, s12\n"
"v_and_b32 v61, s8, v216\n"
"v_alignbit_b32 v62, v216, v216, 4\n"
"v_smfmac_f32_16x16x64_bf16 v[40:43], v[24:27], v[32:39], v14\n"
"v_perm_b32 v32, v61, v214, s11\n"
"v_and_b32 v62, s8, v62\n"
"v_perm_b32 v33, v61, v214, s12\n"
"v_perm_b32 v34, v62, v215, s11\n"
"v_perm_b32 v35, v62, v215, s12\n"
"v_and_b32 v61, s8, v228\n"
"v_alignbit_b32 v62, v228, v228, 4\n"
"v_perm_b32 v36, v61, v226, s11\n"
"v_and_b32 v62, s8, v62\n"
"v_perm_b32 v37, v61, v226, s12\n"
"v_perm_b32 v38, v62, v227, s11\n"
"v_perm_b32 v39, v62, v227, s12\n"
"v_and_b32 v61, s8, v213\n"
"v_alignbit_b32 v62, v213, v213, 4\n"
"v_smfmac_f32_16x16x64_bf16 v[20:23], v[24:27], v[32:39], v14\n"
"v_perm_b32 v32, v61, v211, s11\n"
"v_and_b32 v62, s8, v62\n"
"v_perm_b32 v33, v61, v211, s12\n"
"v_perm_b32 v34, v62, v212, s11\n"
"v_perm_b32 v35, v62, v212, s12\n"
"v_mov_b32_dpp v28, v10 quad_perm:[1,1,1,1] row_mask:0xf bank_mask:0x5\n"
"v_mov_b32_dpp v29, v50 quad_perm:[2,2,2,2] row_mask:0xf bank_mask:0xa\n"
"v_mov_b32_dpp v30, v11 quad_perm:[1,1,1,1] row_mask:0xf bank_mask:0x5\n"
"s_add_u32 s52, s53, 0x10a000\n"
"s_and_b32 s52, s52, s54\n"
"buffer_load_dwordx4 a[248:251], v2, s[32:35], s52 offen offset:0 nt\n"
"v_mov_b32_dpp v31, v51 quad_perm:[2,2,2,2] row_mask:0xf bank_mask:0xa\n"
"v_and_b32 v61, s8, v225\n"
"v_alignbit_b32 v62, v225, v225, 4\n"
"v_perm_b32 v36, v61, v223, s11\n"
"v_and_b32 v62, s8, v62\n"
"v_perm_b32 v37, v61, v223, s12\n"
"v_perm_b32 v38, v62, v224, s11\n"
"v_perm_b32 v39, v62, v224, s12\n"
"v_and_b32 v61, s8, v219\n"
"v_alignbit_b32 v62, v219, v219, 4\n"
"v_smfmac_f32_16x16x64_bf16 v[40:43], v[28:31], v[32:39], v14\n"
"v_perm_b32 v32, v61, v217, s11\n"
"v_and_b32 v62, s8, v62\n"
"v_perm_b32 v33, v61, v217, s12\n"
"v_perm_b32 v34, v62, v218, s11\n"
"v_perm_b32 v35, v62, v218, s12\n"
"v_and_b32 v61, s8, v231\n"
"v_alignbit_b32 v62, v231, v231, 4\n"
"v_perm_b32 v36, v61, v229, s11\n"
"v_and_b32 v62, s8, v62\n"
"v_perm_b32 v37, v61, v229, s12\n"
"v_perm_b32 v38, v62, v230, s11\n"
"v_perm_b32 v39, v62, v230, s12\n"
"v_and_b32 v61, s8, v234\n"
"v_alignbit_b32 v62, v234, v234, 4\n"
"v_smfmac_f32_16x16x64_bf16 v[20:23], v[28:31], v[32:39], v14\n"
"v_perm_b32 v32, v61, v232, s11\n"
"v_and_b32 v62, s8, v62\n"
"v_perm_b32 v33, v61, v232, s12\n"
"v_perm_b32 v34, v62, v233, s11\n"
"v_perm_b32 v35, v62, v233, s12\n"
"v_mov_b32_dpp v24, v50 quad_perm:[3,3,3,3] row_mask:0xf bank_mask:0x5\n"
"v_mov_b32_dpp v25, v10 quad_perm:[0,0,0,0] row_mask:0xf bank_mask:0xa\n"
"v_mov_b32_dpp v26, v51 quad_perm:[3,3,3,3] row_mask:0xf bank_mask:0x5\n"
"v_mov_b32_dpp v27, v11 quad_perm:[0,0,0,0] row_mask:0xf bank_mask:0xa\n"
"v_and_b32 v61, s8, v246\n"
"v_alignbit_b32 v62, v246, v246, 4\n"
"v_perm_b32 v36, v61, v244, s11\n"
"v_and_b32 v62, s8, v62\n"
"v_perm_b32 v37, v61, v244, s12\n"
"v_perm_b32 v38, v62, v245, s11\n"
"v_perm_b32 v39, v62, v245, s12\n"
"v_and_b32 v61, s8, v240\n"
"v_alignbit_b32 v62, v240, v240, 4\n"
"v_smfmac_f32_16x16x64_bf16 v[40:43], v[24:27], v[32:39], v14\n"
"v_perm_b32 v32, v61, v238, s11\n"
"v_and_b32 v62, s8, v62\n"
"v_perm_b32 v33, v61, v238, s12\n"
"v_perm_b32 v34, v62, v239, s11\n"
"v_perm_b32 v35, v62, v239, s12\n"
"v_and_b32 v61, s8, v252\n"
"v_alignbit_b32 v62, v252, v252, 4\n"
"v_perm_b32 v36, v61, v250, s11\n"
"v_and_b32 v62, s8, v62\n"
"v_perm_b32 v37, v61, v250, s12\n"
"v_perm_b32 v38, v62, v251, s11\n"
"v_perm_b32 v39, v62, v251, s12\n"
"v_and_b32 v61, s8, v237\n"
"v_alignbit_b32 v62, v237, v237, 4\n"
"v_smfmac_f32_16x16x64_bf16 v[20:23], v[24:27], v[32:39], v14\n"
"s_add_u32 s52, s53, 0x17a000\n"
"s_and_b32 s52, s52, s54\n"
"buffer_load_dwordx4 a[252:255], v2, s[32:35], s52 offen offset:0 nt\n"
"v_perm_b32 v32, v61, v235, s11\n"
"v_and_b32 v62, s8, v62\n"
"v_perm_b32 v33, v61, v235, s12\n"
"v_perm_b32 v34, v62, v236, s11\n"
"v_perm_b32 v35, v62, v236, s12\n"
"v_mov_b32_dpp v28, v10 quad_perm:[0,0,0,0] row_mask:0xf bank_mask:0x5\n"
"v_mov_b32_dpp v29, v50 quad_perm:[3,3,3,3] row_mask:0xf bank_mask:0xa\n"
"v_mov_b32_dpp v30, v11 quad_perm:[0,0,0,0] row_mask:0xf bank_mask:0x5\n"
"v_mov_b32_dpp v31, v51 quad_perm:[3,3,3,3] row_mask:0xf bank_mask:0xa\n"
"v_and_b32 v61, s8, v249\n"
"v_alignbit_b32 v62, v249, v249, 4\n"
"v_perm_b32 v36, v61, v247, s11\n"
"v_and_b32 v62, s8, v62\n"
"v_perm_b32 v37, v61, v247, s12\n"
"v_perm_b32 v38, v62, v248, s11\n"
"v_perm_b32 v39, v62, v248, s12\n"
"v_and_b32 v61, s8, v243\n"
"v_alignbit_b32 v62, v243, v243, 4\n"
"v_smfmac_f32_16x16x64_bf16 v[40:43], v[28:31], v[32:39], v14\n"
"v_perm_b32 v32, v61, v241, s11\n"
"v_and_b32 v62, s8, v62\n"
"v_perm_b32 v33, v61, v241, s12\n"
"v_perm_b32 v34, v62, v242, s11\n"
"v_perm_b32 v35, v62, v242, s12\n"
"v_and_b32 v61, s8, v255\n"
"v_alignbit_b32 v62, v255, v255, 4\n"
"v_perm_b32 v36, v61, v253, s11\n"
"v_and_b32 v62, s8, v62\n"
"v_perm_b32 v37, v61, v253, s12\n"
"v_perm_b32 v38, v62, v254, s11\n"
"v_perm_b32 v39, v62, v254, s12\n"
"v_mov_b32_dpp v24, v44 quad_perm:[0,0,0,0] row_mask:0xf bank_mask:0x5\n"
"v_mov_b32_dpp v25, v4 quad_perm:[3,3,3,3] row_mask:0xf bank_mask:0xa\n"
"v_smfmac_f32_16x16x64_bf16 v[20:23], v[28:31], v[32:39], v14\n"
"v_mov_b32_dpp v26, v45 quad_perm:[0,0,0,0] row_mask:0xf bank_mask:0x5\n"
"v_mov_b32_dpp v27, v5 quad_perm:[3,3,3,3] row_mask:0xf bank_mask:0xa\n"
"v_mov_b32_dpp v28, v8 quad_perm:[2,2,2,2] row_mask:0xf bank_mask:0x5\n"
"v_mov_b32_dpp v29, v48 quad_perm:[1,1,1,1] row_mask:0xf bank_mask:0xa\n"
"v_smfmac_f32_16x16x64_bf16 v[40:43], v[24:27], v[64:71], v14\n"
"v_mov_b32_dpp v30, v9 quad_perm:[2,2,2,2] row_mask:0xf bank_mask:0x5\n"
"v_mov_b32_dpp v31, v49 quad_perm:[1,1,1,1] row_mask:0xf bank_mask:0xa\n"
"v_smfmac_f32_16x16x64_bf16 v[20:23], v[24:27], v[80:87], v14\n"
"s_add_u32 s52, s53, 0x103000\n"
"s_and_b32 s52, s52, s54\n"
"buffer_load_dwordx4 v[72:75], v2, s[32:35], s52 offen offset:0 nt\n"
"v_mov_b32_dpp v24, v48 quad_perm:[1,1,1,1] row_mask:0xf bank_mask:0x5\n"
"v_mov_b32_dpp v25, v8 quad_perm:[2,2,2,2] row_mask:0xf bank_mask:0xa\n"
"v_smfmac_f32_16x16x64_bf16 v[40:43], v[28:31], a[0:7], v14\n"
"v_mov_b32_dpp v26, v49 quad_perm:[1,1,1,1] row_mask:0xf bank_mask:0x5\n"
"v_mov_b32_dpp v27, v9 quad_perm:[2,2,2,2] row_mask:0xf bank_mask:0xa\n"
"v_mov_b32_dpp v28, v4 quad_perm:[3,3,3,3] row_mask:0xf bank_mask:0x5\n"
"v_mov_b32_dpp v29, v44 quad_perm:[0,0,0,0] row_mask:0xf bank_mask:0xa\n"
"v_smfmac_f32_16x16x64_bf16 v[40:43], v[24:27], a[16:23], v14\n"
"v_mov_b32_dpp v30, v5 quad_perm:[3,3,3,3] row_mask:0xf bank_mask:0x5\n"
"v_mov_b32_dpp v31, v45 quad_perm:[0,0,0,0] row_mask:0xf bank_mask:0xa\n"
"v_smfmac_f32_16x16x64_bf16 v[20:23], v[24:27], a[24:31], v14\n"
"v_mov_b32_dpp v24, v8 quad_perm:[3,3,3,3] row_mask:0xf bank_mask:0x5\n"
"v_mov_b32_dpp v25, v48 quad_perm:[0,0,0,0] row_mask:0xf bank_mask:0xa\n"
"v_smfmac_f32_16x16x64_bf16 v[40:43], v[28:31], a[8:15], v14\n"
"v_mov_b32_dpp v26, v9 quad_perm:[3,3,3,3] row_mask:0xf bank_mask:0x5\n"
"v_mov_b32_dpp v27, v49 quad_perm:[0,0,0,0] row_mask:0xf bank_mask:0xa\n"
"v_smfmac_f32_16x16x64_bf16 v[20:23], v[28:31], a[32:39], v14\n"
"v_mov_b32_dpp v28, v48 quad_perm:[0,0,0,0] row_mask:0xf bank_mask:0x5\n"
"v_mov_b32_dpp v29, v8 quad_perm:[3,3,3,3] row_mask:0xf bank_mask:0xa\n"
"v_smfmac_f32_16x16x64_bf16 v[40:43], v[24:27], a[40:47], v14\n"
"v_mov_b32_dpp v30, v49 quad_perm:[0,0,0,0] row_mask:0xf bank_mask:0x5\n"
"v_mov_b32_dpp v31, v9 quad_perm:[3,3,3,3] row_mask:0xf bank_mask:0xa\n"
"s_waitcnt vmcnt(55)\n"
"v_smfmac_f32_16x16x64_bf16 v[20:23], v[24:27], a[48:55], v14\n"
"v_mov_b32_dpp v24, v44 quad_perm:[1,1,1,1] row_mask:0xf bank_mask:0x5\n"
"v_mov_b32_dpp v25, v4 quad_perm:[2,2,2,2] row_mask:0xf bank_mask:0xa\n"
"s_waitcnt vmcnt(52)\n"
"v_smfmac_f32_16x16x64_bf16 v[40:43], v[28:31], a[64:71], v14\n"
"v_mov_b32_dpp v26, v45 quad_perm:[1,1,1,1] row_mask:0xf bank_mask:0x5\n"
"v_mov_b32_dpp v27, v5 quad_perm:[2,2,2,2] row_mask:0xf bank_mask:0xa\n"
"s_waitcnt vmcnt(49)\n"
"v_smfmac_f32_16x16x64_bf16 v[20:23], v[28:31], a[72:79], v14\n"
"v_mov_b32_dpp v28, v6 quad_perm:[0,0,0,0] row_mask:0xf bank_mask:0x5\n"
"v_mov_b32_dpp v29, v46 quad_perm:[3,3,3,3] row_mask:0xf bank_mask:0xa\n"
"v_smfmac_f32_16x16x64_bf16 v[40:43], v[24:27], a[56:63], v14\n"
"v_mov_b32_dpp v30, v7 quad_perm:[0,0,0,0] row_mask:0xf bank_mask:0x5\n"
"v_mov_b32_dpp v31, v47 quad_perm:[3,3,3,3] row_mask:0xf bank_mask:0xa\n"
"s_waitcnt vmcnt(45)\n"
"v_smfmac_f32_16x16x64_bf16 v[20:23], v[24:27], a[80:87], v14\n"
"s_add_u32 s52, s53, 0x173000\n"
"s_and_b32 s52, s52, s54\n"
"buffer_load_dwordx4 v[76:79], v2, s[32:35], s52 offen offset:0 nt\n"
"v_mov_b32_dpp v24, v46 quad_perm:[3,3,3,3] row_mask:0xf bank_mask:0x5\n"
"v_mov_b32_dpp v25, v6 quad_perm:[0,0,0,0] row_mask:0xf bank_mask:0xa\n"
"v_smfmac_f32_16x16x64_bf16 v[40:43], v[28:31], a[96:103], v14\n"
"v_mov_b32_dpp v26, v47 quad_perm:[3,3,3,3] row_mask:0xf bank_mask:0x5\n"
"v_mov_b32_dpp v27, v7 quad_perm:[0,0,0,0] row_mask:0xf bank_mask:0xa\n"
"s_waitcnt vmcnt(44)\n"
"v_smfmac_f32_16x16x64_bf16 v[20:23], v[28:31], a[104:111], v14\n"
"v_mov_b32_dpp v28, v4 quad_perm:[2,2,2,2] row_mask:0xf bank_mask:0x5\n"
"v_mov_b32_dpp v29, v44 quad_perm:[1,1,1,1] row_mask:0xf bank_mask:0xa\n"
"s_waitcnt vmcnt(41)\n"
"v_smfmac_f32_16x16x64_bf16 v[40:43], v[24:27], a[120:127], v14\n"
"v_mov_b32_dpp v30, v5 quad_perm:[2,2,2,2] row_mask:0xf bank_mask:0x5\n"
"v_mov_b32_dpp v31, v45 quad_perm:[1,1,1,1] row_mask:0xf bank_mask:0xa\n"
"s_waitcnt vmcnt(38)\n"
"v_smfmac_f32_16x16x64_bf16 v[20:23], v[24:27], a[128:135], v14\n"
"s_add_u32 s52, s53, 0x111000\n"
"s_and_b32 s52, s52, s54\n"
"buffer_load_dwordx4 a[88:91], v2, s[32:35], s52 offen offset:0 nt\n"
"s_add_u32 s52, s53, 0x181000\n"
"s_and_b32 s52, s52, s54\n"
"buffer_load_dwordx4 a[92:95], v2, s[32:35], s52 offen offset:0 nt\n"
"s_add_u32 s52, s53, 0x118000\n"
"s_and_b32 s52, s52, s54\n"
"buffer_load_dwordx4 v[112:115], v2, s[32:35], s52 offen offset:0 nt\n"
"s_add_u32 s52, s53, 0x188000\n"
"s_and_b32 s52, s52, s54\n"
"buffer_load_dwordx4 v[116:119], v2, s[32:35], s52 offen offset:0 nt\n"
"v_mov_b32_dpp v24, v6 quad_perm:[1,1,1,1] row_mask:0xf bank_mask:0x5\n"
"v_mov_b32_dpp v25, v46 quad_perm:[2,2,2,2] row_mask:0xf bank_mask:0xa\n"
"v_smfmac_f32_16x16x64_bf16 v[40:43], v[28:31], a[112:119], v14\n"
"v_mov_b32_dpp v26, v7 quad_perm:[1,1,1,1] row_mask:0xf bank_mask:0x5\n"
"v_mov_b32_dpp v27, v47 quad_perm:[2,2,2,2] row_mask:0xf bank_mask:0xa\n"
"s_waitcnt vmcnt(38)\n"
"v_smfmac_f32_16x16x64_bf16 v[20:23], v[28:31], a[136:143], v14\n"
"v_mov_b32_dpp v28, v46 quad_perm:[2,2,2,2] row_mask:0xf bank_mask:0x5\n"
"v_mov_b32_dpp v29, v6 quad_perm:[1,1,1,1] row_mask:0xf bank_mask:0xa\n"
"v_smfmac_f32_16x16x64_bf16 v[40:43], v[24:27], a[144:151], v14\n"
"v_mov_b32_dpp v30, v47 quad_perm:[2,2,2,2] row_mask:0xf bank_mask:0x5\n"
"v_mov_b32_dpp v31, v7 quad_perm:[1,1,1,1] row_mask:0xf bank_mask:0xa\n"
"s_waitcnt vmcnt(36)\n"
"v_smfmac_f32_16x16x64_bf16 v[20:23], v[24:27], a[152:159], v14\n"
"v_mov_b32_dpp v24, v44 quad_perm:[2,2,2,2] row_mask:0xf bank_mask:0x5\n"
"v_mov_b32_dpp v25, v4 quad_perm:[1,1,1,1] row_mask:0xf bank_mask:0xa\n"
"s_waitcnt vmcnt(33)\n"
"v_smfmac_f32_16x16x64_bf16 v[20:23], v[28:31], a[168:175], v14\n"
"v_mov_b32_dpp v26, v45 quad_perm:[2,2,2,2] row_mask:0xf bank_mask:0x5\n"
"v_mov_b32_dpp v27, v5 quad_perm:[1,1,1,1] row_mask:0xf bank_mask:0xa\n"
"v_mov_b32_dpp v28, v4 quad_perm:[1,1,1,1] row_mask:0xf bank_mask:0x5\n"
"v_mov_b32_dpp v29, v44 quad_perm:[2,2,2,2] row_mask:0xf bank_mask:0xa\n"
"s_waitcnt vmcnt(32)\n"
"v_smfmac_f32_16x16x64_bf16 v[40:43], v[24:27], a[160:167], v14\n"
"v_mov_b32_dpp v30, v5 quad_perm:[1,1,1,1] row_mask:0xf bank_mask:0x5\n"
"v_mov_b32_dpp v31, v45 quad_perm:[2,2,2,2] row_mask:0xf bank_mask:0xa\n"
"s_waitcnt vmcnt(30)\n"
"v_smfmac_f32_16x16x64_bf16 v[20:23], v[24:27], a[176:183], v14\n"
"v_mov_b32_dpp v24, v44 quad_perm:[3,3,3,3] row_mask:0xf bank_mask:0x5\n"
"v_mov_b32_dpp v25, v4 quad_perm:[0,0,0,0] row_mask:0xf bank_mask:0xa\n"
"s_waitcnt vmcnt(28)\n"
"v_smfmac_f32_16x16x64_bf16 v[40:43], v[28:31], a[184:191], v14\n"
"v_mov_b32_dpp v26, v45 quad_perm:[3,3,3,3] row_mask:0xf bank_mask:0x5\n"
"v_mov_b32_dpp v27, v5 quad_perm:[0,0,0,0] row_mask:0xf bank_mask:0xa\n"
"s_waitcnt vmcnt(26)\n"
"v_smfmac_f32_16x16x64_bf16 v[20:23], v[28:31], a[192:199], v14\n"
"v_mov_b32_dpp v28, v4 quad_perm:[0,0,0,0] row_mask:0xf bank_mask:0x5\n"
"v_mov_b32_dpp v29, v44 quad_perm:[3,3,3,3] row_mask:0xf bank_mask:0xa\n"
"s_waitcnt vmcnt(24)\n"
"v_smfmac_f32_16x16x64_bf16 v[40:43], v[24:27], a[200:207], v14\n"
"v_mov_b32_dpp v30, v5 quad_perm:[0,0,0,0] row_mask:0xf bank_mask:0x5\n"
"v_mov_b32_dpp v31, v45 quad_perm:[3,3,3,3] row_mask:0xf bank_mask:0xa\n"
"s_waitcnt vmcnt(22)\n"
"v_smfmac_f32_16x16x64_bf16 v[20:23], v[24:27], a[208:215], v14\n"
"v_mov_b32_dpp v24, v46 quad_perm:[0,0,0,0] row_mask:0xf bank_mask:0x5\n"
"v_mov_b32_dpp v25, v6 quad_perm:[3,3,3,3] row_mask:0xf bank_mask:0xa\n"
"s_waitcnt vmcnt(20)\n"
"v_smfmac_f32_16x16x64_bf16 v[40:43], v[28:31], v[104:111], v14\n"
"v_mov_b32_dpp v26, v47 quad_perm:[0,0,0,0] row_mask:0xf bank_mask:0x5\n"
"v_mov_b32_dpp v27, v7 quad_perm:[3,3,3,3] row_mask:0xf bank_mask:0xa\n"
"s_waitcnt vmcnt(18)\n"
"v_smfmac_f32_16x16x64_bf16 v[20:23], v[28:31], v[96:103], v14\n"
"v_mov_b32_dpp v28, v6 quad_perm:[3,3,3,3] row_mask:0xf bank_mask:0x5\n"
"v_mov_b32_dpp v29, v46 quad_perm:[0,0,0,0] row_mask:0xf bank_mask:0xa\n"
"s_waitcnt vmcnt(16)\n"
"v_smfmac_f32_16x16x64_bf16 v[40:43], v[24:27], v[88:95], v14\n"
"v_mov_b32_dpp v30, v7 quad_perm:[3,3,3,3] row_mask:0xf bank_mask:0x5\n"
"v_mov_b32_dpp v31, v47 quad_perm:[0,0,0,0] row_mask:0xf bank_mask:0xa\n"
"s_waitcnt vmcnt(14)\n"
"v_smfmac_f32_16x16x64_bf16 v[20:23], v[24:27], a[216:223], v14\n"
"v_mov_b32_dpp v24, v46 quad_perm:[1,1,1,1] row_mask:0xf bank_mask:0x5\n"
"v_mov_b32_dpp v25, v6 quad_perm:[2,2,2,2] row_mask:0xf bank_mask:0xa\n"
"s_waitcnt vmcnt(12)\n"
"v_smfmac_f32_16x16x64_bf16 v[40:43], v[28:31], a[224:231], v14\n"
"v_mov_b32_dpp v26, v47 quad_perm:[1,1,1,1] row_mask:0xf bank_mask:0x5\n"
"v_mov_b32_dpp v27, v7 quad_perm:[2,2,2,2] row_mask:0xf bank_mask:0xa\n"
"s_waitcnt vmcnt(10)\n"
"v_smfmac_f32_16x16x64_bf16 v[20:23], v[28:31], a[232:239], v14\n"
"s_waitcnt vmcnt(8)\n"
"v_smfmac_f32_16x16x64_bf16 v[40:43], v[24:27], a[240:247], v14\n"
"s_waitcnt vmcnt(6)\n"
"v_smfmac_f32_16x16x64_bf16 v[20:23], v[24:27], a[248:255], v14\n"
"v_min_u32 v62, 32, v0\n"
"v_lshlrev_b32 v62, 4, v62\n"
"v_mov_b32 v61, 0x7ff00000\n"
"v_cmp_gt_u32 vcc, 33, v0\n"
"s_nop 1\n"
"v_cndmask_b32 v61, v61, v62, vcc\n"
"v_add_u32 v62, 159744, v62\n"
"v_mov_b32 v63, 160256\n"
"s_mul_i32 s70, s48, 32768\n"
"s_mul_i32 s56, s49, 640\n"
"s_add_u32 s70, s70, s56\n"
"s_add_u32 s70, s70, 0x104000\n"
"s_add_u32 s70, s70, s99\n"
"v_and_b32 v58, 15, v0\n"
"v_lshlrev_b32 v58, 5, v58\n"
"v_lshrrev_b32 v59, 4, v0\n"
"v_lshl_add_u32 v58, v59, 4, v58\n"
"s_mul_i32 s56, s50, 576\n"
"s_add_u32 s56, s56, 159744\n"
"v_add_u32 v58, s56, v58\n"
"v_mov_b32 v60, v58\n"
"v_permlane32_swap_b32 v20, v16\n"
"v_permlane32_swap_b32 v21, v17\n"
"v_permlane32_swap_b32 v22, v18\n"
"v_permlane32_swap_b32 v23, v19\n"
"v_pk_add_f32 v[40:41], v[40:41], v[16:17]\n"
"v_pk_add_f32 v[42:43], v[42:43], v[18:19]\n"
"v_mov_b32_dpp v28, v46 row_newbcast:5 row_mask:0xf bank_mask:0x1\n"
"v_mov_b32_dpp v29, v46 row_newbcast:5 row_mask:0xf bank_mask:0x2\n"
"v_mov_b32_dpp v30, v47 row_newbcast:5 row_mask:0xf bank_mask:0x1\n"
"v_mov_b32_dpp v31, v47 row_newbcast:5 row_mask:0xf bank_mask:0x2\n"
"v_mov_b32_dpp v24, v46 row_newbcast:13 row_mask:0xf bank_mask:0x1\n"
"v_mov_b32_dpp v25, v46 row_newbcast:13 row_mask:0xf bank_mask:0x2\n"
"s_waitcnt vmcnt(4)\n"
"v_smfmac_f32_16x16x64_bf16 v[40:43], v[28:31], v[72:79], v14\n"
"v_mov_b32_dpp v26, v47 row_newbcast:13 row_mask:0xf bank_mask:0x1\n"
"v_mov_b32_dpp v27, v47 row_newbcast:13 row_mask:0xf bank_mask:0x2\n"
"v_mov_b32_dpp v28, v46 row_newbcast:2 row_mask:0xf bank_mask:0x1\n"
"v_mov_b32_dpp v29, v46 row_newbcast:2 row_mask:0xf bank_mask:0x2\n"
"s_waitcnt vmcnt(2)\n"
"v_smfmac_f32_16x16x64_bf16 v[40:43], v[24:27], a[88:95], v14\n"
"v_mov_b32_dpp v30, v47 row_newbcast:2 row_mask:0xf bank_mask:0x1\n"
"v_mov_b32_dpp v31, v47 row_newbcast:2 row_mask:0xf bank_mask:0x2\n"
"s_waitcnt vmcnt(0)\n"
"s_nop 0\n"
"v_smfmac_f32_16x16x64_bf16 v[40:43], v[28:31], v[112:119], v14\n"
"s_cbranch_execz LDRYR\n"
"s_mov_b32 exec_hi, 0\n"
"s_nop 5\n"
"ds_write_b128 v60, v[40:43]\n"
"s_mov_b64 exec, -1\n"
"s_waitcnt lgkmcnt(0)\n"
"s_barrier\n"
"s_cmp_lg_u32 s50, 0\n"
"s_cbranch_scc1 Lnomerge_506\n"
"ds_read_b32 v24, v63 offset:0\n"
"ds_read_b32 v25, v63 offset:576\n"
"ds_read_b32 v26, v63 offset:1152\n"
"ds_read_b32 v27, v63 offset:1728\n"
"ds_read_b128 v[4:7], v62 offset:0\n"
"ds_read_b128 v[8:11], v62 offset:576\n"
"ds_read_b128 v[12:15], v62 offset:1152\n"
"ds_read_b128 v[16:19], v62 offset:1728\n"
"s_waitcnt lgkmcnt(4)\n"
"v_max3_f32 v28, v24, v25, v26\n"
"v_max_f32 v28, v28, v27\n"
"v_sub_f32 v24, v24, v28\n"
"v_sub_f32 v25, v25, v28\n"
"v_sub_f32 v26, v26, v28\n"
"v_sub_f32 v27, v27, v28\n"
"v_exp_f32 v24, v24\n"
"v_exp_f32 v25, v25\n"
"v_exp_f32 v26, v26\n"
"v_exp_f32 v27, v27\n"
"s_waitcnt lgkmcnt(0)\n"
"v_mul_f32 v4, v4, v24\n"
"v_mul_f32 v5, v5, v24\n"
"v_mul_f32 v6, v6, v24\n"
"v_mul_f32 v7, v7, v24\n"
"v_fmac_f32 v4, v8, v25\n"
"v_fmac_f32 v5, v9, v25\n"
"v_fmac_f32 v6, v10, v25\n"
"v_fmac_f32 v7, v11, v25\n"
"v_fmac_f32 v4, v12, v26\n"
"v_fmac_f32 v5, v13, v26\n"
"v_fmac_f32 v6, v14, v26\n"
"v_fmac_f32 v7, v15, v26\n"
"v_fmac_f32 v4, v16, v27\n"
"v_fmac_f32 v5, v17, v27\n"
"v_fmac_f32 v6, v18, v27\n"
"v_fmac_f32 v7, v19, v27\n"
"v_cmp_eq_u32 vcc, 32, v0\n"
"s_nop 1\n"
"v_cndmask_b32 v4, v4, v28, vcc\n"
"buffer_store_dwordx4 v[4:7], v61, s[36:39], s70 offen sc1\n"
"s_xor_b32 s70, s70, 8192\n"
"buffer_store_dwordx4 v[4:7], v61, s[36:39], s70 offen sc1\n"
"Lnomerge_506:\n"
"LA_END:\n"
"s_branch LEND\n"
"LCHAIN:\n"
"v_bfe_u32 v56, v0, 2, 2\n"
"v_lshlrev_b32 v1, 8, v56\n"
"v_lshrrev_b32 v56, 4, v0\n"
"v_lshl_or_b32 v1, v56, 6, v1\n"
"v_and_b32 v56, 3, v0\n"
"v_lshl_or_b32 v1, v56, 4, v1\n"
"s_mul_i32 s56, s50, 23552\n"
"s_add_u32 s56, s56, 65536\n"
"s_mov_b32 s57, s56\n"
"v_add_u32 v3, s56, v2\n"
"buffer_load_dword v55, off, s[36:39], 0 sc1\n"
"v_mov_b32 v4, -1\n"
"v_mov_b32 v5, -1\n"
"v_mov_b32 v6, -1\n"
"v_mov_b32 v7, -1\n"
"s_mul_i32 s70, s48, 32768\n"
"s_lshl_b32 s56, s50, 13\n"
"s_add_u32 s70, s70, s56\n"
"s_add_u32 s70, s70, 0x100000\n"
"s_add_u32 s71, s70, 4096\n"
"buffer_store_dwordx4 v[4:7], v2, s[36:39], s70 offen offset:0 sc1\n"
"buffer_store_dwordx4 v[4:7], v2, s[36:39], s70 offen offset:1024 sc1\n"
"buffer_store_dwordx4 v[4:7], v2, s[36:39], s70 offen offset:2048 sc1\n"
"buffer_store_dwordx4 v[4:7], v2, s[36:39], s70 offen offset:3072 sc1\n"
"buffer_store_dwordx4 v[4:7], v2, s[36:39], s71 offen offset:0 sc1\n"
"buffer_store_dwordx4 v[4:7], v2, s[36:39], s71 offen offset:1024 sc1\n"
"buffer_store_dwordx4 v[4:7], v2, s[36:39], s71 offen offset:2048 sc1\n"
"buffer_store_dwordx4 v[4:7], v2, s[36:39], s71 offen offset:3072 sc1\n"
"v_lshrrev_b32 v57, 5, v0\n"
"v_lshlrev_b32 v57, 16, v57\n"
"v_bfe_u32 v58, v0, 4, 1\n"
"v_lshl_add_u32 v57, v58, 13, v57\n"
"v_and_b32 v58, 15, v0\n"
"v_lshl_add_u32 v57, v58, 4, v57\n"
"s_and_b32 s70, s48, 24\n"
"s_add_u32 s70, s70, s48\n"
"s_lshl_b32 s70, s70, 10\n"
"s_lshl_b32 s56, s50, 8\n"
"s_add_u32 s70, s70, s56\n"
"s_add_u32 s70, s70, 0x20000\n"
"s_add_u32 s71, s70, 0x20000\n"
"buffer_store_dwordx4 v[4:7], v57, s[36:39], s70 offen sc1\n"
"buffer_store_dwordx4 v[4:7], v57, s[36:39], s71 offen sc1\n"
"v_lshrrev_b32 v57, 5, v0\n"
"v_lshlrev_b32 v57, 13, v57\n"
"v_and_b32 v58, 31, v0\n"
"v_lshl_add_u32 v58, v58, 4, v57\n"
"s_and_b32 s70, s48, 28\n"
"s_add_u32 s70, s70, s48\n"
"s_lshl_b32 s70, s70, 11\n"
"s_lshl_b32 s56, s50, 9\n"
"s_add_u32 s70, s70, s56\n"
"s_add_u32 s70, s70, 0x60000\n"
"buffer_store_dwordx4 v[4:7], v58, s[36:39], s70 offen sc1\n"
"s_mov_b32 s44, s8\n"
"s_and_b32 s45, s9, 0xffff\n"
"s_mov_b32 s46, 0x400\n"
"s_mov_b32 s47, 0x20000\n"
"v_lshlrev_b32 v57, 2, v0\n"
"s_lshl_b32 s56, s50, 8\n"
"buffer_load_dword v40, v57, s[44:47], s56 offen\n"
"s_mov_b32 s40, s14\n"
"s_and_b32 s41, s15, 0xffff\n"
"s_mov_b32 s42, 0x400000\n"
"s_mov_b32 s43, 0x20000\n"
"v_lshrrev_b32 v58, 4, v0\n"
"v_lshlrev_b32 v58, 13, v58\n"
"v_and_b32 v57, 15, v0\n"
"v_lshl_add_u32 v58, v57, 4, v58\n"
"s_lshl_b32 s56, s48, 8\n"
"s_lshl_b32 s71, s50, 20\n"
"s_add_u32 s71, s71, s56\n"
"s_mul_i32 s70, s50, 32832\n"
"s_add_u32 s56, s71, 0x0\n"
"s_add_u32 m0, s70, 0x0\n"
"s_nop 0\n"
"buffer_load_dwordx4 v58, s[40:43], s56 offen nt lds\n"
"s_add_u32 s56, s71, 0x8000\n"
"s_add_u32 m0, s70, 0x400\n"
"s_nop 0\n"
"buffer_load_dwordx4 v58, s[40:43], s56 offen nt lds\n"
"s_add_u32 s56, s71, 0x10000\n"
"s_add_u32 m0, s70, 0x800\n"
"s_nop 0\n"
"buffer_load_dwordx4 v58, s[40:43], s56 offen nt lds\n"
"s_add_u32 s56, s71, 0x18000\n"
"s_add_u32 m0, s70, 0xc00\n"
"s_nop 0\n"
"buffer_load_dwordx4 v58, s[40:43], s56 offen nt lds\n"
"s_add_u32 s56, s71, 0x20000\n"
"s_add_u32 m0, s70, 0x1000\n"
"s_nop 0\n"
"buffer_load_dwordx4 v58, s[40:43], s56 offen nt lds\n"
"s_add_u32 s56, s71, 0x28000\n"
"s_add_u32 m0, s70, 0x1400\n"
"s_nop 0\n"
"buffer_load_dwordx4 v58, s[40:43], s56 offen nt lds\n"
"s_add_u32 s56, s71, 0x30000\n"
"s_add_u32 m0, s70, 0x1800\n"
"s_nop 0\n"
"buffer_load_dwordx4 v58, s[40:43], s56 offen nt lds\n"
"s_add_u32 s56, s71, 0x38000\n"
"s_add_u32 m0, s70, 0x1c00\n"
"s_nop 0\n"
"buffer_load_dwordx4 v58, s[40:43], s56 offen nt lds\n"
"s_add_u32 s56, s71, 0x40000\n"
"s_add_u32 m0, s70, 0x2000\n"
"s_nop 0\n"
"buffer_load_dwordx4 v58, s[40:43], s56 offen nt lds\n"
"s_add_u32 s56, s71, 0x48000\n"
"s_add_u32 m0, s70, 0x2400\n"
"s_nop 0\n"
"buffer_load_dwordx4 v58, s[40:43], s56 offen nt lds\n"
"s_add_u32 s56, s71, 0x50000\n"
"s_add_u32 m0, s70, 0x2800\n"
"s_nop 0\n"
"buffer_load_dwordx4 v58, s[40:43], s56 offen nt lds\n"
"s_add_u32 s56, s71, 0x58000\n"
"s_add_u32 m0, s70, 0x2c00\n"
"s_nop 0\n"
"buffer_load_dwordx4 v58, s[40:43], s56 offen nt lds\n"
"s_add_u32 s56, s71, 0x60000\n"
"s_add_u32 m0, s70, 0x3000\n"
"s_nop 0\n"
"buffer_load_dwordx4 v58, s[40:43], s56 offen nt lds\n"
"s_add_u32 s56, s71, 0x68000\n"
"s_add_u32 m0, s70, 0x3400\n"
"s_nop 0\n"
"buffer_load_dwordx4 v58, s[40:43], s56 offen nt lds\n"
"s_add_u32 s56, s71, 0x70000\n"
"s_add_u32 m0, s70, 0x3800\n"
"s_nop 0\n"
"buffer_load_dwordx4 v58, s[40:43], s56 offen nt lds\n"
"s_add_u32 s56, s71, 0x78000\n"
"s_add_u32 m0, s70, 0x3c00\n"
"s_nop 0\n"
"buffer_load_dwordx4 v58, s[40:43], s56 offen nt lds\n"
"s_add_u32 s56, s71, 0x80000\n"
"s_add_u32 m0, s70, 0x4000\n"
"s_nop 0\n"
"buffer_load_dwordx4 v58, s[40:43], s56 offen nt lds\n"
"s_add_u32 s56, s71, 0x88000\n"
"s_add_u32 m0, s70, 0x4400\n"
"s_nop 0\n"
"buffer_load_dwordx4 v58, s[40:43], s56 offen nt lds\n"
"s_add_u32 s56, s71, 0x90000\n"
"s_add_u32 m0, s70, 0x4800\n"
"s_nop 0\n"
"buffer_load_dwordx4 v58, s[40:43], s56 offen nt lds\n"
"s_add_u32 s56, s71, 0x98000\n"
"s_add_u32 m0, s70, 0x4c00\n"
"s_nop 0\n"
"buffer_load_dwordx4 v58, s[40:43], s56 offen nt lds\n"
"s_add_u32 s56, s71, 0xa0000\n"
"s_add_u32 m0, s70, 0x5000\n"
"s_nop 0\n"
"buffer_load_dwordx4 v58, s[40:43], s56 offen nt lds\n"
"s_add_u32 s56, s71, 0xa8000\n"
"s_add_u32 m0, s70, 0x5400\n"
"s_nop 0\n"
"buffer_load_dwordx4 v58, s[40:43], s56 offen nt lds\n"
"s_add_u32 s56, s71, 0xb0000\n"
"s_add_u32 m0, s70, 0x5800\n"
"s_nop 0\n"
"buffer_load_dwordx4 v58, s[40:43], s56 offen nt lds\n"
"s_add_u32 s56, s71, 0xb8000\n"
"s_add_u32 m0, s70, 0x5c00\n"
"s_nop 0\n"
"buffer_load_dwordx4 v58, s[40:43], s56 offen nt lds\n"
"s_add_u32 s56, s71, 0xc0000\n"
"s_add_u32 m0, s70, 0x6000\n"
"s_nop 0\n"
"buffer_load_dwordx4 v58, s[40:43], s56 offen nt lds\n"
"s_add_u32 s56, s71, 0xc8000\n"
"s_add_u32 m0, s70, 0x6400\n"
"s_nop 0\n"
"buffer_load_dwordx4 v58, s[40:43], s56 offen nt lds\n"
"s_add_u32 s56, s71, 0xd0000\n"
"s_add_u32 m0, s70, 0x6800\n"
"s_nop 0\n"
"buffer_load_dwordx4 v58, s[40:43], s56 offen nt lds\n"
"s_add_u32 s56, s71, 0xd8000\n"
"s_add_u32 m0, s70, 0x6c00\n"
"s_nop 0\n"
"buffer_load_dwordx4 v58, s[40:43], s56 offen nt lds\n"
"s_add_u32 s56, s71, 0xe0000\n"
"s_add_u32 m0, s70, 0x7000\n"
"s_nop 0\n"
"buffer_load_dwordx4 v58, s[40:43], s56 offen nt lds\n"
"s_add_u32 s56, s71, 0xe8000\n"
"s_add_u32 m0, s70, 0x7400\n"
"s_nop 0\n"
"buffer_load_dwordx4 v58, s[40:43], s56 offen nt lds\n"
"s_add_u32 s56, s71, 0xf0000\n"
"s_add_u32 m0, s70, 0x7800\n"
"s_nop 0\n"
"buffer_load_dwordx4 v58, s[40:43], s56 offen nt lds\n"
"s_add_u32 s56, s71, 0xf8000\n"
"s_add_u32 m0, s70, 0x7c00\n"
"s_nop 0\n"
"buffer_load_dwordx4 v58, s[40:43], s56 offen nt lds\n"
"s_mov_b32 s40, s16\n"
"s_and_b32 s41, s17, 0xffff\n"
"s_mov_b32 s42, 0x400000\n"
"s_mov_b32 s43, 0x20000\n"
"v_lshrrev_b32 v62, 4, v0\n"
"v_lshlrev_b32 v62, 13, v62\n"
"v_lshl_add_u32 v62, v57, 4, v62\n"
"s_lshl_b32 s56, s50, 8\n"
"v_add_u32 v62, s56, v62\n"
"s_lshl_b32 s71, s48, 17\n"
"s_add_u32 s56, s71, 0x0\n"
"buffer_load_dwordx4 v[64:67], v62, s[40:43], s56 offen nt\n"
"s_add_u32 s56, s71, 0x400\n"
"buffer_load_dwordx4 v[68:71], v62, s[40:43], s56 offen nt\n"
"s_add_u32 s56, s71, 0x800\n"
"buffer_load_dwordx4 v[72:75], v62, s[40:43], s56 offen nt\n"
"s_add_u32 s56, s71, 0xc00\n"
"buffer_load_dwordx4 v[76:79], v62, s[40:43], s56 offen nt\n"
"s_add_u32 s56, s71, 0x1000\n"
"buffer_load_dwordx4 v[80:83], v62, s[40:43], s56 offen nt\n"
"s_add_u32 s56, s71, 0x1400\n"
"buffer_load_dwordx4 v[84:87], v62, s[40:43], s56 offen nt\n"
"s_add_u32 s56, s71, 0x1800\n"
"buffer_load_dwordx4 v[88:91], v62, s[40:43], s56 offen nt\n"
"s_add_u32 s56, s71, 0x1c00\n"
"buffer_load_dwordx4 v[92:95], v62, s[40:43], s56 offen nt\n"
"s_add_u32 s56, s71, 0x8000\n"
"buffer_load_dwordx4 v[96:99], v62, s[40:43], s56 offen nt\n"
"s_add_u32 s56, s71, 0x8400\n"
"buffer_load_dwordx4 v[100:103], v62, s[40:43], s56 offen nt\n"
"s_add_u32 s56, s71, 0x8800\n"
"buffer_load_dwordx4 v[104:107], v62, s[40:43], s56 offen nt\n"
"s_add_u32 s56, s71, 0x8c00\n"
"buffer_load_dwordx4 v[108:111], v62, s[40:43], s56 offen nt\n"
"s_add_u32 s56, s71, 0x9000\n"
"buffer_load_dwordx4 v[112:115], v62, s[40:43], s56 offen nt\n"
"s_add_u32 s56, s71, 0x9400\n"
"buffer_load_dwordx4 v[116:119], v62, s[40:43], s56 offen nt\n"
"s_add_u32 s56, s71, 0x9800\n"
"buffer_load_dwordx4 v[120:123], v62, s[40:43], s56 offen nt\n"
"s_add_u32 s56, s71, 0x9c00\n"
"buffer_load_dwordx4 v[124:127], v62, s[40:43], s56 offen nt\n"
"s_lshl_b32 s56, s50, 14\n"
"v_add_u32 v59, s56, v2\n"
"v_lshrrev_b32 v58, 4, v0\n"
"v_mul_u32_u24 v58, 32832, v58\n"
"v_lshl_add_u32 v58, v57, 2, v58\n"
"s_lshl_b32 s56, s50, 6\n"
"v_add_u32 v58, s56, v58\n"
"s_waitcnt vmcnt(48)\n"
"v_readfirstlane_b32 s59, v55\n"
"s_mul_i32 s59, s59, 1664525\n"
"s_add_u32 s59, s59, 1013904223\n"
"v_lshrrev_b32 v21, 2, v0\n"
"v_and_b32 v22, 3, v0\n"
"v_lshrrev_b32 v23, 3, v21\n"
"v_mul_u32_u24 v23, 576, v23\n"
"v_and_b32 v20, 7, v21\n"
"v_lshl_add_u32 v20, v20, 4, v23\n"
"v_and_b32 v23, 1, v22\n"
"v_mul_u32_u24 v23, 144, v23\n"
"v_add_u32 v20, v23, v20\n"
"v_lshrrev_b32 v23, 1, v22\n"
"v_lshl_add_u32 v20, v23, 2, v20\n"
"s_lshr_b32 s56, s50, 1\n"
"s_mul_i32 s56, s56, 272\n"
"v_add_u32 v20, s56, v20\n"
"s_lshl_b32 s56, s50, 3\n"
"v_add_u32 v20, s56, v20\n"
"v_add_u32 v20, 162560, v20\n"
"ds_write_b32 v20, v40\n"
"v_and_b32 v20, 31, v0\n"
"v_lshlrev_b32 v20, 2, v20\n"
"v_add_u32 v20, 163712, v20\n"
"v_mov_b32 v21, 0x3f803f80\n"
"ds_write_b32 v20, v21\n"
"s_waitcnt vmcnt(16)\n"
"s_waitcnt lgkmcnt(0)\n"
"s_barrier\n"
"v_mov_b32 v21, s59\n"
"v_add_u32 v22, s50, v0\n"
"v_cmp_eq_u32 vcc, 0, v22\n"
"v_mov_b32 v22, 0x7ff00000\n"
"s_nop 0\n"
"v_cndmask_b32 v22, v22, v0, vcc\n"
"s_lshl_b32 s56, s48, 2\n"
"buffer_store_dword v21, v22, s[36:39], s56 offen offset:256 sc1\n"
"v_lshrrev_b32 v21, 3, v0\n"
"v_lshl_add_u32 v21, v21, 4, v2\n"
"v_add_u32 v21, 162560, v21\n"
"ds_read_b128 v[36:39], v21\n"
"s_waitcnt lgkmcnt(0)\n"
"ds_read_b32 v4, v58 offset:0\n"
"ds_read_b32 v5, v58 offset:256\n"
"ds_read_b32 v6, v58 offset:512\n"
"ds_read_b32 v7, v58 offset:768\n"
"ds_read_b32 v8, v58 offset:1024\n"
"ds_read_b32 v9, v58 offset:1280\n"
"ds_read_b32 v10, v58 offset:1536\n"
"ds_read_b32 v11, v58 offset:1792\n"
"ds_read_b32 v12, v58 offset:16384\n"
"ds_read_b32 v13, v58 offset:16640\n"
"ds_read_b32 v14, v58 offset:16896\n"
"ds_read_b32 v15, v58 offset:17152\n"
"ds_read_b32 v16, v58 offset:17408\n"
"ds_read_b32 v17, v58 offset:17664\n"
"ds_read_b32 v18, v58 offset:17920\n"
"ds_read_b32 v19, v58 offset:18176\n"
"s_waitcnt lgkmcnt(8)\n"
"v_perm_b32 v128, v5, v4, s68\n"
"v_perm_b32 v129, v7, v6, s68\n"
"v_perm_b32 v130, v9, v8, s68\n"
"v_perm_b32 v131, v11, v10, s68\n"
"v_perm_b32 v136, v5, v4, s69\n"
"v_perm_b32 v137, v7, v6, s69\n"
"v_perm_b32 v138, v9, v8, s69\n"
"v_perm_b32 v139, v11, v10, s69\n"
"ds_read_b32 v4, v58 offset:2048\n"
"ds_read_b32 v5, v58 offset:2304\n"
"ds_read_b32 v6, v58 offset:2560\n"
"ds_read_b32 v7, v58 offset:2816\n"
"ds_read_b32 v8, v58 offset:3072\n"
"ds_read_b32 v9, v58 offset:3328\n"
"ds_read_b32 v10, v58 offset:3584\n"
"ds_read_b32 v11, v58 offset:3840\n"
"s_waitcnt lgkmcnt(8)\n"
"v_perm_b32 v132, v13, v12, s68\n"
"v_perm_b32 v133, v15, v14, s68\n"
"v_perm_b32 v134, v17, v16, s68\n"
"v_perm_b32 v135, v19, v18, s68\n"
"v_perm_b32 v140, v13, v12, s69\n"
"v_perm_b32 v141, v15, v14, s69\n"
"v_perm_b32 v142, v17, v16, s69\n"
"v_perm_b32 v143, v19, v18, s69\n"
"ds_read_b32 v12, v58 offset:18432\n"
"ds_read_b32 v13, v58 offset:18688\n"
"ds_read_b32 v14, v58 offset:18944\n"
"ds_read_b32 v15, v58 offset:19200\n"
"ds_read_b32 v16, v58 offset:19456\n"
"ds_read_b32 v17, v58 offset:19712\n"
"ds_read_b32 v18, v58 offset:19968\n"
"ds_read_b32 v19, v58 offset:20224\n"
"s_waitcnt lgkmcnt(8)\n"
"v_perm_b32 v144, v5, v4, s68\n"
"v_perm_b32 v145, v7, v6, s68\n"
"v_perm_b32 v146, v9, v8, s68\n"
"v_perm_b32 v147, v11, v10, s68\n"
"v_perm_b32 v152, v5, v4, s69\n"
"v_perm_b32 v153, v7, v6, s69\n"
"v_perm_b32 v154, v9, v8, s69\n"
"v_perm_b32 v155, v11, v10, s69\n"
"ds_read_b32 v4, v58 offset:4096\n"
"ds_read_b32 v5, v58 offset:4352\n"
"ds_read_b32 v6, v58 offset:4608\n"
"ds_read_b32 v7, v58 offset:4864\n"
"ds_read_b32 v8, v58 offset:5120\n"
"ds_read_b32 v9, v58 offset:5376\n"
"ds_read_b32 v10, v58 offset:5632\n"
"ds_read_b32 v11, v58 offset:5888\n"
"s_waitcnt lgkmcnt(8)\n"
"v_perm_b32 v148, v13, v12, s68\n"
"v_perm_b32 v149, v15, v14, s68\n"
"v_perm_b32 v150, v17, v16, s68\n"
"v_perm_b32 v151, v19, v18, s68\n"
"v_perm_b32 v156, v13, v12, s69\n"
"v_perm_b32 v157, v15, v14, s69\n"
"v_perm_b32 v158, v17, v16, s69\n"
"v_perm_b32 v159, v19, v18, s69\n"
"ds_read_b32 v12, v58 offset:20480\n"
"ds_read_b32 v13, v58 offset:20736\n"
"ds_read_b32 v14, v58 offset:20992\n"
"ds_read_b32 v15, v58 offset:21248\n"
"ds_read_b32 v16, v58 offset:21504\n"
"ds_read_b32 v17, v58 offset:21760\n"
"ds_read_b32 v18, v58 offset:22016\n"
"ds_read_b32 v19, v58 offset:22272\n"
"s_waitcnt lgkmcnt(8)\n"
"v_perm_b32 v160, v5, v4, s68\n"
"v_perm_b32 v161, v7, v6, s68\n"
"v_perm_b32 v162, v9, v8, s68\n"
"v_perm_b32 v163, v11, v10, s68\n"
"v_perm_b32 v168, v5, v4, s69\n"
"v_perm_b32 v169, v7, v6, s69\n"
"v_perm_b32 v170, v9, v8, s69\n"
"v_perm_b32 v171, v11, v10, s69\n"
"ds_read_b32 v4, v58 offset:6144\n"
"ds_read_b32 v5, v58 offset:6400\n"
"ds_read_b32 v6, v58 offset:6656\n"
"ds_read_b32 v7, v58 offset:6912\n"
"ds_read_b32 v8, v58 offset:7168\n"
"ds_read_b32 v9, v58 offset:7424\n"
"ds_read_b32 v10, v58 offset:7680\n"
"ds_read_b32 v11, v58 offset:7936\n"
"s_waitcnt lgkmcnt(8)\n"
"v_perm_b32 v164, v13, v12, s68\n"
"v_perm_b32 v165, v15, v14, s68\n"
"v_perm_b32 v166, v17, v16, s68\n"
"v_perm_b32 v167, v19, v18, s68\n"
"v_perm_b32 v172, v13, v12, s69\n"
"v_perm_b32 v173, v15, v14, s69\n"
"v_perm_b32 v174, v17, v16, s69\n"
"v_perm_b32 v175, v19, v18, s69\n"
"ds_read_b32 v12, v58 offset:22528\n"
"ds_read_b32 v13, v58 offset:22784\n"
"ds_read_b32 v14, v58 offset:23040\n"
"ds_read_b32 v15, v58 offset:23296\n"
"ds_read_b32 v16, v58 offset:23552\n"
"ds_read_b32 v17, v58 offset:23808\n"
"ds_read_b32 v18, v58 offset:24064\n"
"ds_read_b32 v19, v58 offset:24320\n"
"s_waitcnt lgkmcnt(8)\n"
"v_perm_b32 v176, v5, v4, s68\n"
"v_perm_b32 v177, v7, v6, s68\n"
"v_perm_b32 v178, v9, v8, s68\n"
"v_perm_b32 v179, v11, v10, s68\n"
"v_perm_b32 v184, v5, v4, s69\n"
"v_perm_b32 v185, v7, v6, s69\n"
"v_perm_b32 v186, v9, v8, s69\n"
"v_perm_b32 v187, v11, v10, s69\n"
"ds_read_b32 v4, v58 offset:8192\n"
"ds_read_b32 v5, v58 offset:8448\n"
"ds_read_b32 v6, v58 offset:8704\n"
"ds_read_b32 v7, v58 offset:8960\n"
"ds_read_b32 v8, v58 offset:9216\n"
"ds_read_b32 v9, v58 offset:9472\n"
"ds_read_b32 v10, v58 offset:9728\n"
"ds_read_b32 v11, v58 offset:9984\n"
"s_waitcnt lgkmcnt(8)\n"
"v_perm_b32 v180, v13, v12, s68\n"
"v_perm_b32 v181, v15, v14, s68\n"
"v_perm_b32 v182, v17, v16, s68\n"
"v_perm_b32 v183, v19, v18, s68\n"
"v_perm_b32 v188, v13, v12, s69\n"
"v_perm_b32 v189, v15, v14, s69\n"
"v_perm_b32 v190, v17, v16, s69\n"
"v_perm_b32 v191, v19, v18, s69\n"
"ds_read_b32 v12, v58 offset:24576\n"
"ds_read_b32 v13, v58 offset:24832\n"
"ds_read_b32 v14, v58 offset:25088\n"
"ds_read_b32 v15, v58 offset:25344\n"
"ds_read_b32 v16, v58 offset:25600\n"
"ds_read_b32 v17, v58 offset:25856\n"
"ds_read_b32 v18, v58 offset:26112\n"
"ds_read_b32 v19, v58 offset:26368\n"
"s_waitcnt lgkmcnt(8)\n"
"v_perm_b32 v192, v5, v4, s68\n"
"v_perm_b32 v193, v7, v6, s68\n"
"v_perm_b32 v194, v9, v8, s68\n"
"v_perm_b32 v195, v11, v10, s68\n"
"v_perm_b32 v200, v5, v4, s69\n"
"v_perm_b32 v201, v7, v6, s69\n"
"v_perm_b32 v202, v9, v8, s69\n"
"v_perm_b32 v203, v11, v10, s69\n"
"ds_read_b32 v4, v58 offset:10240\n"
"ds_read_b32 v5, v58 offset:10496\n"
"ds_read_b32 v6, v58 offset:10752\n"
"ds_read_b32 v7, v58 offset:11008\n"
"ds_read_b32 v8, v58 offset:11264\n"
"ds_read_b32 v9, v58 offset:11520\n"
"ds_read_b32 v10, v58 offset:11776\n"
"ds_read_b32 v11, v58 offset:12032\n"
"s_waitcnt lgkmcnt(8)\n"
"v_perm_b32 v196, v13, v12, s68\n"
"v_perm_b32 v197, v15, v14, s68\n"
"v_perm_b32 v198, v17, v16, s68\n"
"v_perm_b32 v199, v19, v18, s68\n"
"v_perm_b32 v204, v13, v12, s69\n"
"v_perm_b32 v205, v15, v14, s69\n"
"v_perm_b32 v206, v17, v16, s69\n"
"v_perm_b32 v207, v19, v18, s69\n"
"ds_read_b32 v12, v58 offset:26624\n"
"ds_read_b32 v13, v58 offset:26880\n"
"ds_read_b32 v14, v58 offset:27136\n"
"ds_read_b32 v15, v58 offset:27392\n"
"ds_read_b32 v16, v58 offset:27648\n"
"ds_read_b32 v17, v58 offset:27904\n"
"ds_read_b32 v18, v58 offset:28160\n"
"ds_read_b32 v19, v58 offset:28416\n"
"s_waitcnt lgkmcnt(8)\n"
"v_perm_b32 v208, v5, v4, s68\n"
"v_perm_b32 v209, v7, v6, s68\n"
"v_perm_b32 v210, v9, v8, s68\n"
"v_perm_b32 v211, v11, v10, s68\n"
"v_perm_b32 v216, v5, v4, s69\n"
"v_perm_b32 v217, v7, v6, s69\n"
"v_perm_b32 v218, v9, v8, s69\n"
"v_perm_b32 v219, v11, v10, s69\n"
"ds_read_b32 v4, v58 offset:12288\n"
"ds_read_b32 v5, v58 offset:12544\n"
"ds_read_b32 v6, v58 offset:12800\n"
"ds_read_b32 v7, v58 offset:13056\n"
"ds_read_b32 v8, v58 offset:13312\n"
"ds_read_b32 v9, v58 offset:13568\n"
"ds_read_b32 v10, v58 offset:13824\n"
"ds_read_b32 v11, v58 offset:14080\n"
"s_waitcnt lgkmcnt(8)\n"
"v_perm_b32 v212, v13, v12, s68\n"
"v_perm_b32 v213, v15, v14, s68\n"
"v_perm_b32 v214, v17, v16, s68\n"
"v_perm_b32 v215, v19, v18, s68\n"
"v_perm_b32 v220, v13, v12, s69\n"
"v_perm_b32 v221, v15, v14, s69\n"
"v_perm_b32 v222, v17, v16, s69\n"
"v_perm_b32 v223, v19, v18, s69\n"
"ds_read_b32 v12, v58 offset:28672\n"
"ds_read_b32 v13, v58 offset:28928\n"
"ds_read_b32 v14, v58 offset:29184\n"
"ds_read_b32 v15, v58 offset:29440\n"
"ds_read_b32 v16, v58 offset:29696\n"
"ds_read_b32 v17, v58 offset:29952\n"
"ds_read_b32 v18, v58 offset:30208\n"
"ds_read_b32 v19, v58 offset:30464\n"
"s_waitcnt lgkmcnt(8)\n"
"v_perm_b32 v224, v5, v4, s68\n"
"v_perm_b32 v225, v7, v6, s68\n"
"v_perm_b32 v226, v9, v8, s68\n"
"v_perm_b32 v227, v11, v10, s68\n"
"v_perm_b32 v232, v5, v4, s69\n"
"v_perm_b32 v233, v7, v6, s69\n"
"v_perm_b32 v234, v9, v8, s69\n"
"v_perm_b32 v235, v11, v10, s69\n"
"ds_read_b32 v4, v58 offset:14336\n"
"ds_read_b32 v5, v58 offset:14592\n"
"ds_read_b32 v6, v58 offset:14848\n"
"ds_read_b32 v7, v58 offset:15104\n"
"ds_read_b32 v8, v58 offset:15360\n"
"ds_read_b32 v9, v58 offset:15616\n"
"ds_read_b32 v10, v58 offset:15872\n"
"ds_read_b32 v11, v58 offset:16128\n"
"s_waitcnt lgkmcnt(8)\n"
"v_perm_b32 v228, v13, v12, s68\n"
"v_perm_b32 v229, v15, v14, s68\n"
"v_perm_b32 v230, v17, v16, s68\n"
"v_perm_b32 v231, v19, v18, s68\n"
"v_perm_b32 v236, v13, v12, s69\n"
"v_perm_b32 v237, v15, v14, s69\n"
"v_perm_b32 v238, v17, v16, s69\n"
"v_perm_b32 v239, v19, v18, s69\n"
"ds_read_b32 v12, v58 offset:30720\n"
"ds_read_b32 v13, v58 offset:30976\n"
"ds_read_b32 v14, v58 offset:31232\n"
"ds_read_b32 v15, v58 offset:31488\n"
"ds_read_b32 v16, v58 offset:31744\n"
"ds_read_b32 v17, v58 offset:32000\n"
"ds_read_b32 v18, v58 offset:32256\n"
"ds_read_b32 v19, v58 offset:32512\n"
"s_waitcnt lgkmcnt(8)\n"
"v_perm_b32 v240, v5, v4, s68\n"
"v_perm_b32 v241, v7, v6, s68\n"
"v_perm_b32 v242, v9, v8, s68\n"
"v_perm_b32 v243, v11, v10, s68\n"
"v_perm_b32 v248, v5, v4, s69\n"
"v_perm_b32 v249, v7, v6, s69\n"
"v_perm_b32 v250, v9, v8, s69\n"
"v_perm_b32 v251, v11, v10, s69\n"
"s_waitcnt lgkmcnt(0)\n"
"v_perm_b32 v244, v13, v12, s68\n"
"v_perm_b32 v245, v15, v14, s68\n"
"v_perm_b32 v246, v17, v16, s68\n"
"v_perm_b32 v247, v19, v18, s68\n"
"v_perm_b32 v252, v13, v12, s69\n"
"v_perm_b32 v253, v15, v14, s69\n"
"v_perm_b32 v254, v17, v16, s69\n"
"v_perm_b32 v255, v19, v18, s69\n"
"s_barrier\n"
"ds_write_b128 v59, v[192:195] offset:0\n"
"ds_write_b128 v59, v[196:199] offset:1024\n"
"ds_write_b128 v59, v[200:203] offset:2048\n"
"ds_write_b128 v59, v[204:207] offset:3072\n"
"ds_write_b128 v59, v[208:211] offset:4096\n"
"ds_write_b128 v59, v[212:215] offset:5120\n"
"ds_write_b128 v59, v[216:219] offset:6144\n"
"ds_write_b128 v59, v[220:223] offset:7168\n"
"ds_write_b128 v59, v[224:227] offset:8192\n"
"ds_write_b128 v59, v[228:231] offset:9216\n"
"ds_write_b128 v59, v[232:235] offset:10240\n"
"ds_write_b128 v59, v[236:239] offset:11264\n"
"ds_write_b128 v59, v[240:243] offset:12288\n"
"ds_write_b128 v59, v[244:247] offset:13312\n"
"ds_write_b128 v59, v[248:251] offset:14336\n"
"ds_write_b128 v59, v[252:255] offset:15360\n"
"s_waitcnt lgkmcnt(0)\n"
"s_mov_b32 s51, 0\n"
"s_branch LS5_pre\n"
"LC_P1:\n"
"s_nop 7\n"
"v_mov_b32 v192, v4\n"
"v_mov_b32 v193, v5\n"
"v_mov_b32 v194, v6\n"
"v_mov_b32 v195, v7\n"
"v_mov_b32 v196, v8\n"
"v_mov_b32 v197, v9\n"
"v_mov_b32 v198, v10\n"
"v_mov_b32 v199, v11\n"
"v_mov_b32 v200, v12\n"
"v_mov_b32 v201, v13\n"
"v_mov_b32 v202, v14\n"
"v_mov_b32 v203, v15\n"
"v_mov_b32 v204, v16\n"
"v_mov_b32 v205, v17\n"
"v_mov_b32 v206, v18\n"
"v_mov_b32 v207, v19\n"
"v_mov_b32 v208, v20\n"
"v_mov_b32 v209, v21\n"
"v_mov_b32 v210, v22\n"
"v_mov_b32 v211, v23\n"
"v_mov_b32 v212, v24\n"
"v_mov_b32 v213, v25\n"
"v_mov_b32 v214, v26\n"
"v_mov_b32 v215, v27\n"
"v_mov_b32 v216, v28\n"
"v_mov_b32 v217, v29\n"
"v_mov_b32 v218, v30\n"
"v_mov_b32 v219, v31\n"
"v_mov_b32 v220, v32\n"
"v_mov_b32 v221, v33\n"
"v_mov_b32 v222, v34\n"
"v_mov_b32 v223, v35\n"
"s_barrier\n"
"s_mov_b32 s40, s18\n"
"s_and_b32 s41, s19, 0xffff\n"
"s_mov_b32 s42, 0x158000\n"
"s_mov_b32 s43, 0x20000\n"
"s_mov_b32 s44, s20\n"
"s_and_b32 s45, s21, 0xffff\n"
"s_mov_b32 s46, 0x158000\n"
"s_mov_b32 s47, 0x20000\n"
"v_lshrrev_b32 v56, 3, v0\n"
"v_mul_u32_u24 v56, 2752, v56\n"
"v_and_b32 v57, 7, v0\n"
"v_lshl_add_u32 v56, v57, 4, v56\n"
"s_mul_i32 s71, s48, 86\n"
"s_and_b32 s71, s71, 0xfffffff0\n"
"s_mul_i32 s56, s50, 352256\n"
"s_add_u32 s71, s71, s56\n"
"s_mul_i32 s70, s50, 16416\n"
"s_add_u32 s56, s71, 0x0\n"
"s_add_u32 m0, s70, 0x0\n"
"s_nop 0\n"
"buffer_load_dwordx4 v56, s[40:43], s56 offen lds\n"
"s_add_u32 s56, s71, 0x5600\n"
"s_add_u32 m0, s70, 0x400\n"
"s_nop 0\n"
"buffer_load_dwordx4 v56, s[40:43], s56 offen lds\n"
"s_add_u32 s56, s71, 0xac00\n"
"s_add_u32 m0, s70, 0x800\n"
"s_nop 0\n"
"buffer_load_dwordx4 v56, s[40:43], s56 offen lds\n"
"s_add_u32 s56, s71, 0x10200\n"
"s_add_u32 m0, s70, 0xc00\n"
"s_nop 0\n"
"buffer_load_dwordx4 v56, s[40:43], s56 offen lds\n"
"s_add_u32 s56, s71, 0x15800\n"
"s_add_u32 m0, s70, 0x1000\n"
"s_nop 0\n"
"buffer_load_dwordx4 v56, s[40:43], s56 offen lds\n"
"s_add_u32 s56, s71, 0x1ae00\n"
"s_add_u32 m0, s70, 0x1400\n"
"s_nop 0\n"
"buffer_load_dwordx4 v56, s[40:43], s56 offen lds\n"
"s_add_u32 s56, s71, 0x20400\n"
"s_add_u32 m0, s70, 0x1800\n"
"s_nop 0\n"
"buffer_load_dwordx4 v56, s[40:43], s56 offen lds\n"
"s_add_u32 s56, s71, 0x25a00\n"
"s_add_u32 m0, s70, 0x1c00\n"
"s_nop 0\n"
"buffer_load_dwordx4 v56, s[40:43], s56 offen lds\n"
"s_add_u32 s56, s71, 0x2b000\n"
"s_add_u32 m0, s70, 0x2000\n"
"s_nop 0\n"
"buffer_load_dwordx4 v56, s[40:43], s56 offen lds\n"
"s_add_u32 s56, s71, 0x30600\n"
"s_add_u32 m0, s70, 0x2400\n"
"s_nop 0\n"
"buffer_load_dwordx4 v56, s[40:43], s56 offen lds\n"
"s_add_u32 s56, s71, 0x35c00\n"
"s_add_u32 m0, s70, 0x2800\n"
"s_nop 0\n"
"buffer_load_dwordx4 v56, s[40:43], s56 offen lds\n"
"s_add_u32 s56, s71, 0x3b200\n"
"s_add_u32 m0, s70, 0x2c00\n"
"s_nop 0\n"
"buffer_load_dwordx4 v56, s[40:43], s56 offen lds\n"
"s_add_u32 s56, s71, 0x40800\n"
"s_add_u32 m0, s70, 0x3000\n"
"s_nop 0\n"
"buffer_load_dwordx4 v56, s[40:43], s56 offen lds\n"
"s_add_u32 s56, s71, 0x45e00\n"
"s_add_u32 m0, s70, 0x3400\n"
"s_nop 0\n"
"buffer_load_dwordx4 v56, s[40:43], s56 offen lds\n"
"s_add_u32 s56, s71, 0x4b400\n"
"s_add_u32 m0, s70, 0x3800\n"
"s_nop 0\n"
"buffer_load_dwordx4 v56, s[40:43], s56 offen lds\n"
"s_add_u32 s56, s71, 0x50a00\n"
"s_add_u32 m0, s70, 0x3c00\n"
"s_nop 0\n"
"buffer_load_dwordx4 v56, s[40:43], s56 offen lds\n"
"s_add_u32 s56, s71, 0x0\n"
"s_add_u32 m0, s70, 0x10080\n"
"s_nop 0\n"
"buffer_load_dwordx4 v56, s[44:47], s56 offen lds\n"
"s_add_u32 s56, s71, 0x5600\n"
"s_add_u32 m0, s70, 0x10480\n"
"s_nop 0\n"
"buffer_load_dwordx4 v56, s[44:47], s56 offen lds\n"
"s_add_u32 s56, s71, 0xac00\n"
"s_add_u32 m0, s70, 0x10880\n"
"s_nop 0\n"
"buffer_load_dwordx4 v56, s[44:47], s56 offen lds\n"
"s_add_u32 s56, s71, 0x10200\n"
"s_add_u32 m0, s70, 0x10c80\n"
"s_nop 0\n"
"buffer_load_dwordx4 v56, s[44:47], s56 offen lds\n"
"s_add_u32 s56, s71, 0x15800\n"
"s_add_u32 m0, s70, 0x11080\n"
"s_nop 0\n"
"buffer_load_dwordx4 v56, s[44:47], s56 offen lds\n"
"s_add_u32 s56, s71, 0x1ae00\n"
"s_add_u32 m0, s70, 0x11480\n"
"s_nop 0\n"
"buffer_load_dwordx4 v56, s[44:47], s56 offen lds\n"
"s_add_u32 s56, s71, 0x20400\n"
"s_add_u32 m0, s70, 0x11880\n"
"s_nop 0\n"
"buffer_load_dwordx4 v56, s[44:47], s56 offen lds\n"
"s_add_u32 s56, s71, 0x25a00\n"
"s_add_u32 m0, s70, 0x11c80\n"
"s_nop 0\n"
"buffer_load_dwordx4 v56, s[44:47], s56 offen lds\n"
"s_add_u32 s56, s71, 0x2b000\n"
"s_add_u32 m0, s70, 0x12080\n"
"s_nop 0\n"
"buffer_load_dwordx4 v56, s[44:47], s56 offen lds\n"
"s_add_u32 s56, s71, 0x30600\n"
"s_add_u32 m0, s70, 0x12480\n"
"s_nop 0\n"
"buffer_load_dwordx4 v56, s[44:47], s56 offen lds\n"
"s_add_u32 s56, s71, 0x35c00\n"
"s_add_u32 m0, s70, 0x12880\n"
"s_nop 0\n"
"buffer_load_dwordx4 v56, s[44:47], s56 offen lds\n"
"s_add_u32 s56, s71, 0x3b200\n"
"s_add_u32 m0, s70, 0x12c80\n"
"s_nop 0\n"
"buffer_load_dwordx4 v56, s[44:47], s56 offen lds\n"
"s_add_u32 s56, s71, 0x40800\n"
"s_add_u32 m0, s70, 0x13080\n"
"s_nop 0\n"
"buffer_load_dwordx4 v56, s[44:47], s56 offen lds\n"
"s_add_u32 s56, s71, 0x45e00\n"
"s_add_u32 m0, s70, 0x13480\n"
"s_nop 0\n"
"buffer_load_dwordx4 v56, s[44:47], s56 offen lds\n"
"s_add_u32 s56, s71, 0x4b400\n"
"s_add_u32 m0, s70, 0x13880\n"
"s_nop 0\n"
"buffer_load_dwordx4 v56, s[44:47], s56 offen lds\n"
"s_add_u32 s56, s71, 0x50a00\n"
"s_add_u32 m0, s70, 0x13c80\n"
"s_nop 0\n"
"buffer_load_dwordx4 v56, s[44:47], s56 offen lds\n"
"s_waitcnt vmcnt(32)\n"
"v_perm_b32 v4, v68, v64, s68\n"
"v_accvgpr_write_b32 a0, v4\n"
"v_perm_b32 v5, v76, v72, s68\n"
"v_accvgpr_write_b32 a1, v5\n"
"v_perm_b32 v6, v84, v80, s68\n"
"v_accvgpr_write_b32 a2, v6\n"
"v_perm_b32 v7, v92, v88, s68\n"
"v_accvgpr_write_b32 a3, v7\n"
"v_perm_b32 v8, v68, v64, s69\n"
"v_accvgpr_write_b32 a16, v8\n"
"v_perm_b32 v9, v76, v72, s69\n"
"v_accvgpr_write_b32 a17, v9\n"
"v_perm_b32 v10, v84, v80, s69\n"
"v_accvgpr_write_b32 a18, v10\n"
"v_perm_b32 v11, v92, v88, s69\n"
"v_accvgpr_write_b32 a19, v11\n"
"v_perm_b32 v12, v69, v65, s68\n"
"v_accvgpr_write_b32 a32, v12\n"
"v_perm_b32 v13, v77, v73, s68\n"
"v_accvgpr_write_b32 a33, v13\n"
"v_perm_b32 v14, v85, v81, s68\n"
"v_accvgpr_write_b32 a34, v14\n"
"v_perm_b32 v15, v93, v89, s68\n"
"v_accvgpr_write_b32 a35, v15\n"
"v_perm_b32 v16, v69, v65, s69\n"
"v_accvgpr_write_b32 a48, v16\n"
"v_perm_b32 v17, v77, v73, s69\n"
"v_accvgpr_write_b32 a49, v17\n"
"v_perm_b32 v18, v85, v81, s69\n"
"v_accvgpr_write_b32 a50, v18\n"
"v_perm_b32 v19, v93, v89, s69\n"
"v_accvgpr_write_b32 a51, v19\n"
"v_perm_b32 v4, v70, v66, s68\n"
"v_accvgpr_write_b32 a64, v4\n"
"v_perm_b32 v5, v78, v74, s68\n"
"v_accvgpr_write_b32 a65, v5\n"
"v_perm_b32 v6, v86, v82, s68\n"
"v_accvgpr_write_b32 a66, v6\n"
"v_perm_b32 v7, v94, v90, s68\n"
"v_accvgpr_write_b32 a67, v7\n"
"v_perm_b32 v8, v70, v66, s69\n"
"v_accvgpr_write_b32 a80, v8\n"
"v_perm_b32 v9, v78, v74, s69\n"
"v_accvgpr_write_b32 a81, v9\n"
"v_perm_b32 v10, v86, v82, s69\n"
"v_accvgpr_write_b32 a82, v10\n"
"v_perm_b32 v11, v94, v90, s69\n"
"v_accvgpr_write_b32 a83, v11\n"
"v_perm_b32 v12, v71, v67, s68\n"
"v_accvgpr_write_b32 a96, v12\n"
"v_perm_b32 v13, v79, v75, s68\n"
"v_accvgpr_write_b32 a97, v13\n"
"v_perm_b32 v14, v87, v83, s68\n"
"v_accvgpr_write_b32 a98, v14\n"
"v_perm_b32 v15, v95, v91, s68\n"
"v_accvgpr_write_b32 a99, v15\n"
"v_perm_b32 v16, v71, v67, s69\n"
"v_accvgpr_write_b32 a112, v16\n"
"v_perm_b32 v17, v79, v75, s69\n"
"v_accvgpr_write_b32 a113, v17\n"
"v_perm_b32 v18, v87, v83, s69\n"
"v_accvgpr_write_b32 a114, v18\n"
"v_perm_b32 v19, v95, v91, s69\n"
"v_accvgpr_write_b32 a115, v19\n"
"v_perm_b32 v4, v100, v96, s68\n"
"v_accvgpr_write_b32 a4, v4\n"
"v_perm_b32 v5, v108, v104, s68\n"
"v_accvgpr_write_b32 a5, v5\n"
"v_perm_b32 v6, v116, v112, s68\n"
"v_accvgpr_write_b32 a6, v6\n"
"v_perm_b32 v7, v124, v120, s68\n"
"v_accvgpr_write_b32 a7, v7\n"
"v_perm_b32 v8, v100, v96, s69\n"
"v_accvgpr_write_b32 a20, v8\n"
"v_perm_b32 v9, v108, v104, s69\n"
"v_accvgpr_write_b32 a21, v9\n"
"v_perm_b32 v10, v116, v112, s69\n"
"v_accvgpr_write_b32 a22, v10\n"
"v_perm_b32 v11, v124, v120, s69\n"
"v_accvgpr_write_b32 a23, v11\n"
"v_perm_b32 v12, v101, v97, s68\n"
"v_accvgpr_write_b32 a36, v12\n"
"v_perm_b32 v13, v109, v105, s68\n"
"v_accvgpr_write_b32 a37, v13\n"
"v_perm_b32 v14, v117, v113, s68\n"
"v_accvgpr_write_b32 a38, v14\n"
"v_perm_b32 v15, v125, v121, s68\n"
"v_accvgpr_write_b32 a39, v15\n"
"v_perm_b32 v16, v101, v97, s69\n"
"v_accvgpr_write_b32 a52, v16\n"
"v_perm_b32 v17, v109, v105, s69\n"
"v_accvgpr_write_b32 a53, v17\n"
"v_perm_b32 v18, v117, v113, s69\n"
"v_accvgpr_write_b32 a54, v18\n"
"v_perm_b32 v19, v125, v121, s69\n"
"v_accvgpr_write_b32 a55, v19\n"
"v_perm_b32 v4, v102, v98, s68\n"
"v_accvgpr_write_b32 a68, v4\n"
"v_perm_b32 v5, v110, v106, s68\n"
"v_accvgpr_write_b32 a69, v5\n"
"v_perm_b32 v6, v118, v114, s68\n"
"v_accvgpr_write_b32 a70, v6\n"
"v_perm_b32 v7, v126, v122, s68\n"
"v_accvgpr_write_b32 a71, v7\n"
"v_perm_b32 v8, v102, v98, s69\n"
"v_accvgpr_write_b32 a84, v8\n"
"v_perm_b32 v9, v110, v106, s69\n"
"v_accvgpr_write_b32 a85, v9\n"
"v_perm_b32 v10, v118, v114, s69\n"
"v_accvgpr_write_b32 a86, v10\n"
"v_perm_b32 v11, v126, v122, s69\n"
"v_accvgpr_write_b32 a87, v11\n"
"v_perm_b32 v12, v103, v99, s68\n"
"v_accvgpr_write_b32 a100, v12\n"
"v_perm_b32 v13, v111, v107, s68\n"
"v_accvgpr_write_b32 a101, v13\n"
"v_perm_b32 v14, v119, v115, s68\n"
"v_accvgpr_write_b32 a102, v14\n"
"v_perm_b32 v15, v127, v123, s68\n"
"v_accvgpr_write_b32 a103, v15\n"
"v_perm_b32 v16, v103, v99, s69\n"
"v_accvgpr_write_b32 a116, v16\n"
"v_perm_b32 v17, v111, v107, s69\n"
"v_accvgpr_write_b32 a117, v17\n"
"v_perm_b32 v18, v119, v115, s69\n"
"v_accvgpr_write_b32 a118, v18\n"
"v_perm_b32 v19, v127, v123, s69\n"
"v_accvgpr_write_b32 a119, v19\n"
"s_mov_b32 s40, s16\n"
"s_and_b32 s41, s17, 0xffff\n"
"s_mov_b32 s42, 0x400000\n"
"s_mov_b32 s43, 0x20000\n"
"v_and_b32 v57, 15, v0\n"
"v_lshrrev_b32 v62, 4, v0\n"
"v_lshlrev_b32 v62, 13, v62\n"
"v_lshl_add_u32 v62, v57, 4, v62\n"
"s_lshl_b32 s56, s50, 8\n"
"v_add_u32 v62, s56, v62\n"
"s_lshl_b32 s71, s48, 17\n"
"s_add_u32 s56, s71, 0x10000\n"
"buffer_load_dwordx4 v[64:67], v62, s[40:43], s56 offen nt\n"
"s_add_u32 s56, s71, 0x10400\n"
"buffer_load_dwordx4 v[68:71], v62, s[40:43], s56 offen nt\n"
"s_add_u32 s56, s71, 0x10800\n"
"buffer_load_dwordx4 v[72:75], v62, s[40:43], s56 offen nt\n"
"s_add_u32 s56, s71, 0x10c00\n"
"buffer_load_dwordx4 v[76:79], v62, s[40:43], s56 offen nt\n"
"s_add_u32 s56, s71, 0x11000\n"
"buffer_load_dwordx4 v[80:83], v62, s[40:43], s56 offen nt\n"
"s_add_u32 s56, s71, 0x11400\n"
"buffer_load_dwordx4 v[84:87], v62, s[40:43], s56 offen nt\n"
"s_add_u32 s56, s71, 0x11800\n"
"buffer_load_dwordx4 v[88:91], v62, s[40:43], s56 offen nt\n"
"s_add_u32 s56, s71, 0x11c00\n"
"buffer_load_dwordx4 v[92:95], v62, s[40:43], s56 offen nt\n"
"s_add_u32 s56, s71, 0x18000\n"
"buffer_load_dwordx4 v[96:99], v62, s[40:43], s56 offen nt\n"
"s_add_u32 s56, s71, 0x18400\n"
"buffer_load_dwordx4 v[100:103], v62, s[40:43], s56 offen nt\n"
"s_add_u32 s56, s71, 0x18800\n"
"buffer_load_dwordx4 v[104:107], v62, s[40:43], s56 offen nt\n"
"s_add_u32 s56, s71, 0x18c00\n"
"buffer_load_dwordx4 v[108:111], v62, s[40:43], s56 offen nt\n"
"s_add_u32 s56, s71, 0x19000\n"
"buffer_load_dwordx4 v[112:115], v62, s[40:43], s56 offen nt\n"
"s_add_u32 s56, s71, 0x19400\n"
"buffer_load_dwordx4 v[116:119], v62, s[40:43], s56 offen nt\n"
"s_add_u32 s56, s71, 0x19800\n"
"buffer_load_dwordx4 v[120:123], v62, s[40:43], s56 offen nt\n"
"s_add_u32 s56, s71, 0x19c00\n"
"buffer_load_dwordx4 v[124:127], v62, s[40:43], s56 offen nt\n"
"v_lshrrev_b32 v56, 4, v0\n"
"v_mul_u32_u24 v56, 16416, v56\n"
"v_lshl_add_u32 v56, v57, 1, v56\n"
"s_mul_i32 s56, s48, 86\n"
"s_and_b32 s56, s56, 15\n"
"s_mul_i32 s71, s50, 22\n"
"s_add_u32 s56, s56, s71\n"
"v_add_u32 v56, s56, v56\n"
"v_add_u32 v58, 65664, v56\n"
"s_cmp_eq_u32 s50, 3\n"
"s_cselect_b32 s56, 10, 11\n"
"v_cmp_gt_u32 vcc, s56, v57\n"
"s_nop 1\n"
"v_cndmask_b32 v61, 0, -1, vcc\n"
"s_waitcnt vmcnt(16)\n"
"s_barrier\n"
"ds_read_u16 v20, v56 offset:0\n"
"ds_read_u16 v21, v56 offset:128\n"
"ds_read_u16 v22, v56 offset:256\n"
"ds_read_u16 v23, v56 offset:384\n"
"ds_read_u16 v24, v56 offset:512\n"
"ds_read_u16 v25, v56 offset:640\n"
"ds_read_u16 v26, v56 offset:768\n"
"ds_read_u16 v27, v56 offset:896\n"
"ds_read_u16 v28, v58 offset:0\n"
"ds_read_u16 v29, v58 offset:128\n"
"ds_read_u16 v30, v58 offset:256\n"
"ds_read_u16 v31, v58 offset:384\n"
"ds_read_u16 v32, v58 offset:512\n"
"ds_read_u16 v33, v58 offset:640\n"
"ds_read_u16 v34, v58 offset:768\n"
"ds_read_u16 v35, v58 offset:896\n"
"s_waitcnt lgkmcnt(8)\n"
"v_lshl_or_b32 v4, v21, 16, v20\n"
"v_and_b32 v4, v4, v61\n"
"v_accvgpr_write_b32 a128, v4\n"
"v_lshl_or_b32 v5, v23, 16, v22\n"
"v_and_b32 v5, v5, v61\n"
"v_accvgpr_write_b32 a129, v5\n"
"v_lshl_or_b32 v6, v25, 16, v24\n"
"v_and_b32 v6, v6, v61\n"
"v_accvgpr_write_b32 a130, v6\n"
"v_lshl_or_b32 v7, v27, 16, v26\n"
"v_and_b32 v7, v7, v61\n"
"v_accvgpr_write_b32 a131, v7\n"
"ds_read_u16 v20, v56 offset:8192\n"
"ds_read_u16 v21, v56 offset:8320\n"
"ds_read_u16 v22, v56 offset:8448\n"
"ds_read_u16 v23, v56 offset:8576\n"
"ds_read_u16 v24, v56 offset:8704\n"
"ds_read_u16 v25, v56 offset:8832\n"
"ds_read_u16 v26, v56 offset:8960\n"
"ds_read_u16 v27, v56 offset:9088\n"
"s_waitcnt lgkmcnt(8)\n"
"v_lshl_or_b32 v8, v29, 16, v28\n"
"v_and_b32 v8, v8, v61\n"
"v_accvgpr_write_b32 a192, v8\n"
"v_lshl_or_b32 v9, v31, 16, v30\n"
"v_and_b32 v9, v9, v61\n"
"v_accvgpr_write_b32 a193, v9\n"
"v_lshl_or_b32 v10, v33, 16, v32\n"
"v_and_b32 v10, v10, v61\n"
"v_accvgpr_write_b32 a194, v10\n"
"v_lshl_or_b32 v11, v35, 16, v34\n"
"v_and_b32 v11, v11, v61\n"
"v_accvgpr_write_b32 a195, v11\n"
"ds_read_u16 v28, v58 offset:8192\n"
"ds_read_u16 v29, v58 offset:8320\n"
"ds_read_u16 v30, v58 offset:8448\n"
"ds_read_u16 v31, v58 offset:8576\n"
"ds_read_u16 v32, v58 offset:8704\n"
"ds_read_u16 v33, v58 offset:8832\n"
"ds_read_u16 v34, v58 offset:8960\n"
"ds_read_u16 v35, v58 offset:9088\n"
"s_waitcnt lgkmcnt(8)\n"
"v_lshl_or_b32 v12, v21, 16, v20\n"
"v_and_b32 v12, v12, v61\n"
"v_accvgpr_write_b32 a132, v12\n"
"v_lshl_or_b32 v13, v23, 16, v22\n"
"v_and_b32 v13, v13, v61\n"
"v_accvgpr_write_b32 a133, v13\n"
"v_lshl_or_b32 v14, v25, 16, v24\n"
"v_and_b32 v14, v14, v61\n"
"v_accvgpr_write_b32 a134, v14\n"
"v_lshl_or_b32 v15, v27, 16, v26\n"
"v_and_b32 v15, v15, v61\n"
"v_accvgpr_write_b32 a135, v15\n"
"ds_read_u16 v20, v56 offset:1024\n"
"ds_read_u16 v21, v56 offset:1152\n"
"ds_read_u16 v22, v56 offset:1280\n"
"ds_read_u16 v23, v56 offset:1408\n"
"ds_read_u16 v24, v56 offset:1536\n"
"ds_read_u16 v25, v56 offset:1664\n"
"ds_read_u16 v26, v56 offset:1792\n"
"ds_read_u16 v27, v56 offset:1920\n"
"s_waitcnt lgkmcnt(8)\n"
"v_lshl_or_b32 v16, v29, 16, v28\n"
"v_and_b32 v16, v16, v61\n"
"v_accvgpr_write_b32 a196, v16\n"
"v_lshl_or_b32 v17, v31, 16, v30\n"
"v_and_b32 v17, v17, v61\n"
"v_accvgpr_write_b32 a197, v17\n"
"v_lshl_or_b32 v18, v33, 16, v32\n"
"v_and_b32 v18, v18, v61\n"
"v_accvgpr_write_b32 a198, v18\n"
"v_lshl_or_b32 v19, v35, 16, v34\n"
"v_and_b32 v19, v19, v61\n"
"v_accvgpr_write_b32 a199, v19\n"
"ds_read_u16 v28, v58 offset:1024\n"
"ds_read_u16 v29, v58 offset:1152\n"
"ds_read_u16 v30, v58 offset:1280\n"
"ds_read_u16 v31, v58 offset:1408\n"
"ds_read_u16 v32, v58 offset:1536\n"
"ds_read_u16 v33, v58 offset:1664\n"
"ds_read_u16 v34, v58 offset:1792\n"
"ds_read_u16 v35, v58 offset:1920\n"
"s_waitcnt lgkmcnt(8)\n"
"v_lshl_or_b32 v4, v21, 16, v20\n"
"v_and_b32 v4, v4, v61\n"
"v_accvgpr_write_b32 a136, v4\n"
"v_lshl_or_b32 v5, v23, 16, v22\n"
"v_and_b32 v5, v5, v61\n"
"v_accvgpr_write_b32 a137, v5\n"
"v_lshl_or_b32 v6, v25, 16, v24\n"
"v_and_b32 v6, v6, v61\n"
"v_accvgpr_write_b32 a138, v6\n"
"v_lshl_or_b32 v7, v27, 16, v26\n"
"v_and_b32 v7, v7, v61\n"
"v_accvgpr_write_b32 a139, v7\n"
"ds_read_u16 v20, v56 offset:9216\n"
"ds_read_u16 v21, v56 offset:9344\n"
"ds_read_u16 v22, v56 offset:9472\n"
"ds_read_u16 v23, v56 offset:9600\n"
"ds_read_u16 v24, v56 offset:9728\n"
"ds_read_u16 v25, v56 offset:9856\n"
"ds_read_u16 v26, v56 offset:9984\n"
"ds_read_u16 v27, v56 offset:10112\n"
"s_waitcnt lgkmcnt(8)\n"
"v_lshl_or_b32 v8, v29, 16, v28\n"
"v_and_b32 v8, v8, v61\n"
"v_accvgpr_write_b32 a200, v8\n"
"v_lshl_or_b32 v9, v31, 16, v30\n"
"v_and_b32 v9, v9, v61\n"
"v_accvgpr_write_b32 a201, v9\n"
"v_lshl_or_b32 v10, v33, 16, v32\n"
"v_and_b32 v10, v10, v61\n"
"v_accvgpr_write_b32 a202, v10\n"
"v_lshl_or_b32 v11, v35, 16, v34\n"
"v_and_b32 v11, v11, v61\n"
"v_accvgpr_write_b32 a203, v11\n"
"ds_read_u16 v28, v58 offset:9216\n"
"ds_read_u16 v29, v58 offset:9344\n"
"ds_read_u16 v30, v58 offset:9472\n"
"ds_read_u16 v31, v58 offset:9600\n"
"ds_read_u16 v32, v58 offset:9728\n"
"ds_read_u16 v33, v58 offset:9856\n"
"ds_read_u16 v34, v58 offset:9984\n"
"ds_read_u16 v35, v58 offset:10112\n"
"s_waitcnt lgkmcnt(8)\n"
"v_lshl_or_b32 v12, v21, 16, v20\n"
"v_and_b32 v12, v12, v61\n"
"v_accvgpr_write_b32 a140, v12\n"
"v_lshl_or_b32 v13, v23, 16, v22\n"
"v_and_b32 v13, v13, v61\n"
"v_accvgpr_write_b32 a141, v13\n"
"v_lshl_or_b32 v14, v25, 16, v24\n"
"v_and_b32 v14, v14, v61\n"
"v_accvgpr_write_b32 a142, v14\n"
"v_lshl_or_b32 v15, v27, 16, v26\n"
"v_and_b32 v15, v15, v61\n"
"v_accvgpr_write_b32 a143, v15\n"
"ds_read_u16 v20, v56 offset:2048\n"
"ds_read_u16 v21, v56 offset:2176\n"
"ds_read_u16 v22, v56 offset:2304\n"
"ds_read_u16 v23, v56 offset:2432\n"
"ds_read_u16 v24, v56 offset:2560\n"
"ds_read_u16 v25, v56 offset:2688\n"
"ds_read_u16 v26, v56 offset:2816\n"
"ds_read_u16 v27, v56 offset:2944\n"
"s_waitcnt lgkmcnt(8)\n"
"v_lshl_or_b32 v16, v29, 16, v28\n"
"v_and_b32 v16, v16, v61\n"
"v_accvgpr_write_b32 a204, v16\n"
"v_lshl_or_b32 v17, v31, 16, v30\n"
"v_and_b32 v17, v17, v61\n"
"v_accvgpr_write_b32 a205, v17\n"
"v_lshl_or_b32 v18, v33, 16, v32\n"
"v_and_b32 v18, v18, v61\n"
"v_accvgpr_write_b32 a206, v18\n"
"v_lshl_or_b32 v19, v35, 16, v34\n"
"v_and_b32 v19, v19, v61\n"
"v_accvgpr_write_b32 a207, v19\n"
"ds_read_u16 v28, v58 offset:2048\n"
"ds_read_u16 v29, v58 offset:2176\n"
"ds_read_u16 v30, v58 offset:2304\n"
"ds_read_u16 v31, v58 offset:2432\n"
"ds_read_u16 v32, v58 offset:2560\n"
"ds_read_u16 v33, v58 offset:2688\n"
"ds_read_u16 v34, v58 offset:2816\n"
"ds_read_u16 v35, v58 offset:2944\n"
"s_waitcnt lgkmcnt(8)\n"
"v_lshl_or_b32 v4, v21, 16, v20\n"
"v_and_b32 v4, v4, v61\n"
"v_accvgpr_write_b32 a144, v4\n"
"v_lshl_or_b32 v5, v23, 16, v22\n"
"v_and_b32 v5, v5, v61\n"
"v_accvgpr_write_b32 a145, v5\n"
"v_lshl_or_b32 v6, v25, 16, v24\n"
"v_and_b32 v6, v6, v61\n"
"v_accvgpr_write_b32 a146, v6\n"
"v_lshl_or_b32 v7, v27, 16, v26\n"
"v_and_b32 v7, v7, v61\n"
"v_accvgpr_write_b32 a147, v7\n"
"ds_read_u16 v20, v56 offset:10240\n"
"ds_read_u16 v21, v56 offset:10368\n"
"ds_read_u16 v22, v56 offset:10496\n"
"ds_read_u16 v23, v56 offset:10624\n"
"ds_read_u16 v24, v56 offset:10752\n"
"ds_read_u16 v25, v56 offset:10880\n"
"ds_read_u16 v26, v56 offset:11008\n"
"ds_read_u16 v27, v56 offset:11136\n"
"s_waitcnt lgkmcnt(8)\n"
"v_lshl_or_b32 v8, v29, 16, v28\n"
"v_and_b32 v8, v8, v61\n"
"v_accvgpr_write_b32 a208, v8\n"
"v_lshl_or_b32 v9, v31, 16, v30\n"
"v_and_b32 v9, v9, v61\n"
"v_accvgpr_write_b32 a209, v9\n"
"v_lshl_or_b32 v10, v33, 16, v32\n"
"v_and_b32 v10, v10, v61\n"
"v_accvgpr_write_b32 a210, v10\n"
"v_lshl_or_b32 v11, v35, 16, v34\n"
"v_and_b32 v11, v11, v61\n"
"v_accvgpr_write_b32 a211, v11\n"
"ds_read_u16 v28, v58 offset:10240\n"
"ds_read_u16 v29, v58 offset:10368\n"
"ds_read_u16 v30, v58 offset:10496\n"
"ds_read_u16 v31, v58 offset:10624\n"
"ds_read_u16 v32, v58 offset:10752\n"
"ds_read_u16 v33, v58 offset:10880\n"
"ds_read_u16 v34, v58 offset:11008\n"
"ds_read_u16 v35, v58 offset:11136\n"
"s_waitcnt lgkmcnt(8)\n"
"v_lshl_or_b32 v12, v21, 16, v20\n"
"v_and_b32 v12, v12, v61\n"
"v_accvgpr_write_b32 a148, v12\n"
"v_lshl_or_b32 v13, v23, 16, v22\n"
"v_and_b32 v13, v13, v61\n"
"v_accvgpr_write_b32 a149, v13\n"
"v_lshl_or_b32 v14, v25, 16, v24\n"
"v_and_b32 v14, v14, v61\n"
"v_accvgpr_write_b32 a150, v14\n"
"v_lshl_or_b32 v15, v27, 16, v26\n"
"v_and_b32 v15, v15, v61\n"
"v_accvgpr_write_b32 a151, v15\n"
"ds_read_u16 v20, v56 offset:3072\n"
"ds_read_u16 v21, v56 offset:3200\n"
"ds_read_u16 v22, v56 offset:3328\n"
"ds_read_u16 v23, v56 offset:3456\n"
"ds_read_u16 v24, v56 offset:3584\n"
"ds_read_u16 v25, v56 offset:3712\n"
"ds_read_u16 v26, v56 offset:3840\n"
"ds_read_u16 v27, v56 offset:3968\n"
"s_waitcnt lgkmcnt(8)\n"
"v_lshl_or_b32 v16, v29, 16, v28\n"
"v_and_b32 v16, v16, v61\n"
"v_accvgpr_write_b32 a212, v16\n"
"v_lshl_or_b32 v17, v31, 16, v30\n"
"v_and_b32 v17, v17, v61\n"
"v_accvgpr_write_b32 a213, v17\n"
"v_lshl_or_b32 v18, v33, 16, v32\n"
"v_and_b32 v18, v18, v61\n"
"v_accvgpr_write_b32 a214, v18\n"
"v_lshl_or_b32 v19, v35, 16, v34\n"
"v_and_b32 v19, v19, v61\n"
"v_accvgpr_write_b32 a215, v19\n"
"ds_read_u16 v28, v58 offset:3072\n"
"ds_read_u16 v29, v58 offset:3200\n"
"ds_read_u16 v30, v58 offset:3328\n"
"ds_read_u16 v31, v58 offset:3456\n"
"ds_read_u16 v32, v58 offset:3584\n"
"ds_read_u16 v33, v58 offset:3712\n"
"ds_read_u16 v34, v58 offset:3840\n"
"ds_read_u16 v35, v58 offset:3968\n"
"s_waitcnt lgkmcnt(8)\n"
"v_lshl_or_b32 v4, v21, 16, v20\n"
"v_and_b32 v4, v4, v61\n"
"v_accvgpr_write_b32 a152, v4\n"
"v_lshl_or_b32 v5, v23, 16, v22\n"
"v_and_b32 v5, v5, v61\n"
"v_accvgpr_write_b32 a153, v5\n"
"v_lshl_or_b32 v6, v25, 16, v24\n"
"v_and_b32 v6, v6, v61\n"
"v_accvgpr_write_b32 a154, v6\n"
"v_lshl_or_b32 v7, v27, 16, v26\n"
"v_and_b32 v7, v7, v61\n"
"v_accvgpr_write_b32 a155, v7\n"
"ds_read_u16 v20, v56 offset:11264\n"
"ds_read_u16 v21, v56 offset:11392\n"
"ds_read_u16 v22, v56 offset:11520\n"
"ds_read_u16 v23, v56 offset:11648\n"
"ds_read_u16 v24, v56 offset:11776\n"
"ds_read_u16 v25, v56 offset:11904\n"
"ds_read_u16 v26, v56 offset:12032\n"
"ds_read_u16 v27, v56 offset:12160\n"
"s_waitcnt lgkmcnt(8)\n"
"v_lshl_or_b32 v8, v29, 16, v28\n"
"v_and_b32 v8, v8, v61\n"
"v_accvgpr_write_b32 a216, v8\n"
"v_lshl_or_b32 v9, v31, 16, v30\n"
"v_and_b32 v9, v9, v61\n"
"v_accvgpr_write_b32 a217, v9\n"
"v_lshl_or_b32 v10, v33, 16, v32\n"
"v_and_b32 v10, v10, v61\n"
"v_accvgpr_write_b32 a218, v10\n"
"v_lshl_or_b32 v11, v35, 16, v34\n"
"v_and_b32 v11, v11, v61\n"
"v_accvgpr_write_b32 a219, v11\n"
"ds_read_u16 v28, v58 offset:11264\n"
"ds_read_u16 v29, v58 offset:11392\n"
"ds_read_u16 v30, v58 offset:11520\n"
"ds_read_u16 v31, v58 offset:11648\n"
"ds_read_u16 v32, v58 offset:11776\n"
"ds_read_u16 v33, v58 offset:11904\n"
"ds_read_u16 v34, v58 offset:12032\n"
"ds_read_u16 v35, v58 offset:12160\n"
"s_waitcnt lgkmcnt(8)\n"
"v_lshl_or_b32 v12, v21, 16, v20\n"
"v_and_b32 v12, v12, v61\n"
"v_accvgpr_write_b32 a156, v12\n"
"v_lshl_or_b32 v13, v23, 16, v22\n"
"v_and_b32 v13, v13, v61\n"
"v_accvgpr_write_b32 a157, v13\n"
"v_lshl_or_b32 v14, v25, 16, v24\n"
"v_and_b32 v14, v14, v61\n"
"v_accvgpr_write_b32 a158, v14\n"
"v_lshl_or_b32 v15, v27, 16, v26\n"
"v_and_b32 v15, v15, v61\n"
"v_accvgpr_write_b32 a159, v15\n"
"ds_read_u16 v20, v56 offset:4096\n"
"ds_read_u16 v21, v56 offset:4224\n"
"ds_read_u16 v22, v56 offset:4352\n"
"ds_read_u16 v23, v56 offset:4480\n"
"ds_read_u16 v24, v56 offset:4608\n"
"ds_read_u16 v25, v56 offset:4736\n"
"ds_read_u16 v26, v56 offset:4864\n"
"ds_read_u16 v27, v56 offset:4992\n"
"s_waitcnt lgkmcnt(8)\n"
"v_lshl_or_b32 v16, v29, 16, v28\n"
"v_and_b32 v16, v16, v61\n"
"v_accvgpr_write_b32 a220, v16\n"
"v_lshl_or_b32 v17, v31, 16, v30\n"
"v_and_b32 v17, v17, v61\n"
"v_accvgpr_write_b32 a221, v17\n"
"v_lshl_or_b32 v18, v33, 16, v32\n"
"v_and_b32 v18, v18, v61\n"
"v_accvgpr_write_b32 a222, v18\n"
"v_lshl_or_b32 v19, v35, 16, v34\n"
"v_and_b32 v19, v19, v61\n"
"v_accvgpr_write_b32 a223, v19\n"
"ds_read_u16 v28, v58 offset:4096\n"
"ds_read_u16 v29, v58 offset:4224\n"
"ds_read_u16 v30, v58 offset:4352\n"
"ds_read_u16 v31, v58 offset:4480\n"
"ds_read_u16 v32, v58 offset:4608\n"
"ds_read_u16 v33, v58 offset:4736\n"
"ds_read_u16 v34, v58 offset:4864\n"
"ds_read_u16 v35, v58 offset:4992\n"
"s_waitcnt lgkmcnt(8)\n"
"v_lshl_or_b32 v4, v21, 16, v20\n"
"v_and_b32 v4, v4, v61\n"
"v_accvgpr_write_b32 a160, v4\n"
"v_lshl_or_b32 v5, v23, 16, v22\n"
"v_and_b32 v5, v5, v61\n"
"v_accvgpr_write_b32 a161, v5\n"
"v_lshl_or_b32 v6, v25, 16, v24\n"
"v_and_b32 v6, v6, v61\n"
"v_accvgpr_write_b32 a162, v6\n"
"v_lshl_or_b32 v7, v27, 16, v26\n"
"v_and_b32 v7, v7, v61\n"
"v_accvgpr_write_b32 a163, v7\n"
"ds_read_u16 v20, v56 offset:12288\n"
"ds_read_u16 v21, v56 offset:12416\n"
"ds_read_u16 v22, v56 offset:12544\n"
"ds_read_u16 v23, v56 offset:12672\n"
"ds_read_u16 v24, v56 offset:12800\n"
"ds_read_u16 v25, v56 offset:12928\n"
"ds_read_u16 v26, v56 offset:13056\n"
"ds_read_u16 v27, v56 offset:13184\n"
"s_waitcnt lgkmcnt(8)\n"
"v_lshl_or_b32 v8, v29, 16, v28\n"
"v_and_b32 v8, v8, v61\n"
"v_accvgpr_write_b32 a224, v8\n"
"v_lshl_or_b32 v9, v31, 16, v30\n"
"v_and_b32 v9, v9, v61\n"
"v_accvgpr_write_b32 a225, v9\n"
"v_lshl_or_b32 v10, v33, 16, v32\n"
"v_and_b32 v10, v10, v61\n"
"v_accvgpr_write_b32 a226, v10\n"
"v_lshl_or_b32 v11, v35, 16, v34\n"
"v_and_b32 v11, v11, v61\n"
"v_accvgpr_write_b32 a227, v11\n"
"ds_read_u16 v28, v58 offset:12288\n"
"ds_read_u16 v29, v58 offset:12416\n"
"ds_read_u16 v30, v58 offset:12544\n"
"ds_read_u16 v31, v58 offset:12672\n"
"ds_read_u16 v32, v58 offset:12800\n"
"ds_read_u16 v33, v58 offset:12928\n"
"ds_read_u16 v34, v58 offset:13056\n"
"ds_read_u16 v35, v58 offset:13184\n"
"s_waitcnt lgkmcnt(8)\n"
"v_lshl_or_b32 v12, v21, 16, v20\n"
"v_and_b32 v12, v12, v61\n"
"v_accvgpr_write_b32 a164, v12\n"
"v_lshl_or_b32 v13, v23, 16, v22\n"
"v_and_b32 v13, v13, v61\n"
"v_accvgpr_write_b32 a165, v13\n"
"v_lshl_or_b32 v14, v25, 16, v24\n"
"v_and_b32 v14, v14, v61\n"
"v_accvgpr_write_b32 a166, v14\n"
"v_lshl_or_b32 v15, v27, 16, v26\n"
"v_and_b32 v15, v15, v61\n"
"v_accvgpr_write_b32 a167, v15\n"
"ds_read_u16 v20, v56 offset:5120\n"
"ds_read_u16 v21, v56 offset:5248\n"
"ds_read_u16 v22, v56 offset:5376\n"
"ds_read_u16 v23, v56 offset:5504\n"
"ds_read_u16 v24, v56 offset:5632\n"
"ds_read_u16 v25, v56 offset:5760\n"
"ds_read_u16 v26, v56 offset:5888\n"
"ds_read_u16 v27, v56 offset:6016\n"
"s_waitcnt lgkmcnt(8)\n"
"v_lshl_or_b32 v16, v29, 16, v28\n"
"v_and_b32 v16, v16, v61\n"
"v_accvgpr_write_b32 a228, v16\n"
"v_lshl_or_b32 v17, v31, 16, v30\n"
"v_and_b32 v17, v17, v61\n"
"v_accvgpr_write_b32 a229, v17\n"
"v_lshl_or_b32 v18, v33, 16, v32\n"
"v_and_b32 v18, v18, v61\n"
"v_accvgpr_write_b32 a230, v18\n"
"v_lshl_or_b32 v19, v35, 16, v34\n"
"v_and_b32 v19, v19, v61\n"
"v_accvgpr_write_b32 a231, v19\n"
"ds_read_u16 v28, v58 offset:5120\n"
"ds_read_u16 v29, v58 offset:5248\n"
"ds_read_u16 v30, v58 offset:5376\n"
"ds_read_u16 v31, v58 offset:5504\n"
"ds_read_u16 v32, v58 offset:5632\n"
"ds_read_u16 v33, v58 offset:5760\n"
"ds_read_u16 v34, v58 offset:5888\n"
"ds_read_u16 v35, v58 offset:6016\n"
"s_waitcnt lgkmcnt(8)\n"
"v_lshl_or_b32 v4, v21, 16, v20\n"
"v_and_b32 v4, v4, v61\n"
"v_accvgpr_write_b32 a168, v4\n"
"v_lshl_or_b32 v5, v23, 16, v22\n"
"v_and_b32 v5, v5, v61\n"
"v_accvgpr_write_b32 a169, v5\n"
"v_lshl_or_b32 v6, v25, 16, v24\n"
"v_and_b32 v6, v6, v61\n"
"v_accvgpr_write_b32 a170, v6\n"
"v_lshl_or_b32 v7, v27, 16, v26\n"
"v_and_b32 v7, v7, v61\n"
"v_accvgpr_write_b32 a171, v7\n"
"ds_read_u16 v20, v56 offset:13312\n"
"ds_read_u16 v21, v56 offset:13440\n"
"ds_read_u16 v22, v56 offset:13568\n"
"ds_read_u16 v23, v56 offset:13696\n"
"ds_read_u16 v24, v56 offset:13824\n"
"ds_read_u16 v25, v56 offset:13952\n"
"ds_read_u16 v26, v56 offset:14080\n"
"ds_read_u16 v27, v56 offset:14208\n"
"s_waitcnt lgkmcnt(8)\n"
"v_lshl_or_b32 v8, v29, 16, v28\n"
"v_and_b32 v8, v8, v61\n"
"v_accvgpr_write_b32 a232, v8\n"
"v_lshl_or_b32 v9, v31, 16, v30\n"
"v_and_b32 v9, v9, v61\n"
"v_accvgpr_write_b32 a233, v9\n"
"v_lshl_or_b32 v10, v33, 16, v32\n"
"v_and_b32 v10, v10, v61\n"
"v_accvgpr_write_b32 a234, v10\n"
"v_lshl_or_b32 v11, v35, 16, v34\n"
"v_and_b32 v11, v11, v61\n"
"v_accvgpr_write_b32 a235, v11\n"
"ds_read_u16 v28, v58 offset:13312\n"
"ds_read_u16 v29, v58 offset:13440\n"
"ds_read_u16 v30, v58 offset:13568\n"
"ds_read_u16 v31, v58 offset:13696\n"
"ds_read_u16 v32, v58 offset:13824\n"
"ds_read_u16 v33, v58 offset:13952\n"
"ds_read_u16 v34, v58 offset:14080\n"
"ds_read_u16 v35, v58 offset:14208\n"
"s_waitcnt lgkmcnt(8)\n"
"v_lshl_or_b32 v12, v21, 16, v20\n"
"v_and_b32 v12, v12, v61\n"
"v_accvgpr_write_b32 a172, v12\n"
"v_lshl_or_b32 v13, v23, 16, v22\n"
"v_and_b32 v13, v13, v61\n"
"v_accvgpr_write_b32 a173, v13\n"
"v_lshl_or_b32 v14, v25, 16, v24\n"
"v_and_b32 v14, v14, v61\n"
"v_accvgpr_write_b32 a174, v14\n"
"v_lshl_or_b32 v15, v27, 16, v26\n"
"v_and_b32 v15, v15, v61\n"
"v_accvgpr_write_b32 a175, v15\n"
"ds_read_u16 v20, v56 offset:6144\n"
"ds_read_u16 v21, v56 offset:6272\n"
"ds_read_u16 v22, v56 offset:6400\n"
"ds_read_u16 v23, v56 offset:6528\n"
"ds_read_u16 v24, v56 offset:6656\n"
"ds_read_u16 v25, v56 offset:6784\n"
"ds_read_u16 v26, v56 offset:6912\n"
"ds_read_u16 v27, v56 offset:7040\n"
"s_waitcnt lgkmcnt(8)\n"
"v_lshl_or_b32 v16, v29, 16, v28\n"
"v_and_b32 v16, v16, v61\n"
"v_accvgpr_write_b32 a236, v16\n"
"v_lshl_or_b32 v17, v31, 16, v30\n"
"v_and_b32 v17, v17, v61\n"
"v_accvgpr_write_b32 a237, v17\n"
"v_lshl_or_b32 v18, v33, 16, v32\n"
"v_and_b32 v18, v18, v61\n"
"v_accvgpr_write_b32 a238, v18\n"
"v_lshl_or_b32 v19, v35, 16, v34\n"
"v_and_b32 v19, v19, v61\n"
"v_accvgpr_write_b32 a239, v19\n"
"ds_read_u16 v28, v58 offset:6144\n"
"ds_read_u16 v29, v58 offset:6272\n"
"ds_read_u16 v30, v58 offset:6400\n"
"ds_read_u16 v31, v58 offset:6528\n"
"ds_read_u16 v32, v58 offset:6656\n"
"ds_read_u16 v33, v58 offset:6784\n"
"ds_read_u16 v34, v58 offset:6912\n"
"ds_read_u16 v35, v58 offset:7040\n"
"s_waitcnt lgkmcnt(8)\n"
"v_lshl_or_b32 v4, v21, 16, v20\n"
"v_and_b32 v4, v4, v61\n"
"v_accvgpr_write_b32 a176, v4\n"
"v_lshl_or_b32 v5, v23, 16, v22\n"
"v_and_b32 v5, v5, v61\n"
"v_accvgpr_write_b32 a177, v5\n"
"v_lshl_or_b32 v6, v25, 16, v24\n"
"v_and_b32 v6, v6, v61\n"
"v_accvgpr_write_b32 a178, v6\n"
"v_lshl_or_b32 v7, v27, 16, v26\n"
"v_and_b32 v7, v7, v61\n"
"v_accvgpr_write_b32 a179, v7\n"
"ds_read_u16 v20, v56 offset:14336\n"
"ds_read_u16 v21, v56 offset:14464\n"
"ds_read_u16 v22, v56 offset:14592\n"
"ds_read_u16 v23, v56 offset:14720\n"
"ds_read_u16 v24, v56 offset:14848\n"
"ds_read_u16 v25, v56 offset:14976\n"
"ds_read_u16 v26, v56 offset:15104\n"
"ds_read_u16 v27, v56 offset:15232\n"
"s_waitcnt lgkmcnt(8)\n"
"v_lshl_or_b32 v8, v29, 16, v28\n"
"v_and_b32 v8, v8, v61\n"
"v_accvgpr_write_b32 a240, v8\n"
"v_lshl_or_b32 v9, v31, 16, v30\n"
"v_and_b32 v9, v9, v61\n"
"v_accvgpr_write_b32 a241, v9\n"
"v_lshl_or_b32 v10, v33, 16, v32\n"
"v_and_b32 v10, v10, v61\n"
"v_accvgpr_write_b32 a242, v10\n"
"v_lshl_or_b32 v11, v35, 16, v34\n"
"v_and_b32 v11, v11, v61\n"
"v_accvgpr_write_b32 a243, v11\n"
"ds_read_u16 v28, v58 offset:14336\n"
"ds_read_u16 v29, v58 offset:14464\n"
"ds_read_u16 v30, v58 offset:14592\n"
"ds_read_u16 v31, v58 offset:14720\n"
"ds_read_u16 v32, v58 offset:14848\n"
"ds_read_u16 v33, v58 offset:14976\n"
"ds_read_u16 v34, v58 offset:15104\n"
"ds_read_u16 v35, v58 offset:15232\n"
"s_waitcnt lgkmcnt(8)\n"
"v_lshl_or_b32 v12, v21, 16, v20\n"
"v_and_b32 v12, v12, v61\n"
"v_accvgpr_write_b32 a180, v12\n"
"v_lshl_or_b32 v13, v23, 16, v22\n"
"v_and_b32 v13, v13, v61\n"
"v_accvgpr_write_b32 a181, v13\n"
"v_lshl_or_b32 v14, v25, 16, v24\n"
"v_and_b32 v14, v14, v61\n"
"v_accvgpr_write_b32 a182, v14\n"
"v_lshl_or_b32 v15, v27, 16, v26\n"
"v_and_b32 v15, v15, v61\n"
"v_accvgpr_write_b32 a183, v15\n"
"ds_read_u16 v20, v56 offset:7168\n"
"ds_read_u16 v21, v56 offset:7296\n"
"ds_read_u16 v22, v56 offset:7424\n"
"ds_read_u16 v23, v56 offset:7552\n"
"ds_read_u16 v24, v56 offset:7680\n"
"ds_read_u16 v25, v56 offset:7808\n"
"ds_read_u16 v26, v56 offset:7936\n"
"ds_read_u16 v27, v56 offset:8064\n"
"s_waitcnt lgkmcnt(8)\n"
"v_lshl_or_b32 v16, v29, 16, v28\n"
"v_and_b32 v16, v16, v61\n"
"v_accvgpr_write_b32 a244, v16\n"
"v_lshl_or_b32 v17, v31, 16, v30\n"
"v_and_b32 v17, v17, v61\n"
"v_accvgpr_write_b32 a245, v17\n"
"v_lshl_or_b32 v18, v33, 16, v32\n"
"v_and_b32 v18, v18, v61\n"
"v_accvgpr_write_b32 a246, v18\n"
"v_lshl_or_b32 v19, v35, 16, v34\n"
"v_and_b32 v19, v19, v61\n"
"v_accvgpr_write_b32 a247, v19\n"
"ds_read_u16 v28, v58 offset:7168\n"
"ds_read_u16 v29, v58 offset:7296\n"
"ds_read_u16 v30, v58 offset:7424\n"
"ds_read_u16 v31, v58 offset:7552\n"
"ds_read_u16 v32, v58 offset:7680\n"
"ds_read_u16 v33, v58 offset:7808\n"
"ds_read_u16 v34, v58 offset:7936\n"
"ds_read_u16 v35, v58 offset:8064\n"
"s_waitcnt lgkmcnt(8)\n"
"v_lshl_or_b32 v4, v21, 16, v20\n"
"v_and_b32 v4, v4, v61\n"
"v_accvgpr_write_b32 a184, v4\n"
"v_lshl_or_b32 v5, v23, 16, v22\n"
"v_and_b32 v5, v5, v61\n"
"v_accvgpr_write_b32 a185, v5\n"
"v_lshl_or_b32 v6, v25, 16, v24\n"
"v_and_b32 v6, v6, v61\n"
"v_accvgpr_write_b32 a186, v6\n"
"v_lshl_or_b32 v7, v27, 16, v26\n"
"v_and_b32 v7, v7, v61\n"
"v_accvgpr_write_b32 a187, v7\n"
"ds_read_u16 v20, v56 offset:15360\n"
"ds_read_u16 v21, v56 offset:15488\n"
"ds_read_u16 v22, v56 offset:15616\n"
"ds_read_u16 v23, v56 offset:15744\n"
"ds_read_u16 v24, v56 offset:15872\n"
"ds_read_u16 v25, v56 offset:16000\n"
"ds_read_u16 v26, v56 offset:16128\n"
"ds_read_u16 v27, v56 offset:16256\n"
"s_waitcnt lgkmcnt(8)\n"
"v_lshl_or_b32 v8, v29, 16, v28\n"
"v_and_b32 v8, v8, v61\n"
"v_accvgpr_write_b32 a248, v8\n"
"v_lshl_or_b32 v9, v31, 16, v30\n"
"v_and_b32 v9, v9, v61\n"
"v_accvgpr_write_b32 a249, v9\n"
"v_lshl_or_b32 v10, v33, 16, v32\n"
"v_and_b32 v10, v10, v61\n"
"v_accvgpr_write_b32 a250, v10\n"
"v_lshl_or_b32 v11, v35, 16, v34\n"
"v_and_b32 v11, v11, v61\n"
"v_accvgpr_write_b32 a251, v11\n"
"ds_read_u16 v28, v58 offset:15360\n"
"ds_read_u16 v29, v58 offset:15488\n"
"ds_read_u16 v30, v58 offset:15616\n"
"ds_read_u16 v31, v58 offset:15744\n"
"ds_read_u16 v32, v58 offset:15872\n"
"ds_read_u16 v33, v58 offset:16000\n"
"ds_read_u16 v34, v58 offset:16128\n"
"ds_read_u16 v35, v58 offset:16256\n"
"s_waitcnt lgkmcnt(8)\n"
"v_lshl_or_b32 v12, v21, 16, v20\n"
"v_and_b32 v12, v12, v61\n"
"v_accvgpr_write_b32 a188, v12\n"
"v_lshl_or_b32 v13, v23, 16, v22\n"
"v_and_b32 v13, v13, v61\n"
"v_accvgpr_write_b32 a189, v13\n"
"v_lshl_or_b32 v14, v25, 16, v24\n"
"v_and_b32 v14, v14, v61\n"
"v_accvgpr_write_b32 a190, v14\n"
"v_lshl_or_b32 v15, v27, 16, v26\n"
"v_and_b32 v15, v15, v61\n"
"v_accvgpr_write_b32 a191, v15\n"
"s_waitcnt lgkmcnt(0)\n"
"v_lshl_or_b32 v16, v29, 16, v28\n"
"v_and_b32 v16, v16, v61\n"
"v_accvgpr_write_b32 a252, v16\n"
"v_lshl_or_b32 v17, v31, 16, v30\n"
"v_and_b32 v17, v17, v61\n"
"v_accvgpr_write_b32 a253, v17\n"
"v_lshl_or_b32 v18, v33, 16, v32\n"
"v_and_b32 v18, v18, v61\n"
"v_accvgpr_write_b32 a254, v18\n"
"v_lshl_or_b32 v19, v35, 16, v34\n"
"v_and_b32 v19, v19, v61\n"
"v_accvgpr_write_b32 a255, v19\n"
"s_barrier\n"
"s_lshl_b32 s56, s50, 14\n"
"v_add_u32 v59, s56, v2\n"
"ds_write_b128 v59, v[192:195] offset:0\n"
"ds_write_b128 v59, v[196:199] offset:1024\n"
"ds_write_b128 v59, v[200:203] offset:2048\n"
"ds_write_b128 v59, v[204:207] offset:3072\n"
"ds_write_b128 v59, v[208:211] offset:4096\n"
"ds_write_b128 v59, v[212:215] offset:5120\n"
"ds_write_b128 v59, v[216:219] offset:6144\n"
"ds_write_b128 v59, v[220:223] offset:7168\n"
"ds_write_b128 v59, v[224:227] offset:8192\n"
"ds_write_b128 v59, v[228:231] offset:9216\n"
"ds_write_b128 v59, v[232:235] offset:10240\n"
"ds_write_b128 v59, v[236:239] offset:11264\n"
"ds_write_b128 v59, v[240:243] offset:12288\n"
"ds_write_b128 v59, v[244:247] offset:13312\n"
"ds_write_b128 v59, v[248:251] offset:14336\n"
"ds_write_b128 v59, v[252:255] offset:15360\n"
"s_waitcnt lgkmcnt(0)\n"
"s_mul_i32 s53, s50, 1024\n"
"s_add_u32 s53, s53, 0x3d4000\n"
"s_mul_i32 s56, s48, 65536\n"
"s_add_u32 s53, s53, s56\n"
"s_add_u32 s52, s53, 0x0\n"
"s_and_b32 s52, s52, s54\n"
"s_add_u32 m0, s57, 0x0\n"
"s_nop 0\n"
"buffer_load_dwordx4 v1, s[28:31], s52 offen offset:0 nt lds\n"
"s_add_u32 s52, s53, 0x1000\n"
"s_and_b32 s52, s52, s54\n"
"s_add_u32 m0, s57, 0x400\n"
"s_nop 0\n"
"buffer_load_dwordx4 v1, s[28:31], s52 offen offset:0 nt lds\n"
"s_add_u32 s52, s53, 0x2000\n"
"s_and_b32 s52, s52, s54\n"
"s_add_u32 m0, s57, 0x800\n"
"s_nop 0\n"
"buffer_load_dwordx4 v1, s[28:31], s52 offen offset:0 nt lds\n"
"s_add_u32 s52, s53, 0x3000\n"
"s_and_b32 s52, s52, s54\n"
"s_add_u32 m0, s57, 0xc00\n"
"s_nop 0\n"
"buffer_load_dwordx4 v1, s[28:31], s52 offen offset:0 nt lds\n"
"s_add_u32 s52, s53, 0x4000\n"
"s_and_b32 s52, s52, s54\n"
"s_add_u32 m0, s57, 0x1000\n"
"s_nop 0\n"
"buffer_load_dwordx4 v1, s[28:31], s52 offen offset:0 nt lds\n"
"s_add_u32 s52, s53, 0x5000\n"
"s_and_b32 s52, s52, s54\n"
"s_add_u32 m0, s57, 0x1400\n"
"s_nop 0\n"
"buffer_load_dwordx4 v1, s[28:31], s52 offen offset:0 nt lds\n"
"s_add_u32 s52, s53, 0x6000\n"
"s_and_b32 s52, s52, s54\n"
"s_add_u32 m0, s57, 0x1800\n"
"s_nop 0\n"
"buffer_load_dwordx4 v1, s[28:31], s52 offen offset:0 nt lds\n"
"s_add_u32 s52, s53, 0x7000\n"
"s_and_b32 s52, s52, s54\n"
"s_add_u32 m0, s57, 0x1c00\n"
"s_nop 0\n"
"buffer_load_dwordx4 v1, s[28:31], s52 offen offset:0 nt lds\n"
"s_add_u32 s52, s53, 0x8000\n"
"s_and_b32 s52, s52, s54\n"
"s_add_u32 m0, s57, 0x2000\n"
"s_nop 0\n"
"buffer_load_dwordx4 v1, s[28:31], s52 offen offset:0 nt lds\n"
"s_add_u32 s52, s53, 0x9000\n"
"s_and_b32 s52, s52, s54\n"
"s_add_u32 m0, s57, 0x2400\n"
"s_nop 0\n"
"buffer_load_dwordx4 v1, s[28:31], s52 offen offset:0 nt lds\n"
"s_add_u32 s52, s53, 0xa000\n"
"s_and_b32 s52, s52, s54\n"
"s_add_u32 m0, s57, 0x2800\n"
"s_nop 0\n"
"buffer_load_dwordx4 v1, s[28:31], s52 offen offset:0 nt lds\n"
"s_add_u32 s52, s53, 0xb000\n"
"s_and_b32 s52, s52, s54\n"
"s_add_u32 m0, s57, 0x2c00\n"
"s_nop 0\n"
"buffer_load_dwordx4 v1, s[28:31], s52 offen offset:0 nt lds\n"
"s_add_u32 s52, s53, 0xc000\n"
"s_and_b32 s52, s52, s54\n"
"s_add_u32 m0, s57, 0x3000\n"
"s_nop 0\n"
"buffer_load_dwordx4 v1, s[28:31], s52 offen offset:0 nt lds\n"
"s_add_u32 s52, s53, 0xd000\n"
"s_and_b32 s52, s52, s54\n"
"s_add_u32 m0, s57, 0x3400\n"
"s_nop 0\n"
"buffer_load_dwordx4 v1, s[28:31], s52 offen offset:0 nt lds\n"
"s_add_u32 s52, s53, 0xe000\n"
"s_and_b32 s52, s52, s54\n"
"s_add_u32 m0, s57, 0x3800\n"
"s_nop 0\n"
"buffer_load_dwordx4 v1, s[28:31], s52 offen offset:0 nt lds\n"
"s_add_u32 s52, s53, 0xf000\n"
"s_and_b32 s52, s52, s54\n"
"s_add_u32 m0, s57, 0x3c00\n"
"s_nop 0\n"
"buffer_load_dwordx4 v1, s[28:31], s52 offen offset:0 nt lds\n"
"s_add_u32 s52, s53, 0x10000\n"
"s_and_b32 s52, s52, s54\n"
"s_add_u32 m0, s57, 0x4000\n"
"s_nop 0\n"
"buffer_load_dwordx4 v1, s[28:31], s52 offen offset:0 nt lds\n"
"s_add_u32 s52, s53, 0x11000\n"
"s_and_b32 s52, s52, s54\n"
"s_add_u32 m0, s57, 0x4400\n"
"s_nop 0\n"
"buffer_load_dwordx4 v1, s[28:31], s52 offen offset:0 nt lds\n"
"s_add_u32 s52, s53, 0x12000\n"
"s_and_b32 s52, s52, s54\n"
"s_add_u32 m0, s57, 0x4800\n"
"s_nop 0\n"
"buffer_load_dwordx4 v1, s[28:31], s52 offen offset:0 nt lds\n"
"s_add_u32 s52, s53, 0x13000\n"
"s_and_b32 s52, s52, s54\n"
"s_add_u32 m0, s57, 0x4c00\n"
"s_nop 0\n"
"buffer_load_dwordx4 v1, s[28:31], s52 offen offset:0 nt lds\n"
"s_add_u32 s52, s53, 0x14000\n"
"s_and_b32 s52, s52, s54\n"
"s_add_u32 m0, s57, 0x5000\n"
"s_nop 0\n"
"buffer_load_dwordx4 v1, s[28:31], s52 offen offset:0 nt lds\n"
"s_add_u32 s52, s53, 0x15000\n"
"s_and_b32 s52, s52, s54\n"
"s_add_u32 m0, s57, 0x5400\n"
"s_nop 0\n"
"buffer_load_dwordx4 v1, s[28:31], s52 offen offset:0 nt lds\n"
"s_add_u32 s52, s53, 0x16000\n"
"s_and_b32 s52, s52, s54\n"
"s_add_u32 m0, s57, 0x5800\n"
"s_nop 0\n"
"buffer_load_dwordx4 v1, s[28:31], s52 offen offset:0 nt lds\n"
"s_add_u32 s52, s53, 0x17000\n"
"s_and_b32 s52, s52, s54\n"
"buffer_load_dwordx4 v[192:195], v1, s[28:31], s52 offen offset:0\n"
"s_add_u32 s52, s53, 0x18000\n"
"s_and_b32 s52, s52, s54\n"
"buffer_load_dwordx4 v[196:199], v1, s[28:31], s52 offen offset:0\n"
"s_add_u32 s52, s53, 0x19000\n"
"s_and_b32 s52, s52, s54\n"
"buffer_load_dwordx4 v[200:203], v1, s[28:31], s52 offen offset:0\n"
"s_add_u32 s52, s53, 0x1a000\n"
"s_and_b32 s52, s52, s54\n"
"buffer_load_dwordx4 v[204:207], v1, s[28:31], s52 offen offset:0\n"
"s_add_u32 s52, s53, 0x1b000\n"
"s_and_b32 s52, s52, s54\n"
"buffer_load_dwordx4 v[208:211], v1, s[28:31], s52 offen offset:0\n"
"s_add_u32 s52, s53, 0x1c000\n"
"s_and_b32 s52, s52, s54\n"
"buffer_load_dwordx4 v[212:215], v1, s[28:31], s52 offen offset:0\n"
"s_add_u32 s52, s53, 0x1d000\n"
"s_and_b32 s52, s52, s54\n"
"buffer_load_dwordx4 v[216:219], v1, s[28:31], s52 offen offset:0\n"
"s_add_u32 s52, s53, 0x1e000\n"
"s_and_b32 s52, s52, s54\n"
"buffer_load_dwordx4 v[220:223], v1, s[28:31], s52 offen offset:0\n"
"s_waitcnt vmcnt(39)\n"
"v_perm_b32 v4, v68, v64, s68\n"
"v_accvgpr_write_b32 a8, v4\n"
"v_perm_b32 v5, v76, v72, s68\n"
"v_accvgpr_write_b32 a9, v5\n"
"v_perm_b32 v6, v84, v80, s68\n"
"v_accvgpr_write_b32 a10, v6\n"
"v_perm_b32 v7, v92, v88, s68\n"
"v_accvgpr_write_b32 a11, v7\n"
"v_perm_b32 v8, v68, v64, s69\n"
"v_accvgpr_write_b32 a24, v8\n"
"v_perm_b32 v9, v76, v72, s69\n"
"v_accvgpr_write_b32 a25, v9\n"
"v_perm_b32 v10, v84, v80, s69\n"
"v_accvgpr_write_b32 a26, v10\n"
"v_perm_b32 v11, v92, v88, s69\n"
"v_accvgpr_write_b32 a27, v11\n"
"v_perm_b32 v12, v69, v65, s68\n"
"v_accvgpr_write_b32 a40, v12\n"
"v_perm_b32 v13, v77, v73, s68\n"
"v_accvgpr_write_b32 a41, v13\n"
"v_perm_b32 v14, v85, v81, s68\n"
"v_accvgpr_write_b32 a42, v14\n"
"v_perm_b32 v15, v93, v89, s68\n"
"v_accvgpr_write_b32 a43, v15\n"
"v_perm_b32 v16, v69, v65, s69\n"
"v_accvgpr_write_b32 a56, v16\n"
"v_perm_b32 v17, v77, v73, s69\n"
"v_accvgpr_write_b32 a57, v17\n"
"v_perm_b32 v18, v85, v81, s69\n"
"v_accvgpr_write_b32 a58, v18\n"
"v_perm_b32 v19, v93, v89, s69\n"
"v_accvgpr_write_b32 a59, v19\n"
"v_perm_b32 v4, v70, v66, s68\n"
"v_accvgpr_write_b32 a72, v4\n"
"v_perm_b32 v5, v78, v74, s68\n"
"v_accvgpr_write_b32 a73, v5\n"
"v_perm_b32 v6, v86, v82, s68\n"
"v_accvgpr_write_b32 a74, v6\n"
"v_perm_b32 v7, v94, v90, s68\n"
"v_accvgpr_write_b32 a75, v7\n"
"v_perm_b32 v8, v70, v66, s69\n"
"v_accvgpr_write_b32 a88, v8\n"
"v_perm_b32 v9, v78, v74, s69\n"
"v_accvgpr_write_b32 a89, v9\n"
"v_perm_b32 v10, v86, v82, s69\n"
"v_accvgpr_write_b32 a90, v10\n"
"v_perm_b32 v11, v94, v90, s69\n"
"v_accvgpr_write_b32 a91, v11\n"
"v_perm_b32 v12, v71, v67, s68\n"
"v_accvgpr_write_b32 a104, v12\n"
"v_perm_b32 v13, v79, v75, s68\n"
"v_accvgpr_write_b32 a105, v13\n"
"v_perm_b32 v14, v87, v83, s68\n"
"v_accvgpr_write_b32 a106, v14\n"
"v_perm_b32 v15, v95, v91, s68\n"
"v_accvgpr_write_b32 a107, v15\n"
"v_perm_b32 v16, v71, v67, s69\n"
"v_accvgpr_write_b32 a120, v16\n"
"v_perm_b32 v17, v79, v75, s69\n"
"v_accvgpr_write_b32 a121, v17\n"
"v_perm_b32 v18, v87, v83, s69\n"
"v_accvgpr_write_b32 a122, v18\n"
"v_perm_b32 v19, v95, v91, s69\n"
"v_accvgpr_write_b32 a123, v19\n"
"s_waitcnt vmcnt(31)\n"
"v_perm_b32 v4, v100, v96, s68\n"
"v_accvgpr_write_b32 a12, v4\n"
"v_perm_b32 v5, v108, v104, s68\n"
"v_accvgpr_write_b32 a13, v5\n"
"v_perm_b32 v6, v116, v112, s68\n"
"v_accvgpr_write_b32 a14, v6\n"
"v_perm_b32 v7, v124, v120, s68\n"
"v_accvgpr_write_b32 a15, v7\n"
"v_perm_b32 v8, v100, v96, s69\n"
"v_accvgpr_write_b32 a28, v8\n"
"v_perm_b32 v9, v108, v104, s69\n"
"v_accvgpr_write_b32 a29, v9\n"
"v_perm_b32 v10, v116, v112, s69\n"
"v_accvgpr_write_b32 a30, v10\n"
"v_perm_b32 v11, v124, v120, s69\n"
"v_accvgpr_write_b32 a31, v11\n"
"v_perm_b32 v12, v101, v97, s68\n"
"v_accvgpr_write_b32 a44, v12\n"
"v_perm_b32 v13, v109, v105, s68\n"
"v_accvgpr_write_b32 a45, v13\n"
"v_perm_b32 v14, v117, v113, s68\n"
"v_accvgpr_write_b32 a46, v14\n"
"v_perm_b32 v15, v125, v121, s68\n"
"v_accvgpr_write_b32 a47, v15\n"
"v_perm_b32 v16, v101, v97, s69\n"
"v_accvgpr_write_b32 a60, v16\n"
"v_perm_b32 v17, v109, v105, s69\n"
"v_accvgpr_write_b32 a61, v17\n"
"v_perm_b32 v18, v117, v113, s69\n"
"v_accvgpr_write_b32 a62, v18\n"
"v_perm_b32 v19, v125, v121, s69\n"
"v_accvgpr_write_b32 a63, v19\n"
"v_perm_b32 v4, v102, v98, s68\n"
"v_accvgpr_write_b32 a76, v4\n"
"v_perm_b32 v5, v110, v106, s68\n"
"v_accvgpr_write_b32 a77, v5\n"
"v_perm_b32 v6, v118, v114, s68\n"
"v_accvgpr_write_b32 a78, v6\n"
"v_perm_b32 v7, v126, v122, s68\n"
"v_accvgpr_write_b32 a79, v7\n"
"v_perm_b32 v8, v102, v98, s69\n"
"v_accvgpr_write_b32 a92, v8\n"
"v_perm_b32 v9, v110, v106, s69\n"
"v_accvgpr_write_b32 a93, v9\n"
"v_perm_b32 v10, v118, v114, s69\n"
"v_accvgpr_write_b32 a94, v10\n"
"v_perm_b32 v11, v126, v122, s69\n"
"v_accvgpr_write_b32 a95, v11\n"
"v_perm_b32 v12, v103, v99, s68\n"
"v_accvgpr_write_b32 a108, v12\n"
"v_perm_b32 v13, v111, v107, s68\n"
"v_accvgpr_write_b32 a109, v13\n"
"v_perm_b32 v14, v119, v115, s68\n"
"v_accvgpr_write_b32 a110, v14\n"
"v_perm_b32 v15, v127, v123, s68\n"
"v_accvgpr_write_b32 a111, v15\n"
"v_perm_b32 v16, v103, v99, s69\n"
"v_accvgpr_write_b32 a124, v16\n"
"v_perm_b32 v17, v111, v107, s69\n"
"v_accvgpr_write_b32 a125, v17\n"
"v_perm_b32 v18, v119, v115, s69\n"
"v_accvgpr_write_b32 a126, v18\n"
"v_perm_b32 v19, v127, v123, s69\n"
"v_accvgpr_write_b32 a127, v19\n"
"s_add_u32 s52, s53, 0x1f000\n"
"s_and_b32 s52, s52, s54\n"
"buffer_load_dwordx4 v[224:227], v1, s[28:31], s52 offen offset:0\n"
"s_add_u32 s52, s53, 0x20000\n"
"s_and_b32 s52, s52, s54\n"
"buffer_load_dwordx4 v[228:231], v1, s[28:31], s52 offen offset:0\n"
"s_add_u32 s52, s53, 0x21000\n"
"s_and_b32 s52, s52, s54\n"
"buffer_load_dwordx4 v[232:235], v1, s[28:31], s52 offen offset:0\n"
"s_add_u32 s52, s53, 0x22000\n"
"s_and_b32 s52, s52, s54\n"
"buffer_load_dwordx4 v[236:239], v1, s[28:31], s52 offen offset:0\n"
"s_add_u32 s52, s53, 0x23000\n"
"s_and_b32 s52, s52, s54\n"
"buffer_load_dwordx4 v[240:243], v1, s[28:31], s52 offen offset:0\n"
"s_add_u32 s52, s53, 0x24000\n"
"s_and_b32 s52, s52, s54\n"
"buffer_load_dwordx4 v[244:247], v1, s[28:31], s52 offen offset:0\n"
"s_add_u32 s52, s53, 0x25000\n"
"s_and_b32 s52, s52, s54\n"
"buffer_load_dwordx4 v[248:251], v1, s[28:31], s52 offen offset:0\n"
"s_add_u32 s52, s53, 0x26000\n"
"s_and_b32 s52, s52, s54\n"
"buffer_load_dwordx4 v[252:255], v1, s[28:31], s52 offen offset:0\n"
"s_add_u32 s52, s53, 0x27000\n"
"s_and_b32 s52, s52, s54\n"
"buffer_load_dwordx4 v[64:67], v1, s[28:31], s52 offen offset:0\n"
"s_mov_b32 s44, s24\n"
"s_and_b32 s45, s25, 0xffff\n"
"s_mov_b32 s46, 0x400\n"
"s_mov_b32 s47, 0x20000\n"
"v_and_b32 v56, 1, v0\n"
"v_lshlrev_b32 v56, 4, v56\n"
"v_mov_b32 v57, 0xffff\n"
"v_lshlrev_b32 v56, v56, v57\n"
"v_bfe_u32 v57, v0, 1, 3\n"
"v_cmp_eq_u32 vcc, 0, v57\n"
"s_nop 1\n"
"v_cndmask_b32 v20, 0, v56, vcc\n"
"v_cmp_eq_u32 vcc, 1, v57\n"
"s_nop 1\n"
"v_cndmask_b32 v21, 0, v56, vcc\n"
"v_cmp_eq_u32 vcc, 2, v57\n"
"s_nop 1\n"
"v_cndmask_b32 v22, 0, v56, vcc\n"
"v_cmp_eq_u32 vcc, 3, v57\n"
"s_nop 1\n"
"v_cndmask_b32 v23, 0, v56, vcc\n"
"v_mov_b32 v4, 0\n"
"v_mov_b32 v5, 0\n"
"v_mov_b32 v6, 0\n"
"v_mov_b32 v7, 0\n"
"v_mov_b32 v8, 0\n"
"v_mov_b32 v9, 0\n"
"v_mov_b32 v10, 0\n"
"v_mov_b32 v11, 0\n"
"v_mov_b32 v12, 0\n"
"v_mov_b32 v13, 0\n"
"v_mov_b32 v14, 0\n"
"v_mov_b32 v15, 0\n"
"v_mov_b32 v16, 0\n"
"v_mov_b32 v17, 0\n"
"v_mov_b32 v18, 0\n"
"v_mov_b32 v19, 0\n"
"v_mov_b32 v44, 0xff800000\n"
"v_mov_b32 v45, 0xff800000\n"
"v_mov_b32 v46, 0xff800000\n"
"v_mov_b32 v47, 0xff800000\n"
"v_mov_b32 v48, 0xff800000\n"
"v_mov_b32 v49, 0xff800000\n"
"v_mov_b32 v50, 0xff800000\n"
"v_mov_b32 v51, 0xff800000\n"
"v_mov_b32 v52, 0xff800000\n"
"v_lshlrev_b32 v53, 7, v0\n"
"v_mov_b32 v54, 0x7ff00000\n"
"v_cmp_gt_u32 vcc, 8, v0\n"
"s_nop 1\n"
"v_cndmask_b32 v53, v54, v53, vcc\n"
"v_lshrrev_b32 v57, 4, v0\n"
"v_lshlrev_b32 v57, 7, v57\n"
"v_and_b32 v58, 3, v0\n"
"v_lshl_or_b32 v57, v58, 5, v57\n"
"s_lshr_b32 s56, s48, 4\n"
"s_lshl_b32 s56, s56, 4\n"
"s_add_u32 s56, s56, s48\n"
"s_lshl_b32 s56, s56, 9\n"
"s_add_u32 s56, s56, 0x8000\n"
"s_add_u32 s56, s56, s99\n"
"v_and_b32 v56, 31, v0\n"
"v_lshlrev_b32 v56, 2, v56\n"
"s_mov_b32 s55, 4194304\n"
"Ltpoll_507:\n"
"buffer_load_dwordx4 v[24:27], v57, s[36:39], s56 offen sc1\n"
"buffer_load_dwordx4 v[28:31], v57, s[36:39], s56 offen offset:16 sc1\n"
"buffer_load_dword v32, v56, s[36:39], 0 offen offset:256 sc1\n"
"s_waitcnt vmcnt(0)\n"
"v_cmp_ne_u32_e64 vcc, s59, v25\n"
"v_cmp_ne_u32_e64 s[72:73], s59, v27\n"
"v_cmp_ne_u32_e64 s[74:75], s59, v29\n"
"v_cmp_ne_u32_e64 s[76:77], s59, v31\n"
"v_cmp_ne_u32_e64 s[78:79], s59, v32\n"
"s_or_b64 vcc, vcc, s[72:73]\n"
"s_or_b64 vcc, vcc, s[74:75]\n"
"s_or_b64 vcc, vcc, s[76:77]\n"
"s_or_b64 vcc, vcc, s[78:79]\n"
"s_cbranch_scc0 Ltdone_508\n"
"s_sleep 2\n"
"s_sub_u32 s55, s55, 1\n"
"s_cbranch_scc0 Ltpoll_507\n"
"Ltdone_508:\n"
"s_lshl_b32 s70, s48, 15\n"
"s_lshl_b32 s56, s50, 8\n"
"s_add_u32 s70, s70, s56\n"
"s_add_u32 s70, s70, 0x101c00\n"
"s_add_u32 s72, s70, 8192\n"
"s_mov_b32 s71, 0\n"
"s_mov_b64 exec, 1\n"
"s_memtime s[8:9]\n"
"buffer_store_dwordx4 v[56:59], v2, s[36:39], s72 offen sc1\n"
"s_waitcnt vmcnt(0) lgkmcnt(0)\n"
"s_mov_b32 s12, s8\n"
"s_memtime s[8:9]\n"
"s_waitcnt lgkmcnt(0)\n"
"s_sub_u32 s15, s8, s12\n"
"buffer_store_dwordx4 v[56:59], v2, s[36:39], s70 offen sc1\n"
"s_waitcnt vmcnt(0) lgkmcnt(0)\n"
"s_mov_b32 s12, s8\n"
"s_memtime s[8:9]\n"
"s_waitcnt lgkmcnt(0)\n"
"s_sub_u32 s14, s8, s12\n"
"s_sub_u32 s16, s15, s14\n"
"s_max_i32 s16, s16, -400\n"
"s_min_i32 s16, s16, 400\n"
"s_add_u32 s71, s71, s16\n"
"s_xor_b32 s70, s70, 16384\n"
"s_xor_b32 s72, s72, 16384\n"
"s_getreg_b32 s16, hwreg(HW_REG_XCC_ID, 2, 1)\n"
"s_sub_u32 s12, 0, s71\n"
"s_cmp_eq_u32 s16, 0\n"
"s_cselect_b32 s71, s71, s12\n"
"v_mov_b32 v58, s71\n"
"v_mov_b32 v59, s59\n"
"s_lshl_b32 s56, s48, 2\n"
"s_add_u32 s56, s56, s50\n"
"s_lshl_b32 s56, s56, 3\n"
"s_add_u32 s56, s56, 0x1000\n"
"buffer_store_dwordx2 v[58:59], off, s[36:39], s56 sc1\n"
"s_mov_b64 exec, -1\n"
"s_add_u32 s52, s53, 0x28000\n"
"s_and_b32 s52, s52, s54\n"
"buffer_load_dwordx4 v[68:71], v1, s[28:31], s52 offen offset:0\n"
"s_add_u32 s52, s53, 0x29000\n"
"s_and_b32 s52, s52, s54\n"
"buffer_load_dwordx4 v[72:75], v1, s[28:31], s52 offen offset:0\n"
"s_add_u32 s52, s53, 0x2a000\n"
"s_and_b32 s52, s52, s54\n"
"buffer_load_dwordx4 v[76:79], v1, s[28:31], s52 offen offset:0\n"
"s_add_u32 s52, s53, 0x2b000\n"
"s_and_b32 s52, s52, s54\n"
"buffer_load_dwordx4 v[80:83], v1, s[28:31], s52 offen offset:0\n"
"s_add_u32 s52, s53, 0x0\n"
"s_and_b32 s52, s52, s54\n"
"buffer_load_dwordx4 v[84:87], v2, s[32:35], s52 offen offset:0\n"
"s_add_u32 s52, s53, 0x1000\n"
"s_and_b32 s52, s52, s54\n"
"buffer_load_dwordx4 v[88:91], v2, s[32:35], s52 offen offset:0\n"
"s_add_u32 s52, s53, 0x2000\n"
"s_and_b32 s52, s52, s54\n"
"buffer_load_dwordx4 v[92:95], v2, s[32:35], s52 offen offset:0\n"
"v_mov_b32_dpp v4, v24 quad_perm:[0,1,2,3] row_mask:0xf bank_mask:0x1\n"
"v_mov_b32_dpp v5, v26 quad_perm:[0,1,2,3] row_mask:0xf bank_mask:0x1\n"
"v_mov_b32_dpp v6, v28 quad_perm:[0,1,2,3] row_mask:0xf bank_mask:0x1\n"
"v_mov_b32_dpp v7, v30 quad_perm:[0,1,2,3] row_mask:0xf bank_mask:0x1\n"
"v_mov_b32_dpp v8, v24 quad_perm:[0,1,2,3] row_mask:0xf bank_mask:0x2\n"
"v_mov_b32_dpp v9, v26 quad_perm:[0,1,2,3] row_mask:0xf bank_mask:0x2\n"
"v_mov_b32_dpp v10, v28 quad_perm:[0,1,2,3] row_mask:0xf bank_mask:0x2\n"
"v_mov_b32_dpp v11, v30 quad_perm:[0,1,2,3] row_mask:0xf bank_mask:0x2\n"
"v_mov_b32_dpp v12, v24 quad_perm:[0,1,2,3] row_mask:0xf bank_mask:0x4\n"
"v_mov_b32_dpp v13, v26 quad_perm:[0,1,2,3] row_mask:0xf bank_mask:0x4\n"
"v_mov_b32_dpp v14, v28 quad_perm:[0,1,2,3] row_mask:0xf bank_mask:0x4\n"
"v_mov_b32_dpp v15, v30 quad_perm:[0,1,2,3] row_mask:0xf bank_mask:0x4\n"
"v_mov_b32_dpp v16, v24 quad_perm:[0,1,2,3] row_mask:0xf bank_mask:0x8\n"
"v_mov_b32_dpp v17, v26 quad_perm:[0,1,2,3] row_mask:0xf bank_mask:0x8\n"
"v_mov_b32_dpp v18, v28 quad_perm:[0,1,2,3] row_mask:0xf bank_mask:0x8\n"
"v_mov_b32_dpp v19, v30 quad_perm:[0,1,2,3] row_mask:0xf bank_mask:0x8\n"
"ds_read_b128 v[124:127], v3 offset:0\n"
"ds_read_b128 v[120:123], v3 offset:1024\n"
"ds_read_b128 v[116:119], v3 offset:2048\n"
"ds_read_b128 v[112:115], v3 offset:3072\n"
"ds_read_b128 v[108:111], v3 offset:4096\n"
"ds_read_b128 v[104:107], v3 offset:5120\n"
"ds_read_b128 v[100:103], v3 offset:6144\n"
"ds_read_b128 v[96:99], v3 offset:7168\n"
"s_waitcnt lgkmcnt(4)\n"
"v_mfma_f32_16x16x32_bf16 v[32:35], v[124:127], v[4:7], 0\n"
"v_mfma_f32_16x16x32_bf16 v[32:35], v[120:123], v[8:11], v[32:35]\n"
"v_mfma_f32_16x16x32_bf16 v[32:35], v[116:119], v[12:15], v[32:35]\n"
"v_mfma_f32_16x16x32_bf16 v[32:35], v[112:115], v[16:19], v[32:35]\n"
"ds_read_b128 v[112:115], v3 offset:8192\n"
"ds_read_b128 v[116:119], v3 offset:9216\n"
"ds_read_b128 v[120:123], v3 offset:10240\n"
"ds_read_b128 v[124:127], v3 offset:11264\n"
"s_waitcnt lgkmcnt(4)\n"
"v_mfma_f32_16x16x32_bf16 v[36:39], v[108:111], v[4:7], 0\n"
"v_mfma_f32_16x16x32_bf16 v[36:39], v[104:107], v[8:11], v[36:39]\n"
"v_mfma_f32_16x16x32_bf16 v[36:39], v[100:103], v[12:15], v[36:39]\n"
"v_mov_b32_dpp v63, v32 quad_perm:[0,0,0,0] row_mask:0xf bank_mask:0xf\n"
"v_add_f32_dpp v63, v33, v63 quad_perm:[1,1,1,1] row_mask:0xf bank_mask:0xf\n"
"v_add_f32_dpp v63, v34, v63 quad_perm:[2,2,2,2] row_mask:0xf bank_mask:0xf\n"
"v_add_f32_dpp v63, v35, v63 quad_perm:[3,3,3,3] row_mask:0xf bank_mask:0xf\n"
"v_cndmask_b32_e64 v44, v44, v63, s[60:61]\n"
"v_mfma_f32_16x16x32_bf16 v[36:39], v[96:99], v[16:19], v[36:39]\n"
"ds_read_b128 v[96:99], v3 offset:12288\n"
"ds_read_b128 v[100:103], v3 offset:13312\n"
"ds_read_b128 v[104:107], v3 offset:14336\n"
"ds_read_b128 v[108:111], v3 offset:15360\n"
"s_waitcnt lgkmcnt(4)\n"
"v_mfma_f32_16x16x32_bf16 v[32:35], v[112:115], v[4:7], 0\n"
"v_mfma_f32_16x16x32_bf16 v[32:35], v[116:119], v[8:11], v[32:35]\n"
"v_mfma_f32_16x16x32_bf16 v[32:35], v[120:123], v[12:15], v[32:35]\n"
"v_mov_b32_dpp v63, v36 quad_perm:[0,0,0,0] row_mask:0xf bank_mask:0xf\n"
"v_add_f32_dpp v63, v37, v63 quad_perm:[1,1,1,1] row_mask:0xf bank_mask:0xf\n"
"v_add_f32_dpp v63, v38, v63 quad_perm:[2,2,2,2] row_mask:0xf bank_mask:0xf\n"
"v_add_f32_dpp v63, v39, v63 quad_perm:[3,3,3,3] row_mask:0xf bank_mask:0xf\n"
"v_cndmask_b32_e64 v44, v44, v63, s[62:63]\n"
"v_mfma_f32_16x16x32_bf16 v[32:35], v[124:127], v[16:19], v[32:35]\n"
"ds_read_b128 v[124:127], v3 offset:16384\n"
"ds_read_b128 v[120:123], v3 offset:17408\n"
"ds_read_b128 v[116:119], v3 offset:18432\n"
"ds_read_b128 v[112:115], v3 offset:19456\n"
"s_waitcnt lgkmcnt(4)\n"
"v_mfma_f32_16x16x32_bf16 v[36:39], v[96:99], v[4:7], 0\n"
"v_mfma_f32_16x16x32_bf16 v[36:39], v[100:103], v[8:11], v[36:39]\n"
"v_mfma_f32_16x16x32_bf16 v[36:39], v[104:107], v[12:15], v[36:39]\n"
"v_mov_b32_dpp v63, v32 quad_perm:[0,0,0,0] row_mask:0xf bank_mask:0xf\n"
"v_add_f32_dpp v63, v33, v63 quad_perm:[1,1,1,1] row_mask:0xf bank_mask:0xf\n"
"v_add_f32_dpp v63, v34, v63 quad_perm:[2,2,2,2] row_mask:0xf bank_mask:0xf\n"
"v_add_f32_dpp v63, v35, v63 quad_perm:[3,3,3,3] row_mask:0xf bank_mask:0xf\n"
"v_cndmask_b32_e64 v44, v44, v63, s[64:65]\n"
"v_mfma_f32_16x16x32_bf16 v[36:39], v[108:111], v[16:19], v[36:39]\n"
"s_add_u32 s52, s53, 0x3000\n"
"s_and_b32 s52, s52, s54\n"
"buffer_load_dwordx4 v[96:99], v2, s[32:35], s52 offen offset:0\n"
"ds_read_b128 v[108:111], v3 offset:20480\n"
"ds_read_b128 v[104:107], v3 offset:21504\n"
"ds_read_b128 v[100:103], v3 offset:22528\n"
"s_waitcnt lgkmcnt(3)\n"
"v_mfma_f32_16x16x32_bf16 v[32:35], v[124:127], v[4:7], 0\n"
"s_add_u32 s52, s53, 0x4000\n"
"s_and_b32 s52, s52, s54\n"
"buffer_load_dwordx4 v[124:127], v2, s[32:35], s52 offen offset:0\n"
"v_mfma_f32_16x16x32_bf16 v[32:35], v[120:123], v[8:11], v[32:35]\n"
"s_add_u32 s52, s53, 0x5000\n"
"s_and_b32 s52, s52, s54\n"
"buffer_load_dwordx4 v[120:123], v2, s[32:35], s52 offen offset:0\n"
"v_mfma_f32_16x16x32_bf16 v[32:35], v[116:119], v[12:15], v[32:35]\n"
"s_add_u32 s52, s53, 0x6000\n"
"s_and_b32 s52, s52, s54\n"
"buffer_load_dwordx4 v[116:119], v2, s[32:35], s52 offen offset:0\n"
"v_mov_b32_dpp v63, v36 quad_perm:[0,0,0,0] row_mask:0xf bank_mask:0xf\n"
"v_add_f32_dpp v63, v37, v63 quad_perm:[1,1,1,1] row_mask:0xf bank_mask:0xf\n"
"v_add_f32_dpp v63, v38, v63 quad_perm:[2,2,2,2] row_mask:0xf bank_mask:0xf\n"
"v_add_f32_dpp v63, v39, v63 quad_perm:[3,3,3,3] row_mask:0xf bank_mask:0xf\n"
"v_cndmask_b32_e64 v44, v44, v63, s[66:67]\n"
"v_mfma_f32_16x16x32_bf16 v[32:35], v[112:115], v[16:19], v[32:35]\n"
"s_add_u32 s52, s53, 0x7000\n"
"s_and_b32 s52, s52, s54\n"
"buffer_load_dwordx4 v[112:115], v2, s[32:35], s52 offen offset:0\n"
"s_waitcnt lgkmcnt(0)\n"
"v_mfma_f32_16x16x32_bf16 v[36:39], v[108:111], v[4:7], 0\n"
"s_add_u32 s52, s53, 0x8000\n"
"s_and_b32 s52, s52, s54\n"
"buffer_load_dwordx4 v[108:111], v2, s[32:35], s52 offen offset:0\n"
"v_mfma_f32_16x16x32_bf16 v[36:39], v[104:107], v[8:11], v[36:39]\n"
"s_add_u32 s52, s53, 0x9000\n"
"s_and_b32 s52, s52, s54\n"
"buffer_load_dwordx4 v[104:107], v2, s[32:35], s52 offen offset:0\n"
"v_mfma_f32_16x16x32_bf16 v[36:39], v[100:103], v[12:15], v[36:39]\n"
"s_add_u32 s52, s53, 0xa000\n"
"s_and_b32 s52, s52, s54\n"
"buffer_load_dwordx4 v[100:103], v2, s[32:35], s52 offen offset:0\n"
"v_mov_b32_dpp v63, v32 quad_perm:[0,0,0,0] row_mask:0xf bank_mask:0xf\n"
"v_add_f32_dpp v63, v33, v63 quad_perm:[1,1,1,1] row_mask:0xf bank_mask:0xf\n"
"v_add_f32_dpp v63, v34, v63 quad_perm:[2,2,2,2] row_mask:0xf bank_mask:0xf\n"
"v_add_f32_dpp v63, v35, v63 quad_perm:[3,3,3,3] row_mask:0xf bank_mask:0xf\n"
"v_cndmask_b32_e64 v45, v45, v63, s[60:61]\n"
"v_mfma_f32_16x16x32_bf16 v[36:39], v[192:195], v[16:19], v[36:39]\n"
"s_add_u32 s52, s53, 0xb000\n"
"s_and_b32 s52, s52, s54\n"
"buffer_load_dwordx4 v[192:195], v2, s[32:35], s52 offen offset:0\n"
"v_mfma_f32_16x16x32_bf16 v[32:35], v[196:199], v[4:7], 0\n"
"s_add_u32 s52, s53, 0xc000\n"
"s_and_b32 s52, s52, s54\n"
"buffer_load_dwordx4 v[196:199], v2, s[32:35], s52 offen offset:0\n"
"v_mfma_f32_16x16x32_bf16 v[32:35], v[200:203], v[8:11], v[32:35]\n"
"s_add_u32 s52, s53, 0xd000\n"
"s_and_b32 s52, s52, s54\n"
"buffer_load_dwordx4 v[200:203], v2, s[32:35], s52 offen offset:0\n"
"v_mfma_f32_16x16x32_bf16 v[32:35], v[204:207], v[12:15], v[32:35]\n"
"s_add_u32 s52, s53, 0xe000\n"
"s_and_b32 s52, s52, s54\n"
"buffer_load_dwordx4 v[204:207], v2, s[32:35], s52 offen offset:0\n"
"v_mov_b32_dpp v63, v36 quad_perm:[0,0,0,0] row_mask:0xf bank_mask:0xf\n"
"v_add_f32_dpp v63, v37, v63 quad_perm:[1,1,1,1] row_mask:0xf bank_mask:0xf\n"
"v_add_f32_dpp v63, v38, v63 quad_perm:[2,2,2,2] row_mask:0xf bank_mask:0xf\n"
"v_add_f32_dpp v63, v39, v63 quad_perm:[3,3,3,3] row_mask:0xf bank_mask:0xf\n"
"v_cndmask_b32_e64 v45, v45, v63, s[62:63]\n"
"v_mfma_f32_16x16x32_bf16 v[32:35], v[208:211], v[16:19], v[32:35]\n"
"s_add_u32 s52, s53, 0xf000\n"
"s_and_b32 s52, s52, s54\n"
"buffer_load_dwordx4 v[208:211], v2, s[32:35], s52 offen offset:0\n"
"v_mfma_f32_16x16x32_bf16 v[36:39], v[212:215], v[4:7], 0\n"
"s_add_u32 s52, s53, 0x10000\n"
"s_and_b32 s52, s52, s54\n"
"buffer_load_dwordx4 v[212:215], v2, s[32:35], s52 offen offset:0\n"
"v_mfma_f32_16x16x32_bf16 v[36:39], v[216:219], v[8:11], v[36:39]\n"
"s_add_u32 s52, s53, 0x11000\n"
"s_and_b32 s52, s52, s54\n"
"buffer_load_dwordx4 v[216:219], v2, s[32:35], s52 offen offset:0\n"
"v_mfma_f32_16x16x32_bf16 v[36:39], v[220:223], v[12:15], v[36:39]\n"
"s_add_u32 s52, s53, 0x12000\n"
"s_and_b32 s52, s52, s54\n"
"buffer_load_dwordx4 v[220:223], v2, s[32:35], s52 offen offset:0\n"
"v_mov_b32_dpp v63, v32 quad_perm:[0,0,0,0] row_mask:0xf bank_mask:0xf\n"
"v_add_f32_dpp v63, v33, v63 quad_perm:[1,1,1,1] row_mask:0xf bank_mask:0xf\n"
"v_add_f32_dpp v63, v34, v63 quad_perm:[2,2,2,2] row_mask:0xf bank_mask:0xf\n"
"v_add_f32_dpp v63, v35, v63 quad_perm:[3,3,3,3] row_mask:0xf bank_mask:0xf\n"
"v_cndmask_b32_e64 v45, v45, v63, s[64:65]\n"
"v_mfma_f32_16x16x32_bf16 v[36:39], v[224:227], v[16:19], v[36:39]\n"
"s_add_u32 s52, s53, 0x13000\n"
"s_and_b32 s52, s52, s54\n"
"buffer_load_dwordx4 v[224:227], v2, s[32:35], s52 offen offset:0\n"
"v_mfma_f32_16x16x32_bf16 v[32:35], v[228:231], v[4:7], 0\n"
"s_add_u32 s52, s53, 0x14000\n"
"s_and_b32 s52, s52, s54\n"
"buffer_load_dwordx4 v[228:231], v2, s[32:35], s52 offen offset:0\n"
"v_mfma_f32_16x16x32_bf16 v[32:35], v[232:235], v[8:11], v[32:35]\n"
"s_add_u32 s52, s53, 0x15000\n"
"s_and_b32 s52, s52, s54\n"
"buffer_load_dwordx4 v[232:235], v2, s[32:35], s52 offen offset:0\n"
"v_mfma_f32_16x16x32_bf16 v[32:35], v[236:239], v[12:15], v[32:35]\n"
"s_add_u32 s52, s53, 0x16000\n"
"s_and_b32 s52, s52, s54\n"
"buffer_load_dwordx4 v[236:239], v2, s[32:35], s52 offen offset:0\n"
"v_mov_b32_dpp v63, v36 quad_perm:[0,0,0,0] row_mask:0xf bank_mask:0xf\n"
"v_add_f32_dpp v63, v37, v63 quad_perm:[1,1,1,1] row_mask:0xf bank_mask:0xf\n"
"v_add_f32_dpp v63, v38, v63 quad_perm:[2,2,2,2] row_mask:0xf bank_mask:0xf\n"
"v_add_f32_dpp v63, v39, v63 quad_perm:[3,3,3,3] row_mask:0xf bank_mask:0xf\n"
"v_cndmask_b32_e64 v45, v45, v63, s[66:67]\n"
"v_mfma_f32_16x16x32_bf16 v[32:35], v[240:243], v[16:19], v[32:35]\n"
"s_add_u32 s52, s53, 0x17000\n"
"s_and_b32 s52, s52, s54\n"
"buffer_load_dwordx4 v[240:243], v2, s[32:35], s52 offen offset:0\n"
"v_mfma_f32_16x16x32_bf16 v[36:39], v[244:247], v[4:7], 0\n"
"s_add_u32 s52, s53, 0x18000\n"
"s_and_b32 s52, s52, s54\n"
"buffer_load_dwordx4 v[244:247], v2, s[32:35], s52 offen offset:0\n"
"v_mfma_f32_16x16x32_bf16 v[36:39], v[248:251], v[8:11], v[36:39]\n"
"s_add_u32 s52, s53, 0x19000\n"
"s_and_b32 s52, s52, s54\n"
"buffer_load_dwordx4 v[248:251], v2, s[32:35], s52 offen offset:0\n"
"v_mfma_f32_16x16x32_bf16 v[36:39], v[252:255], v[12:15], v[36:39]\n"
"s_add_u32 s52, s53, 0x1a000\n"
"s_and_b32 s52, s52, s54\n"
"buffer_load_dwordx4 v[252:255], v2, s[32:35], s52 offen offset:0\n"
"v_mov_b32_dpp v63, v32 quad_perm:[0,0,0,0] row_mask:0xf bank_mask:0xf\n"
"v_add_f32_dpp v63, v33, v63 quad_perm:[1,1,1,1] row_mask:0xf bank_mask:0xf\n"
"v_add_f32_dpp v63, v34, v63 quad_perm:[2,2,2,2] row_mask:0xf bank_mask:0xf\n"
"v_add_f32_dpp v63, v35, v63 quad_perm:[3,3,3,3] row_mask:0xf bank_mask:0xf\n"
"v_cndmask_b32_e64 v46, v46, v63, s[60:61]\n"
"v_mfma_f32_16x16x32_bf16 v[36:39], v[64:67], v[16:19], v[36:39]\n"
"s_waitcnt vmcnt(30)\n"
"v_mfma_f32_16x16x32_bf16 v[32:35], v[68:71], v[4:7], 0\n"
"s_add_u32 s52, s53, 0x1b000\n"
"s_and_b32 s52, s52, s54\n"
"buffer_load_dwordx4 v[64:67], v2, s[32:35], s52 offen offset:0\n"
"s_waitcnt vmcnt(30)\n"
"v_mfma_f32_16x16x32_bf16 v[32:35], v[72:75], v[8:11], v[32:35]\n"
"s_add_u32 s52, s53, 0x1c000\n"
"s_and_b32 s52, s52, s54\n"
"buffer_load_dwordx4 v[68:71], v2, s[32:35], s52 offen offset:0\n"
"s_waitcnt vmcnt(30)\n"
"v_mfma_f32_16x16x32_bf16 v[32:35], v[76:79], v[12:15], v[32:35]\n"
"s_add_u32 s52, s53, 0x1d000\n"
"s_and_b32 s52, s52, s54\n"
"buffer_load_dwordx4 v[72:75], v2, s[32:35], s52 offen offset:0\n"
"v_mov_b32_dpp v63, v36 quad_perm:[0,0,0,0] row_mask:0xf bank_mask:0xf\n"
"v_add_f32_dpp v63, v37, v63 quad_perm:[1,1,1,1] row_mask:0xf bank_mask:0xf\n"
"v_add_f32_dpp v63, v38, v63 quad_perm:[2,2,2,2] row_mask:0xf bank_mask:0xf\n"
"v_add_f32_dpp v63, v39, v63 quad_perm:[3,3,3,3] row_mask:0xf bank_mask:0xf\n"
"v_cndmask_b32_e64 v46, v46, v63, s[62:63]\n"
"s_waitcnt vmcnt(30)\n"
"v_mfma_f32_16x16x32_bf16 v[32:35], v[80:83], v[16:19], v[32:35]\n"
"s_add_u32 s52, s53, 0x1e000\n"
"s_and_b32 s52, s52, s54\n"
"buffer_load_dwordx4 v[76:79], v2, s[32:35], s52 offen offset:0\n"
"s_add_u32 s52, s53, 0x1f000\n"
"s_and_b32 s52, s52, s54\n"
"buffer_load_dwordx4 v[80:83], v2, s[32:35], s52 offen offset:0\n"
"s_nop 1\n"
"v_mov_b32_dpp v63, v32 quad_perm:[0,0,0,0] row_mask:0xf bank_mask:0xf\n"
"v_add_f32_dpp v63, v33, v63 quad_perm:[1,1,1,1] row_mask:0xf bank_mask:0xf\n"
"v_add_f32_dpp v63, v34, v63 quad_perm:[2,2,2,2] row_mask:0xf bank_mask:0xf\n"
"v_add_f32_dpp v63, v35, v63 quad_perm:[3,3,3,3] row_mask:0xf bank_mask:0xf\n"
"v_cndmask_b32_e64 v46, v46, v63, s[64:65]\n"
"v_cvt_pk_bf16_f32 v58, v44, v45\n"
"v_lshlrev_b32 v44, 16, v58\n"
"v_and_b32 v45, 0xffff0000, v58\n"
"v_mul_f32 v44, 0x3db504f3, v44\n"
"v_mul_f32 v45, 0x3db504f3, v45\n"
"v_cvt_pk_bf16_f32 v58, v44, v45\n"
"v_lshlrev_b32 v44, 16, v58\n"
"v_and_b32 v45, 0xffff0000, v58\n"
"v_cvt_pk_bf16_f32 v58, v46, v46\n"
"v_lshlrev_b32 v46, 16, v58\n"
"v_mul_f32 v46, 0x3db504f3, v46\n"
"v_cvt_pk_bf16_f32 v58, v46, v46\n"
"v_lshlrev_b32 v46, 16, v58\n"
"v_max_f32 v58, v44, v45\n"
"v_max_f32 v58, v58, v46\n"
"s_nop 1\n"
"v_max_f32_dpp v58, v58, v58 quad_perm:[1,0,3,2] row_mask:0xf bank_mask:0xf\n"
"s_nop 1\n"
"v_max_f32_dpp v58, v58, v58 quad_perm:[2,3,0,1] row_mask:0xf bank_mask:0xf\n"
"s_nop 1\n"
"v_max_f32_dpp v58, v58, v58 row_half_mirror row_mask:0xf bank_mask:0xf\n"
"s_nop 1\n"
"v_max_f32_dpp v58, v58, v58 row_mirror row_mask:0xf bank_mask:0xf\n"
"s_nop 1\n"
"v_max_f32_dpp v58, v58, v58 row_bcast:15 row_mask:0xa bank_mask:0xf\n"
"s_nop 1\n"
"v_max_f32_dpp v58, v58, v58 row_bcast:31 row_mask:0xc bank_mask:0xf\n"
"v_mul_f32 v58, 0x3fb8aa3b, v58\n"
"s_nop 0\n"
"v_readlane_b32 s58, v58, 63\n"
"v_mov_b32 v59, 0x3fb8aa3b\n"
"s_nop 0\n"
"v_fma_f32 v44, v44, v59, -s58\n"
"v_fma_f32 v45, v45, v59, -s58\n"
"v_fma_f32 v46, v46, v59, -s58\n"
"v_exp_f32 v44, v44\n"
"v_exp_f32 v45, v45\n"
"v_exp_f32 v46, v46\n"
"v_add_f32 v58, v44, v45\n"
"v_add_f32 v58, v58, v46\n"
"v_cvt_pk_bf16_f32 v44, v44, v44\n"
"v_cvt_pk_bf16_f32 v45, v45, v45\n"
"v_cvt_pk_bf16_f32 v46, v46, v46\n"
"v_add_f32_dpp v58, v58, v58 quad_perm:[1,0,3,2] row_mask:0xf bank_mask:0xf\n"
"s_nop 1\n"
"v_add_f32_dpp v58, v58, v58 quad_perm:[2,3,0,1] row_mask:0xf bank_mask:0xf\n"
"s_nop 1\n"
"v_add_f32_dpp v58, v58, v58 row_half_mirror row_mask:0xf bank_mask:0xf\n"
"s_nop 1\n"
"v_add_f32_dpp v58, v58, v58 row_mirror row_mask:0xf bank_mask:0xf\n"
"s_nop 1\n"
"v_add_f32_dpp v58, v58, v58 row_bcast:15 row_mask:0xa bank_mask:0xf\n"
"s_nop 1\n"
"v_add_f32_dpp v58, v58, v58 row_bcast:31 row_mask:0xc bank_mask:0xf\n"
"v_mov_b32 v61, v58\n"
"v_mov_b32 v60, s58\n"
"s_mul_i32 s56, s50, 576\n"
"s_add_u32 s56, s56, 160256\n"
"v_mov_b32 v59, s56\n"
"s_mov_b32 exec_lo, 0\n"
"s_and_b32 exec_hi, exec_hi, 0x80000000\n"
"ds_write_b64 v59, v[60:61]\n"
"s_mov_b64 exec, -1\n"
"v_and_b32_dpp v24, v44, v20 row_newbcast:0 row_mask:0xf bank_mask:0xf\n"
"v_and_b32_dpp v25, v44, v21 row_newbcast:0 row_mask:0xf bank_mask:0xf\n"
"v_and_b32_dpp v26, v44, v22 row_newbcast:0 row_mask:0xf bank_mask:0xf\n"
"v_and_b32_dpp v27, v44, v23 row_newbcast:0 row_mask:0xf bank_mask:0xf\n"
"s_waitcnt vmcnt(31)\n"
"s_nop 0\n"
"v_mfma_f32_16x16x32_bf16 v[40:43], v[24:27], v[84:87], 0\n"
"s_add_u32 s52, s53, 0x20000\n"
"s_and_b32 s52, s52, s54\n"
"buffer_load_dwordx4 v[84:87], v2, s[32:35], s52 offen offset:0\n"
"v_and_b32_dpp v24, v44, v20 row_newbcast:4 row_mask:0xf bank_mask:0xf\n"
"v_and_b32_dpp v25, v44, v21 row_newbcast:4 row_mask:0xf bank_mask:0xf\n"
"v_and_b32_dpp v26, v44, v22 row_newbcast:4 row_mask:0xf bank_mask:0xf\n"
"v_and_b32_dpp v27, v44, v23 row_newbcast:4 row_mask:0xf bank_mask:0xf\n"
"s_waitcnt vmcnt(31)\n"
"s_nop 0\n"
"v_mfma_f32_16x16x32_bf16 v[40:43], v[24:27], v[88:91], v[40:43]\n"
"s_add_u32 s52, s53, 0x21000\n"
"s_and_b32 s52, s52, s54\n"
"buffer_load_dwordx4 v[88:91], v2, s[32:35], s52 offen offset:0\n"
"v_and_b32_dpp v24, v44, v20 row_newbcast:8 row_mask:0xf bank_mask:0xf\n"
"v_and_b32_dpp v25, v44, v21 row_newbcast:8 row_mask:0xf bank_mask:0xf\n"
"v_and_b32_dpp v26, v44, v22 row_newbcast:8 row_mask:0xf bank_mask:0xf\n"
"v_and_b32_dpp v27, v44, v23 row_newbcast:8 row_mask:0xf bank_mask:0xf\n"
"s_waitcnt vmcnt(31)\n"
"s_nop 0\n"
"v_mfma_f32_16x16x32_bf16 v[40:43], v[24:27], v[92:95], v[40:43]\n"
"s_add_u32 s52, s53, 0x22000\n"
"s_and_b32 s52, s52, s54\n"
"buffer_load_dwordx4 v[92:95], v2, s[32:35], s52 offen offset:0\n"
"v_and_b32_dpp v24, v44, v20 row_newbcast:12 row_mask:0xf bank_mask:0xf\n"
"v_and_b32_dpp v25, v44, v21 row_newbcast:12 row_mask:0xf bank_mask:0xf\n"
"v_and_b32_dpp v26, v44, v22 row_newbcast:12 row_mask:0xf bank_mask:0xf\n"
"v_and_b32_dpp v27, v44, v23 row_newbcast:12 row_mask:0xf bank_mask:0xf\n"
"s_waitcnt vmcnt(31)\n"
"s_nop 0\n"
"v_mfma_f32_16x16x32_bf16 v[40:43], v[24:27], v[96:99], v[40:43]\n"
"s_add_u32 s52, s53, 0x23000\n"
"s_and_b32 s52, s52, s54\n"
"buffer_load_dwordx4 v[96:99], v2, s[32:35], s52 offen offset:0\n"
"v_and_b32_dpp v24, v44, v20 row_newbcast:1 row_mask:0xf bank_mask:0xf\n"
"v_and_b32_dpp v25, v44, v21 row_newbcast:1 row_mask:0xf bank_mask:0xf\n"
"v_and_b32_dpp v26, v44, v22 row_newbcast:1 row_mask:0xf bank_mask:0xf\n"
"v_and_b32_dpp v27, v44, v23 row_newbcast:1 row_mask:0xf bank_mask:0xf\n"
"s_waitcnt vmcnt(31)\n"
"s_nop 0\n"
"v_mfma_f32_16x16x32_bf16 v[40:43], v[24:27], v[124:127], v[40:43]\n"
"s_add_u32 s52, s53, 0x24000\n"
"s_and_b32 s52, s52, s54\n"
"buffer_load_dwordx4 v[124:127], v2, s[32:35], s52 offen offset:0\n"
"v_and_b32_dpp v24, v44, v20 row_newbcast:5 row_mask:0xf bank_mask:0xf\n"
"v_and_b32_dpp v25, v44, v21 row_newbcast:5 row_mask:0xf bank_mask:0xf\n"
"v_and_b32_dpp v26, v44, v22 row_newbcast:5 row_mask:0xf bank_mask:0xf\n"
"v_and_b32_dpp v27, v44, v23 row_newbcast:5 row_mask:0xf bank_mask:0xf\n"
"s_waitcnt vmcnt(31)\n"
"s_nop 0\n"
"v_mfma_f32_16x16x32_bf16 v[40:43], v[24:27], v[120:123], v[40:43]\n"
"s_add_u32 s52, s53, 0x25000\n"
"s_and_b32 s52, s52, s54\n"
"buffer_load_dwordx4 v[120:123], v2, s[32:35], s52 offen offset:0\n"
"v_and_b32_dpp v24, v44, v20 row_newbcast:9 row_mask:0xf bank_mask:0xf\n"
"v_and_b32_dpp v25, v44, v21 row_newbcast:9 row_mask:0xf bank_mask:0xf\n"
"v_and_b32_dpp v26, v44, v22 row_newbcast:9 row_mask:0xf bank_mask:0xf\n"
"v_and_b32_dpp v27, v44, v23 row_newbcast:9 row_mask:0xf bank_mask:0xf\n"
"s_waitcnt vmcnt(31)\n"
"s_nop 0\n"
"v_mfma_f32_16x16x32_bf16 v[40:43], v[24:27], v[116:119], v[40:43]\n"
"s_add_u32 s52, s53, 0x26000\n"
"s_and_b32 s52, s52, s54\n"
"buffer_load_dwordx4 v[116:119], v2, s[32:35], s52 offen offset:0\n"
"v_and_b32_dpp v24, v44, v20 row_newbcast:13 row_mask:0xf bank_mask:0xf\n"
"v_and_b32_dpp v25, v44, v21 row_newbcast:13 row_mask:0xf bank_mask:0xf\n"
"v_and_b32_dpp v26, v44, v22 row_newbcast:13 row_mask:0xf bank_mask:0xf\n"
"v_and_b32_dpp v27, v44, v23 row_newbcast:13 row_mask:0xf bank_mask:0xf\n"
"s_waitcnt vmcnt(31)\n"
"s_nop 0\n"
"v_mfma_f32_16x16x32_bf16 v[40:43], v[24:27], v[112:115], v[40:43]\n"
"s_add_u32 s52, s53, 0x27000\n"
"s_and_b32 s52, s52, s54\n"
"buffer_load_dwordx4 v[112:115], v2, s[32:35], s52 offen offset:0\n"
"v_and_b32_dpp v24, v44, v20 row_newbcast:2 row_mask:0xf bank_mask:0xf\n"
"v_and_b32_dpp v25, v44, v21 row_newbcast:2 row_mask:0xf bank_mask:0xf\n"
"v_and_b32_dpp v26, v44, v22 row_newbcast:2 row_mask:0xf bank_mask:0xf\n"
"v_and_b32_dpp v27, v44, v23 row_newbcast:2 row_mask:0xf bank_mask:0xf\n"
"s_waitcnt vmcnt(31)\n"
"s_nop 0\n"
"v_mfma_f32_16x16x32_bf16 v[40:43], v[24:27], v[108:111], v[40:43]\n"
"s_add_u32 s52, s53, 0x28000\n"
"s_and_b32 s52, s52, s54\n"
"buffer_load_dwordx4 v[108:111], v2, s[32:35], s52 offen offset:0\n"
"v_and_b32_dpp v24, v44, v20 row_newbcast:6 row_mask:0xf bank_mask:0xf\n"
"v_and_b32_dpp v25, v44, v21 row_newbcast:6 row_mask:0xf bank_mask:0xf\n"
"v_and_b32_dpp v26, v44, v22 row_newbcast:6 row_mask:0xf bank_mask:0xf\n"
"v_and_b32_dpp v27, v44, v23 row_newbcast:6 row_mask:0xf bank_mask:0xf\n"
"s_waitcnt vmcnt(31)\n"
"s_nop 0\n"
"v_mfma_f32_16x16x32_bf16 v[40:43], v[24:27], v[104:107], v[40:43]\n"
"s_add_u32 s52, s53, 0x29000\n"
"s_and_b32 s52, s52, s54\n"
"buffer_load_dwordx4 v[104:107], v2, s[32:35], s52 offen offset:0\n"
"v_and_b32_dpp v24, v44, v20 row_newbcast:10 row_mask:0xf bank_mask:0xf\n"
"v_and_b32_dpp v25, v44, v21 row_newbcast:10 row_mask:0xf bank_mask:0xf\n"
"v_and_b32_dpp v26, v44, v22 row_newbcast:10 row_mask:0xf bank_mask:0xf\n"
"v_and_b32_dpp v27, v44, v23 row_newbcast:10 row_mask:0xf bank_mask:0xf\n"
"s_waitcnt vmcnt(31)\n"
"s_nop 0\n"
"v_mfma_f32_16x16x32_bf16 v[40:43], v[24:27], v[100:103], v[40:43]\n"
"s_add_u32 s52, s53, 0x2a000\n"
"s_and_b32 s52, s52, s54\n"
"buffer_load_dwordx4 v[100:103], v2, s[32:35], s52 offen offset:0\n"
"v_and_b32_dpp v24, v44, v20 row_newbcast:14 row_mask:0xf bank_mask:0xf\n"
"v_and_b32_dpp v25, v44, v21 row_newbcast:14 row_mask:0xf bank_mask:0xf\n"
"v_and_b32_dpp v26, v44, v22 row_newbcast:14 row_mask:0xf bank_mask:0xf\n"
"v_and_b32_dpp v27, v44, v23 row_newbcast:14 row_mask:0xf bank_mask:0xf\n"
"s_waitcnt vmcnt(31)\n"
"s_nop 0\n"
"v_mfma_f32_16x16x32_bf16 v[40:43], v[24:27], v[192:195], v[40:43]\n"
"v_and_b32_dpp v24, v44, v20 row_newbcast:3 row_mask:0xf bank_mask:0xf\n"
"v_and_b32_dpp v25, v44, v21 row_newbcast:3 row_mask:0xf bank_mask:0xf\n"
"v_and_b32_dpp v26, v44, v22 row_newbcast:3 row_mask:0xf bank_mask:0xf\n"
"v_and_b32_dpp v27, v44, v23 row_newbcast:3 row_mask:0xf bank_mask:0xf\n"
"s_waitcnt vmcnt(30)\n"
"s_nop 0\n"
"v_mfma_f32_16x16x32_bf16 v[40:43], v[24:27], v[196:199], v[40:43]\n"
"v_and_b32_dpp v24, v44, v20 row_newbcast:7 row_mask:0xf bank_mask:0xf\n"
"v_and_b32_dpp v25, v44, v21 row_newbcast:7 row_mask:0xf bank_mask:0xf\n"
"v_and_b32_dpp v26, v44, v22 row_newbcast:7 row_mask:0xf bank_mask:0xf\n"
"v_and_b32_dpp v27, v44, v23 row_newbcast:7 row_mask:0xf bank_mask:0xf\n"
"s_waitcnt vmcnt(29)\n"
"s_nop 0\n"
"v_mfma_f32_16x16x32_bf16 v[40:43], v[24:27], v[200:203], v[40:43]\n"
"v_and_b32_dpp v24, v44, v20 row_newbcast:11 row_mask:0xf bank_mask:0xf\n"
"v_and_b32_dpp v25, v44, v21 row_newbcast:11 row_mask:0xf bank_mask:0xf\n"
"v_and_b32_dpp v26, v44, v22 row_newbcast:11 row_mask:0xf bank_mask:0xf\n"
"v_and_b32_dpp v27, v44, v23 row_newbcast:11 row_mask:0xf bank_mask:0xf\n"
"s_waitcnt vmcnt(28)\n"
"s_nop 0\n"
"v_mfma_f32_16x16x32_bf16 v[40:43], v[24:27], v[204:207], v[40:43]\n"
"v_and_b32_dpp v24, v44, v20 row_newbcast:15 row_mask:0xf bank_mask:0xf\n"
"v_and_b32_dpp v25, v44, v21 row_newbcast:15 row_mask:0xf bank_mask:0xf\n"
"v_and_b32_dpp v26, v44, v22 row_newbcast:15 row_mask:0xf bank_mask:0xf\n"
"v_and_b32_dpp v27, v44, v23 row_newbcast:15 row_mask:0xf bank_mask:0xf\n"
"s_waitcnt vmcnt(27)\n"
"s_nop 0\n"
"v_mfma_f32_16x16x32_bf16 v[40:43], v[24:27], v[208:211], v[40:43]\n"
"v_and_b32_dpp v24, v45, v20 row_newbcast:0 row_mask:0xf bank_mask:0xf\n"
"v_and_b32_dpp v25, v45, v21 row_newbcast:0 row_mask:0xf bank_mask:0xf\n"
"v_and_b32_dpp v26, v45, v22 row_newbcast:0 row_mask:0xf bank_mask:0xf\n"
"v_and_b32_dpp v27, v45, v23 row_newbcast:0 row_mask:0xf bank_mask:0xf\n"
"s_waitcnt vmcnt(26)\n"
"s_nop 0\n"
"v_mfma_f32_16x16x32_bf16 v[40:43], v[24:27], v[212:215], v[40:43]\n"
"v_and_b32_dpp v24, v45, v20 row_newbcast:4 row_mask:0xf bank_mask:0xf\n"
"v_and_b32_dpp v25, v45, v21 row_newbcast:4 row_mask:0xf bank_mask:0xf\n"
"v_and_b32_dpp v26, v45, v22 row_newbcast:4 row_mask:0xf bank_mask:0xf\n"
"v_and_b32_dpp v27, v45, v23 row_newbcast:4 row_mask:0xf bank_mask:0xf\n"
"s_waitcnt vmcnt(25)\n"
"s_nop 0\n"
"v_mfma_f32_16x16x32_bf16 v[40:43], v[24:27], v[216:219], v[40:43]\n"
"v_and_b32_dpp v24, v45, v20 row_newbcast:8 row_mask:0xf bank_mask:0xf\n"
"v_and_b32_dpp v25, v45, v21 row_newbcast:8 row_mask:0xf bank_mask:0xf\n"
"v_and_b32_dpp v26, v45, v22 row_newbcast:8 row_mask:0xf bank_mask:0xf\n"
"v_and_b32_dpp v27, v45, v23 row_newbcast:8 row_mask:0xf bank_mask:0xf\n"
"s_waitcnt vmcnt(24)\n"
"s_nop 0\n"
"v_mfma_f32_16x16x32_bf16 v[40:43], v[24:27], v[220:223], v[40:43]\n"
"v_and_b32_dpp v24, v45, v20 row_newbcast:12 row_mask:0xf bank_mask:0xf\n"
"v_and_b32_dpp v25, v45, v21 row_newbcast:12 row_mask:0xf bank_mask:0xf\n"
"v_and_b32_dpp v26, v45, v22 row_newbcast:12 row_mask:0xf bank_mask:0xf\n"
"v_and_b32_dpp v27, v45, v23 row_newbcast:12 row_mask:0xf bank_mask:0xf\n"
"s_waitcnt vmcnt(23)\n"
"s_nop 0\n"
"v_mfma_f32_16x16x32_bf16 v[40:43], v[24:27], v[224:227], v[40:43]\n"
"v_and_b32_dpp v24, v45, v20 row_newbcast:1 row_mask:0xf bank_mask:0xf\n"
"v_and_b32_dpp v25, v45, v21 row_newbcast:1 row_mask:0xf bank_mask:0xf\n"
"v_and_b32_dpp v26, v45, v22 row_newbcast:1 row_mask:0xf bank_mask:0xf\n"
"v_and_b32_dpp v27, v45, v23 row_newbcast:1 row_mask:0xf bank_mask:0xf\n"
"s_waitcnt vmcnt(22)\n"
"s_nop 0\n"
"v_mfma_f32_16x16x32_bf16 v[40:43], v[24:27], v[228:231], v[40:43]\n"
"v_and_b32_dpp v24, v45, v20 row_newbcast:5 row_mask:0xf bank_mask:0xf\n"
"v_and_b32_dpp v25, v45, v21 row_newbcast:5 row_mask:0xf bank_mask:0xf\n"
"v_and_b32_dpp v26, v45, v22 row_newbcast:5 row_mask:0xf bank_mask:0xf\n"
"v_and_b32_dpp v27, v45, v23 row_newbcast:5 row_mask:0xf bank_mask:0xf\n"
"s_waitcnt vmcnt(21)\n"
"s_nop 0\n"
"v_mfma_f32_16x16x32_bf16 v[40:43], v[24:27], v[232:235], v[40:43]\n"
"v_and_b32_dpp v24, v45, v20 row_newbcast:9 row_mask:0xf bank_mask:0xf\n"
"v_and_b32_dpp v25, v45, v21 row_newbcast:9 row_mask:0xf bank_mask:0xf\n"
"v_and_b32_dpp v26, v45, v22 row_newbcast:9 row_mask:0xf bank_mask:0xf\n"
"v_and_b32_dpp v27, v45, v23 row_newbcast:9 row_mask:0xf bank_mask:0xf\n"
"s_waitcnt vmcnt(20)\n"
"s_nop 0\n"
"v_mfma_f32_16x16x32_bf16 v[40:43], v[24:27], v[236:239], v[40:43]\n"
"v_and_b32_dpp v24, v45, v20 row_newbcast:13 row_mask:0xf bank_mask:0xf\n"
"v_and_b32_dpp v25, v45, v21 row_newbcast:13 row_mask:0xf bank_mask:0xf\n"
"v_and_b32_dpp v26, v45, v22 row_newbcast:13 row_mask:0xf bank_mask:0xf\n"
"v_and_b32_dpp v27, v45, v23 row_newbcast:13 row_mask:0xf bank_mask:0xf\n"
"s_waitcnt vmcnt(19)\n"
"s_nop 0\n"
"v_mfma_f32_16x16x32_bf16 v[40:43], v[24:27], v[240:243], v[40:43]\n"
"v_and_b32_dpp v24, v45, v20 row_newbcast:2 row_mask:0xf bank_mask:0xf\n"
"v_and_b32_dpp v25, v45, v21 row_newbcast:2 row_mask:0xf bank_mask:0xf\n"
"v_and_b32_dpp v26, v45, v22 row_newbcast:2 row_mask:0xf bank_mask:0xf\n"
"v_and_b32_dpp v27, v45, v23 row_newbcast:2 row_mask:0xf bank_mask:0xf\n"
"s_waitcnt vmcnt(18)\n"
"s_nop 0\n"
"v_mfma_f32_16x16x32_bf16 v[40:43], v[24:27], v[244:247], v[40:43]\n"
"v_and_b32_dpp v24, v45, v20 row_newbcast:6 row_mask:0xf bank_mask:0xf\n"
"v_and_b32_dpp v25, v45, v21 row_newbcast:6 row_mask:0xf bank_mask:0xf\n"
"v_and_b32_dpp v26, v45, v22 row_newbcast:6 row_mask:0xf bank_mask:0xf\n"
"v_and_b32_dpp v27, v45, v23 row_newbcast:6 row_mask:0xf bank_mask:0xf\n"
"s_waitcnt vmcnt(17)\n"
"s_nop 0\n"
"v_mfma_f32_16x16x32_bf16 v[40:43], v[24:27], v[248:251], v[40:43]\n"
"v_and_b32_dpp v24, v45, v20 row_newbcast:10 row_mask:0xf bank_mask:0xf\n"
"v_and_b32_dpp v25, v45, v21 row_newbcast:10 row_mask:0xf bank_mask:0xf\n"
"v_and_b32_dpp v26, v45, v22 row_newbcast:10 row_mask:0xf bank_mask:0xf\n"
"v_and_b32_dpp v27, v45, v23 row_newbcast:10 row_mask:0xf bank_mask:0xf\n"
"s_waitcnt vmcnt(16)\n"
"s_nop 0\n"
"v_mfma_f32_16x16x32_bf16 v[40:43], v[24:27], v[252:255], v[40:43]\n"
"v_and_b32_dpp v24, v45, v20 row_newbcast:14 row_mask:0xf bank_mask:0xf\n"
"v_and_b32_dpp v25, v45, v21 row_newbcast:14 row_mask:0xf bank_mask:0xf\n"
"v_and_b32_dpp v26, v45, v22 row_newbcast:14 row_mask:0xf bank_mask:0xf\n"
"v_and_b32_dpp v27, v45, v23 row_newbcast:14 row_mask:0xf bank_mask:0xf\n"
"s_waitcnt vmcnt(15)\n"
"s_nop 0\n"
"v_mfma_f32_16x16x32_bf16 v[40:43], v[24:27], v[64:67], v[40:43]\n"
"s_add_u32 s52, s53, 0x2b000\n"
"s_and_b32 s52, s52, s54\n"
"buffer_load_dwordx4 v[64:67], v2, s[32:35], s52 offen offset:0\n"
"v_and_b32 v57, 15, v0\n"
"s_mov_b32 s40, s22\n"
"s_and_b32 s41, s23, 0xffff\n"
"s_mov_b32 s42, 0x158000\n"
"s_mov_b32 s43, 0x20000\n"
"v_lshrrev_b32 v56, 5, v0\n"
"v_mul_u32_u24 v56, 11, v56\n"
"v_bfe_u32 v58, v0, 4, 1\n"
"v_lshl_add_u32 v56, v58, 3, v56\n"
"v_lshlrev_b32 v56, 10, v56\n"
"v_lshl_add_u32 v56, v57, 4, v56\n"
"s_lshl_b32 s56, s50, 8\n"
"v_add_u32 v56, s56, v56\n"
"v_mov_b32 v60, 0x7ff00000\n"
"v_cmp_eq_u32 vcc, 0, v58\n"
"s_nop 1\n"
"v_cndmask_b32 v58, v60, v56, vcc\n"
"v_cmp_gt_u32 vcc, 48, v0\n"
"s_nop 1\n"
"v_cndmask_b32 v59, v60, v56, vcc\n"
"s_mul_i32 s71, s48, 44032\n"
"s_add_u32 s56, s71, 0x0\n"
"buffer_load_dwordx4 v[192:195], v56, s[40:43], s56 offen nt\n"
"s_add_u32 s56, s71, 0x400\n"
"buffer_load_dwordx4 v[196:199], v56, s[40:43], s56 offen nt\n"
"s_add_u32 s56, s71, 0x800\n"
"buffer_load_dwordx4 v[200:203], v56, s[40:43], s56 offen nt\n"
"s_add_u32 s56, s71, 0xc00\n"
"buffer_load_dwordx4 v[204:207], v58, s[40:43], s56 offen nt\n"
"s_add_u32 s56, s71, 0x1000\n"
"buffer_load_dwordx4 v[208:211], v58, s[40:43], s56 offen nt\n"
"s_add_u32 s56, s71, 0x1400\n"
"buffer_load_dwordx4 v[212:215], v58, s[40:43], s56 offen nt\n"
"s_add_u32 s56, s71, 0x1800\n"
"buffer_load_dwordx4 v[216:219], v58, s[40:43], s56 offen nt\n"
"s_add_u32 s56, s71, 0x1c00\n"
"buffer_load_dwordx4 v[220:223], v58, s[40:43], s56 offen nt\n"
"s_add_u32 s56, s71, 0x5800\n"
"buffer_load_dwordx4 v[224:227], v56, s[40:43], s56 offen nt\n"
"s_add_u32 s56, s71, 0x5c00\n"
"buffer_load_dwordx4 v[228:231], v56, s[40:43], s56 offen nt\n"
"s_add_u32 s56, s71, 0x6000\n"
"buffer_load_dwordx4 v[232:235], v59, s[40:43], s56 offen nt\n"
"s_add_u32 s56, s71, 0x6400\n"
"buffer_load_dwordx4 v[236:239], v58, s[40:43], s56 offen nt\n"
"s_add_u32 s56, s71, 0x6800\n"
"buffer_load_dwordx4 v[240:243], v58, s[40:43], s56 offen nt\n"
"s_add_u32 s56, s71, 0x6c00\n"
"buffer_load_dwordx4 v[244:247], v58, s[40:43], s56 offen nt\n"
"s_add_u32 s56, s71, 0x7000\n"
"buffer_load_dwordx4 v[248:251], v58, s[40:43], s56 offen nt\n"
"s_add_u32 s56, s71, 0x7400\n"
"buffer_load_dwordx4 v[252:255], v58, s[40:43], s56 offen nt\n"
"v_and_b32_dpp v24, v45, v20 row_newbcast:3 row_mask:0xf bank_mask:0xf\n"
"v_and_b32_dpp v25, v45, v21 row_newbcast:3 row_mask:0xf bank_mask:0xf\n"
"v_and_b32_dpp v26, v45, v22 row_newbcast:3 row_mask:0xf bank_mask:0xf\n"
"v_and_b32_dpp v27, v45, v23 row_newbcast:3 row_mask:0xf bank_mask:0xf\n"
"s_waitcnt vmcnt(31)\n"
"s_nop 0\n"
"v_mfma_f32_16x16x32_bf16 v[40:43], v[24:27], v[68:71], v[40:43]\n"
"v_and_b32_dpp v24, v45, v20 row_newbcast:7 row_mask:0xf bank_mask:0xf\n"
"v_and_b32_dpp v25, v45, v21 row_newbcast:7 row_mask:0xf bank_mask:0xf\n"
"v_and_b32_dpp v26, v45, v22 row_newbcast:7 row_mask:0xf bank_mask:0xf\n"
"v_and_b32_dpp v27, v45, v23 row_newbcast:7 row_mask:0xf bank_mask:0xf\n"
"s_waitcnt vmcnt(30)\n"
"s_nop 0\n"
"v_mfma_f32_16x16x32_bf16 v[40:43], v[24:27], v[72:75], v[40:43]\n"
"v_and_b32_dpp v24, v45, v20 row_newbcast:11 row_mask:0xf bank_mask:0xf\n"
"v_and_b32_dpp v25, v45, v21 row_newbcast:11 row_mask:0xf bank_mask:0xf\n"
"v_and_b32_dpp v26, v45, v22 row_newbcast:11 row_mask:0xf bank_mask:0xf\n"
"v_and_b32_dpp v27, v45, v23 row_newbcast:11 row_mask:0xf bank_mask:0xf\n"
"s_waitcnt vmcnt(29)\n"
"s_nop 0\n"
"v_mfma_f32_16x16x32_bf16 v[40:43], v[24:27], v[76:79], v[40:43]\n"
"v_and_b32_dpp v24, v45, v20 row_newbcast:15 row_mask:0xf bank_mask:0xf\n"
"v_and_b32_dpp v25, v45, v21 row_newbcast:15 row_mask:0xf bank_mask:0xf\n"
"v_and_b32_dpp v26, v45, v22 row_newbcast:15 row_mask:0xf bank_mask:0xf\n"
"v_and_b32_dpp v27, v45, v23 row_newbcast:15 row_mask:0xf bank_mask:0xf\n"
"s_waitcnt vmcnt(28)\n"
"s_nop 0\n"
"v_mfma_f32_16x16x32_bf16 v[40:43], v[24:27], v[80:83], v[40:43]\n"
"v_and_b32_dpp v24, v46, v20 row_newbcast:0 row_mask:0xf bank_mask:0xf\n"
"v_and_b32_dpp v25, v46, v21 row_newbcast:0 row_mask:0xf bank_mask:0xf\n"
"v_and_b32_dpp v26, v46, v22 row_newbcast:0 row_mask:0xf bank_mask:0xf\n"
"v_and_b32_dpp v27, v46, v23 row_newbcast:0 row_mask:0xf bank_mask:0xf\n"
"s_waitcnt vmcnt(27)\n"
"s_nop 0\n"
"v_mfma_f32_16x16x32_bf16 v[40:43], v[24:27], v[84:87], v[40:43]\n"
"v_and_b32_dpp v24, v46, v20 row_newbcast:4 row_mask:0xf bank_mask:0xf\n"
"v_and_b32_dpp v25, v46, v21 row_newbcast:4 row_mask:0xf bank_mask:0xf\n"
"v_and_b32_dpp v26, v46, v22 row_newbcast:4 row_mask:0xf bank_mask:0xf\n"
"v_and_b32_dpp v27, v46, v23 row_newbcast:4 row_mask:0xf bank_mask:0xf\n"
"s_waitcnt vmcnt(26)\n"
"s_nop 0\n"
"v_mfma_f32_16x16x32_bf16 v[40:43], v[24:27], v[88:91], v[40:43]\n"
"v_and_b32_dpp v24, v46, v20 row_newbcast:8 row_mask:0xf bank_mask:0xf\n"
"v_and_b32_dpp v25, v46, v21 row_newbcast:8 row_mask:0xf bank_mask:0xf\n"
"v_and_b32_dpp v26, v46, v22 row_newbcast:8 row_mask:0xf bank_mask:0xf\n"
"v_and_b32_dpp v27, v46, v23 row_newbcast:8 row_mask:0xf bank_mask:0xf\n"
"s_waitcnt vmcnt(25)\n"
"s_nop 0\n"
"v_mfma_f32_16x16x32_bf16 v[40:43], v[24:27], v[92:95], v[40:43]\n"
"v_and_b32_dpp v24, v46, v20 row_newbcast:12 row_mask:0xf bank_mask:0xf\n"
"v_and_b32_dpp v25, v46, v21 row_newbcast:12 row_mask:0xf bank_mask:0xf\n"
"v_and_b32_dpp v26, v46, v22 row_newbcast:12 row_mask:0xf bank_mask:0xf\n"
"v_and_b32_dpp v27, v46, v23 row_newbcast:12 row_mask:0xf bank_mask:0xf\n"
"s_waitcnt vmcnt(24)\n"
"s_nop 0\n"
"v_mfma_f32_16x16x32_bf16 v[40:43], v[24:27], v[96:99], v[40:43]\n"
"v_and_b32_dpp v24, v46, v20 row_newbcast:1 row_mask:0xf bank_mask:0xf\n"
"v_and_b32_dpp v25, v46, v21 row_newbcast:1 row_mask:0xf bank_mask:0xf\n"
"v_and_b32_dpp v26, v46, v22 row_newbcast:1 row_mask:0xf bank_mask:0xf\n"
"v_and_b32_dpp v27, v46, v23 row_newbcast:1 row_mask:0xf bank_mask:0xf\n"
"s_waitcnt vmcnt(23)\n"
"s_nop 0\n"
"v_mfma_f32_16x16x32_bf16 v[40:43], v[24:27], v[124:127], v[40:43]\n"
"v_and_b32_dpp v24, v46, v20 row_newbcast:5 row_mask:0xf bank_mask:0xf\n"
"v_and_b32_dpp v25, v46, v21 row_newbcast:5 row_mask:0xf bank_mask:0xf\n"
"v_and_b32_dpp v26, v46, v22 row_newbcast:5 row_mask:0xf bank_mask:0xf\n"
"v_and_b32_dpp v27, v46, v23 row_newbcast:5 row_mask:0xf bank_mask:0xf\n"
"s_waitcnt vmcnt(22)\n"
"s_nop 0\n"
"v_mfma_f32_16x16x32_bf16 v[40:43], v[24:27], v[120:123], v[40:43]\n"
"s_mov_b32 s56, 0x1000\n"
"buffer_load_dwordx4 v[32:35], v2, s[36:39], s56 offen sc1\n"
"v_min_u32 v62, 32, v0\n"
"v_lshlrev_b32 v62, 4, v62\n"
"v_mov_b32 v61, 0x7ff00000\n"
"v_cmp_gt_u32 vcc, 33, v0\n"
"s_nop 1\n"
"v_cndmask_b32 v61, v61, v62, vcc\n"
"v_add_u32 v62, 159744, v62\n"
"v_mov_b32 v63, 160256\n"
"s_mul_i32 s70, s48, 32768\n"
"s_mul_i32 s56, s49, 640\n"
"s_add_u32 s70, s70, s56\n"
"s_add_u32 s70, s70, 0x100000\n"
"s_add_u32 s70, s70, s99\n"
"v_and_b32 v58, 15, v0\n"
"v_lshlrev_b32 v58, 5, v58\n"
"v_lshrrev_b32 v59, 4, v0\n"
"v_lshl_add_u32 v58, v59, 4, v58\n"
"s_mul_i32 s56, s50, 576\n"
"s_add_u32 s56, s56, 159744\n"
"v_add_u32 v58, s56, v58\n"
"v_mov_b32 v60, v58\n"
"v_and_b32_dpp v24, v46, v20 row_newbcast:9 row_mask:0xf bank_mask:0xf\n"
"v_and_b32_dpp v25, v46, v21 row_newbcast:9 row_mask:0xf bank_mask:0xf\n"
"v_and_b32_dpp v26, v46, v22 row_newbcast:9 row_mask:0xf bank_mask:0xf\n"
"v_and_b32_dpp v27, v46, v23 row_newbcast:9 row_mask:0xf bank_mask:0xf\n"
"s_waitcnt vmcnt(22)\n"
"s_nop 0\n"
"v_mfma_f32_16x16x32_bf16 v[40:43], v[24:27], v[116:119], v[40:43]\n"
"v_and_b32_dpp v24, v46, v20 row_newbcast:13 row_mask:0xf bank_mask:0xf\n"
"v_and_b32_dpp v25, v46, v21 row_newbcast:13 row_mask:0xf bank_mask:0xf\n"
"v_and_b32_dpp v26, v46, v22 row_newbcast:13 row_mask:0xf bank_mask:0xf\n"
"v_and_b32_dpp v27, v46, v23 row_newbcast:13 row_mask:0xf bank_mask:0xf\n"
"s_waitcnt vmcnt(21)\n"
"s_nop 0\n"
"v_mfma_f32_16x16x32_bf16 v[40:43], v[24:27], v[112:115], v[40:43]\n"
"v_and_b32_dpp v24, v46, v20 row_newbcast:2 row_mask:0xf bank_mask:0xf\n"
"v_and_b32_dpp v25, v46, v21 row_newbcast:2 row_mask:0xf bank_mask:0xf\n"
"v_and_b32_dpp v26, v46, v22 row_newbcast:2 row_mask:0xf bank_mask:0xf\n"
"v_and_b32_dpp v27, v46, v23 row_newbcast:2 row_mask:0xf bank_mask:0xf\n"
"s_waitcnt vmcnt(20)\n"
"s_nop 0\n"
"v_mfma_f32_16x16x32_bf16 v[40:43], v[24:27], v[108:111], v[40:43]\n"
"v_and_b32_dpp v24, v46, v20 row_newbcast:6 row_mask:0xf bank_mask:0xf\n"
"v_and_b32_dpp v25, v46, v21 row_newbcast:6 row_mask:0xf bank_mask:0xf\n"
"v_and_b32_dpp v26, v46, v22 row_newbcast:6 row_mask:0xf bank_mask:0xf\n"
"v_and_b32_dpp v27, v46, v23 row_newbcast:6 row_mask:0xf bank_mask:0xf\n"
"s_waitcnt vmcnt(19)\n"
"s_nop 0\n"
"v_mfma_f32_16x16x32_bf16 v[40:43], v[24:27], v[104:107], v[40:43]\n"
"v_and_b32_dpp v24, v46, v20 row_newbcast:10 row_mask:0xf bank_mask:0xf\n"
"v_and_b32_dpp v25, v46, v21 row_newbcast:10 row_mask:0xf bank_mask:0xf\n"
"v_and_b32_dpp v26, v46, v22 row_newbcast:10 row_mask:0xf bank_mask:0xf\n"
"v_and_b32_dpp v27, v46, v23 row_newbcast:10 row_mask:0xf bank_mask:0xf\n"
"s_waitcnt vmcnt(18)\n"
"s_nop 0\n"
"v_mfma_f32_16x16x32_bf16 v[40:43], v[24:27], v[100:103], v[40:43]\n"
"v_and_b32_dpp v24, v46, v20 row_newbcast:14 row_mask:0xf bank_mask:0xf\n"
"v_and_b32_dpp v25, v46, v21 row_newbcast:14 row_mask:0xf bank_mask:0xf\n"
"v_and_b32_dpp v26, v46, v22 row_newbcast:14 row_mask:0xf bank_mask:0xf\n"
"v_and_b32_dpp v27, v46, v23 row_newbcast:14 row_mask:0xf bank_mask:0xf\n"
"s_waitcnt vmcnt(17)\n"
"s_nop 0\n"
"v_mfma_f32_16x16x32_bf16 v[40:43], v[24:27], v[64:67], v[40:43]\n"
"s_mov_b32 exec_hi, 0\n"
"s_nop 6\n"
"ds_write_b128 v60, v[40:43]\n"
"s_mov_b64 exec, -1\n"
"s_waitcnt lgkmcnt(0)\n"
"s_barrier\n"
"s_cmp_lg_u32 s50, 0\n"
"s_cbranch_scc1 Lnomerge_509\n"
"ds_read_b32 v24, v63 offset:0\n"
"ds_read_b32 v25, v63 offset:576\n"
"ds_read_b32 v26, v63 offset:1152\n"
"ds_read_b32 v27, v63 offset:1728\n"
"ds_read_b128 v[4:7], v62 offset:0\n"
"ds_read_b128 v[8:11], v62 offset:576\n"
"ds_read_b128 v[12:15], v62 offset:1152\n"
"ds_read_b128 v[16:19], v62 offset:1728\n"
"s_waitcnt lgkmcnt(4)\n"
"v_max3_f32 v28, v24, v25, v26\n"
"v_max_f32 v28, v28, v27\n"
"v_sub_f32 v24, v24, v28\n"
"v_sub_f32 v25, v25, v28\n"
"v_sub_f32 v26, v26, v28\n"
"v_sub_f32 v27, v27, v28\n"
"v_exp_f32 v24, v24\n"
"v_exp_f32 v25, v25\n"
"v_exp_f32 v26, v26\n"
"v_exp_f32 v27, v27\n"
"s_waitcnt lgkmcnt(0)\n"
"v_mul_f32 v4, v4, v24\n"
"v_mul_f32 v5, v5, v24\n"
"v_mul_f32 v6, v6, v24\n"
"v_mul_f32 v7, v7, v24\n"
"v_fmac_f32 v4, v8, v25\n"
"v_fmac_f32 v5, v9, v25\n"
"v_fmac_f32 v6, v10, v25\n"
"v_fmac_f32 v7, v11, v25\n"
"v_fmac_f32 v4, v12, v26\n"
"v_fmac_f32 v5, v13, v26\n"
"v_fmac_f32 v6, v14, v26\n"
"v_fmac_f32 v7, v15, v26\n"
"v_fmac_f32 v4, v16, v27\n"
"v_fmac_f32 v5, v17, v27\n"
"v_fmac_f32 v6, v18, v27\n"
"v_fmac_f32 v7, v19, v27\n"
"v_cmp_eq_u32 vcc, 32, v0\n"
"s_nop 1\n"
"v_cndmask_b32 v4, v4, v28, vcc\n"
"buffer_store_dwordx4 v[4:7], v61, s[36:39], s70 offen sc1\n"
"s_xor_b32 s70, s70, 8192\n"
"buffer_store_dwordx4 v[4:7], v61, s[36:39], s70 offen sc1\n"
"Lnomerge_509:\n"
"s_waitcnt vmcnt(9)\n"
"v_perm_b32 v64, v196, v192, s68\n"
"v_perm_b32 v65, v204, v200, s68\n"
"v_perm_b32 v66, v212, v208, s68\n"
"v_perm_b32 v67, v220, v216, s68\n"
"v_perm_b32 v72, v196, v192, s69\n"
"v_perm_b32 v73, v204, v200, s69\n"
"v_perm_b32 v74, v212, v208, s69\n"
"v_perm_b32 v75, v220, v216, s69\n"
"v_perm_b32 v80, v197, v193, s68\n"
"v_perm_b32 v81, v205, v201, s68\n"
"v_perm_b32 v82, v213, v209, s68\n"
"v_perm_b32 v83, v221, v217, s68\n"
"v_perm_b32 v88, v197, v193, s69\n"
"v_perm_b32 v89, v205, v201, s69\n"
"v_perm_b32 v90, v213, v209, s69\n"
"v_perm_b32 v91, v221, v217, s69\n"
"v_perm_b32 v96, v198, v194, s68\n"
"v_perm_b32 v97, v206, v202, s68\n"
"v_perm_b32 v98, v214, v210, s68\n"
"v_perm_b32 v99, v222, v218, s68\n"
"v_perm_b32 v104, v198, v194, s69\n"
"v_perm_b32 v105, v206, v202, s69\n"
"v_perm_b32 v106, v214, v210, s69\n"
"v_perm_b32 v107, v222, v218, s69\n"
"v_perm_b32 v112, v199, v195, s68\n"
"v_perm_b32 v113, v207, v203, s68\n"
"v_perm_b32 v114, v215, v211, s68\n"
"v_perm_b32 v115, v223, v219, s68\n"
"v_perm_b32 v120, v199, v195, s69\n"
"v_perm_b32 v121, v207, v203, s69\n"
"v_perm_b32 v122, v215, v211, s69\n"
"v_perm_b32 v123, v223, v219, s69\n"
"s_waitcnt vmcnt(1)\n"
"v_perm_b32 v68, v228, v224, s68\n"
"v_perm_b32 v69, v236, v232, s68\n"
"v_perm_b32 v70, v244, v240, s68\n"
"v_perm_b32 v71, v252, v248, s68\n"
"v_perm_b32 v76, v228, v224, s69\n"
"v_perm_b32 v77, v236, v232, s69\n"
"v_perm_b32 v78, v244, v240, s69\n"
"v_perm_b32 v79, v252, v248, s69\n"
"v_perm_b32 v84, v229, v225, s68\n"
"v_perm_b32 v85, v237, v233, s68\n"
"v_perm_b32 v86, v245, v241, s68\n"
"v_perm_b32 v87, v253, v249, s68\n"
"v_perm_b32 v92, v229, v225, s69\n"
"v_perm_b32 v93, v237, v233, s69\n"
"v_perm_b32 v94, v245, v241, s69\n"
"v_perm_b32 v95, v253, v249, s69\n"
"v_perm_b32 v100, v230, v226, s68\n"
"v_perm_b32 v101, v238, v234, s68\n"
"v_perm_b32 v102, v246, v242, s68\n"
"v_perm_b32 v103, v254, v250, s68\n"
"v_perm_b32 v108, v230, v226, s69\n"
"v_perm_b32 v109, v238, v234, s69\n"
"v_perm_b32 v110, v246, v242, s69\n"
"v_perm_b32 v111, v254, v250, s69\n"
"v_perm_b32 v116, v231, v227, s68\n"
"v_perm_b32 v117, v239, v235, s68\n"
"v_perm_b32 v118, v247, v243, s68\n"
"v_perm_b32 v119, v255, v251, s68\n"
"v_perm_b32 v124, v231, v227, s69\n"
"v_perm_b32 v125, v239, v235, s69\n"
"v_perm_b32 v126, v247, v243, s69\n"
"v_perm_b32 v127, v255, v251, s69\n"
"s_mov_b64 exec, 0\n"
"s_cmp_eq_u32 s50, 1\n"
"s_cbranch_scc1 LCD1\n"
"s_cmp_eq_u32 s50, 2\n"
"s_cbranch_scc1 LRTY\n"
"s_cmp_eq_u32 s50, 3\n"
"s_cbranch_scc1 LCD3\n"
"s_branch LS1\n"
"LCDRYR:\n"
"s_waitcnt vmcnt(0) lgkmcnt(0)\n"
"s_mov_b32 s51, 0\n"
"s_mov_b64 exec, -1\n"
"v_cmp_eq_u32 vcc, s59, v33\n"
"s_nop 1\n"
"v_cndmask_b32 v32, 0, v32, vcc\n"
"v_cmp_eq_u32 vcc, s59, v35\n"
"s_nop 1\n"
"v_cndmask_b32 v34, 0, v34, vcc\n"
"v_add_u32 v32, v32, v34\n"
"v_cvt_f32_i32 v32, v32\n"
"s_nop 1\n"
"v_add_f32_dpp v32, v32, v32 quad_perm:[1,0,3,2] row_mask:0xf bank_mask:0xf\n"
"s_nop 1\n"
"v_add_f32_dpp v32, v32, v32 quad_perm:[2,3,0,1] row_mask:0xf bank_mask:0xf\n"
"s_nop 1\n"
"v_add_f32_dpp v32, v32, v32 row_half_mirror row_mask:0xf bank_mask:0xf\n"
"s_nop 1\n"
"v_add_f32_dpp v32, v32, v32 row_mirror row_mask:0xf bank_mask:0xf\n"
"s_nop 1\n"
"v_add_f32_dpp v32, v32, v32 row_bcast:15 row_mask:0xa bank_mask:0xf\n"
"s_nop 1\n"
"v_add_f32_dpp v32, v32, v32 row_bcast:31 row_mask:0xc bank_mask:0xf\n"
"s_nop 0\n"
"v_readlane_b32 s70, v32, 63\n"
"s_getreg_b32 s71, hwreg(HW_REG_XCC_ID, 2, 1)\n"
"s_nop 3\n"
"s_lshr_b32 s70, s70, 31\n"
"s_xor_b32 s70, s70, s71\n"
"s_lshl_b32 s99, s70, 13\n"
"s_branch LS1\n"
"LC_P2:\n"
"s_nop 7\n"
"s_add_u32 s52, s53, 0x17000\n"
"s_and_b32 s52, s52, s54\n"
"buffer_load_dwordx4 v[192:195], v1, s[28:31], s52 offen offset:0 nt\n"
"s_add_u32 s52, s53, 0x18000\n"
"s_and_b32 s52, s52, s54\n"
"buffer_load_dwordx4 v[196:199], v1, s[28:31], s52 offen offset:0 nt\n"
"s_add_u32 s52, s53, 0x19000\n"
"s_and_b32 s52, s52, s54\n"
"buffer_load_dwordx4 v[200:203], v1, s[28:31], s52 offen offset:0 nt\n"
"s_add_u32 s52, s53, 0x1a000\n"
"s_and_b32 s52, s52, s54\n"
"buffer_load_dwordx4 v[204:207], v1, s[28:31], s52 offen offset:0 nt\n"
"s_add_u32 s52, s53, 0x1b000\n"
"s_and_b32 s52, s52, s54\n"
"buffer_load_dwordx4 v[208:211], v1, s[28:31], s52 offen offset:0 nt\n"
"s_add_u32 s52, s53, 0x1c000\n"
"s_and_b32 s52, s52, s54\n"
"buffer_load_dwordx4 v[212:215], v1, s[28:31], s52 offen offset:0 nt\n"
"s_add_u32 s52, s53, 0x1d000\n"
"s_and_b32 s52, s52, s54\n"
"buffer_load_dwordx4 v[216:219], v1, s[28:31], s52 offen offset:0 nt\n"
"s_add_u32 s52, s53, 0x1e000\n"
"s_and_b32 s52, s52, s54\n"
"buffer_load_dwordx4 v[220:223], v1, s[28:31], s52 offen offset:0 nt\n"
"s_add_u32 s52, s53, 0x1f000\n"
"s_and_b32 s52, s52, s54\n"
"buffer_load_dwordx4 v[224:227], v1, s[28:31], s52 offen offset:0 nt\n"
"s_add_u32 s52, s53, 0x20000\n"
"s_and_b32 s52, s52, s54\n"
"buffer_load_dwordx4 v[228:231], v1, s[28:31], s52 offen offset:0 nt\n"
"s_add_u32 s52, s53, 0x21000\n"
"s_and_b32 s52, s52, s54\n"
"buffer_load_dwordx4 v[232:235], v1, s[28:31], s52 offen offset:0 nt\n"
"s_add_u32 s52, s53, 0x22000\n"
"s_and_b32 s52, s52, s54\n"
"buffer_load_dwordx4 v[236:239], v1, s[28:31], s52 offen offset:0 nt\n"
"s_add_u32 s52, s53, 0x23000\n"
"s_and_b32 s52, s52, s54\n"
"buffer_load_dwordx4 v[240:243], v1, s[28:31], s52 offen offset:0 nt\n"
"s_add_u32 s52, s53, 0x24000\n"
"s_and_b32 s52, s52, s54\n"
"buffer_load_dwordx4 v[244:247], v1, s[28:31], s52 offen offset:0 nt\n"
"s_add_u32 s52, s53, 0x25000\n"
"s_and_b32 s52, s52, s54\n"
"buffer_load_dwordx4 v[248:251], v1, s[28:31], s52 offen offset:0 nt\n"
"s_add_u32 s52, s53, 0x26000\n"
"s_and_b32 s52, s52, s54\n"
"buffer_load_dwordx4 v[252:255], v1, s[28:31], s52 offen offset:0 nt\n"
"v_and_b32 v56, 1, v0\n"
"v_lshlrev_b32 v56, 4, v56\n"
"v_mov_b32 v57, 0xffff\n"
"v_lshlrev_b32 v56, v56, v57\n"
"v_bfe_u32 v57, v0, 1, 3\n"
"v_cmp_eq_u32 vcc, 0, v57\n"
"s_nop 1\n"
"v_cndmask_b32 v20, 0, v56, vcc\n"
"v_cmp_eq_u32 vcc, 1, v57\n"
"s_nop 1\n"
"v_cndmask_b32 v21, 0, v56, vcc\n"
"v_cmp_eq_u32 vcc, 2, v57\n"
"s_nop 1\n"
"v_cndmask_b32 v22, 0, v56, vcc\n"
"v_cmp_eq_u32 vcc, 3, v57\n"
"s_nop 1\n"
"v_cndmask_b32 v23, 0, v56, vcc\n"
"v_mov_b32 v4, 0\n"
"v_mov_b32 v5, 0\n"
"v_mov_b32 v6, 0\n"
"v_mov_b32 v7, 0\n"
"v_mov_b32 v8, 0\n"
"v_mov_b32 v9, 0\n"
"v_mov_b32 v10, 0\n"
"v_mov_b32 v11, 0\n"
"v_mov_b32 v12, 0\n"
"v_mov_b32 v13, 0\n"
"v_mov_b32 v14, 0\n"
"v_mov_b32 v15, 0\n"
"v_mov_b32 v16, 0\n"
"v_mov_b32 v17, 0\n"
"v_mov_b32 v18, 0\n"
"v_mov_b32 v19, 0\n"
"v_mov_b32 v44, 0xff800000\n"
"v_mov_b32 v45, 0xff800000\n"
"v_mov_b32 v46, 0xff800000\n"
"v_mov_b32 v47, 0xff800000\n"
"v_mov_b32 v48, 0xff800000\n"
"v_mov_b32 v49, 0xff800000\n"
"v_mov_b32 v50, 0xff800000\n"
"v_mov_b32 v51, 0xff800000\n"
"v_mov_b32 v52, 0xff800000\n"
"v_lshlrev_b32 v53, 7, v0\n"
"v_mov_b32 v54, 0x7ff00000\n"
"v_cmp_gt_u32 vcc, 8, v0\n"
"s_nop 1\n"
"v_cndmask_b32 v53, v54, v53, vcc\n"
"v_lshrrev_b32 v57, 4, v0\n"
"v_lshlrev_b32 v57, 7, v57\n"
"v_and_b32 v58, 3, v0\n"
"v_lshl_or_b32 v57, v58, 5, v57\n"
"s_lshr_b32 s56, s48, 4\n"
"s_lshl_b32 s56, s56, 4\n"
"s_add_u32 s56, s56, s48\n"
"s_lshl_b32 s56, s56, 9\n"
"s_add_u32 s56, s56, 0x10000\n"
"s_add_u32 s56, s56, s99\n"
"buffer_load_dwordx4 v[24:27], v57, s[36:39], s56 offen sc1\n"
"buffer_load_dwordx4 v[28:31], v57, s[36:39], s56 offen offset:16 sc1\n"
"s_waitcnt vmcnt(0)\n"
"v_cmp_ne_u32_e64 vcc, s59, v25\n"
"v_cmp_ne_u32_e64 s[72:73], s59, v27\n"
"v_cmp_ne_u32_e64 s[74:75], s59, v29\n"
"v_cmp_ne_u32_e64 s[76:77], s59, v31\n"
"s_or_b64 vcc, vcc, s[72:73]\n"
"s_or_b64 vcc, vcc, s[74:75]\n"
"s_or_b64 vcc, vcc, s[76:77]\n"
"s_cbranch_scc0 Lqok_510\n"
"s_mov_b32 s55, 4194304\n"
"Ltpoll_511:\n"
"buffer_load_dwordx4 v[24:27], v57, s[36:39], s56 offen sc1\n"
"buffer_load_dwordx4 v[28:31], v57, s[36:39], s56 offen offset:16 sc1\n"
"s_waitcnt vmcnt(0)\n"
"v_cmp_ne_u32_e64 vcc, s59, v25\n"
"v_cmp_ne_u32_e64 s[72:73], s59, v27\n"
"v_cmp_ne_u32_e64 s[74:75], s59, v29\n"
"v_cmp_ne_u32_e64 s[76:77], s59, v31\n"
"s_or_b64 vcc, vcc, s[72:73]\n"
"s_or_b64 vcc, vcc, s[74:75]\n"
"s_or_b64 vcc, vcc, s[76:77]\n"
"s_cbranch_scc0 Ltdone_512\n"
"s_sub_u32 s55, s55, 1\n"
"s_cbranch_scc0 Ltpoll_511\n"
"Ltdone_512:\n"
"Lqok_510:\n"
"v_mov_b32_dpp v4, v24 quad_perm:[0,1,2,3] row_mask:0xf bank_mask:0x1\n"
"v_mov_b32_dpp v5, v26 quad_perm:[0,1,2,3] row_mask:0xf bank_mask:0x1\n"
"v_mov_b32_dpp v6, v28 quad_perm:[0,1,2,3] row_mask:0xf bank_mask:0x1\n"
"v_mov_b32_dpp v7, v30 quad_perm:[0,1,2,3] row_mask:0xf bank_mask:0x1\n"
"v_mov_b32_dpp v8, v24 quad_perm:[0,1,2,3] row_mask:0xf bank_mask:0x2\n"
"v_mov_b32_dpp v9, v26 quad_perm:[0,1,2,3] row_mask:0xf bank_mask:0x2\n"
"v_mov_b32_dpp v10, v28 quad_perm:[0,1,2,3] row_mask:0xf bank_mask:0x2\n"
"v_mov_b32_dpp v11, v30 quad_perm:[0,1,2,3] row_mask:0xf bank_mask:0x2\n"
"v_mov_b32_dpp v12, v24 quad_perm:[0,1,2,3] row_mask:0xf bank_mask:0x4\n"
"v_mov_b32_dpp v13, v26 quad_perm:[0,1,2,3] row_mask:0xf bank_mask:0x4\n"
"v_mov_b32_dpp v14, v28 quad_perm:[0,1,2,3] row_mask:0xf bank_mask:0x4\n"
"v_mov_b32_dpp v15, v30 quad_perm:[0,1,2,3] row_mask:0xf bank_mask:0x4\n"
"v_mov_b32_dpp v16, v24 quad_perm:[0,1,2,3] row_mask:0xf bank_mask:0x8\n"
"v_mov_b32_dpp v17, v26 quad_perm:[0,1,2,3] row_mask:0xf bank_mask:0x8\n"
"v_mov_b32_dpp v18, v28 quad_perm:[0,1,2,3] row_mask:0xf bank_mask:0x8\n"
"v_mov_b32_dpp v19, v30 quad_perm:[0,1,2,3] row_mask:0xf bank_mask:0x8\n"
"ds_read_b128 v[188:191], v3 offset:0\n"
"ds_read_b128 v[184:187], v3 offset:1024\n"
"ds_read_b128 v[180:183], v3 offset:2048\n"
"ds_read_b128 v[176:179], v3 offset:3072\n"
"ds_read_b128 v[172:175], v3 offset:4096\n"
"ds_read_b128 v[168:171], v3 offset:5120\n"
"ds_read_b128 v[164:167], v3 offset:6144\n"
"ds_read_b128 v[160:163], v3 offset:7168\n"
"s_waitcnt lgkmcnt(4)\n"
"v_mfma_f32_16x16x32_bf16 v[32:35], v[188:191], v[4:7], 0\n"
"s_add_u32 s52, s53, 0x27000\n"
"s_and_b32 s52, s52, s54\n"
"buffer_load_dwordx4 v[128:131], v1, s[28:31], s52 offen offset:0 nt\n"
"v_mfma_f32_16x16x32_bf16 v[32:35], v[184:187], v[8:11], v[32:35]\n"
"s_add_u32 s52, s53, 0x28000\n"
"s_and_b32 s52, s52, s54\n"
"buffer_load_dwordx4 v[132:135], v1, s[28:31], s52 offen offset:0 nt\n"
"v_mfma_f32_16x16x32_bf16 v[32:35], v[180:183], v[12:15], v[32:35]\n"
"s_add_u32 s52, s53, 0x29000\n"
"s_and_b32 s52, s52, s54\n"
"buffer_load_dwordx4 v[136:139], v1, s[28:31], s52 offen offset:0 nt\n"
"v_mfma_f32_16x16x32_bf16 v[32:35], v[176:179], v[16:19], v[32:35]\n"
"s_add_u32 s52, s53, 0x2a000\n"
"s_and_b32 s52, s52, s54\n"
"buffer_load_dwordx4 v[140:143], v1, s[28:31], s52 offen offset:0 nt\n"
"ds_read_b128 v[176:179], v3 offset:8192\n"
"ds_read_b128 v[180:183], v3 offset:9216\n"
"ds_read_b128 v[184:187], v3 offset:10240\n"
"ds_read_b128 v[188:191], v3 offset:11264\n"
"s_waitcnt lgkmcnt(4)\n"
"v_mfma_f32_16x16x32_bf16 v[36:39], v[172:175], v[4:7], 0\n"
"v_mfma_f32_16x16x32_bf16 v[36:39], v[168:171], v[8:11], v[36:39]\n"
"v_mfma_f32_16x16x32_bf16 v[36:39], v[164:167], v[12:15], v[36:39]\n"
"v_mov_b32_dpp v63, v32 quad_perm:[0,0,0,0] row_mask:0xf bank_mask:0xf\n"
"v_add_f32_dpp v63, v33, v63 quad_perm:[1,1,1,1] row_mask:0xf bank_mask:0xf\n"
"v_add_f32_dpp v63, v34, v63 quad_perm:[2,2,2,2] row_mask:0xf bank_mask:0xf\n"
"v_add_f32_dpp v63, v35, v63 quad_perm:[3,3,3,3] row_mask:0xf bank_mask:0xf\n"
"v_cndmask_b32_e64 v44, v44, v63, s[60:61]\n"
"v_mfma_f32_16x16x32_bf16 v[36:39], v[160:163], v[16:19], v[36:39]\n"
"ds_read_b128 v[160:163], v3 offset:12288\n"
"ds_read_b128 v[164:167], v3 offset:13312\n"
"ds_read_b128 v[168:171], v3 offset:14336\n"
"ds_read_b128 v[172:175], v3 offset:15360\n"
"s_waitcnt lgkmcnt(4)\n"
"v_mfma_f32_16x16x32_bf16 v[32:35], v[176:179], v[4:7], 0\n"
"v_mfma_f32_16x16x32_bf16 v[32:35], v[180:183], v[8:11], v[32:35]\n"
"v_mfma_f32_16x16x32_bf16 v[32:35], v[184:187], v[12:15], v[32:35]\n"
"v_mov_b32_dpp v63, v36 quad_perm:[0,0,0,0] row_mask:0xf bank_mask:0xf\n"
"v_add_f32_dpp v63, v37, v63 quad_perm:[1,1,1,1] row_mask:0xf bank_mask:0xf\n"
"v_add_f32_dpp v63, v38, v63 quad_perm:[2,2,2,2] row_mask:0xf bank_mask:0xf\n"
"v_add_f32_dpp v63, v39, v63 quad_perm:[3,3,3,3] row_mask:0xf bank_mask:0xf\n"
"v_cndmask_b32_e64 v44, v44, v63, s[62:63]\n"
"v_mfma_f32_16x16x32_bf16 v[32:35], v[188:191], v[16:19], v[32:35]\n"
"s_add_u32 s52, s53, 0x2b000\n"
"s_and_b32 s52, s52, s54\n"
"buffer_load_dwordx4 v[144:147], v1, s[28:31], s52 offen offset:0 nt\n"
"ds_read_b128 v[188:191], v3 offset:16384\n"
"ds_read_b128 v[184:187], v3 offset:17408\n"
"ds_read_b128 v[180:183], v3 offset:18432\n"
"ds_read_b128 v[176:179], v3 offset:19456\n"
"s_waitcnt lgkmcnt(4)\n"
"v_mfma_f32_16x16x32_bf16 v[36:39], v[160:163], v[4:7], 0\n"
"buffer_load_dwordx4 v[148:151], v2, s[32:35], s52 offen offset:0 nt\n"
"v_mfma_f32_16x16x32_bf16 v[36:39], v[164:167], v[8:11], v[36:39]\n"
"s_add_u32 s52, s53, 0x2a000\n"
"s_and_b32 s52, s52, s54\n"
"buffer_load_dwordx4 v[152:155], v2, s[32:35], s52 offen offset:0 nt\n"
"v_mfma_f32_16x16x32_bf16 v[36:39], v[168:171], v[12:15], v[36:39]\n"
"s_add_u32 s52, s53, 0x29000\n"
"s_and_b32 s52, s52, s54\n"
"buffer_load_dwordx4 v[156:159], v2, s[32:35], s52 offen offset:0 nt\n"
"v_mov_b32_dpp v63, v32 quad_perm:[0,0,0,0] row_mask:0xf bank_mask:0xf\n"
"v_add_f32_dpp v63, v33, v63 quad_perm:[1,1,1,1] row_mask:0xf bank_mask:0xf\n"
"v_add_f32_dpp v63, v34, v63 quad_perm:[2,2,2,2] row_mask:0xf bank_mask:0xf\n"
"v_add_f32_dpp v63, v35, v63 quad_perm:[3,3,3,3] row_mask:0xf bank_mask:0xf\n"
"v_cndmask_b32_e64 v44, v44, v63, s[64:65]\n"
"v_mfma_f32_16x16x32_bf16 v[36:39], v[172:175], v[16:19], v[36:39]\n"
"s_add_u32 s52, s53, 0x28000\n"
"s_and_b32 s52, s52, s54\n"
"buffer_load_dwordx4 v[160:163], v2, s[32:35], s52 offen offset:0 nt\n"
"ds_read_b128 v[172:175], v3 offset:20480\n"
"ds_read_b128 v[168:171], v3 offset:21504\n"
"ds_read_b128 v[164:167], v3 offset:22528\n"
"s_waitcnt lgkmcnt(3)\n"
"v_mfma_f32_16x16x32_bf16 v[32:35], v[188:191], v[4:7], 0\n"
"s_add_u32 s52, s53, 0x27000\n"
"s_and_b32 s52, s52, s54\n"
"buffer_load_dwordx4 v[188:191], v2, s[32:35], s52 offen offset:0 nt\n"
"v_mfma_f32_16x16x32_bf16 v[32:35], v[184:187], v[8:11], v[32:35]\n"
"s_add_u32 s52, s53, 0x26000\n"
"s_and_b32 s52, s52, s54\n"
"buffer_load_dwordx4 v[184:187], v2, s[32:35], s52 offen offset:0 nt\n"
"v_mfma_f32_16x16x32_bf16 v[32:35], v[180:183], v[12:15], v[32:35]\n"
"s_add_u32 s52, s53, 0x25000\n"
"s_and_b32 s52, s52, s54\n"
"buffer_load_dwordx4 v[180:183], v2, s[32:35], s52 offen offset:0 nt\n"
"v_mov_b32_dpp v63, v36 quad_perm:[0,0,0,0] row_mask:0xf bank_mask:0xf\n"
"v_add_f32_dpp v63, v37, v63 quad_perm:[1,1,1,1] row_mask:0xf bank_mask:0xf\n"
"v_add_f32_dpp v63, v38, v63 quad_perm:[2,2,2,2] row_mask:0xf bank_mask:0xf\n"
"v_add_f32_dpp v63, v39, v63 quad_perm:[3,3,3,3] row_mask:0xf bank_mask:0xf\n"
"v_cndmask_b32_e64 v44, v44, v63, s[66:67]\n"
"v_mfma_f32_16x16x32_bf16 v[32:35], v[176:179], v[16:19], v[32:35]\n"
"s_add_u32 s52, s53, 0x24000\n"
"s_and_b32 s52, s52, s54\n"
"buffer_load_dwordx4 v[176:179], v2, s[32:35], s52 offen offset:0 nt\n"
"s_waitcnt lgkmcnt(0)\n"
"v_mfma_f32_16x16x32_bf16 v[36:39], v[172:175], v[4:7], 0\n"
"s_add_u32 s52, s53, 0x23000\n"
"s_and_b32 s52, s52, s54\n"
"buffer_load_dwordx4 v[172:175], v2, s[32:35], s52 offen offset:0 nt\n"
"v_mfma_f32_16x16x32_bf16 v[36:39], v[168:171], v[8:11], v[36:39]\n"
"s_add_u32 s52, s53, 0x22000\n"
"s_and_b32 s52, s52, s54\n"
"buffer_load_dwordx4 v[168:171], v2, s[32:35], s52 offen offset:0 nt\n"
"v_mfma_f32_16x16x32_bf16 v[36:39], v[164:167], v[12:15], v[36:39]\n"
"s_add_u32 s52, s53, 0x21000\n"
"s_and_b32 s52, s52, s54\n"
"buffer_load_dwordx4 v[164:167], v2, s[32:35], s52 offen offset:0 nt\n"
"v_mov_b32_dpp v63, v32 quad_perm:[0,0,0,0] row_mask:0xf bank_mask:0xf\n"
"v_add_f32_dpp v63, v33, v63 quad_perm:[1,1,1,1] row_mask:0xf bank_mask:0xf\n"
"v_add_f32_dpp v63, v34, v63 quad_perm:[2,2,2,2] row_mask:0xf bank_mask:0xf\n"
"v_add_f32_dpp v63, v35, v63 quad_perm:[3,3,3,3] row_mask:0xf bank_mask:0xf\n"
"v_cndmask_b32_e64 v45, v45, v63, s[60:61]\n"
"v_mfma_f32_16x16x32_bf16 v[36:39], v[192:195], v[16:19], v[36:39]\n"
"v_mfma_f32_16x16x32_bf16 v[32:35], v[196:199], v[4:7], 0\n"
"v_mfma_f32_16x16x32_bf16 v[32:35], v[200:203], v[8:11], v[32:35]\n"
"v_mfma_f32_16x16x32_bf16 v[32:35], v[204:207], v[12:15], v[32:35]\n"
"s_nop 4\n"
"v_mov_b32_dpp v63, v36 quad_perm:[0,0,0,0] row_mask:0xf bank_mask:0xf\n"
"v_add_f32_dpp v63, v37, v63 quad_perm:[1,1,1,1] row_mask:0xf bank_mask:0xf\n"
"v_add_f32_dpp v63, v38, v63 quad_perm:[2,2,2,2] row_mask:0xf bank_mask:0xf\n"
"v_add_f32_dpp v63, v39, v63 quad_perm:[3,3,3,3] row_mask:0xf bank_mask:0xf\n"
"v_cndmask_b32_e64 v45, v45, v63, s[62:63]\n"
"v_mfma_f32_16x16x32_bf16 v[32:35], v[208:211], v[16:19], v[32:35]\n"
"v_mfma_f32_16x16x32_bf16 v[36:39], v[212:215], v[4:7], 0\n"
"v_mfma_f32_16x16x32_bf16 v[36:39], v[216:219], v[8:11], v[36:39]\n"
"v_mfma_f32_16x16x32_bf16 v[36:39], v[220:223], v[12:15], v[36:39]\n"
"s_nop 4\n"
"v_mov_b32_dpp v63, v32 quad_perm:[0,0,0,0] row_mask:0xf bank_mask:0xf\n"
"v_add_f32_dpp v63, v33, v63 quad_perm:[1,1,1,1] row_mask:0xf bank_mask:0xf\n"
"v_add_f32_dpp v63, v34, v63 quad_perm:[2,2,2,2] row_mask:0xf bank_mask:0xf\n"
"v_add_f32_dpp v63, v35, v63 quad_perm:[3,3,3,3] row_mask:0xf bank_mask:0xf\n"
"v_cndmask_b32_e64 v45, v45, v63, s[64:65]\n"
"v_mfma_f32_16x16x32_bf16 v[36:39], v[224:227], v[16:19], v[36:39]\n"
"v_mfma_f32_16x16x32_bf16 v[32:35], v[228:231], v[4:7], 0\n"
"v_mfma_f32_16x16x32_bf16 v[32:35], v[232:235], v[8:11], v[32:35]\n"
"v_mfma_f32_16x16x32_bf16 v[32:35], v[236:239], v[12:15], v[32:35]\n"
"s_nop 4\n"
"v_mov_b32_dpp v63, v36 quad_perm:[0,0,0,0] row_mask:0xf bank_mask:0xf\n"
"v_add_f32_dpp v63, v37, v63 quad_perm:[1,1,1,1] row_mask:0xf bank_mask:0xf\n"
"v_add_f32_dpp v63, v38, v63 quad_perm:[2,2,2,2] row_mask:0xf bank_mask:0xf\n"
"v_add_f32_dpp v63, v39, v63 quad_perm:[3,3,3,3] row_mask:0xf bank_mask:0xf\n"
"v_cndmask_b32_e64 v45, v45, v63, s[66:67]\n"
"v_mfma_f32_16x16x32_bf16 v[32:35], v[240:243], v[16:19], v[32:35]\n"
"v_mfma_f32_16x16x32_bf16 v[36:39], v[244:247], v[4:7], 0\n"
"v_mfma_f32_16x16x32_bf16 v[36:39], v[248:251], v[8:11], v[36:39]\n"
"v_mfma_f32_16x16x32_bf16 v[36:39], v[252:255], v[12:15], v[36:39]\n"
"s_nop 4\n"
"v_mov_b32_dpp v63, v32 quad_perm:[0,0,0,0] row_mask:0xf bank_mask:0xf\n"
"v_add_f32_dpp v63, v33, v63 quad_perm:[1,1,1,1] row_mask:0xf bank_mask:0xf\n"
"v_add_f32_dpp v63, v34, v63 quad_perm:[2,2,2,2] row_mask:0xf bank_mask:0xf\n"
"v_add_f32_dpp v63, v35, v63 quad_perm:[3,3,3,3] row_mask:0xf bank_mask:0xf\n"
"v_cndmask_b32_e64 v46, v46, v63, s[60:61]\n"
"s_waitcnt vmcnt(15)\n"
"v_mfma_f32_16x16x32_bf16 v[36:39], v[128:131], v[16:19], v[36:39]\n"
"s_add_u32 s52, s53, 0x20000\n"
"s_and_b32 s52, s52, s54\n"
"buffer_load_dwordx4 v[192:195], v2, s[32:35], s52 offen offset:0 nt\n"
"s_waitcnt vmcnt(15)\n"
"v_mfma_f32_16x16x32_bf16 v[32:35], v[132:135], v[4:7], 0\n"
"s_add_u32 s52, s53, 0x1f000\n"
"s_and_b32 s52, s52, s54\n"
"buffer_load_dwordx4 v[196:199], v2, s[32:35], s52 offen offset:0 nt\n"
"s_waitcnt vmcnt(15)\n"
"v_mfma_f32_16x16x32_bf16 v[32:35], v[136:139], v[8:11], v[32:35]\n"
"s_add_u32 s52, s53, 0x1e000\n"
"s_and_b32 s52, s52, s54\n"
"buffer_load_dwordx4 v[200:203], v2, s[32:35], s52 offen offset:0 nt\n"
"s_waitcnt vmcnt(15)\n"
"v_mfma_f32_16x16x32_bf16 v[32:35], v[140:143], v[12:15], v[32:35]\n"
"s_add_u32 s52, s53, 0x1d000\n"
"s_and_b32 s52, s52, s54\n"
"buffer_load_dwordx4 v[204:207], v2, s[32:35], s52 offen offset:0 nt\n"
"v_mov_b32_dpp v63, v36 quad_perm:[0,0,0,0] row_mask:0xf bank_mask:0xf\n"
"v_add_f32_dpp v63, v37, v63 quad_perm:[1,1,1,1] row_mask:0xf bank_mask:0xf\n"
"v_add_f32_dpp v63, v38, v63 quad_perm:[2,2,2,2] row_mask:0xf bank_mask:0xf\n"
"v_add_f32_dpp v63, v39, v63 quad_perm:[3,3,3,3] row_mask:0xf bank_mask:0xf\n"
"v_cndmask_b32_e64 v46, v46, v63, s[62:63]\n"
"s_waitcnt vmcnt(15)\n"
"v_mfma_f32_16x16x32_bf16 v[32:35], v[144:147], v[16:19], v[32:35]\n"
"s_add_u32 s52, s53, 0x1c000\n"
"s_and_b32 s52, s52, s54\n"
"buffer_load_dwordx4 v[208:211], v2, s[32:35], s52 offen offset:0 nt\n"
"s_nop 4\n"
"v_mov_b32_dpp v63, v32 quad_perm:[0,0,0,0] row_mask:0xf bank_mask:0xf\n"
"v_add_f32_dpp v63, v33, v63 quad_perm:[1,1,1,1] row_mask:0xf bank_mask:0xf\n"
"v_add_f32_dpp v63, v34, v63 quad_perm:[2,2,2,2] row_mask:0xf bank_mask:0xf\n"
"v_add_f32_dpp v63, v35, v63 quad_perm:[3,3,3,3] row_mask:0xf bank_mask:0xf\n"
"v_cndmask_b32_e64 v46, v46, v63, s[64:65]\n"
"v_cvt_pk_bf16_f32 v58, v44, v45\n"
"v_lshlrev_b32 v44, 16, v58\n"
"v_and_b32 v45, 0xffff0000, v58\n"
"v_mul_f32 v44, 0x3db504f3, v44\n"
"v_mul_f32 v45, 0x3db504f3, v45\n"
"v_cvt_pk_bf16_f32 v58, v44, v45\n"
"v_lshlrev_b32 v44, 16, v58\n"
"v_and_b32 v45, 0xffff0000, v58\n"
"v_cvt_pk_bf16_f32 v58, v46, v46\n"
"v_lshlrev_b32 v46, 16, v58\n"
"v_mul_f32 v46, 0x3db504f3, v46\n"
"v_cvt_pk_bf16_f32 v58, v46, v46\n"
"v_lshlrev_b32 v46, 16, v58\n"
"v_max_f32 v58, v44, v45\n"
"v_max_f32 v58, v58, v46\n"
"s_nop 1\n"
"v_max_f32_dpp v58, v58, v58 quad_perm:[1,0,3,2] row_mask:0xf bank_mask:0xf\n"
"s_nop 1\n"
"v_max_f32_dpp v58, v58, v58 quad_perm:[2,3,0,1] row_mask:0xf bank_mask:0xf\n"
"s_nop 1\n"
"v_max_f32_dpp v58, v58, v58 row_half_mirror row_mask:0xf bank_mask:0xf\n"
"s_nop 1\n"
"v_max_f32_dpp v58, v58, v58 row_mirror row_mask:0xf bank_mask:0xf\n"
"s_nop 1\n"
"v_max_f32_dpp v58, v58, v58 row_bcast:15 row_mask:0xa bank_mask:0xf\n"
"s_nop 1\n"
"v_max_f32_dpp v58, v58, v58 row_bcast:31 row_mask:0xc bank_mask:0xf\n"
"v_mul_f32 v58, 0x3fb8aa3b, v58\n"
"s_nop 0\n"
"v_readlane_b32 s58, v58, 63\n"
"v_mov_b32 v59, 0x3fb8aa3b\n"
"s_nop 0\n"
"v_fma_f32 v44, v44, v59, -s58\n"
"v_fma_f32 v45, v45, v59, -s58\n"
"v_fma_f32 v46, v46, v59, -s58\n"
"v_exp_f32 v44, v44\n"
"v_exp_f32 v45, v45\n"
"v_exp_f32 v46, v46\n"
"v_add_f32 v58, v44, v45\n"
"v_add_f32 v58, v58, v46\n"
"v_cvt_pk_bf16_f32 v44, v44, v44\n"
"v_cvt_pk_bf16_f32 v45, v45, v45\n"
"v_cvt_pk_bf16_f32 v46, v46, v46\n"
"v_add_f32_dpp v58, v58, v58 quad_perm:[1,0,3,2] row_mask:0xf bank_mask:0xf\n"
"s_nop 1\n"
"v_add_f32_dpp v58, v58, v58 quad_perm:[2,3,0,1] row_mask:0xf bank_mask:0xf\n"
"s_nop 1\n"
"v_add_f32_dpp v58, v58, v58 row_half_mirror row_mask:0xf bank_mask:0xf\n"
"s_nop 1\n"
"v_add_f32_dpp v58, v58, v58 row_mirror row_mask:0xf bank_mask:0xf\n"
"s_nop 1\n"
"v_add_f32_dpp v58, v58, v58 row_bcast:15 row_mask:0xa bank_mask:0xf\n"
"s_nop 1\n"
"v_add_f32_dpp v58, v58, v58 row_bcast:31 row_mask:0xc bank_mask:0xf\n"
"v_mov_b32 v61, v58\n"
"v_mov_b32 v60, s58\n"
"s_mul_i32 s56, s50, 576\n"
"s_add_u32 s56, s56, 160256\n"
"v_mov_b32 v59, s56\n"
"s_mov_b32 exec_lo, 0\n"
"s_and_b32 exec_hi, exec_hi, 0x80000000\n"
"ds_write_b64 v59, v[60:61]\n"
"s_mov_b64 exec, -1\n"
"v_and_b32_dpp v24, v46, v20 row_newbcast:14 row_mask:0xf bank_mask:0xf\n"
"v_and_b32_dpp v25, v46, v21 row_newbcast:14 row_mask:0xf bank_mask:0xf\n"
"v_and_b32_dpp v26, v46, v22 row_newbcast:14 row_mask:0xf bank_mask:0xf\n"
"v_and_b32_dpp v27, v46, v23 row_newbcast:14 row_mask:0xf bank_mask:0xf\n"
"s_waitcnt vmcnt(15)\n"
"s_nop 0\n"
"v_mfma_f32_16x16x32_bf16 v[40:43], v[24:27], v[148:151], 0\n"
"s_add_u32 s52, s53, 0x1b000\n"
"s_and_b32 s52, s52, s54\n"
"buffer_load_dwordx4 v[212:215], v2, s[32:35], s52 offen offset:0 nt\n"
"v_and_b32_dpp v24, v46, v20 row_newbcast:10 row_mask:0xf bank_mask:0xf\n"
"v_and_b32_dpp v25, v46, v21 row_newbcast:10 row_mask:0xf bank_mask:0xf\n"
"v_and_b32_dpp v26, v46, v22 row_newbcast:10 row_mask:0xf bank_mask:0xf\n"
"v_and_b32_dpp v27, v46, v23 row_newbcast:10 row_mask:0xf bank_mask:0xf\n"
"s_waitcnt vmcnt(15)\n"
"s_nop 0\n"
"v_mfma_f32_16x16x32_bf16 v[40:43], v[24:27], v[152:155], v[40:43]\n"
"s_add_u32 s52, s53, 0x1a000\n"
"s_and_b32 s52, s52, s54\n"
"buffer_load_dwordx4 v[216:219], v2, s[32:35], s52 offen offset:0 nt\n"
"v_and_b32_dpp v24, v46, v20 row_newbcast:6 row_mask:0xf bank_mask:0xf\n"
"v_and_b32_dpp v25, v46, v21 row_newbcast:6 row_mask:0xf bank_mask:0xf\n"
"v_and_b32_dpp v26, v46, v22 row_newbcast:6 row_mask:0xf bank_mask:0xf\n"
"v_and_b32_dpp v27, v46, v23 row_newbcast:6 row_mask:0xf bank_mask:0xf\n"
"s_waitcnt vmcnt(15)\n"
"s_nop 0\n"
"v_mfma_f32_16x16x32_bf16 v[40:43], v[24:27], v[156:159], v[40:43]\n"
"s_add_u32 s52, s53, 0x19000\n"
"s_and_b32 s52, s52, s54\n"
"buffer_load_dwordx4 v[220:223], v2, s[32:35], s52 offen offset:0 nt\n"
"v_and_b32_dpp v24, v46, v20 row_newbcast:2 row_mask:0xf bank_mask:0xf\n"
"v_and_b32_dpp v25, v46, v21 row_newbcast:2 row_mask:0xf bank_mask:0xf\n"
"v_and_b32_dpp v26, v46, v22 row_newbcast:2 row_mask:0xf bank_mask:0xf\n"
"v_and_b32_dpp v27, v46, v23 row_newbcast:2 row_mask:0xf bank_mask:0xf\n"
"s_waitcnt vmcnt(15)\n"
"s_nop 0\n"
"v_mfma_f32_16x16x32_bf16 v[40:43], v[24:27], v[160:163], v[40:43]\n"
"s_add_u32 s52, s53, 0x18000\n"
"s_and_b32 s52, s52, s54\n"
"buffer_load_dwordx4 v[224:227], v2, s[32:35], s52 offen offset:0 nt\n"
"v_and_b32_dpp v24, v46, v20 row_newbcast:13 row_mask:0xf bank_mask:0xf\n"
"v_and_b32_dpp v25, v46, v21 row_newbcast:13 row_mask:0xf bank_mask:0xf\n"
"v_and_b32_dpp v26, v46, v22 row_newbcast:13 row_mask:0xf bank_mask:0xf\n"
"v_and_b32_dpp v27, v46, v23 row_newbcast:13 row_mask:0xf bank_mask:0xf\n"
"s_waitcnt vmcnt(15)\n"
"s_nop 0\n"
"v_mfma_f32_16x16x32_bf16 v[40:43], v[24:27], v[188:191], v[40:43]\n"
"s_add_u32 s52, s53, 0x17000\n"
"s_and_b32 s52, s52, s54\n"
"buffer_load_dwordx4 v[228:231], v2, s[32:35], s52 offen offset:0 nt\n"
"v_and_b32_dpp v24, v46, v20 row_newbcast:9 row_mask:0xf bank_mask:0xf\n"
"v_and_b32_dpp v25, v46, v21 row_newbcast:9 row_mask:0xf bank_mask:0xf\n"
"v_and_b32_dpp v26, v46, v22 row_newbcast:9 row_mask:0xf bank_mask:0xf\n"
"v_and_b32_dpp v27, v46, v23 row_newbcast:9 row_mask:0xf bank_mask:0xf\n"
"s_waitcnt vmcnt(15)\n"
"s_nop 0\n"
"v_mfma_f32_16x16x32_bf16 v[40:43], v[24:27], v[184:187], v[40:43]\n"
"s_add_u32 s52, s53, 0x16000\n"
"s_and_b32 s52, s52, s54\n"
"buffer_load_dwordx4 v[232:235], v2, s[32:35], s52 offen offset:0 nt\n"
"v_and_b32_dpp v24, v46, v20 row_newbcast:5 row_mask:0xf bank_mask:0xf\n"
"v_and_b32_dpp v25, v46, v21 row_newbcast:5 row_mask:0xf bank_mask:0xf\n"
"v_and_b32_dpp v26, v46, v22 row_newbcast:5 row_mask:0xf bank_mask:0xf\n"
"v_and_b32_dpp v27, v46, v23 row_newbcast:5 row_mask:0xf bank_mask:0xf\n"
"s_waitcnt vmcnt(15)\n"
"s_nop 0\n"
"v_mfma_f32_16x16x32_bf16 v[40:43], v[24:27], v[180:183], v[40:43]\n"
"s_add_u32 s52, s53, 0x15000\n"
"s_and_b32 s52, s52, s54\n"
"buffer_load_dwordx4 v[236:239], v2, s[32:35], s52 offen offset:0 nt\n"
"v_and_b32_dpp v24, v46, v20 row_newbcast:1 row_mask:0xf bank_mask:0xf\n"
"v_and_b32_dpp v25, v46, v21 row_newbcast:1 row_mask:0xf bank_mask:0xf\n"
"v_and_b32_dpp v26, v46, v22 row_newbcast:1 row_mask:0xf bank_mask:0xf\n"
"v_and_b32_dpp v27, v46, v23 row_newbcast:1 row_mask:0xf bank_mask:0xf\n"
"s_waitcnt vmcnt(15)\n"
"s_nop 0\n"
"v_mfma_f32_16x16x32_bf16 v[40:43], v[24:27], v[176:179], v[40:43]\n"
"s_add_u32 s52, s53, 0x14000\n"
"s_and_b32 s52, s52, s54\n"
"buffer_load_dwordx4 v[240:243], v2, s[32:35], s52 offen offset:0 nt\n"
"v_and_b32_dpp v24, v46, v20 row_newbcast:12 row_mask:0xf bank_mask:0xf\n"
"v_and_b32_dpp v25, v46, v21 row_newbcast:12 row_mask:0xf bank_mask:0xf\n"
"v_and_b32_dpp v26, v46, v22 row_newbcast:12 row_mask:0xf bank_mask:0xf\n"
"v_and_b32_dpp v27, v46, v23 row_newbcast:12 row_mask:0xf bank_mask:0xf\n"
"s_waitcnt vmcnt(15)\n"
"s_nop 0\n"
"v_mfma_f32_16x16x32_bf16 v[40:43], v[24:27], v[172:175], v[40:43]\n"
"s_add_u32 s52, s53, 0x13000\n"
"s_and_b32 s52, s52, s54\n"
"buffer_load_dwordx4 v[244:247], v2, s[32:35], s52 offen offset:0 nt\n"
"v_and_b32_dpp v24, v46, v20 row_newbcast:8 row_mask:0xf bank_mask:0xf\n"
"v_and_b32_dpp v25, v46, v21 row_newbcast:8 row_mask:0xf bank_mask:0xf\n"
"v_and_b32_dpp v26, v46, v22 row_newbcast:8 row_mask:0xf bank_mask:0xf\n"
"v_and_b32_dpp v27, v46, v23 row_newbcast:8 row_mask:0xf bank_mask:0xf\n"
"s_waitcnt vmcnt(15)\n"
"s_nop 0\n"
"v_mfma_f32_16x16x32_bf16 v[40:43], v[24:27], v[168:171], v[40:43]\n"
"s_add_u32 s52, s53, 0x12000\n"
"s_and_b32 s52, s52, s54\n"
"buffer_load_dwordx4 v[248:251], v2, s[32:35], s52 offen offset:0 nt\n"
"v_and_b32_dpp v24, v46, v20 row_newbcast:4 row_mask:0xf bank_mask:0xf\n"
"v_and_b32_dpp v25, v46, v21 row_newbcast:4 row_mask:0xf bank_mask:0xf\n"
"v_and_b32_dpp v26, v46, v22 row_newbcast:4 row_mask:0xf bank_mask:0xf\n"
"v_and_b32_dpp v27, v46, v23 row_newbcast:4 row_mask:0xf bank_mask:0xf\n"
"s_waitcnt vmcnt(15)\n"
"s_nop 0\n"
"v_mfma_f32_16x16x32_bf16 v[40:43], v[24:27], v[164:167], v[40:43]\n"
"s_add_u32 s52, s53, 0x11000\n"
"s_and_b32 s52, s52, s54\n"
"buffer_load_dwordx4 v[252:255], v2, s[32:35], s52 offen offset:0 nt\n"
"v_and_b32_dpp v24, v46, v20 row_newbcast:0 row_mask:0xf bank_mask:0xf\n"
"v_and_b32_dpp v25, v46, v21 row_newbcast:0 row_mask:0xf bank_mask:0xf\n"
"v_and_b32_dpp v26, v46, v22 row_newbcast:0 row_mask:0xf bank_mask:0xf\n"
"v_and_b32_dpp v27, v46, v23 row_newbcast:0 row_mask:0xf bank_mask:0xf\n"
"s_waitcnt vmcnt(15)\n"
"s_nop 0\n"
"v_mfma_f32_16x16x32_bf16 v[40:43], v[24:27], v[192:195], v[40:43]\n"
"s_add_u32 s52, s53, 0x10000\n"
"s_and_b32 s52, s52, s54\n"
"buffer_load_dwordx4 v[128:131], v2, s[32:35], s52 offen offset:0 nt\n"
"v_and_b32_dpp v24, v45, v20 row_newbcast:15 row_mask:0xf bank_mask:0xf\n"
"v_and_b32_dpp v25, v45, v21 row_newbcast:15 row_mask:0xf bank_mask:0xf\n"
"v_and_b32_dpp v26, v45, v22 row_newbcast:15 row_mask:0xf bank_mask:0xf\n"
"v_and_b32_dpp v27, v45, v23 row_newbcast:15 row_mask:0xf bank_mask:0xf\n"
"s_waitcnt vmcnt(15)\n"
"s_nop 0\n"
"v_mfma_f32_16x16x32_bf16 v[40:43], v[24:27], v[196:199], v[40:43]\n"
"s_add_u32 s52, s53, 0xf000\n"
"s_and_b32 s52, s52, s54\n"
"buffer_load_dwordx4 v[132:135], v2, s[32:35], s52 offen offset:0 nt\n"
"v_and_b32_dpp v24, v45, v20 row_newbcast:11 row_mask:0xf bank_mask:0xf\n"
"v_and_b32_dpp v25, v45, v21 row_newbcast:11 row_mask:0xf bank_mask:0xf\n"
"v_and_b32_dpp v26, v45, v22 row_newbcast:11 row_mask:0xf bank_mask:0xf\n"
"v_and_b32_dpp v27, v45, v23 row_newbcast:11 row_mask:0xf bank_mask:0xf\n"
"s_waitcnt vmcnt(15)\n"
"s_nop 0\n"
"v_mfma_f32_16x16x32_bf16 v[40:43], v[24:27], v[200:203], v[40:43]\n"
"s_add_u32 s52, s53, 0xe000\n"
"s_and_b32 s52, s52, s54\n"
"buffer_load_dwordx4 v[136:139], v2, s[32:35], s52 offen offset:0 nt\n"
"v_and_b32_dpp v24, v45, v20 row_newbcast:7 row_mask:0xf bank_mask:0xf\n"
"v_and_b32_dpp v25, v45, v21 row_newbcast:7 row_mask:0xf bank_mask:0xf\n"
"v_and_b32_dpp v26, v45, v22 row_newbcast:7 row_mask:0xf bank_mask:0xf\n"
"v_and_b32_dpp v27, v45, v23 row_newbcast:7 row_mask:0xf bank_mask:0xf\n"
"s_waitcnt vmcnt(15)\n"
"s_nop 0\n"
"v_mfma_f32_16x16x32_bf16 v[40:43], v[24:27], v[204:207], v[40:43]\n"
"s_add_u32 s52, s53, 0xd000\n"
"s_and_b32 s52, s52, s54\n"
"buffer_load_dwordx4 v[140:143], v2, s[32:35], s52 offen offset:0 nt\n"
"v_and_b32_dpp v24, v45, v20 row_newbcast:3 row_mask:0xf bank_mask:0xf\n"
"v_and_b32_dpp v25, v45, v21 row_newbcast:3 row_mask:0xf bank_mask:0xf\n"
"v_and_b32_dpp v26, v45, v22 row_newbcast:3 row_mask:0xf bank_mask:0xf\n"
"v_and_b32_dpp v27, v45, v23 row_newbcast:3 row_mask:0xf bank_mask:0xf\n"
"s_waitcnt vmcnt(15)\n"
"s_nop 0\n"
"v_mfma_f32_16x16x32_bf16 v[40:43], v[24:27], v[208:211], v[40:43]\n"
"s_add_u32 s52, s53, 0xc000\n"
"s_and_b32 s52, s52, s54\n"
"buffer_load_dwordx4 v[144:147], v2, s[32:35], s52 offen offset:0 nt\n"
"v_and_b32_dpp v24, v45, v20 row_newbcast:14 row_mask:0xf bank_mask:0xf\n"
"v_and_b32_dpp v25, v45, v21 row_newbcast:14 row_mask:0xf bank_mask:0xf\n"
"v_and_b32_dpp v26, v45, v22 row_newbcast:14 row_mask:0xf bank_mask:0xf\n"
"v_and_b32_dpp v27, v45, v23 row_newbcast:14 row_mask:0xf bank_mask:0xf\n"
"s_waitcnt vmcnt(15)\n"
"s_nop 0\n"
"v_mfma_f32_16x16x32_bf16 v[40:43], v[24:27], v[212:215], v[40:43]\n"
"s_add_u32 s52, s53, 0xb000\n"
"s_and_b32 s52, s52, s54\n"
"buffer_load_dwordx4 v[148:151], v2, s[32:35], s52 offen offset:0 nt\n"
"v_and_b32_dpp v24, v45, v20 row_newbcast:10 row_mask:0xf bank_mask:0xf\n"
"v_and_b32_dpp v25, v45, v21 row_newbcast:10 row_mask:0xf bank_mask:0xf\n"
"v_and_b32_dpp v26, v45, v22 row_newbcast:10 row_mask:0xf bank_mask:0xf\n"
"v_and_b32_dpp v27, v45, v23 row_newbcast:10 row_mask:0xf bank_mask:0xf\n"
"s_waitcnt vmcnt(15)\n"
"s_nop 0\n"
"v_mfma_f32_16x16x32_bf16 v[40:43], v[24:27], v[216:219], v[40:43]\n"
"s_add_u32 s52, s53, 0xa000\n"
"s_and_b32 s52, s52, s54\n"
"buffer_load_dwordx4 v[152:155], v2, s[32:35], s52 offen offset:0 nt\n"
"v_and_b32_dpp v24, v45, v20 row_newbcast:6 row_mask:0xf bank_mask:0xf\n"
"v_and_b32_dpp v25, v45, v21 row_newbcast:6 row_mask:0xf bank_mask:0xf\n"
"v_and_b32_dpp v26, v45, v22 row_newbcast:6 row_mask:0xf bank_mask:0xf\n"
"v_and_b32_dpp v27, v45, v23 row_newbcast:6 row_mask:0xf bank_mask:0xf\n"
"s_waitcnt vmcnt(15)\n"
"s_nop 0\n"
"v_mfma_f32_16x16x32_bf16 v[40:43], v[24:27], v[220:223], v[40:43]\n"
"s_add_u32 s52, s53, 0x9000\n"
"s_and_b32 s52, s52, s54\n"
"buffer_load_dwordx4 v[156:159], v2, s[32:35], s52 offen offset:0 nt\n"
"v_and_b32_dpp v24, v45, v20 row_newbcast:2 row_mask:0xf bank_mask:0xf\n"
"v_and_b32_dpp v25, v45, v21 row_newbcast:2 row_mask:0xf bank_mask:0xf\n"
"v_and_b32_dpp v26, v45, v22 row_newbcast:2 row_mask:0xf bank_mask:0xf\n"
"v_and_b32_dpp v27, v45, v23 row_newbcast:2 row_mask:0xf bank_mask:0xf\n"
"s_waitcnt vmcnt(15)\n"
"s_nop 0\n"
"v_mfma_f32_16x16x32_bf16 v[40:43], v[24:27], v[224:227], v[40:43]\n"
"s_add_u32 s52, s53, 0x8000\n"
"s_and_b32 s52, s52, s54\n"
"buffer_load_dwordx4 v[160:163], v2, s[32:35], s52 offen offset:0 nt\n"
"v_and_b32_dpp v24, v45, v20 row_newbcast:13 row_mask:0xf bank_mask:0xf\n"
"v_and_b32_dpp v25, v45, v21 row_newbcast:13 row_mask:0xf bank_mask:0xf\n"
"v_and_b32_dpp v26, v45, v22 row_newbcast:13 row_mask:0xf bank_mask:0xf\n"
"v_and_b32_dpp v27, v45, v23 row_newbcast:13 row_mask:0xf bank_mask:0xf\n"
"s_waitcnt vmcnt(15)\n"
"s_nop 0\n"
"v_mfma_f32_16x16x32_bf16 v[40:43], v[24:27], v[228:231], v[40:43]\n"
"s_add_u32 s52, s53, 0x7000\n"
"s_and_b32 s52, s52, s54\n"
"buffer_load_dwordx4 v[188:191], v2, s[32:35], s52 offen offset:0 nt\n"
"v_and_b32_dpp v24, v45, v20 row_newbcast:9 row_mask:0xf bank_mask:0xf\n"
"v_and_b32_dpp v25, v45, v21 row_newbcast:9 row_mask:0xf bank_mask:0xf\n"
"v_and_b32_dpp v26, v45, v22 row_newbcast:9 row_mask:0xf bank_mask:0xf\n"
"v_and_b32_dpp v27, v45, v23 row_newbcast:9 row_mask:0xf bank_mask:0xf\n"
"s_waitcnt vmcnt(15)\n"
"s_nop 0\n"
"v_mfma_f32_16x16x32_bf16 v[40:43], v[24:27], v[232:235], v[40:43]\n"
"s_add_u32 s52, s53, 0x6000\n"
"s_and_b32 s52, s52, s54\n"
"buffer_load_dwordx4 v[184:187], v2, s[32:35], s52 offen offset:0 nt\n"
"v_and_b32_dpp v24, v45, v20 row_newbcast:5 row_mask:0xf bank_mask:0xf\n"
"v_and_b32_dpp v25, v45, v21 row_newbcast:5 row_mask:0xf bank_mask:0xf\n"
"v_and_b32_dpp v26, v45, v22 row_newbcast:5 row_mask:0xf bank_mask:0xf\n"
"v_and_b32_dpp v27, v45, v23 row_newbcast:5 row_mask:0xf bank_mask:0xf\n"
"s_waitcnt vmcnt(15)\n"
"s_nop 0\n"
"v_mfma_f32_16x16x32_bf16 v[40:43], v[24:27], v[236:239], v[40:43]\n"
"s_add_u32 s52, s53, 0x5000\n"
"s_and_b32 s52, s52, s54\n"
"buffer_load_dwordx4 v[180:183], v2, s[32:35], s52 offen offset:0 nt\n"
"v_and_b32_dpp v24, v45, v20 row_newbcast:1 row_mask:0xf bank_mask:0xf\n"
"v_and_b32_dpp v25, v45, v21 row_newbcast:1 row_mask:0xf bank_mask:0xf\n"
"v_and_b32_dpp v26, v45, v22 row_newbcast:1 row_mask:0xf bank_mask:0xf\n"
"v_and_b32_dpp v27, v45, v23 row_newbcast:1 row_mask:0xf bank_mask:0xf\n"
"s_waitcnt vmcnt(15)\n"
"s_nop 0\n"
"v_mfma_f32_16x16x32_bf16 v[40:43], v[24:27], v[240:243], v[40:43]\n"
"s_add_u32 s52, s53, 0x4000\n"
"s_and_b32 s52, s52, s54\n"
"buffer_load_dwordx4 v[176:179], v2, s[32:35], s52 offen offset:0 nt\n"
"v_and_b32_dpp v24, v45, v20 row_newbcast:12 row_mask:0xf bank_mask:0xf\n"
"v_and_b32_dpp v25, v45, v21 row_newbcast:12 row_mask:0xf bank_mask:0xf\n"
"v_and_b32_dpp v26, v45, v22 row_newbcast:12 row_mask:0xf bank_mask:0xf\n"
"v_and_b32_dpp v27, v45, v23 row_newbcast:12 row_mask:0xf bank_mask:0xf\n"
"s_waitcnt vmcnt(15)\n"
"s_nop 0\n"
"v_mfma_f32_16x16x32_bf16 v[40:43], v[24:27], v[244:247], v[40:43]\n"
"s_add_u32 s52, s53, 0x3000\n"
"s_and_b32 s52, s52, s54\n"
"buffer_load_dwordx4 v[172:175], v2, s[32:35], s52 offen offset:0 nt\n"
"v_and_b32_dpp v24, v45, v20 row_newbcast:8 row_mask:0xf bank_mask:0xf\n"
"v_and_b32_dpp v25, v45, v21 row_newbcast:8 row_mask:0xf bank_mask:0xf\n"
"v_and_b32_dpp v26, v45, v22 row_newbcast:8 row_mask:0xf bank_mask:0xf\n"
"v_and_b32_dpp v27, v45, v23 row_newbcast:8 row_mask:0xf bank_mask:0xf\n"
"s_waitcnt vmcnt(15)\n"
"s_nop 0\n"
"v_mfma_f32_16x16x32_bf16 v[40:43], v[24:27], v[248:251], v[40:43]\n"
"s_add_u32 s52, s53, 0x2000\n"
"s_and_b32 s52, s52, s54\n"
"buffer_load_dwordx4 v[168:171], v2, s[32:35], s52 offen offset:0 nt\n"
"v_and_b32_dpp v24, v45, v20 row_newbcast:4 row_mask:0xf bank_mask:0xf\n"
"v_and_b32_dpp v25, v45, v21 row_newbcast:4 row_mask:0xf bank_mask:0xf\n"
"v_and_b32_dpp v26, v45, v22 row_newbcast:4 row_mask:0xf bank_mask:0xf\n"
"v_and_b32_dpp v27, v45, v23 row_newbcast:4 row_mask:0xf bank_mask:0xf\n"
"s_waitcnt vmcnt(15)\n"
"s_nop 0\n"
"v_mfma_f32_16x16x32_bf16 v[40:43], v[24:27], v[252:255], v[40:43]\n"
"s_add_u32 s52, s53, 0x1000\n"
"s_and_b32 s52, s52, s54\n"
"buffer_load_dwordx4 v[164:167], v2, s[32:35], s52 offen offset:0 nt\n"
"v_and_b32_dpp v24, v45, v20 row_newbcast:0 row_mask:0xf bank_mask:0xf\n"
"v_and_b32_dpp v25, v45, v21 row_newbcast:0 row_mask:0xf bank_mask:0xf\n"
"v_and_b32_dpp v26, v45, v22 row_newbcast:0 row_mask:0xf bank_mask:0xf\n"
"v_and_b32_dpp v27, v45, v23 row_newbcast:0 row_mask:0xf bank_mask:0xf\n"
"s_waitcnt vmcnt(15)\n"
"s_nop 0\n"
"v_mfma_f32_16x16x32_bf16 v[40:43], v[24:27], v[128:131], v[40:43]\n"
"s_add_u32 s52, s53, 0x0\n"
"s_and_b32 s52, s52, s54\n"
"buffer_load_dwordx4 v[192:195], v2, s[32:35], s52 offen offset:0 nt\n"
"v_and_b32_dpp v24, v44, v20 row_newbcast:15 row_mask:0xf bank_mask:0xf\n"
"v_and_b32_dpp v25, v44, v21 row_newbcast:15 row_mask:0xf bank_mask:0xf\n"
"v_and_b32_dpp v26, v44, v22 row_newbcast:15 row_mask:0xf bank_mask:0xf\n"
"v_and_b32_dpp v27, v44, v23 row_newbcast:15 row_mask:0xf bank_mask:0xf\n"
"s_waitcnt vmcnt(15)\n"
"s_nop 0\n"
"v_mfma_f32_16x16x32_bf16 v[40:43], v[24:27], v[132:135], v[40:43]\n"
"v_and_b32_dpp v24, v44, v20 row_newbcast:11 row_mask:0xf bank_mask:0xf\n"
"v_and_b32_dpp v25, v44, v21 row_newbcast:11 row_mask:0xf bank_mask:0xf\n"
"v_and_b32_dpp v26, v44, v22 row_newbcast:11 row_mask:0xf bank_mask:0xf\n"
"v_and_b32_dpp v27, v44, v23 row_newbcast:11 row_mask:0xf bank_mask:0xf\n"
"s_waitcnt vmcnt(14)\n"
"s_nop 0\n"
"v_mfma_f32_16x16x32_bf16 v[40:43], v[24:27], v[136:139], v[40:43]\n"
"v_and_b32_dpp v24, v44, v20 row_newbcast:7 row_mask:0xf bank_mask:0xf\n"
"v_and_b32_dpp v25, v44, v21 row_newbcast:7 row_mask:0xf bank_mask:0xf\n"
"v_and_b32_dpp v26, v44, v22 row_newbcast:7 row_mask:0xf bank_mask:0xf\n"
"v_and_b32_dpp v27, v44, v23 row_newbcast:7 row_mask:0xf bank_mask:0xf\n"
"s_waitcnt vmcnt(13)\n"
"s_nop 0\n"
"v_mfma_f32_16x16x32_bf16 v[40:43], v[24:27], v[140:143], v[40:43]\n"
"v_and_b32_dpp v24, v44, v20 row_newbcast:3 row_mask:0xf bank_mask:0xf\n"
"v_and_b32_dpp v25, v44, v21 row_newbcast:3 row_mask:0xf bank_mask:0xf\n"
"v_and_b32_dpp v26, v44, v22 row_newbcast:3 row_mask:0xf bank_mask:0xf\n"
"v_and_b32_dpp v27, v44, v23 row_newbcast:3 row_mask:0xf bank_mask:0xf\n"
"s_waitcnt vmcnt(12)\n"
"s_nop 0\n"
"v_mfma_f32_16x16x32_bf16 v[40:43], v[24:27], v[144:147], v[40:43]\n"
"v_and_b32_dpp v24, v44, v20 row_newbcast:14 row_mask:0xf bank_mask:0xf\n"
"v_and_b32_dpp v25, v44, v21 row_newbcast:14 row_mask:0xf bank_mask:0xf\n"
"v_and_b32_dpp v26, v44, v22 row_newbcast:14 row_mask:0xf bank_mask:0xf\n"
"v_and_b32_dpp v27, v44, v23 row_newbcast:14 row_mask:0xf bank_mask:0xf\n"
"s_waitcnt vmcnt(11)\n"
"s_nop 0\n"
"v_mfma_f32_16x16x32_bf16 v[40:43], v[24:27], v[148:151], v[40:43]\n"
"v_and_b32_dpp v24, v44, v20 row_newbcast:10 row_mask:0xf bank_mask:0xf\n"
"v_and_b32_dpp v25, v44, v21 row_newbcast:10 row_mask:0xf bank_mask:0xf\n"
"v_and_b32_dpp v26, v44, v22 row_newbcast:10 row_mask:0xf bank_mask:0xf\n"
"v_and_b32_dpp v27, v44, v23 row_newbcast:10 row_mask:0xf bank_mask:0xf\n"
"s_waitcnt vmcnt(10)\n"
"s_nop 0\n"
"v_mfma_f32_16x16x32_bf16 v[40:43], v[24:27], v[152:155], v[40:43]\n"
"v_and_b32_dpp v24, v44, v20 row_newbcast:6 row_mask:0xf bank_mask:0xf\n"
"v_and_b32_dpp v25, v44, v21 row_newbcast:6 row_mask:0xf bank_mask:0xf\n"
"v_and_b32_dpp v26, v44, v22 row_newbcast:6 row_mask:0xf bank_mask:0xf\n"
"v_and_b32_dpp v27, v44, v23 row_newbcast:6 row_mask:0xf bank_mask:0xf\n"
"s_waitcnt vmcnt(9)\n"
"s_nop 0\n"
"v_mfma_f32_16x16x32_bf16 v[40:43], v[24:27], v[156:159], v[40:43]\n"
"v_and_b32_dpp v24, v44, v20 row_newbcast:2 row_mask:0xf bank_mask:0xf\n"
"v_and_b32_dpp v25, v44, v21 row_newbcast:2 row_mask:0xf bank_mask:0xf\n"
"v_and_b32_dpp v26, v44, v22 row_newbcast:2 row_mask:0xf bank_mask:0xf\n"
"v_and_b32_dpp v27, v44, v23 row_newbcast:2 row_mask:0xf bank_mask:0xf\n"
"s_waitcnt vmcnt(8)\n"
"s_nop 0\n"
"v_mfma_f32_16x16x32_bf16 v[40:43], v[24:27], v[160:163], v[40:43]\n"
"v_and_b32_dpp v24, v44, v20 row_newbcast:13 row_mask:0xf bank_mask:0xf\n"
"v_and_b32_dpp v25, v44, v21 row_newbcast:13 row_mask:0xf bank_mask:0xf\n"
"v_and_b32_dpp v26, v44, v22 row_newbcast:13 row_mask:0xf bank_mask:0xf\n"
"v_and_b32_dpp v27, v44, v23 row_newbcast:13 row_mask:0xf bank_mask:0xf\n"
"s_waitcnt vmcnt(7)\n"
"s_nop 0\n"
"v_mfma_f32_16x16x32_bf16 v[40:43], v[24:27], v[188:191], v[40:43]\n"
"v_and_b32_dpp v24, v44, v20 row_newbcast:9 row_mask:0xf bank_mask:0xf\n"
"v_and_b32_dpp v25, v44, v21 row_newbcast:9 row_mask:0xf bank_mask:0xf\n"
"v_and_b32_dpp v26, v44, v22 row_newbcast:9 row_mask:0xf bank_mask:0xf\n"
"v_and_b32_dpp v27, v44, v23 row_newbcast:9 row_mask:0xf bank_mask:0xf\n"
"s_waitcnt vmcnt(6)\n"
"s_nop 0\n"
"v_mfma_f32_16x16x32_bf16 v[40:43], v[24:27], v[184:187], v[40:43]\n"
"v_min_u32 v62, 32, v0\n"
"v_lshlrev_b32 v62, 4, v62\n"
"v_mov_b32 v61, 0x7ff00000\n"
"v_cmp_gt_u32 vcc, 33, v0\n"
"s_nop 1\n"
"v_cndmask_b32 v61, v61, v62, vcc\n"
"v_add_u32 v62, 159744, v62\n"
"v_mov_b32 v63, 160256\n"
"s_mul_i32 s70, s48, 32768\n"
"s_mul_i32 s56, s49, 640\n"
"s_add_u32 s70, s70, s56\n"
"s_add_u32 s70, s70, 0x104000\n"
"s_add_u32 s70, s70, s99\n"
"v_and_b32 v58, 15, v0\n"
"v_lshlrev_b32 v58, 5, v58\n"
"v_lshrrev_b32 v59, 4, v0\n"
"v_lshl_add_u32 v58, v59, 4, v58\n"
"s_mul_i32 s56, s50, 576\n"
"s_add_u32 s56, s56, 159744\n"
"v_add_u32 v58, s56, v58\n"
"v_mov_b32 v60, v58\n"
"v_and_b32_dpp v24, v44, v20 row_newbcast:5 row_mask:0xf bank_mask:0xf\n"
"v_and_b32_dpp v25, v44, v21 row_newbcast:5 row_mask:0xf bank_mask:0xf\n"
"v_and_b32_dpp v26, v44, v22 row_newbcast:5 row_mask:0xf bank_mask:0xf\n"
"v_and_b32_dpp v27, v44, v23 row_newbcast:5 row_mask:0xf bank_mask:0xf\n"
"s_waitcnt vmcnt(5)\n"
"s_nop 0\n"
"v_mfma_f32_16x16x32_bf16 v[40:43], v[24:27], v[180:183], v[40:43]\n"
"v_and_b32_dpp v24, v44, v20 row_newbcast:1 row_mask:0xf bank_mask:0xf\n"
"v_and_b32_dpp v25, v44, v21 row_newbcast:1 row_mask:0xf bank_mask:0xf\n"
"v_and_b32_dpp v26, v44, v22 row_newbcast:1 row_mask:0xf bank_mask:0xf\n"
"v_and_b32_dpp v27, v44, v23 row_newbcast:1 row_mask:0xf bank_mask:0xf\n"
"s_waitcnt vmcnt(4)\n"
"s_nop 0\n"
"v_mfma_f32_16x16x32_bf16 v[40:43], v[24:27], v[176:179], v[40:43]\n"
"v_and_b32_dpp v24, v44, v20 row_newbcast:12 row_mask:0xf bank_mask:0xf\n"
"v_and_b32_dpp v25, v44, v21 row_newbcast:12 row_mask:0xf bank_mask:0xf\n"
"v_and_b32_dpp v26, v44, v22 row_newbcast:12 row_mask:0xf bank_mask:0xf\n"
"v_and_b32_dpp v27, v44, v23 row_newbcast:12 row_mask:0xf bank_mask:0xf\n"
"s_waitcnt vmcnt(3)\n"
"s_nop 0\n"
"v_mfma_f32_16x16x32_bf16 v[40:43], v[24:27], v[172:175], v[40:43]\n"
"v_and_b32_dpp v24, v44, v20 row_newbcast:8 row_mask:0xf bank_mask:0xf\n"
"v_and_b32_dpp v25, v44, v21 row_newbcast:8 row_mask:0xf bank_mask:0xf\n"
"v_and_b32_dpp v26, v44, v22 row_newbcast:8 row_mask:0xf bank_mask:0xf\n"
"v_and_b32_dpp v27, v44, v23 row_newbcast:8 row_mask:0xf bank_mask:0xf\n"
"s_waitcnt vmcnt(2)\n"
"s_nop 0\n"
"v_mfma_f32_16x16x32_bf16 v[40:43], v[24:27], v[168:171], v[40:43]\n"
"v_and_b32_dpp v24, v44, v20 row_newbcast:4 row_mask:0xf bank_mask:0xf\n"
"v_and_b32_dpp v25, v44, v21 row_newbcast:4 row_mask:0xf bank_mask:0xf\n"
"v_and_b32_dpp v26, v44, v22 row_newbcast:4 row_mask:0xf bank_mask:0xf\n"
"v_and_b32_dpp v27, v44, v23 row_newbcast:4 row_mask:0xf bank_mask:0xf\n"
"s_waitcnt vmcnt(1)\n"
"s_nop 0\n"
"v_mfma_f32_16x16x32_bf16 v[40:43], v[24:27], v[164:167], v[40:43]\n"
"v_and_b32_dpp v24, v44, v20 row_newbcast:0 row_mask:0xf bank_mask:0xf\n"
"v_and_b32_dpp v25, v44, v21 row_newbcast:0 row_mask:0xf bank_mask:0xf\n"
"v_and_b32_dpp v26, v44, v22 row_newbcast:0 row_mask:0xf bank_mask:0xf\n"
"v_and_b32_dpp v27, v44, v23 row_newbcast:0 row_mask:0xf bank_mask:0xf\n"
"s_waitcnt vmcnt(0)\n"
"s_nop 0\n"
"v_mfma_f32_16x16x32_bf16 v[40:43], v[24:27], v[192:195], v[40:43]\n"
"s_mov_b32 exec_hi, 0\n"
"s_nop 6\n"
"ds_write_b128 v60, v[40:43]\n"
"s_mov_b64 exec, -1\n"
"s_waitcnt lgkmcnt(0)\n"
"s_barrier\n"
"s_cmp_lg_u32 s50, 0\n"
"s_cbranch_scc1 Lnomerge_513\n"
"ds_read_b32 v24, v63 offset:0\n"
"ds_read_b32 v25, v63 offset:576\n"
"ds_read_b32 v26, v63 offset:1152\n"
"ds_read_b32 v27, v63 offset:1728\n"
"ds_read_b128 v[4:7], v62 offset:0\n"
"ds_read_b128 v[8:11], v62 offset:576\n"
"ds_read_b128 v[12:15], v62 offset:1152\n"
"ds_read_b128 v[16:19], v62 offset:1728\n"
"s_waitcnt lgkmcnt(4)\n"
"v_max3_f32 v28, v24, v25, v26\n"
"v_max_f32 v28, v28, v27\n"
"v_sub_f32 v24, v24, v28\n"
"v_sub_f32 v25, v25, v28\n"
"v_sub_f32 v26, v26, v28\n"
"v_sub_f32 v27, v27, v28\n"
"v_exp_f32 v24, v24\n"
"v_exp_f32 v25, v25\n"
"v_exp_f32 v26, v26\n"
"v_exp_f32 v27, v27\n"
"s_waitcnt lgkmcnt(0)\n"
"v_mul_f32 v4, v4, v24\n"
"v_mul_f32 v5, v5, v24\n"
"v_mul_f32 v6, v6, v24\n"
"v_mul_f32 v7, v7, v24\n"
"v_fmac_f32 v4, v8, v25\n"
"v_fmac_f32 v5, v9, v25\n"
"v_fmac_f32 v6, v10, v25\n"
"v_fmac_f32 v7, v11, v25\n"
"v_fmac_f32 v4, v12, v26\n"
"v_fmac_f32 v5, v13, v26\n"
"v_fmac_f32 v6, v14, v26\n"
"v_fmac_f32 v7, v15, v26\n"
"v_fmac_f32 v4, v16, v27\n"
"v_fmac_f32 v5, v17, v27\n"
"v_fmac_f32 v6, v18, v27\n"
"v_fmac_f32 v7, v19, v27\n"
"v_cmp_eq_u32 vcc, 32, v0\n"
"s_nop 1\n"
"v_cndmask_b32 v4, v4, v28, vcc\n"
"buffer_store_dwordx4 v[4:7], v61, s[36:39], s70 offen sc1\n"
"s_xor_b32 s70, s70, 8192\n"
"buffer_store_dwordx4 v[4:7], v61, s[36:39], s70 offen sc1\n"
"Lnomerge_513:\n"
"s_mov_b64 exec, 0\n"
"s_branch LFIN\n"
"LFINR:\n"
"s_waitcnt vmcnt(0) lgkmcnt(0)\n"
"s_mov_b64 exec, -1\n"
"LS1:\n"
"s_nop 7\n"
"s_mul_i32 s76, s48, 32768\n"
"s_mul_i32 s56, s51, 16384\n"
"s_add_u32 s76, s76, s56\n"
"s_add_u32 s76, s76, 0x101400\n"
"s_lshl_b32 s56, s50, 9\n"
"s_add_u32 s76, s76, s56\n"
"s_and_b32 s77, s48, 24\n"
"s_add_u32 s77, s77, s48\n"
"s_lshl_b32 s77, s77, 10\n"
"s_lshl_b32 s56, s51, 17\n"
"s_add_u32 s77, s77, s56\n"
"s_lshl_b32 s56, s50, 8\n"
"s_add_u32 s77, s77, s56\n"
"s_add_u32 s77, s77, 0x22000\n"
"s_xor_b32 s77, s77, s99\n"
"v_and_b32 v63, 15, v0\n"
"v_lshrrev_b32 v60, 4, v0\n"
"v_lshl_add_u32 v63, v63, 2, v60\n"
"v_lshlrev_b32 v60, 3, v63\n"
"v_lshlrev_b32 v63, 2, v63\n"
"v_mov_b32 v224, 0\n"
"v_mov_b32 v226, 0\n"
"v_mov_b32 v228, 0\n"
"v_mov_b32 v230, 0\n"
"v_mov_b32 v232, 0\n"
"v_mov_b32 v234, 0\n"
"v_mov_b32 v236, 0\n"
"v_mov_b32 v238, 0\n"
"v_mov_b32 v240, 0\n"
"v_mov_b32 v242, 0\n"
"v_mov_b32 v244, 0\n"
"v_mov_b32 v246, 0\n"
"v_mov_b32 v248, 0\n"
"v_mov_b32 v250, 0\n"
"v_mov_b32 v252, 0\n"
"v_mov_b32 v254, 0\n"
"s_mul_i32 s70, s48, 32768\n"
"s_mul_i32 s56, s51, 16384\n"
"s_add_u32 s70, s70, s56\n"
"s_add_u32 s70, s70, 0x100000\n"
"s_add_u32 s70, s70, s99\n"
"v_bfe_u32 v62, v0, 1, 3\n"
"v_mul_u32_u24 v62, 640, v62\n"
"v_and_b32 v61, 1, v0\n"
"v_lshrrev_b32 v58, 4, v0\n"
"v_lshl_add_u32 v61, v58, 1, v61\n"
"s_lshl_b32 s56, s50, 3\n"
"v_add_u32 v61, s56, v61\n"
"v_lshrrev_b32 v26, 2, v61\n"
"v_lshlrev_b32 v26, 5, v26\n"
"v_and_b32 v27, 3, v61\n"
"v_lshl_add_u32 v26, v27, 2, v26\n"
"v_add_u32 v26, 162304, v26\n"
"v_and_b32 v27, 2, v0\n"
"v_lshlrev_b32 v27, 3, v27\n"
"v_lshl_add_u32 v27, v58, 5, v27\n"
"v_add_u32 v27, 162304, v27\n"
"v_bfe_u32 v59, v0, 1, 1\n"
"v_mul_u32_u24 v59, 0xaaaa, v59\n"
"v_add_u32 v59, 0x4444, v59\n"
"v_lshl_add_u32 v61, v61, 4, v62\n"
"s_mov_b32 s55, 4194304\n"
"Lpoll_514:\n"
"buffer_load_dwordx4 v[8:11], v61, s[36:39], s70 offen sc1\n"
"buffer_load_dwordx2 v[20:21], v62, s[36:39], s70 offen offset:512 sc1\n"
"s_waitcnt vmcnt(0)\n"
"v_max3_u32 v24, v8, v9, v10\n"
"v_max3_u32 v25, v11, v20, v21\n"
"v_max_u32 v24, v24, v25\n"
"v_cmp_eq_u32 vcc, -1, v24\n"
"s_cbranch_vccz Lpdone_515\n"
"s_sub_u32 s55, s55, 1\n"
"s_cbranch_scc0 Lpoll_514\n"
"Lpdone_515:\n"
"v_max_f32_dpp v24, v20, v20 row_ror:8 row_mask:0xf bank_mask:0xf\n"
"s_nop 1\n"
"v_max_f32_dpp v24, v24, v24 row_ror:4 row_mask:0xf bank_mask:0xf\n"
"s_nop 1\n"
"v_max_f32_dpp v24, v24, v24 row_ror:2 row_mask:0xf bank_mask:0xf\n"
"v_sub_f32 v44, v20, v24\n"
"v_exp_f32 v44, v44\n"
"s_nop 0\n"
"v_mul_f32 v24, v44, v21\n"
"v_pk_mul_f32 v[48:49], v[8:9], v[44:45] op_sel_hi:[1,0]\n"
"v_pk_mul_f32 v[50:51], v[10:11], v[44:45] op_sel_hi:[1,0]\n"
"v_add_f32_dpp v24, v24, v24 row_ror:8 row_mask:0xf bank_mask:0xf\n"
"v_add_f32_dpp v48, v48, v48 row_ror:8 row_mask:0xf bank_mask:0xf\n"
"v_add_f32_dpp v49, v49, v49 row_ror:8 row_mask:0xf bank_mask:0xf\n"
"v_add_f32_dpp v50, v50, v50 row_ror:8 row_mask:0xf bank_mask:0xf\n"
"v_add_f32_dpp v51, v51, v51 row_ror:8 row_mask:0xf bank_mask:0xf\n"
"v_add_f32_dpp v24, v24, v24 row_ror:4 row_mask:0xf bank_mask:0xf\n"
"v_add_f32_dpp v48, v48, v48 row_ror:4 row_mask:0xf bank_mask:0xf\n"
"v_add_f32_dpp v49, v49, v49 row_ror:4 row_mask:0xf bank_mask:0xf\n"
"v_add_f32_dpp v50, v50, v50 row_ror:4 row_mask:0xf bank_mask:0xf\n"
"v_add_f32_dpp v51, v51, v51 row_ror:4 row_mask:0xf bank_mask:0xf\n"
"v_add_f32_dpp v24, v24, v24 row_ror:2 row_mask:0xf bank_mask:0xf\n"
"v_add_f32_dpp v48, v48, v48 row_ror:2 row_mask:0xf bank_mask:0xf\n"
"v_rcp_f32 v24, v24\n"
"v_add_f32_dpp v49, v49, v49 row_ror:2 row_mask:0xf bank_mask:0xf\n"
"v_add_f32_dpp v50, v50, v50 row_ror:2 row_mask:0xf bank_mask:0xf\n"
"v_add_f32_dpp v51, v51, v51 row_ror:2 row_mask:0xf bank_mask:0xf\n"
"v_pk_mul_f32 v[48:49], v[48:49], v[24:25] op_sel_hi:[1,0]\n"
"v_pk_mul_f32 v[50:51], v[50:51], v[24:25] op_sel_hi:[1,0]\n"
"v_cvt_pk_bf16_f32 v48, v48, v49\n"
"v_cvt_pk_bf16_f32 v49, v50, v51\n"
"ds_write2_b32 v26, v48, v49 offset1:4\n"
"s_waitcnt lgkmcnt(0)\n"
"s_barrier\n"
"ds_read_b128 v[28:31], v27 offset:0\n"
"ds_read_b128 v[32:35], v27 offset:128\n"
"s_waitcnt lgkmcnt(1)\n"
"v_smfmac_f32_16x16x64_bf16 v[224:227], v[28:31], a[0:7], v59\n"
"s_waitcnt lgkmcnt(0)\n"
"v_smfmac_f32_16x16x64_bf16 v[224:227], v[32:35], a[8:15], v59\n"
"v_smfmac_f32_16x16x64_bf16 v[228:231], v[28:31], a[16:23], v59\n"
"v_smfmac_f32_16x16x64_bf16 v[228:231], v[32:35], a[24:31], v59\n"
"v_smfmac_f32_16x16x64_bf16 v[232:235], v[28:31], a[32:39], v59\n"
"v_smfmac_f32_16x16x64_bf16 v[232:235], v[32:35], a[40:47], v59\n"
"v_smfmac_f32_16x16x64_bf16 v[236:239], v[28:31], a[48:55], v59\n"
"v_smfmac_f32_16x16x64_bf16 v[236:239], v[32:35], a[56:63], v59\n"
"v_smfmac_f32_16x16x64_bf16 v[240:243], v[28:31], a[64:71], v59\n"
"v_smfmac_f32_16x16x64_bf16 v[240:243], v[32:35], a[72:79], v59\n"
"v_add_f32_dpp v24, v224, v226 quad_perm:[0,1,2,3] row_mask:0x1 bank_mask:0xf\n"
"v_smfmac_f32_16x16x64_bf16 v[244:247], v[28:31], a[80:87], v59\n"
"v_add_f32_dpp v25, v228, v230 quad_perm:[0,1,2,3] row_mask:0x1 bank_mask:0xf\n"
"v_smfmac_f32_16x16x64_bf16 v[244:247], v[32:35], a[88:95], v59\n"
"v_add_f32_dpp v24, v232, v234 quad_perm:[0,1,2,3] row_mask:0x2 bank_mask:0xf\n"
"v_smfmac_f32_16x16x64_bf16 v[248:251], v[28:31], a[96:103], v59\n"
"v_add_f32_dpp v25, v236, v238 quad_perm:[0,1,2,3] row_mask:0x2 bank_mask:0xf\n"
"v_smfmac_f32_16x16x64_bf16 v[248:251], v[32:35], a[104:111], v59\n"
"v_smfmac_f32_16x16x64_bf16 v[252:255], v[28:31], a[112:119], v59\n"
"v_add_f32_dpp v24, v240, v242 quad_perm:[0,1,2,3] row_mask:0x4 bank_mask:0xf\n"
"v_smfmac_f32_16x16x64_bf16 v[252:255], v[32:35], a[120:127], v59\n"
"s_nop 0\n"
"v_add_f32_dpp v25, v244, v246 quad_perm:[0,1,2,3] row_mask:0x4 bank_mask:0xf\n"
"s_nop 2\n"
"v_add_f32_dpp v24, v248, v250 quad_perm:[0,1,2,3] row_mask:0x8 bank_mask:0xf\n"
"s_nop 1\n"
"v_add_f32_dpp v25, v252, v254 quad_perm:[0,1,2,3] row_mask:0x8 bank_mask:0xf\n"
"v_cvt_pk_f16_f32 v4, v24, v25\n"
"buffer_store_dword v4, v63, s[36:39], s77 offen sc1\n"
"s_xor_b32 s77, s77, 8192\n"
"buffer_store_dword v4, v63, s[36:39], s77 offen sc1\n"
"buffer_store_dwordx2 v[24:25], v60, s[36:39], s76 offen sc1\n"
"s_lshl_b32 s70, s51, 17\n"
"s_lshl_b32 s56, s50, 8\n"
"s_add_u32 s70, s70, s56\n"
"s_add_u32 s70, s70, s99\n"
"s_add_u32 s70, s70, 0x20000\n"
"v_lshrrev_b32 v40, 4, v0\n"
"v_lshlrev_b32 v36, 10, v40\n"
"v_and_b32 v37, 15, v0\n"
"v_lshl_add_u32 v36, v37, 4, v36\n"
"s_mov_b32 s55, 4194304\n"
"s_mov_b32 s78, 0x39800000\n"
"v_mov_b32 v39, v37\n"
"v_lshrrev_b32 v38, 3, v39\n"
"v_mul_u32_u24 v38, 576, v38\n"
"v_and_b32 v37, 7, v39\n"
"v_lshl_add_u32 v37, v37, 4, v38\n"
"v_and_b32 v38, 1, v40\n"
"v_mul_u32_u24 v38, 144, v38\n"
"v_add_u32 v37, v38, v37\n"
"v_lshrrev_b32 v38, 1, v40\n"
"v_lshl_add_u32 v37, v38, 2, v37\n"
"s_lshr_b32 s56, s50, 1\n"
"s_mul_i32 s56, s56, 272\n"
"v_add_u32 v37, s56, v37\n"
"s_lshl_b32 s56, s50, 3\n"
"v_add_u32 v37, s56, v37\n"
"v_add_u32 v38, 162560, v37\n"
"ds_read_b32 v39, v38\n"
"v_add_u32 v37, 159744, v37\n"
"v_lshrrev_b32 v41, 3, v0\n"
"v_lshl_add_u32 v41, v41, 4, v2\n"
"v_add_u32 v41, 159744, v41\n"
"v_and_b32 v42, 1, v0\n"
"v_lshlrev_b32 v42, 4, v42\n"
"v_mov_b32 v43, 0x3c00\n"
"v_lshlrev_b32 v42, v42, v43\n"
"v_bfe_u32 v43, v0, 1, 3\n"
"v_cmp_eq_u32 vcc, 0, v43\n"
"s_nop 1\n"
"v_cndmask_b32 v48, 0, v42, vcc\n"
"v_cmp_eq_u32 vcc, 2, v43\n"
"s_nop 1\n"
"v_cndmask_b32 v49, 0, v42, vcc\n"
"v_cmp_eq_u32 vcc, 4, v43\n"
"s_nop 1\n"
"v_cndmask_b32 v50, 0, v42, vcc\n"
"v_cmp_eq_u32 vcc, 6, v43\n"
"s_nop 1\n"
"v_cndmask_b32 v51, 0, v42, vcc\n"
"v_lshrrev_b32 v56, 4, v0\n"
"v_lshlrev_b32 v56, 2, v56\n"
"v_and_b32 v57, 2, v0\n"
"v_add_u32 v56, v57, v56\n"
"v_mul_u32_u24 v56, 72, v56\n"
"v_add_u32 v56, 159744, v56\n"
"v_mov_b32 v57, 163712\n"
"s_or_b64 vcc, s[62:63], s[66:67]\n"
"v_cndmask_b32 v56, v56, v57, vcc\n"
"v_mov_b32 v57, 0x3b000000\n"
"s_mov_b32 s79, 0x3b003b00\n"
"v_mov_b32 v58, 0x38a7c5ac\n"
"s_cbranch_execz LCDRYR\n"
"LCD1:\n"
"s_mul_i32 s76, s48, 32768\n"
"s_mul_i32 s56, s51, 16384\n"
"s_add_u32 s76, s76, s56\n"
"s_add_u32 s76, s76, 0x103400\n"
"s_and_b32 s56, s48, 28\n"
"s_add_u32 s56, s56, s48\n"
"s_lshl_b32 s56, s56, 11\n"
"s_add_u32 s56, s56, 0x62000\n"
"s_xor_b32 s56, s56, s99\n"
"s_cmp_lg_u32 s51, 0\n"
"s_cselect_b32 s76, s56, s76\n"
"s_lshl_b32 s56, s50, 9\n"
"s_add_u32 s76, s76, s56\n"
"s_and_b32 s77, s48, 24\n"
"s_add_u32 s77, s77, s48\n"
"s_lshl_b32 s77, s77, 10\n"
"s_lshl_b32 s56, s51, 17\n"
"s_add_u32 s77, s77, s56\n"
"s_lshl_b32 s56, s50, 8\n"
"s_add_u32 s77, s77, s56\n"
"s_add_u32 s77, s77, 0x32000\n"
"s_xor_b32 s77, s77, s99\n"
"v_and_b32 v63, 15, v0\n"
"v_lshrrev_b32 v60, 4, v0\n"
"v_lshl_add_u32 v63, v63, 2, v60\n"
"v_lshlrev_b32 v60, 3, v63\n"
"v_lshlrev_b32 v63, 2, v63\n"
"v_mov_b32 v224, 0\n"
"v_mov_b32 v226, 0\n"
"v_mov_b32 v228, 0\n"
"v_mov_b32 v230, 0\n"
"v_mov_b32 v232, 0\n"
"v_mov_b32 v234, 0\n"
"v_mov_b32 v236, 0\n"
"v_mov_b32 v238, 0\n"
"v_mov_b32 v240, 0\n"
"v_mov_b32 v242, 0\n"
"v_mov_b32 v244, 0\n"
"v_mov_b32 v246, 0\n"
"v_mov_b32 v248, 0\n"
"v_mov_b32 v250, 0\n"
"v_mov_b32 v252, 0\n"
"v_mov_b32 v254, 0\n"
"v_and_b32 v62, 2, v0\n"
"v_lshlrev_b32 v62, 3, v62\n"
"v_and_b32 v61, 12, v0\n"
"v_add_u32 v62, v62, v61\n"
"v_and_b32 v61, 1, v0\n"
"v_lshl_add_u32 v62, v61, 1, v62\n"
"s_lshl_b32 s56, s50, 5\n"
"s_add_u32 s56, s56, 162304\n"
"v_add_u32 v62, s56, v62\n"
"s_waitcnt lgkmcnt(0)\n"
"v_lshlrev_b32 v38, 16, v39\n"
"v_and_b32 v39, 0xffff0000, v39\n"
"s_cbranch_execz La2as_523\n"
"s_sleep 7\n"
"La2as_523:\n"
"s_add_u32 s56, s70, 0x0\n"
"buffer_load_dwordx4 v[192:195], v36, s[36:39], s56 offen sc1\n"
"s_add_u32 s56, s70, 0x1000\n"
"buffer_load_dwordx4 v[196:199], v36, s[36:39], s56 offen sc1\n"
"s_add_u32 s56, s70, 0x4000\n"
"buffer_load_dwordx4 v[200:203], v36, s[36:39], s56 offen sc1\n"
"s_add_u32 s56, s70, 0x5000\n"
"buffer_load_dwordx4 v[204:207], v36, s[36:39], s56 offen sc1\n"
"s_add_u32 s56, s70, 0x8000\n"
"buffer_load_dwordx4 v[208:211], v36, s[36:39], s56 offen sc1\n"
"s_add_u32 s56, s70, 0x9000\n"
"buffer_load_dwordx4 v[212:215], v36, s[36:39], s56 offen sc1\n"
"s_add_u32 s56, s70, 0xc000\n"
"buffer_load_dwordx4 v[216:219], v36, s[36:39], s56 offen sc1\n"
"s_add_u32 s56, s70, 0xd000\n"
"buffer_load_dwordx4 v[220:223], v36, s[36:39], s56 offen sc1\n"
"La2ac_522:\n"
"s_waitcnt vmcnt(7)\n"
"v_mfma_f32_16x16x32_f16 v[44:47], v[48:51], v[192:195], 0\n"
"s_waitcnt vmcnt(6)\n"
"v_mfma_f32_16x16x32_f16 v[44:47], v[48:51], v[196:199], v[44:47]\n"
"s_waitcnt vmcnt(5)\n"
"v_mfma_f32_16x16x32_f16 v[44:47], v[48:51], v[200:203], v[44:47]\n"
"s_waitcnt vmcnt(4)\n"
"v_mfma_f32_16x16x32_f16 v[44:47], v[48:51], v[204:207], v[44:47]\n"
"s_waitcnt vmcnt(3)\n"
"v_mfma_f32_16x16x32_f16 v[44:47], v[48:51], v[208:211], v[44:47]\n"
"s_waitcnt vmcnt(2)\n"
"v_mfma_f32_16x16x32_f16 v[44:47], v[48:51], v[212:215], v[44:47]\n"
"s_waitcnt vmcnt(1)\n"
"v_mfma_f32_16x16x32_f16 v[44:47], v[48:51], v[216:219], v[44:47]\n"
"s_waitcnt vmcnt(0)\n"
"v_mfma_f32_16x16x32_f16 v[44:47], v[48:51], v[220:223], v[44:47]\n"
"s_nop 7\n"
"v_cmp_u_f32 vcc, v44, v45\n"
"s_cbranch_vccz La2ad_521\n"
"LRTY:\n"
"s_mov_b32 s72, 0\n"
"v_max3_u32 v42, v192, v193, v194\n"
"v_max_u32 v42, v42, v195\n"
"v_cmp_eq_u32 vcc, -1, v42\n"
"s_cbranch_vccz Lskl_524\n"
"s_mov_b64 exec, vcc\n"
"s_add_u32 s56, s70, 0x0\n"
"buffer_load_dwordx4 v[192:195], v36, s[36:39], s56 offen sc1\n"
"s_mov_b32 s72, 1\n"
"s_mov_b64 exec, -1\n"
"Lskl_524:\n"
"v_max3_u32 v42, v196, v197, v198\n"
"v_max_u32 v42, v42, v199\n"
"v_cmp_eq_u32 vcc, -1, v42\n"
"s_cbranch_vccz Lskl_525\n"
"s_mov_b64 exec, vcc\n"
"s_add_u32 s56, s70, 0x1000\n"
"buffer_load_dwordx4 v[196:199], v36, s[36:39], s56 offen sc1\n"
"s_mov_b32 s72, 1\n"
"s_mov_b64 exec, -1\n"
"Lskl_525:\n"
"v_max3_u32 v42, v200, v201, v202\n"
"v_max_u32 v42, v42, v203\n"
"v_cmp_eq_u32 vcc, -1, v42\n"
"s_cbranch_vccz Lskl_526\n"
"s_mov_b64 exec, vcc\n"
"s_add_u32 s56, s70, 0x4000\n"
"buffer_load_dwordx4 v[200:203], v36, s[36:39], s56 offen sc1\n"
"s_mov_b32 s72, 1\n"
"s_mov_b64 exec, -1\n"
"Lskl_526:\n"
"v_max3_u32 v42, v204, v205, v206\n"
"v_max_u32 v42, v42, v207\n"
"v_cmp_eq_u32 vcc, -1, v42\n"
"s_cbranch_vccz Lskl_527\n"
"s_mov_b64 exec, vcc\n"
"s_add_u32 s56, s70, 0x5000\n"
"buffer_load_dwordx4 v[204:207], v36, s[36:39], s56 offen sc1\n"
"s_mov_b32 s72, 1\n"
"s_mov_b64 exec, -1\n"
"Lskl_527:\n"
"v_max3_u32 v42, v208, v209, v210\n"
"v_max_u32 v42, v42, v211\n"
"v_cmp_eq_u32 vcc, -1, v42\n"
"s_cbranch_vccz Lskl_528\n"
"s_mov_b64 exec, vcc\n"
"s_add_u32 s56, s70, 0x8000\n"
"buffer_load_dwordx4 v[208:211], v36, s[36:39], s56 offen sc1\n"
"s_mov_b32 s72, 1\n"
"s_mov_b64 exec, -1\n"
"Lskl_528:\n"
"v_max3_u32 v42, v212, v213, v214\n"
"v_max_u32 v42, v42, v215\n"
"v_cmp_eq_u32 vcc, -1, v42\n"
"s_cbranch_vccz Lskl_529\n"
"s_mov_b64 exec, vcc\n"
"s_add_u32 s56, s70, 0x9000\n"
"buffer_load_dwordx4 v[212:215], v36, s[36:39], s56 offen sc1\n"
"s_mov_b32 s72, 1\n"
"s_mov_b64 exec, -1\n"
"Lskl_529:\n"
"v_max3_u32 v42, v216, v217, v218\n"
"v_max_u32 v42, v42, v219\n"
"v_cmp_eq_u32 vcc, -1, v42\n"
"s_cbranch_vccz Lskl_530\n"
"s_mov_b64 exec, vcc\n"
"s_add_u32 s56, s70, 0xc000\n"
"buffer_load_dwordx4 v[216:219], v36, s[36:39], s56 offen sc1\n"
"s_mov_b32 s72, 1\n"
"s_mov_b64 exec, -1\n"
"Lskl_530:\n"
"v_max3_u32 v42, v220, v221, v222\n"
"v_max_u32 v42, v42, v223\n"
"v_cmp_eq_u32 vcc, -1, v42\n"
"s_cbranch_vccz Lskl_531\n"
"s_mov_b64 exec, vcc\n"
"s_add_u32 s56, s70, 0xd000\n"
"buffer_load_dwordx4 v[220:223], v36, s[36:39], s56 offen sc1\n"
"s_mov_b32 s72, 1\n"
"s_mov_b64 exec, -1\n"
"Lskl_531:\n"
"s_cbranch_execz LCD2\n"
"s_cmp_eq_u32 s72, 0\n"
"s_cbranch_scc1 LFBY\n"
"s_waitcnt vmcnt(0)\n"
"s_sub_u32 s55, s55, 1\n"
"s_cbranch_scc0 La2ac_522\n"
"La2ad_521:\n"
"v_pk_add_f32 v[46:47], v[44:45], v[38:39]\n"
"v_cvt_pk_bf16_f32 v44, v46, v47\n"
"ds_write_b32 v37, v44\n"
"v_mov_b32 v48, 0\n"
"v_mov_b32 v49, 0\n"
"v_mov_b32 v50, 0\n"
"v_mov_b32 v51, 0\n"
"v_mov_b32 v52, 0\n"
"v_mov_b32 v53, 0\n"
"v_mov_b32 v54, 0\n"
"v_mov_b32 v55, 0\n"
"s_waitcnt lgkmcnt(0)\n"
"s_barrier\n"
"ds_read_b128 v[36:39], v41\n"
"LFBRY:\n"
"ds_read_b128 v[192:195], v56 offset:0\n"
"ds_read_b128 v[196:199], v56 offset:16\n"
"ds_read_b128 v[200:203], v56 offset:32\n"
"s_waitcnt lgkmcnt(3)\n"
"v_dot2_f32_bf16 v40, v36, v36, v58\n"
"v_dot2_f32_bf16 v41, v36, s79, 0\n"
"v_dot2_f32_bf16 v40, v37, v37, v40\n"
"v_dot2_f32_bf16 v41, v37, s79, v41\n"
"v_dot2_f32_bf16 v40, v38, v38, v40\n"
"v_dot2_f32_bf16 v41, v38, s79, v41\n"
"v_dot2_f32_bf16 v40, v39, v39, v40\n"
"v_dot2_f32_bf16 v41, v39, s79, v41\n"
"s_waitcnt lgkmcnt(2)\n"
"v_smfmac_f32_16x16x64_bf16 v[48:51], v[192:195], a[128:135], v59\n"
"ds_read_b128 v[204:207], v56 offset:48\n"
"v_smfmac_f32_16x16x64_bf16 v[52:55], v[192:195], a[192:199], v59\n"
"v_permlane32_swap_b32 v41, v40\n"
"s_waitcnt lgkmcnt(2)\n"
"v_smfmac_f32_16x16x64_bf16 v[48:51], v[196:199], a[136:143], v59\n"
"ds_read_b128 v[208:211], v56 offset:64\n"
"v_add_f32 v40, v41, v40\n"
"v_smfmac_f32_16x16x64_bf16 v[52:55], v[196:199], a[200:207], v59\n"
"s_waitcnt lgkmcnt(2)\n"
"v_smfmac_f32_16x16x64_bf16 v[48:51], v[200:203], a[144:151], v59\n"
"ds_read_b128 v[212:215], v56 offset:80\n"
"v_add_f32_dpp v40, v40, v40 quad_perm:[1,0,3,2] row_mask:0xf bank_mask:0xf\n"
"v_smfmac_f32_16x16x64_bf16 v[52:55], v[200:203], a[208:215], v59\n"
"s_waitcnt lgkmcnt(2)\n"
"v_smfmac_f32_16x16x64_bf16 v[48:51], v[204:207], a[152:159], v59\n"
"ds_read_b128 v[216:219], v56 offset:96\n"
"v_add_f32_dpp v40, v40, v40 quad_perm:[2,3,0,1] row_mask:0xf bank_mask:0xf\n"
"v_smfmac_f32_16x16x64_bf16 v[52:55], v[204:207], a[216:223], v59\n"
"s_waitcnt lgkmcnt(2)\n"
"v_smfmac_f32_16x16x64_bf16 v[48:51], v[208:211], a[160:167], v59\n"
"ds_read_b128 v[220:223], v56 offset:112\n"
"v_add_f32_dpp v40, v40, v40 row_half_mirror row_mask:0xf bank_mask:0xf\n"
"v_smfmac_f32_16x16x64_bf16 v[52:55], v[208:211], a[224:231], v59\n"
"s_waitcnt lgkmcnt(2)\n"
"v_smfmac_f32_16x16x64_bf16 v[48:51], v[212:215], a[168:175], v59\n"
"v_add_f32_dpp v40, v40, v40 row_mirror row_mask:0xf bank_mask:0xf\n"
"v_smfmac_f32_16x16x64_bf16 v[52:55], v[212:215], a[232:239], v59\n"
"s_waitcnt lgkmcnt(1)\n"
"v_smfmac_f32_16x16x64_bf16 v[48:51], v[216:219], a[176:183], v59\n"
"v_add_f32_dpp v40, v40, v40 row_bcast:15 row_mask:0xa bank_mask:0xf\n"
"v_smfmac_f32_16x16x64_bf16 v[52:55], v[216:219], a[240:247], v59\n"
"v_readlane_b32 s75, v40, 63\n"
"s_waitcnt lgkmcnt(0)\n"
"v_smfmac_f32_16x16x64_bf16 v[48:51], v[220:223], a[184:191], v59\n"
"v_readlane_b32 s74, v40, 31\n"
"v_smfmac_f32_16x16x64_bf16 v[52:55], v[220:223], a[248:255], v59\n"
"s_cmp_lt_u32 s75, 0x4e000000\n"
"v_mul_f32 v42, s75, v57\n"
"v_fma_f32 v43, -s74, s74, v42\n"
"s_cselect_b32 s78, s78, 0x7f800000\n"
"v_rsq_f32 v47, v43\n"
"v_fmamk_f32 v42, v42, 0xbc800000, v43\n"
"v_cmp_nlt_f32 vcc, s78, v42\n"
"v_pk_add_f32 v[48:49], v[48:49], v[50:51]\n"
"v_pk_add_f32 v[52:53], v[52:53], v[54:55]\n"
"v_mul_f32 v46, 0xbfb8aa3b, v47\n"
"s_cbranch_vccnz LFB2Y\n"
"v_fma_f32 v44, -s74, v49, v48\n"
"v_fma_f32 v45, -s74, v53, v52\n"
"v_pk_mul_f32 v[44:45], v[44:45], v[46:47]\n"
"LPOSTY:\n"
"v_exp_f32 v49, v44\n"
"s_nop 0\n"
"v_add_f32 v49, 1.0, v49\n"
"v_rcp_f32 v49, v49\n"
"s_nop 0\n"
"v_mul_f32 v49, v49, v45\n"
"v_cvt_pk_bf16_f32 v49, v49, v49\n"
"ds_write_b16 v62, v49\n"
"s_cbranch_execz LRTZ\n"
"LCD2:\n"
"s_waitcnt lgkmcnt(0)\n"
"s_barrier\n"
"ds_read_b128 v[28:31], v27\n"
"s_waitcnt lgkmcnt(0)\n"
"v_smfmac_f32_16x16x64_bf16 v[224:227], v[28:31], v[64:71], v59\n"
"v_smfmac_f32_16x16x64_bf16 v[228:231], v[28:31], v[72:79], v59\n"
"v_smfmac_f32_16x16x64_bf16 v[232:235], v[28:31], v[80:87], v59\n"
"v_smfmac_f32_16x16x64_bf16 v[236:239], v[28:31], v[88:95], v59\n"
"v_smfmac_f32_16x16x64_bf16 v[240:243], v[28:31], v[96:103], v59\n"
"v_smfmac_f32_16x16x64_bf16 v[244:247], v[28:31], v[104:111], v59\n"
"v_smfmac_f32_16x16x64_bf16 v[248:251], v[28:31], v[112:119], v59\n"
"v_smfmac_f32_16x16x64_bf16 v[252:255], v[28:31], v[120:127], v59\n"
"s_cmp_lg_u32 s51, 0\n"
"v_add_f32_dpp v24, v224, v226 quad_perm:[0,1,2,3] row_mask:0x1 bank_mask:0xf\n"
"v_add_f32_dpp v25, v228, v230 quad_perm:[0,1,2,3] row_mask:0x1 bank_mask:0xf\n"
"v_add_f32_dpp v24, v232, v234 quad_perm:[0,1,2,3] row_mask:0x2 bank_mask:0xf\n"
"v_add_f32_dpp v25, v236, v238 quad_perm:[0,1,2,3] row_mask:0x2 bank_mask:0xf\n"
"v_add_f32_dpp v24, v240, v242 quad_perm:[0,1,2,3] row_mask:0x4 bank_mask:0xf\n"
"v_add_f32_dpp v25, v244, v246 quad_perm:[0,1,2,3] row_mask:0x4 bank_mask:0xf\n"
"v_add_f32_dpp v24, v248, v250 quad_perm:[0,1,2,3] row_mask:0x8 bank_mask:0xf\n"
"v_add_f32_dpp v25, v252, v254 quad_perm:[0,1,2,3] row_mask:0x8 bank_mask:0xf\n"
"s_cbranch_scc1 Lh16_547\n"
"v_cvt_pk_f16_f32 v4, v24, v25\n"
"buffer_store_dword v4, v63, s[36:39], s77 offen sc1\n"
"s_xor_b32 s77, s77, 8192\n"
"buffer_store_dword v4, v63, s[36:39], s77 offen sc1\n"
"s_branch Lh16d_548\n"
"Lh16_547:\n"
"buffer_store_dwordx2 v[24:25], v60, s[36:39], s76 offen sc1\n"
"s_xor_b32 s76, s76, 8192\n"
"Lh16d_548:\n"
"buffer_store_dwordx2 v[24:25], v60, s[36:39], s76 offen sc1\n"
"s_cmp_eq_u32 s51, 0\n"
"s_cbranch_scc1 LS4\n"
"s_cmp_lg_u32 s50, 0\n"
"s_cbranch_scc1 LEND\n"
"LFIN:\n"
"s_lshl_b32 s70, s48, 6\n"
"s_add_u32 s70, s70, s99\n"
"s_add_u32 s70, s70, 0x60000\n"
"s_add_u32 s71, s70, 0x10000\n"
"v_lshrrev_b32 v20, 4, v0\n"
"v_lshlrev_b32 v20, 14, v20\n"
"v_bfe_u32 v21, v0, 2, 2\n"
"v_lshl_add_u32 v20, v21, 11, v20\n"
"v_and_b32 v21, 3, v0\n"
"v_lshl_add_u32 v20, v21, 4, v20\n"
"v_mov_b32 v4, -1\n"
"v_mov_b32 v5, -1\n"
"v_mov_b32 v6, -1\n"
"v_mov_b32 v7, -1\n"
"v_mov_b32 v8, -1\n"
"v_mov_b32 v9, -1\n"
"v_mov_b32 v10, -1\n"
"v_mov_b32 v11, -1\n"
"buffer_load_dwordx4 v[4:7], v20, s[36:39], s70 offen sc1\n"
"buffer_load_dwordx4 v[8:11], v20, s[36:39], s71 offen sc1\n"
"v_lshrrev_b32 v23, 5, v0\n"
"v_bfe_u32 v24, v0, 4, 1\n"
"v_mul_u32_u24 v22, 144, v24\n"
"v_lshl_add_u32 v22, v23, 1, v22\n"
"v_and_b32 v25, 2, v0\n"
"v_lshl_add_u32 v22, v25, 3, v22\n"
"v_and_b32 v25, 1, v0\n"
"v_lshl_add_u32 v22, v25, 2, v22\n"
"s_and_b32 s73, s48, 4\n"
"s_mul_i32 s73, s73, 144\n"
"s_and_b32 s56, s48, 16\n"
"s_mul_i32 s56, s56, 18\n"
"s_add_u32 s73, s73, s56\n"
"s_and_b32 s56, s48, 3\n"
"s_lshl_b32 s56, s56, 5\n"
"s_add_u32 s73, s73, s56\n"
"s_and_b32 s56, s48, 8\n"
"s_add_u32 s73, s73, s56\n"
"s_add_u32 s73, s73, 159744\n"
"v_add_u32 v22, s73, v22\n"
"ds_read_u16 v25, v22\n"
"v_lshlrev_b32 v22, 3, v21\n"
"v_lshl_add_u32 v22, v24, 2, v22\n"
"v_lshl_add_u32 v22, v23, 1, v22\n"
"s_lshl_b32 s56, s48, 5\n"
"v_add_u32 v22, s56, v22\n"
"v_and_b32 v23, 12, v0\n"
"v_cmp_eq_u32 vcc, 0, v23\n"
"v_mov_b32 v23, 0x7ff00000\n"
"s_nop 0\n"
"v_cndmask_b32 v22, v23, v22, vcc\n"
"v_mov_b32 v26, s59\n"
"v_add_u32 v27, s48, v0\n"
"v_cmp_eq_u32 vcc, 0, v27\n"
"s_nop 1\n"
"v_cndmask_b32 v27, v23, v27, vcc\n"
"s_waitcnt lgkmcnt(0)\n"
"v_lshlrev_b32 v25, 16, v25\n"
"s_mov_b32 s55, 4194304\n"
"s_mov_b64 s[74:75], exec\n"
"Lspoll_550:\n"
"s_waitcnt vmcnt(16)\n"
"buffer_load_dwordx4 v[4:7], v20, s[36:39], s70 offen sc1\n"
"buffer_load_dwordx4 v[8:11], v20, s[36:39], s71 offen sc1\n"
"v_max3_u32 v12, v4, v5, v6\n"
"v_max3_u32 v13, v8, v9, v10\n"
"v_max_u32 v12, v12, v7\n"
"v_max_u32 v13, v13, v11\n"
"v_max_u32 v12, v12, v13\n"
"v_cmp_eq_u32 vcc, -1, v12\n"
"s_cbranch_vccz Lsdone_551\n"
"s_mov_b64 exec, vcc\n"
"v_max3_u32 v12, v4, v5, v6\n"
"v_max3_u32 v13, v8, v9, v10\n"
"v_max_u32 v12, v12, v7\n"
"v_max_u32 v13, v13, v11\n"
"v_max_u32 v12, v12, v13\n"
"v_cmp_eq_u32 vcc, -1, v12\n"
"s_cbranch_vccz Lsdone_551\n"
"s_mov_b64 exec, vcc\n"
"v_max3_u32 v12, v4, v5, v6\n"
"v_max3_u32 v13, v8, v9, v10\n"
"v_max_u32 v12, v12, v7\n"
"v_max_u32 v13, v13, v11\n"
"v_max_u32 v12, v12, v13\n"
"v_cmp_eq_u32 vcc, -1, v12\n"
"s_cbranch_vccz Lsdone_551\n"
"s_mov_b64 exec, vcc\n"
"v_max3_u32 v12, v4, v5, v6\n"
"v_max3_u32 v13, v8, v9, v10\n"
"v_max_u32 v12, v12, v7\n"
"v_max_u32 v13, v13, v11\n"
"v_max_u32 v12, v12, v13\n"
"v_cmp_eq_u32 vcc, -1, v12\n"
"s_cbranch_vccz Lsdone_551\n"
"s_mov_b64 exec, vcc\n"
"s_sub_u32 s55, s55, 1\n"
"s_cbranch_scc0 Lspoll_550\n"
"Lsdone_551:\n"
"s_mov_b64 exec, s[74:75]\n"
"Lfok_549:\n"
"v_pk_add_f32 v[14:15], v[4:5], v[8:9]\n"
"v_pk_add_f32 v[16:17], v[6:7], v[10:11]\n"
"s_nop 0\n"
"v_permlane32_swap_b32 v14, v15\n"
"v_permlane32_swap_b32 v16, v17\n"
"v_add_f32 v14, v14, v15\n"
"v_add_f32 v16, v16, v17\n"
"s_nop 1\n"
"v_permlane16_swap_b32 v14, v16\n"
"v_add_f32 v14, v14, v16\n"
"s_nop 1\n"
"v_add_f32_dpp v14, v14, v14 row_ror:4 row_mask:0xf bank_mask:0xf\n"
"s_nop 1\n"
"v_add_f32_dpp v14, v14, v14 row_ror:8 row_mask:0xf bank_mask:0xf\n"
"v_add_f32 v14, v14, v25\n"
"v_cvt_pk_bf16_f32 v15, v14, v14\n"
"buffer_store_short v15, v22, s[44:47], 0 offen\n"
"buffer_store_dword v26, v27, s[36:39], 0 offen sc1\n"
"s_cbranch_execz LFINR\n"
"s_waitcnt vmcnt(2)\n"
"s_branch LEND\n"
"LS5_pre:\n"
"s_nop 7\n"
"s_lshl_b32 s56, s50, 14\n"
"v_add_u32 v61, s56, v2\n"
"ds_read_b128 v[4:7], v61 offset:0\n"
"ds_read_b128 v[8:11], v61 offset:1024\n"
"ds_read_b128 v[12:15], v61 offset:2048\n"
"ds_read_b128 v[16:19], v61 offset:3072\n"
"ds_read_b128 v[20:23], v61 offset:4096\n"
"ds_read_b128 v[24:27], v61 offset:5120\n"
"ds_read_b128 v[28:31], v61 offset:6144\n"
"ds_read_b128 v[32:35], v61 offset:7168\n"
"v_and_b32 v60, 15, v0\n"
"v_lshlrev_b32 v60, 3, v60\n"
"v_and_b32 v62, 16, v0\n"
"v_lshl_add_u32 v62, v62, 9, v60\n"
"v_cmp_gt_u32 vcc, 32, v0\n"
"v_mov_b32 v60, 0x7ff00000\n"
"s_nop 0\n"
"v_cndmask_b32 v60, v60, v62, vcc\n"
"v_mov_b32 v63, s59\n"
"s_lshr_b32 s71, s48, 4\n"
"s_lshl_b32 s71, s71, 4\n"
"s_add_u32 s71, s71, s48\n"
"s_lshl_b32 s71, s71, 9\n"
"s_add_u32 s71, s71, 0x8000\n"
"s_lshl_b32 s56, s50, 7\n"
"s_add_u32 s71, s71, s56\n"
"ds_read_b128 v[224:227], v61 offset:8192\n"
"ds_read_b128 v[228:231], v61 offset:9216\n"
"ds_read_b128 v[232:235], v61 offset:10240\n"
"ds_read_b128 v[236:239], v61 offset:11264\n"
"ds_read_b128 v[240:243], v61 offset:12288\n"
"ds_read_b128 v[244:247], v61 offset:13312\n"
"ds_read_b128 v[248:251], v61 offset:14336\n"
"ds_read_b128 v[252:255], v61 offset:15360\n"
"s_waitcnt lgkmcnt(0)\n"
"s_branch LSLOWZ\n"
"LS4:\n"
"s_nop 7\n"
"s_lshl_b32 s70, s51, 17\n"
"s_lshl_b32 s56, s50, 8\n"
"s_add_u32 s70, s70, s56\n"
"s_add_u32 s70, s70, s99\n"
"s_add_u32 s70, s70, 0x30000\n"
"v_lshrrev_b32 v40, 4, v0\n"
"v_lshlrev_b32 v36, 10, v40\n"
"v_and_b32 v37, 15, v0\n"
"v_lshl_add_u32 v36, v37, 4, v36\n"
"s_mov_b32 s55, 4194304\n"
"s_mov_b32 s78, 0x39800000\n"
"v_mov_b32 v39, v37\n"
"v_lshrrev_b32 v38, 3, v39\n"
"v_mul_u32_u24 v38, 576, v38\n"
"v_and_b32 v37, 7, v39\n"
"v_lshl_add_u32 v37, v37, 4, v38\n"
"v_and_b32 v38, 1, v40\n"
"v_mul_u32_u24 v38, 144, v38\n"
"v_add_u32 v37, v38, v37\n"
"v_lshrrev_b32 v38, 1, v40\n"
"v_lshl_add_u32 v37, v38, 2, v37\n"
"s_lshr_b32 s56, s50, 1\n"
"s_mul_i32 s56, s56, 272\n"
"v_add_u32 v37, s56, v37\n"
"s_lshl_b32 s56, s50, 3\n"
"v_add_u32 v37, s56, v37\n"
"v_add_u32 v38, 159744, v37\n"
"ds_read_b32 v39, v38\n"
"v_add_u32 v37, 162560, v37\n"
"v_lshrrev_b32 v41, 3, v0\n"
"v_lshl_add_u32 v41, v41, 4, v2\n"
"v_add_u32 v41, 162560, v41\n"
"v_and_b32 v42, 1, v0\n"
"v_lshlrev_b32 v42, 4, v42\n"
"v_mov_b32 v43, 0x3c00\n"
"v_lshlrev_b32 v42, v42, v43\n"
"v_bfe_u32 v43, v0, 1, 3\n"
"v_cmp_eq_u32 vcc, 0, v43\n"
"s_nop 1\n"
"v_cndmask_b32 v48, 0, v42, vcc\n"
"v_cmp_eq_u32 vcc, 2, v43\n"
"s_nop 1\n"
"v_cndmask_b32 v49, 0, v42, vcc\n"
"v_cmp_eq_u32 vcc, 4, v43\n"
"s_nop 1\n"
"v_cndmask_b32 v50, 0, v42, vcc\n"
"v_cmp_eq_u32 vcc, 6, v43\n"
"s_nop 1\n"
"v_cndmask_b32 v51, 0, v42, vcc\n"
"v_lshrrev_b32 v56, 4, v0\n"
"v_lshlrev_b32 v56, 2, v56\n"
"v_and_b32 v57, 2, v0\n"
"v_add_u32 v56, v57, v56\n"
"v_mul_u32_u24 v56, 72, v56\n"
"v_add_u32 v56, 162560, v56\n"
"v_mov_b32 v57, 163712\n"
"s_or_b64 vcc, s[62:63], s[66:67]\n"
"v_cndmask_b32 v56, v56, v57, vcc\n"
"v_mov_b32 v57, 0x3b000000\n"
"s_mov_b32 s79, 0x3b003b00\n"
"v_mov_b32 v58, 0x38a7c5ac\n"
"s_lshl_b32 s56, s50, 14\n"
"v_add_u32 v61, s56, v2\n"
"ds_read_b128 v[4:7], v61 offset:0\n"
"ds_read_b128 v[8:11], v61 offset:1024\n"
"ds_read_b128 v[12:15], v61 offset:2048\n"
"ds_read_b128 v[16:19], v61 offset:3072\n"
"ds_read_b128 v[20:23], v61 offset:4096\n"
"ds_read_b128 v[24:27], v61 offset:5120\n"
"ds_read_b128 v[28:31], v61 offset:6144\n"
"ds_read_b128 v[32:35], v61 offset:7168\n"
"v_and_b32 v60, 15, v0\n"
"v_lshlrev_b32 v60, 3, v60\n"
"v_and_b32 v62, 16, v0\n"
"v_lshl_add_u32 v62, v62, 9, v60\n"
"v_cmp_gt_u32 vcc, 32, v0\n"
"v_mov_b32 v60, 0x7ff00000\n"
"s_nop 0\n"
"v_cndmask_b32 v60, v60, v62, vcc\n"
"v_mov_b32 v63, s59\n"
"s_lshr_b32 s71, s48, 4\n"
"s_lshl_b32 s71, s71, 4\n"
"s_add_u32 s71, s71, s48\n"
"s_lshl_b32 s71, s71, 9\n"
"s_add_u32 s71, s71, 0x10000\n"
"s_lshl_b32 s56, s50, 7\n"
"s_add_u32 s71, s71, s56\n"
"ds_read_b128 v[224:227], v61 offset:8192\n"
"ds_read_b128 v[228:231], v61 offset:9216\n"
"ds_read_b128 v[232:235], v61 offset:10240\n"
"ds_read_b128 v[236:239], v61 offset:11264\n"
"ds_read_b128 v[240:243], v61 offset:12288\n"
"ds_read_b128 v[244:247], v61 offset:13312\n"
"ds_read_b128 v[248:251], v61 offset:14336\n"
"ds_read_b128 v[252:255], v61 offset:15360\n"
"s_waitcnt lgkmcnt(0)\n"
"s_cbranch_execz LCDRYR\n"
"LCD3:\n"
"s_waitcnt lgkmcnt(0)\n"
"v_lshlrev_b32 v38, 16, v39\n"
"v_and_b32 v39, 0xffff0000, v39\n"
"s_cbranch_execz La2as_555\n"
"s_sleep 4\n"
"La2as_555:\n"
"s_add_u32 s56, s70, 0x0\n"
"buffer_load_dwordx4 v[192:195], v36, s[36:39], s56 offen sc1\n"
"s_add_u32 s56, s70, 0x1000\n"
"buffer_load_dwordx4 v[196:199], v36, s[36:39], s56 offen sc1\n"
"s_add_u32 s56, s70, 0x4000\n"
"buffer_load_dwordx4 v[200:203], v36, s[36:39], s56 offen sc1\n"
"s_add_u32 s56, s70, 0x5000\n"
"buffer_load_dwordx4 v[204:207], v36, s[36:39], s56 offen sc1\n"
"s_add_u32 s56, s70, 0x8000\n"
"buffer_load_dwordx4 v[208:211], v36, s[36:39], s56 offen sc1\n"
"s_add_u32 s56, s70, 0x9000\n"
"buffer_load_dwordx4 v[212:215], v36, s[36:39], s56 offen sc1\n"
"s_add_u32 s56, s70, 0xc000\n"
"buffer_load_dwordx4 v[216:219], v36, s[36:39], s56 offen sc1\n"
"s_add_u32 s56, s70, 0xd000\n"
"buffer_load_dwordx4 v[220:223], v36, s[36:39], s56 offen sc1\n"
"La2ac_554:\n"
"s_waitcnt vmcnt(7)\n"
"v_mfma_f32_16x16x32_f16 v[44:47], v[48:51], v[192:195], 0\n"
"s_waitcnt vmcnt(6)\n"
"v_mfma_f32_16x16x32_f16 v[44:47], v[48:51], v[196:199], v[44:47]\n"
"s_waitcnt vmcnt(5)\n"
"v_mfma_f32_16x16x32_f16 v[44:47], v[48:51], v[200:203], v[44:47]\n"
"s_waitcnt vmcnt(4)\n"
"v_mfma_f32_16x16x32_f16 v[44:47], v[48:51], v[204:207], v[44:47]\n"
"s_waitcnt vmcnt(3)\n"
"v_mfma_f32_16x16x32_f16 v[44:47], v[48:51], v[208:211], v[44:47]\n"
"s_waitcnt vmcnt(2)\n"
"v_mfma_f32_16x16x32_f16 v[44:47], v[48:51], v[212:215], v[44:47]\n"
"s_waitcnt vmcnt(1)\n"
"v_mfma_f32_16x16x32_f16 v[44:47], v[48:51], v[216:219], v[44:47]\n"
"s_waitcnt vmcnt(0)\n"
"v_mfma_f32_16x16x32_f16 v[44:47], v[48:51], v[220:223], v[44:47]\n"
"s_nop 7\n"
"v_cmp_u_f32 vcc, v44, v45\n"
"s_cbranch_vccz La2ad_553\n"
"LRTZ:\n"
"s_mov_b32 s72, 0\n"
"v_max3_u32 v42, v192, v193, v194\n"
"v_max_u32 v42, v42, v195\n"
"v_cmp_eq_u32 vcc, -1, v42\n"
"s_cbranch_vccz Lskl_556\n"
"s_mov_b64 exec, vcc\n"
"s_add_u32 s56, s70, 0x0\n"
"buffer_load_dwordx4 v[192:195], v36, s[36:39], s56 offen sc1\n"
"s_mov_b32 s72, 1\n"
"s_mov_b64 exec, -1\n"
"Lskl_556:\n"
"v_max3_u32 v42, v196, v197, v198\n"
"v_max_u32 v42, v42, v199\n"
"v_cmp_eq_u32 vcc, -1, v42\n"
"s_cbranch_vccz Lskl_557\n"
"s_mov_b64 exec, vcc\n"
"s_add_u32 s56, s70, 0x1000\n"
"buffer_load_dwordx4 v[196:199], v36, s[36:39], s56 offen sc1\n"
"s_mov_b32 s72, 1\n"
"s_mov_b64 exec, -1\n"
"Lskl_557:\n"
"v_max3_u32 v42, v200, v201, v202\n"
"v_max_u32 v42, v42, v203\n"
"v_cmp_eq_u32 vcc, -1, v42\n"
"s_cbranch_vccz Lskl_558\n"
"s_mov_b64 exec, vcc\n"
"s_add_u32 s56, s70, 0x4000\n"
"buffer_load_dwordx4 v[200:203], v36, s[36:39], s56 offen sc1\n"
"s_mov_b32 s72, 1\n"
"s_mov_b64 exec, -1\n"
"Lskl_558:\n"
"v_max3_u32 v42, v204, v205, v206\n"
"v_max_u32 v42, v42, v207\n"
"v_cmp_eq_u32 vcc, -1, v42\n"
"s_cbranch_vccz Lskl_559\n"
"s_mov_b64 exec, vcc\n"
"s_add_u32 s56, s70, 0x5000\n"
"buffer_load_dwordx4 v[204:207], v36, s[36:39], s56 offen sc1\n"
"s_mov_b32 s72, 1\n"
"s_mov_b64 exec, -1\n"
"Lskl_559:\n"
"v_max3_u32 v42, v208, v209, v210\n"
"v_max_u32 v42, v42, v211\n"
"v_cmp_eq_u32 vcc, -1, v42\n"
"s_cbranch_vccz Lskl_560\n"
"s_mov_b64 exec, vcc\n"
"s_add_u32 s56, s70, 0x8000\n"
"buffer_load_dwordx4 v[208:211], v36, s[36:39], s56 offen sc1\n"
"s_mov_b32 s72, 1\n"
"s_mov_b64 exec, -1\n"
"Lskl_560:\n"
"v_max3_u32 v42, v212, v213, v214\n"
"v_max_u32 v42, v42, v215\n"
"v_cmp_eq_u32 vcc, -1, v42\n"
"s_cbranch_vccz Lskl_561\n"
"s_mov_b64 exec, vcc\n"
"s_add_u32 s56, s70, 0x9000\n"
"buffer_load_dwordx4 v[212:215], v36, s[36:39], s56 offen sc1\n"
"s_mov_b32 s72, 1\n"
"s_mov_b64 exec, -1\n"
"Lskl_561:\n"
"v_max3_u32 v42, v216, v217, v218\n"
"v_max_u32 v42, v42, v219\n"
"v_cmp_eq_u32 vcc, -1, v42\n"
"s_cbranch_vccz Lskl_562\n"
"s_mov_b64 exec, vcc\n"
"s_add_u32 s56, s70, 0xc000\n"
"buffer_load_dwordx4 v[216:219], v36, s[36:39], s56 offen sc1\n"
"s_mov_b32 s72, 1\n"
"s_mov_b64 exec, -1\n"
"Lskl_562:\n"
"v_max3_u32 v42, v220, v221, v222\n"
"v_max_u32 v42, v42, v223\n"
"v_cmp_eq_u32 vcc, -1, v42\n"
"s_cbranch_vccz Lskl_563\n"
"s_mov_b64 exec, vcc\n"
"s_add_u32 s56, s70, 0xd000\n"
"buffer_load_dwordx4 v[220:223], v36, s[36:39], s56 offen sc1\n"
"s_mov_b32 s72, 1\n"
"s_mov_b64 exec, -1\n"
"Lskl_563:\n"
"s_cbranch_execz LCDRYR\n"
"s_cmp_eq_u32 s72, 0\n"
"s_cbranch_scc1 LFBZ\n"
"s_waitcnt vmcnt(0)\n"
"s_sub_u32 s55, s55, 1\n"
"s_cbranch_scc0 La2ac_554\n"
"La2ad_553:\n"
"v_pk_add_f32 v[46:47], v[44:45], v[38:39]\n"
"v_cvt_pk_bf16_f32 v44, v46, v47\n"
"ds_write_b32 v37, v44\n"
"v_mov_b32 v48, 0\n"
"v_mov_b32 v49, 0\n"
"v_mov_b32 v50, 0\n"
"v_mov_b32 v51, 0\n"
"v_mov_b32 v52, 0\n"
"v_mov_b32 v53, 0\n"
"v_mov_b32 v54, 0\n"
"v_mov_b32 v55, 0\n"
"s_waitcnt lgkmcnt(0)\n"
"s_barrier\n"
"ds_read_b128 v[36:39], v41\n"
"LFBRZ:\n"
"s_mov_b32 s51, 1\n"
"ds_read_b128 v[192:195], v56 offset:0\n"
"ds_read_b128 v[196:199], v56 offset:16\n"
"ds_read_b128 v[200:203], v56 offset:32\n"
"s_waitcnt lgkmcnt(3)\n"
"v_dot2_f32_bf16 v40, v36, v36, v58\n"
"v_dot2_f32_bf16 v41, v36, s79, 0\n"
"v_dot2_f32_bf16 v40, v37, v37, v40\n"
"v_dot2_f32_bf16 v41, v37, s79, v41\n"
"v_dot2_f32_bf16 v40, v38, v38, v40\n"
"v_dot2_f32_bf16 v41, v38, s79, v41\n"
"v_dot2_f32_bf16 v40, v39, v39, v40\n"
"v_dot2_f32_bf16 v41, v39, s79, v41\n"
"s_waitcnt lgkmcnt(2)\n"
"v_smfmac_f32_16x16x64_bf16 v[48:51], v[192:195], v[128:135], v59\n"
"ds_read_b128 v[204:207], v56 offset:48\n"
"v_smfmac_f32_16x16x64_bf16 v[52:55], v[192:195], v[136:143], v59\n"
"v_permlane32_swap_b32 v41, v40\n"
"s_waitcnt lgkmcnt(2)\n"
"v_smfmac_f32_16x16x64_bf16 v[48:51], v[196:199], v[144:151], v59\n"
"ds_read_b128 v[208:211], v56 offset:64\n"
"v_add_f32 v40, v41, v40\n"
"v_smfmac_f32_16x16x64_bf16 v[52:55], v[196:199], v[152:159], v59\n"
"s_waitcnt lgkmcnt(2)\n"
"v_smfmac_f32_16x16x64_bf16 v[48:51], v[200:203], v[160:167], v59\n"
"ds_read_b128 v[212:215], v56 offset:80\n"
"v_add_f32_dpp v40, v40, v40 quad_perm:[1,0,3,2] row_mask:0xf bank_mask:0xf\n"
"v_smfmac_f32_16x16x64_bf16 v[52:55], v[200:203], v[168:175], v59\n"
"s_waitcnt lgkmcnt(2)\n"
"v_smfmac_f32_16x16x64_bf16 v[48:51], v[204:207], v[176:183], v59\n"
"ds_read_b128 v[216:219], v56 offset:96\n"
"v_add_f32_dpp v40, v40, v40 quad_perm:[2,3,0,1] row_mask:0xf bank_mask:0xf\n"
"v_smfmac_f32_16x16x64_bf16 v[52:55], v[204:207], v[184:191], v59\n"
"s_waitcnt lgkmcnt(2)\n"
"v_smfmac_f32_16x16x64_bf16 v[48:51], v[208:211], v[4:11], v59\n"
"ds_read_b128 v[220:223], v56 offset:112\n"
"v_add_f32_dpp v40, v40, v40 row_half_mirror row_mask:0xf bank_mask:0xf\n"
"v_smfmac_f32_16x16x64_bf16 v[52:55], v[208:211], v[12:19], v59\n"
"s_waitcnt lgkmcnt(2)\n"
"v_smfmac_f32_16x16x64_bf16 v[48:51], v[212:215], v[20:27], v59\n"
"v_add_f32_dpp v40, v40, v40 row_mirror row_mask:0xf bank_mask:0xf\n"
"v_smfmac_f32_16x16x64_bf16 v[52:55], v[212:215], v[28:35], v59\n"
"s_waitcnt lgkmcnt(1)\n"
"v_smfmac_f32_16x16x64_bf16 v[48:51], v[216:219], v[224:231], v59\n"
"v_add_f32_dpp v40, v40, v40 row_bcast:15 row_mask:0xa bank_mask:0xf\n"
"v_smfmac_f32_16x16x64_bf16 v[52:55], v[216:219], v[232:239], v59\n"
"v_readlane_b32 s75, v40, 63\n"
"s_waitcnt lgkmcnt(0)\n"
"v_smfmac_f32_16x16x64_bf16 v[48:51], v[220:223], v[240:247], v59\n"
"v_readlane_b32 s74, v40, 31\n"
"v_smfmac_f32_16x16x64_bf16 v[52:55], v[220:223], v[248:255], v59\n"
"s_cmp_lt_u32 s75, 0x4e000000\n"
"v_mul_f32 v42, s75, v57\n"
"v_fma_f32 v43, -s74, s74, v42\n"
"s_cselect_b32 s78, s78, 0x7f800000\n"
"v_rsq_f32 v47, v43\n"
"v_fmamk_f32 v42, v42, 0xbc800000, v43\n"
"v_cmp_nlt_f32 vcc, s78, v42\n"
"v_pk_add_f32 v[48:49], v[48:49], v[50:51]\n"
"v_pk_add_f32 v[52:53], v[52:53], v[54:55]\n"
"s_cbranch_vccnz LFB2Z\n"
"v_fma_f32 v44, -s74, v49, v48\n"
"v_fma_f32 v45, -s74, v53, v52\n"
"v_pk_mul_f32 v[44:45], v[44:45], v[46:47] op_sel:[0,1] op_sel_hi:[1,1]\n"
"LPOSTZ:\n"
"v_cvt_pk_bf16_f32 v62, v44, v45\n"
"buffer_store_dwordx2 v[62:63], v60, s[36:39], s71 offen sc1\n"
"s_cbranch_execz LCDRYR\n"
"s_cmp_eq_u32 s51, 0\n"
"s_cbranch_scc1 LC_P1\n"
"s_branch LC_P2\n"
"LFB2Y:\n"
"s_waitcnt lgkmcnt(0)\n"
"s_barrier\n"
"s_mov_b32 s78, -1.0\n"
"LFBY:\n"
"s_nop 7\n"
"s_mul_i32 s70, s51, 16384\n"
"s_lshl_b32 s56, s50, 9\n"
"s_add_u32 s70, s70, s56\n"
"s_add_u32 s70, s70, 0x101400\n"
"v_lshrrev_b32 v36, 5, v0\n"
"v_mul_u32_u24 v36, 32768, v36\n"
"v_and_b32 v37, 31, v0\n"
"v_lshl_add_u32 v36, v37, 4, v36\n"
"s_mov_b32 s72, 0xf\n"
"s_mov_b32 s55, 4194304\n"
"v_bfe_u32 v38, v0, 1, 4\n"
"v_and_b32 v39, 1, v0\n"
"v_lshrrev_b32 v40, 5, v0\n"
"v_lshl_add_u32 v39, v39, 1, v40\n"
"v_lshrrev_b32 v40, 3, v38\n"
"v_mul_u32_u24 v40, 576, v40\n"
"v_and_b32 v37, 7, v38\n"
"v_lshl_add_u32 v37, v37, 4, v40\n"
"v_and_b32 v40, 1, v39\n"
"v_mul_u32_u24 v40, 144, v40\n"
"v_add_u32 v37, v40, v37\n"
"v_lshrrev_b32 v40, 1, v39\n"
"v_lshl_add_u32 v37, v40, 2, v37\n"
"s_lshr_b32 s56, s50, 1\n"
"s_mul_i32 s56, s56, 272\n"
"v_add_u32 v37, s56, v37\n"
"s_lshl_b32 s56, s50, 3\n"
"v_add_u32 v37, s56, v37\n"
"v_add_u32 v38, 162560, v37\n"
"ds_read_b32 v39, v38\n"
"v_add_u32 v37, 159744, v37\n"
"v_lshrrev_b32 v41, 3, v0\n"
"v_lshl_add_u32 v41, v41, 4, v2\n"
"v_add_u32 v41, 159744, v41\n"
"s_waitcnt lgkmcnt(0)\n"
"v_lshlrev_b32 v38, 16, v39\n"
"v_and_b32 v39, 0xffff0000, v39\n"
"s_cbranch_execz La2a_532\n"
"s_sleep 7\n"
"La2a_532:\n"
"s_bitcmp1_b32 s72, 0\n"
"s_cbranch_scc0 Lskl_535\n"
"s_add_u32 s56, s70, 0x0\n"
"buffer_load_dwordx4 v[192:195], v36, s[36:39], s56 offen sc1\n"
"s_add_u32 s56, s70, 0x10000\n"
"buffer_load_dwordx4 v[196:199], v36, s[36:39], s56 offen sc1\n"
"s_add_u32 s56, s70, 0x20000\n"
"buffer_load_dwordx4 v[200:203], v36, s[36:39], s56 offen sc1\n"
"s_add_u32 s56, s70, 0x30000\n"
"buffer_load_dwordx4 v[204:207], v36, s[36:39], s56 offen sc1\n"
"Lskl_535:\n"
"s_bitcmp1_b32 s72, 1\n"
"s_cbranch_scc0 Lskl_536\n"
"s_add_u32 s56, s70, 0x40000\n"
"buffer_load_dwordx4 v[208:211], v36, s[36:39], s56 offen sc1\n"
"s_add_u32 s56, s70, 0x50000\n"
"buffer_load_dwordx4 v[212:215], v36, s[36:39], s56 offen sc1\n"
"s_add_u32 s56, s70, 0x60000\n"
"buffer_load_dwordx4 v[216:219], v36, s[36:39], s56 offen sc1\n"
"s_add_u32 s56, s70, 0x70000\n"
"buffer_load_dwordx4 v[220:223], v36, s[36:39], s56 offen sc1\n"
"Lskl_536:\n"
"s_bitcmp1_b32 s72, 2\n"
"s_cbranch_scc0 Lskl_537\n"
"s_add_u32 s56, s70, 0x80000\n"
"buffer_load_dwordx4 v[224:227], v36, s[36:39], s56 offen sc1\n"
"s_add_u32 s56, s70, 0x90000\n"
"buffer_load_dwordx4 v[228:231], v36, s[36:39], s56 offen sc1\n"
"s_add_u32 s56, s70, 0xa0000\n"
"buffer_load_dwordx4 v[232:235], v36, s[36:39], s56 offen sc1\n"
"s_add_u32 s56, s70, 0xb0000\n"
"buffer_load_dwordx4 v[236:239], v36, s[36:39], s56 offen sc1\n"
"Lskl_537:\n"
"s_bitcmp1_b32 s72, 3\n"
"s_cbranch_scc0 Lskl_538\n"
"s_add_u32 s56, s70, 0xc0000\n"
"buffer_load_dwordx4 v[240:243], v36, s[36:39], s56 offen sc1\n"
"s_add_u32 s56, s70, 0xd0000\n"
"buffer_load_dwordx4 v[244:247], v36, s[36:39], s56 offen sc1\n"
"s_add_u32 s56, s70, 0xe0000\n"
"buffer_load_dwordx4 v[248:251], v36, s[36:39], s56 offen sc1\n"
"s_add_u32 s56, s70, 0xf0000\n"
"buffer_load_dwordx4 v[252:255], v36, s[36:39], s56 offen sc1\n"
"Lskl_538:\n"
"La2ac_534:\n"
"s_waitcnt vmcnt(0)\n"
"s_bitcmp1_b32 s72, 0\n"
"s_cbranch_scc0 Lskc_539\n"
"v_pk_add_f32 v[44:45], v[192:193], v[196:197]\n"
"v_pk_add_f32 v[46:47], v[194:195], v[198:199]\n"
"v_pk_add_f32 v[44:45], v[44:45], v[200:201]\n"
"v_pk_add_f32 v[46:47], v[46:47], v[202:203]\n"
"v_pk_add_f32 v[44:45], v[44:45], v[204:205]\n"
"v_pk_add_f32 v[46:47], v[46:47], v[206:207]\n"
"v_cmp_u_f32 vcc, v44, v45\n"
"v_cmp_u_f32_e64 s[74:75], v46, v47\n"
"s_or_b64 vcc, vcc, s[74:75]\n"
"s_cbranch_scc0 Lokc_540\n"
"v_max3_u32 v42, v192, v193, v194\n"
"v_max3_u32 v43, v200, v201, v202\n"
"v_max3_u32 v42, v42, v195, v196\n"
"v_max3_u32 v43, v43, v203, v204\n"
"v_max3_u32 v42, v42, v197, v198\n"
"v_max3_u32 v43, v43, v205, v206\n"
"v_max_u32 v42, v42, v199\n"
"v_max_u32 v43, v43, v207\n"
"v_max_u32 v42, v42, v43\n"
"v_cmp_eq_u32 vcc, -1, v42\n"
"s_cbranch_vccnz Lskc_539\n"
"Lokc_540:\n"
"s_bitset0_b32 s72, 0\n"
"Lskc_539:\n"
"s_bitcmp1_b32 s72, 1\n"
"s_cbranch_scc0 Lskc_541\n"
"v_pk_add_f32 v[48:49], v[208:209], v[212:213]\n"
"v_pk_add_f32 v[50:51], v[210:211], v[214:215]\n"
"v_pk_add_f32 v[48:49], v[48:49], v[216:217]\n"
"v_pk_add_f32 v[50:51], v[50:51], v[218:219]\n"
"v_pk_add_f32 v[48:49], v[48:49], v[220:221]\n"
"v_pk_add_f32 v[50:51], v[50:51], v[222:223]\n"
"v_cmp_u_f32 vcc, v48, v49\n"
"v_cmp_u_f32_e64 s[74:75], v50, v51\n"
"s_or_b64 vcc, vcc, s[74:75]\n"
"s_cbranch_scc0 Lokc_542\n"
"v_max3_u32 v42, v208, v209, v210\n"
"v_max3_u32 v43, v216, v217, v218\n"
"v_max3_u32 v42, v42, v211, v212\n"
"v_max3_u32 v43, v43, v219, v220\n"
"v_max3_u32 v42, v42, v213, v214\n"
"v_max3_u32 v43, v43, v221, v222\n"
"v_max_u32 v42, v42, v215\n"
"v_max_u32 v43, v43, v223\n"
"v_max_u32 v42, v42, v43\n"
"v_cmp_eq_u32 vcc, -1, v42\n"
"s_cbranch_vccnz Lskc_541\n"
"Lokc_542:\n"
"s_bitset0_b32 s72, 1\n"
"Lskc_541:\n"
"s_bitcmp1_b32 s72, 2\n"
"s_cbranch_scc0 Lskc_543\n"
"v_pk_add_f32 v[52:53], v[224:225], v[228:229]\n"
"v_pk_add_f32 v[54:55], v[226:227], v[230:231]\n"
"v_pk_add_f32 v[52:53], v[52:53], v[232:233]\n"
"v_pk_add_f32 v[54:55], v[54:55], v[234:235]\n"
"v_pk_add_f32 v[52:53], v[52:53], v[236:237]\n"
"v_pk_add_f32 v[54:55], v[54:55], v[238:239]\n"
"v_cmp_u_f32 vcc, v52, v53\n"
"v_cmp_u_f32_e64 s[74:75], v54, v55\n"
"s_or_b64 vcc, vcc, s[74:75]\n"
"s_cbranch_scc0 Lokc_544\n"
"v_max3_u32 v42, v224, v225, v226\n"
"v_max3_u32 v43, v232, v233, v234\n"
"v_max3_u32 v42, v42, v227, v228\n"
"v_max3_u32 v43, v43, v235, v236\n"
"v_max3_u32 v42, v42, v229, v230\n"
"v_max3_u32 v43, v43, v237, v238\n"
"v_max_u32 v42, v42, v231\n"
"v_max_u32 v43, v43, v239\n"
"v_max_u32 v42, v42, v43\n"
"v_cmp_eq_u32 vcc, -1, v42\n"
"s_cbranch_vccnz Lskc_543\n"
"Lokc_544:\n"
"s_bitset0_b32 s72, 2\n"
"Lskc_543:\n"
"s_bitcmp1_b32 s72, 3\n"
"s_cbranch_scc0 Lskc_545\n"
"v_pk_add_f32 v[56:57], v[240:241], v[244:245]\n"
"v_pk_add_f32 v[58:59], v[242:243], v[246:247]\n"
"v_pk_add_f32 v[56:57], v[56:57], v[248:249]\n"
"v_pk_add_f32 v[58:59], v[58:59], v[250:251]\n"
"v_pk_add_f32 v[56:57], v[56:57], v[252:253]\n"
"v_pk_add_f32 v[58:59], v[58:59], v[254:255]\n"
"v_cmp_u_f32 vcc, v56, v57\n"
"v_cmp_u_f32_e64 s[74:75], v58, v59\n"
"s_or_b64 vcc, vcc, s[74:75]\n"
"s_cbranch_scc0 Lokc_546\n"
"v_max3_u32 v42, v240, v241, v242\n"
"v_max3_u32 v43, v248, v249, v250\n"
"v_max3_u32 v42, v42, v243, v244\n"
"v_max3_u32 v43, v43, v251, v252\n"
"v_max3_u32 v42, v42, v245, v246\n"
"v_max3_u32 v43, v43, v253, v254\n"
"v_max_u32 v42, v42, v247\n"
"v_max_u32 v43, v43, v255\n"
"v_max_u32 v42, v42, v43\n"
"v_cmp_eq_u32 vcc, -1, v42\n"
"s_cbranch_vccnz Lskc_545\n"
"Lokc_546:\n"
"s_bitset0_b32 s72, 3\n"
"Lskc_545:\n"
"s_cmp_eq_u32 s72, 0\n"
"s_cbranch_scc1 La2ad_533\n"
"s_sub_u32 s55, s55, 1\n"
"s_cbranch_scc0 La2a_532\n"
"La2ad_533:\n"
"v_pk_add_f32 v[44:45], v[44:45], v[48:49]\n"
"v_pk_add_f32 v[46:47], v[46:47], v[50:51]\n"
"v_pk_add_f32 v[52:53], v[52:53], v[56:57]\n"
"v_pk_add_f32 v[54:55], v[54:55], v[58:59]\n"
"v_pk_add_f32 v[44:45], v[44:45], v[52:53]\n"
"v_pk_add_f32 v[46:47], v[46:47], v[54:55]\n"
"s_nop 1\n"
"v_permlane32_swap_b32 v44, v46\n"
"v_permlane32_swap_b32 v45, v47\n"
"v_add_f32 v44, v44, v46\n"
"v_add_f32 v46, v45, v47\n"
"v_cvt_pk_bf16_f32 v44, v44, v46\n"
"v_lshlrev_b32 v46, 16, v44\n"
"v_and_b32 v47, 0xffff0000, v44\n"
"v_pk_add_f32 v[46:47], v[46:47], v[38:39]\n"
"v_cvt_pk_bf16_f32 v44, v46, v47\n"
"ds_write_b32 v37, v44\n"
"s_waitcnt lgkmcnt(0)\n"
"s_barrier\n"
"ds_read_b128 v[36:39], v41\n"
"s_waitcnt lgkmcnt(0)\n"
"v_mov_b32 v224, 0\n"
"v_mov_b32 v226, 0\n"
"v_mov_b32 v228, 0\n"
"v_mov_b32 v230, 0\n"
"v_mov_b32 v232, 0\n"
"v_mov_b32 v234, 0\n"
"v_mov_b32 v236, 0\n"
"v_mov_b32 v238, 0\n"
"v_mov_b32 v240, 0\n"
"v_mov_b32 v242, 0\n"
"v_mov_b32 v244, 0\n"
"v_mov_b32 v246, 0\n"
"v_mov_b32 v248, 0\n"
"v_mov_b32 v250, 0\n"
"v_mov_b32 v252, 0\n"
"v_mov_b32 v254, 0\n"
"s_cmp_eq_u32 s78, -1.0\n"
"s_cbranch_scc1 LSLOWY\n"
"v_lshrrev_b32 v56, 4, v0\n"
"v_lshlrev_b32 v56, 2, v56\n"
"v_and_b32 v57, 2, v0\n"
"v_add_u32 v56, v57, v56\n"
"v_mul_u32_u24 v56, 72, v56\n"
"v_add_u32 v56, 159744, v56\n"
"v_mov_b32 v57, 163712\n"
"s_or_b64 vcc, s[62:63], s[66:67]\n"
"v_cndmask_b32 v56, v56, v57, vcc\n"
"v_mov_b32 v57, 0x3b000000\n"
"s_mov_b32 s79, 0x3b003b00\n"
"v_mov_b32 v58, 0x38a7c5ac\n"
"v_bfe_u32 v59, v0, 1, 1\n"
"v_mul_u32_u24 v59, 0xaaaa, v59\n"
"v_add_u32 v59, 0x4444, v59\n"
"v_mov_b32 v48, 0\n"
"v_mov_b32 v49, 0\n"
"v_mov_b32 v50, 0\n"
"v_mov_b32 v51, 0\n"
"v_mov_b32 v52, 0\n"
"v_mov_b32 v53, 0\n"
"v_mov_b32 v54, 0\n"
"v_mov_b32 v55, 0\n"
"s_nop 1\n"
"s_branch LFBRY\n"
"LSLOWY:\n"
"s_nop 7\n"
"v_lshlrev_b32 v40, 16, v36\n"
"v_and_b32 v41, 0xffff0000, v36\n"
"v_lshlrev_b32 v42, 16, v37\n"
"v_and_b32 v43, 0xffff0000, v37\n"
"v_readfirstlane_b32 s72, v40\n"
"v_lshlrev_b32 v44, 16, v38\n"
"v_and_b32 v45, 0xffff0000, v38\n"
"v_lshlrev_b32 v46, 16, v39\n"
"v_and_b32 v47, 0xffff0000, v39\n"
"v_pk_add_f32 v[48:49], v[40:41], s[72:73] op_sel_hi:[1,0] neg_lo:[0,1] neg_hi:[0,1]\n"
"v_pk_add_f32 v[50:51], v[42:43], s[72:73] op_sel_hi:[1,0] neg_lo:[0,1] neg_hi:[0,1]\n"
"v_pk_add_f32 v[52:53], v[44:45], s[72:73] op_sel_hi:[1,0] neg_lo:[0,1] neg_hi:[0,1]\n"
"v_pk_add_f32 v[54:55], v[46:47], s[72:73] op_sel_hi:[1,0] neg_lo:[0,1] neg_hi:[0,1]\n"
"v_pk_mul_f32 v[36:37], v[48:49], v[48:49]\n"
"v_pk_add_f32 v[38:39], v[48:49], v[50:51]\n"
"v_pk_fma_f32 v[36:37], v[50:51], v[50:51], v[36:37]\n"
"v_pk_add_f32 v[38:39], v[38:39], v[52:53]\n"
"v_pk_fma_f32 v[36:37], v[52:53], v[52:53], v[36:37]\n"
"v_pk_add_f32 v[38:39], v[38:39], v[54:55]\n"
"v_pk_fma_f32 v[36:37], v[54:55], v[54:55], v[36:37]\n"
"v_add_f32 v38, v38, v39\n"
"v_add_f32 v36, v36, v37\n"
"v_mov_b32 v49, s72\n"
"s_nop 0\n"
"v_permlane32_swap_b32 v38, v36\n"
"v_add_f32 v48, v38, v36\n"
"s_nop 1\n"
"v_add_f32_dpp v48, v48, v48 quad_perm:[1,0,3,2] row_mask:0xf bank_mask:0xf\n"
"s_nop 1\n"
"v_add_f32_dpp v48, v48, v48 quad_perm:[2,3,0,1] row_mask:0xf bank_mask:0xf\n"
"s_nop 1\n"
"v_add_f32_dpp v48, v48, v48 row_half_mirror row_mask:0xf bank_mask:0xf\n"
"s_nop 1\n"
"v_add_f32_dpp v48, v48, v48 row_mirror row_mask:0xf bank_mask:0xf\n"
"s_nop 1\n"
"v_add_f32_dpp v48, v48, v48 row_bcast:15 row_mask:0xa bank_mask:0xf\n"
"v_mul_f32 v48, 0x3b000000, v48\n"
"s_nop 0\n"
"v_readlane_b32 s74, v48, 31\n"
"v_readlane_b32 s75, v48, 63\n"
"s_nop 0\n"
"v_add_f32 v49, s74, v49\n"
"v_mov_b32 v50, s75\n"
"v_fma_f32 v50, -s74, s74, v50\n"
"v_max_f32 v50, 0, v50\n"
"v_cvt_pk_bf16_f32 v51, v49, v50\n"
"v_lshlrev_b32 v52, 16, v51\n"
"v_and_b32 v50, 0xffff0000, v51\n"
"v_add_f32 v50, 0x3727c5ac, v50\n"
"v_pk_add_f32 v[40:41], v[40:41], v[52:53] op_sel_hi:[1,0] neg_lo:[0,1] neg_hi:[0,1]\n"
"v_cvt_pk_bf16_f32 v50, v50, v50\n"
"v_pk_add_f32 v[42:43], v[42:43], v[52:53] op_sel_hi:[1,0] neg_lo:[0,1] neg_hi:[0,1]\n"
"v_lshlrev_b32 v50, 16, v50\n"
"v_pk_add_f32 v[44:45], v[44:45], v[52:53] op_sel_hi:[1,0] neg_lo:[0,1] neg_hi:[0,1]\n"
"v_sqrt_f32 v50, v50\n"
"v_pk_add_f32 v[46:47], v[46:47], v[52:53] op_sel_hi:[1,0] neg_lo:[0,1] neg_hi:[0,1]\n"
"v_cvt_pk_bf16_f32 v36, v40, v41\n"
"v_cvt_pk_bf16_f32 v50, v50, v50\n"
"v_cvt_pk_bf16_f32 v37, v42, v43\n"
"v_lshlrev_b32 v50, 16, v50\n"
"v_cvt_pk_bf16_f32 v38, v44, v45\n"
"v_rcp_f32 v50, v50\n"
"v_cvt_pk_bf16_f32 v39, v46, v47\n"
"v_lshlrev_b32 v40, 16, v36\n"
"v_and_b32 v41, 0xffff0000, v36\n"
"v_lshlrev_b32 v42, 16, v37\n"
"v_and_b32 v43, 0xffff0000, v37\n"
"v_lshlrev_b32 v44, 16, v38\n"
"v_and_b32 v45, 0xffff0000, v38\n"
"v_lshlrev_b32 v46, 16, v39\n"
"v_and_b32 v47, 0xffff0000, v39\n"
"v_pk_mul_f32 v[40:41], v[40:41], v[50:51] op_sel_hi:[1,0]\n"
"v_pk_mul_f32 v[42:43], v[42:43], v[50:51] op_sel_hi:[1,0]\n"
"v_pk_mul_f32 v[44:45], v[44:45], v[50:51] op_sel_hi:[1,0]\n"
"v_pk_mul_f32 v[46:47], v[46:47], v[50:51] op_sel_hi:[1,0]\n"
"v_cvt_pk_bf16_f32 v36, v40, v41\n"
"v_cvt_pk_bf16_f32 v37, v42, v43\n"
"v_cvt_pk_bf16_f32 v38, v44, v45\n"
"v_cvt_pk_bf16_f32 v39, v46, v47\n"
"v_lshrrev_b32 v56, 3, v0\n"
"v_lshl_add_u32 v56, v56, 4, v2\n"
"v_add_u32 v56, 161024, v56\n"
"ds_write_b128 v56, v[36:39]\n"
"v_lshrrev_b32 v56, 4, v0\n"
"v_lshlrev_b32 v56, 2, v56\n"
"v_and_b32 v57, 2, v0\n"
"v_add_u32 v56, v57, v56\n"
"v_mul_u32_u24 v56, 72, v56\n"
"v_add_u32 v56, 161024, v56\n"
"v_bfe_u32 v59, v0, 1, 1\n"
"v_mul_u32_u24 v59, 0xaaaa, v59\n"
"v_add_u32 v59, 0x4444, v59\n"
"v_mov_b32 v48, 0\n"
"v_mov_b32 v49, 0\n"
"v_mov_b32 v50, 0\n"
"v_mov_b32 v51, 0\n"
"v_mov_b32 v52, 0\n"
"v_mov_b32 v53, 0\n"
"v_mov_b32 v54, 0\n"
"v_mov_b32 v55, 0\n"
"s_waitcnt lgkmcnt(0)\n"
"ds_read_b128 v[192:195], v56 offset:0\n"
"ds_read_b128 v[196:199], v56 offset:16\n"
"ds_read_b128 v[200:203], v56 offset:32\n"
"ds_read_b128 v[204:207], v56 offset:48\n"
"ds_read_b128 v[208:211], v56 offset:64\n"
"ds_read_b128 v[212:215], v56 offset:80\n"
"ds_read_b128 v[216:219], v56 offset:96\n"
"ds_read_b128 v[220:223], v56 offset:112\n"
"s_waitcnt lgkmcnt(0)\n"
"v_smfmac_f32_16x16x64_bf16 v[48:51], v[192:195], a[128:135], v59\n"
"v_smfmac_f32_16x16x64_bf16 v[52:55], v[192:195], a[192:199], v59\n"
"v_smfmac_f32_16x16x64_bf16 v[48:51], v[196:199], a[136:143], v59\n"
"v_smfmac_f32_16x16x64_bf16 v[52:55], v[196:199], a[200:207], v59\n"
"v_smfmac_f32_16x16x64_bf16 v[48:51], v[200:203], a[144:151], v59\n"
"v_smfmac_f32_16x16x64_bf16 v[52:55], v[200:203], a[208:215], v59\n"
"v_smfmac_f32_16x16x64_bf16 v[48:51], v[204:207], a[152:159], v59\n"
"v_smfmac_f32_16x16x64_bf16 v[52:55], v[204:207], a[216:223], v59\n"
"v_smfmac_f32_16x16x64_bf16 v[48:51], v[208:211], a[160:167], v59\n"
"v_smfmac_f32_16x16x64_bf16 v[52:55], v[208:211], a[224:231], v59\n"
"v_smfmac_f32_16x16x64_bf16 v[48:51], v[212:215], a[168:175], v59\n"
"v_smfmac_f32_16x16x64_bf16 v[52:55], v[212:215], a[232:239], v59\n"
"v_smfmac_f32_16x16x64_bf16 v[48:51], v[216:219], a[176:183], v59\n"
"v_smfmac_f32_16x16x64_bf16 v[52:55], v[216:219], a[240:247], v59\n"
"v_smfmac_f32_16x16x64_bf16 v[48:51], v[220:223], a[184:191], v59\n"
"v_smfmac_f32_16x16x64_bf16 v[52:55], v[220:223], a[248:255], v59\n"
"s_nop 6\n"
"v_add_f32 v44, v48, v50\n"
"v_add_f32 v45, v52, v54\n"
"v_mul_f32 v44, 0xbfb8aa3b, v44\n"
"s_branch LPOSTY\n"
"LFB2Z:\n"
"s_waitcnt lgkmcnt(0)\n"
"s_barrier\n"
"s_mov_b32 s78, -1.0\n"
"s_mov_b32 s51, 0\n"
"LFBZ:\n"
"s_nop 7\n"
"s_mul_i32 s70, s51, 16384\n"
"s_lshl_b32 s56, s50, 9\n"
"s_add_u32 s70, s70, s56\n"
"s_add_u32 s70, s70, 0x103400\n"
"v_lshrrev_b32 v36, 5, v0\n"
"v_mul_u32_u24 v36, 32768, v36\n"
"v_and_b32 v37, 31, v0\n"
"v_lshl_add_u32 v36, v37, 4, v36\n"
"s_mov_b32 s72, 0xf\n"
"s_mov_b32 s55, 4194304\n"
"v_bfe_u32 v38, v0, 1, 4\n"
"v_and_b32 v39, 1, v0\n"
"v_lshrrev_b32 v40, 5, v0\n"
"v_lshl_add_u32 v39, v39, 1, v40\n"
"v_lshrrev_b32 v40, 3, v38\n"
"v_mul_u32_u24 v40, 576, v40\n"
"v_and_b32 v37, 7, v38\n"
"v_lshl_add_u32 v37, v37, 4, v40\n"
"v_and_b32 v40, 1, v39\n"
"v_mul_u32_u24 v40, 144, v40\n"
"v_add_u32 v37, v40, v37\n"
"v_lshrrev_b32 v40, 1, v39\n"
"v_lshl_add_u32 v37, v40, 2, v37\n"
"s_lshr_b32 s56, s50, 1\n"
"s_mul_i32 s56, s56, 272\n"
"v_add_u32 v37, s56, v37\n"
"s_lshl_b32 s56, s50, 3\n"
"v_add_u32 v37, s56, v37\n"
"v_add_u32 v38, 159744, v37\n"
"ds_read_b32 v39, v38\n"
"v_add_u32 v37, 162560, v37\n"
"v_lshrrev_b32 v41, 3, v0\n"
"v_lshl_add_u32 v41, v41, 4, v2\n"
"v_add_u32 v41, 162560, v41\n"
"s_waitcnt lgkmcnt(0)\n"
"v_lshlrev_b32 v38, 16, v39\n"
"v_and_b32 v39, 0xffff0000, v39\n"
"s_cbranch_execz La2a_564\n"
"s_sleep 4\n"
"La2a_564:\n"
"s_bitcmp1_b32 s72, 0\n"
"s_cbranch_scc0 Lskl_567\n"
"s_add_u32 s56, s70, 0x0\n"
"buffer_load_dwordx4 v[192:195], v36, s[36:39], s56 offen sc1\n"
"s_add_u32 s56, s70, 0x10000\n"
"buffer_load_dwordx4 v[196:199], v36, s[36:39], s56 offen sc1\n"
"s_add_u32 s56, s70, 0x20000\n"
"buffer_load_dwordx4 v[200:203], v36, s[36:39], s56 offen sc1\n"
"s_add_u32 s56, s70, 0x30000\n"
"buffer_load_dwordx4 v[204:207], v36, s[36:39], s56 offen sc1\n"
"Lskl_567:\n"
"s_bitcmp1_b32 s72, 1\n"
"s_cbranch_scc0 Lskl_568\n"
"s_add_u32 s56, s70, 0x40000\n"
"buffer_load_dwordx4 v[208:211], v36, s[36:39], s56 offen sc1\n"
"s_add_u32 s56, s70, 0x50000\n"
"buffer_load_dwordx4 v[212:215], v36, s[36:39], s56 offen sc1\n"
"s_add_u32 s56, s70, 0x60000\n"
"buffer_load_dwordx4 v[216:219], v36, s[36:39], s56 offen sc1\n"
"s_add_u32 s56, s70, 0x70000\n"
"buffer_load_dwordx4 v[220:223], v36, s[36:39], s56 offen sc1\n"
"Lskl_568:\n"
"s_bitcmp1_b32 s72, 2\n"
"s_cbranch_scc0 Lskl_569\n"
"s_add_u32 s56, s70, 0x80000\n"
"buffer_load_dwordx4 v[224:227], v36, s[36:39], s56 offen sc1\n"
"s_add_u32 s56, s70, 0x90000\n"
"buffer_load_dwordx4 v[228:231], v36, s[36:39], s56 offen sc1\n"
"s_add_u32 s56, s70, 0xa0000\n"
"buffer_load_dwordx4 v[232:235], v36, s[36:39], s56 offen sc1\n"
"s_add_u32 s56, s70, 0xb0000\n"
"buffer_load_dwordx4 v[236:239], v36, s[36:39], s56 offen sc1\n"
"Lskl_569:\n"
"s_bitcmp1_b32 s72, 3\n"
"s_cbranch_scc0 Lskl_570\n"
"s_add_u32 s56, s70, 0xc0000\n"
"buffer_load_dwordx4 v[240:243], v36, s[36:39], s56 offen sc1\n"
"s_add_u32 s56, s70, 0xd0000\n"
"buffer_load_dwordx4 v[244:247], v36, s[36:39], s56 offen sc1\n"
"s_add_u32 s56, s70, 0xe0000\n"
"buffer_load_dwordx4 v[248:251], v36, s[36:39], s56 offen sc1\n"
"s_add_u32 s56, s70, 0xf0000\n"
"buffer_load_dwordx4 v[252:255], v36, s[36:39], s56 offen sc1\n"
"Lskl_570:\n"
"La2ac_566:\n"
"s_waitcnt vmcnt(0)\n"
"s_bitcmp1_b32 s72, 0\n"
"s_cbranch_scc0 Lskc_571\n"
"v_pk_add_f32 v[44:45], v[192:193], v[196:197]\n"
"v_pk_add_f32 v[46:47], v[194:195], v[198:199]\n"
"v_pk_add_f32 v[44:45], v[44:45], v[200:201]\n"
"v_pk_add_f32 v[46:47], v[46:47], v[202:203]\n"
"v_pk_add_f32 v[44:45], v[44:45], v[204:205]\n"
"v_pk_add_f32 v[46:47], v[46:47], v[206:207]\n"
"v_cmp_u_f32 vcc, v44, v45\n"
"v_cmp_u_f32_e64 s[74:75], v46, v47\n"
"s_or_b64 vcc, vcc, s[74:75]\n"
"s_cbranch_scc0 Lokc_572\n"
"v_max3_u32 v42, v192, v193, v194\n"
"v_max3_u32 v43, v200, v201, v202\n"
"v_max3_u32 v42, v42, v195, v196\n"
"v_max3_u32 v43, v43, v203, v204\n"
"v_max3_u32 v42, v42, v197, v198\n"
"v_max3_u32 v43, v43, v205, v206\n"
"v_max_u32 v42, v42, v199\n"
"v_max_u32 v43, v43, v207\n"
"v_max_u32 v42, v42, v43\n"
"v_cmp_eq_u32 vcc, -1, v42\n"
"s_cbranch_vccnz Lskc_571\n"
"Lokc_572:\n"
"s_bitset0_b32 s72, 0\n"
"Lskc_571:\n"
"s_bitcmp1_b32 s72, 1\n"
"s_cbranch_scc0 Lskc_573\n"
"v_pk_add_f32 v[48:49], v[208:209], v[212:213]\n"
"v_pk_add_f32 v[50:51], v[210:211], v[214:215]\n"
"v_pk_add_f32 v[48:49], v[48:49], v[216:217]\n"
"v_pk_add_f32 v[50:51], v[50:51], v[218:219]\n"
"v_pk_add_f32 v[48:49], v[48:49], v[220:221]\n"
"v_pk_add_f32 v[50:51], v[50:51], v[222:223]\n"
"v_cmp_u_f32 vcc, v48, v49\n"
"v_cmp_u_f32_e64 s[74:75], v50, v51\n"
"s_or_b64 vcc, vcc, s[74:75]\n"
"s_cbranch_scc0 Lokc_574\n"
"v_max3_u32 v42, v208, v209, v210\n"
"v_max3_u32 v43, v216, v217, v218\n"
"v_max3_u32 v42, v42, v211, v212\n"
"v_max3_u32 v43, v43, v219, v220\n"
"v_max3_u32 v42, v42, v213, v214\n"
"v_max3_u32 v43, v43, v221, v222\n"
"v_max_u32 v42, v42, v215\n"
"v_max_u32 v43, v43, v223\n"
"v_max_u32 v42, v42, v43\n"
"v_cmp_eq_u32 vcc, -1, v42\n"
"s_cbranch_vccnz Lskc_573\n"
"Lokc_574:\n"
"s_bitset0_b32 s72, 1\n"
"Lskc_573:\n"
"s_bitcmp1_b32 s72, 2\n"
"s_cbranch_scc0 Lskc_575\n"
"v_pk_add_f32 v[52:53], v[224:225], v[228:229]\n"
"v_pk_add_f32 v[54:55], v[226:227], v[230:231]\n"
"v_pk_add_f32 v[52:53], v[52:53], v[232:233]\n"
"v_pk_add_f32 v[54:55], v[54:55], v[234:235]\n"
"v_pk_add_f32 v[52:53], v[52:53], v[236:237]\n"
"v_pk_add_f32 v[54:55], v[54:55], v[238:239]\n"
"v_cmp_u_f32 vcc, v52, v53\n"
"v_cmp_u_f32_e64 s[74:75], v54, v55\n"
"s_or_b64 vcc, vcc, s[74:75]\n"
"s_cbranch_scc0 Lokc_576\n"
"v_max3_u32 v42, v224, v225, v226\n"
"v_max3_u32 v43, v232, v233, v234\n"
"v_max3_u32 v42, v42, v227, v228\n"
"v_max3_u32 v43, v43, v235, v236\n"
"v_max3_u32 v42, v42, v229, v230\n"
"v_max3_u32 v43, v43, v237, v238\n"
"v_max_u32 v42, v42, v231\n"
"v_max_u32 v43, v43, v239\n"
"v_max_u32 v42, v42, v43\n"
"v_cmp_eq_u32 vcc, -1, v42\n"
"s_cbranch_vccnz Lskc_575\n"
"Lokc_576:\n"
"s_bitset0_b32 s72, 2\n"
"Lskc_575:\n"
"s_bitcmp1_b32 s72, 3\n"
"s_cbranch_scc0 Lskc_577\n"
"v_pk_add_f32 v[56:57], v[240:241], v[244:245]\n"
"v_pk_add_f32 v[58:59], v[242:243], v[246:247]\n"
"v_pk_add_f32 v[56:57], v[56:57], v[248:249]\n"
"v_pk_add_f32 v[58:59], v[58:59], v[250:251]\n"
"v_pk_add_f32 v[56:57], v[56:57], v[252:253]\n"
"v_pk_add_f32 v[58:59], v[58:59], v[254:255]\n"
"v_cmp_u_f32 vcc, v56, v57\n"
"v_cmp_u_f32_e64 s[74:75], v58, v59\n"
"s_or_b64 vcc, vcc, s[74:75]\n"
"s_cbranch_scc0 Lokc_578\n"
"v_max3_u32 v42, v240, v241, v242\n"
"v_max3_u32 v43, v248, v249, v250\n"
"v_max3_u32 v42, v42, v243, v244\n"
"v_max3_u32 v43, v43, v251, v252\n"
"v_max3_u32 v42, v42, v245, v246\n"
"v_max3_u32 v43, v43, v253, v254\n"
"v_max_u32 v42, v42, v247\n"
"v_max_u32 v43, v43, v255\n"
"v_max_u32 v42, v42, v43\n"
"v_cmp_eq_u32 vcc, -1, v42\n"
"s_cbranch_vccnz Lskc_577\n"
"Lokc_578:\n"
"s_bitset0_b32 s72, 3\n"
"Lskc_577:\n"
"s_cmp_eq_u32 s72, 0\n"
"s_cbranch_scc1 La2ad_565\n"
"s_sub_u32 s55, s55, 1\n"
"s_cbranch_scc0 La2a_564\n"
"La2ad_565:\n"
"v_pk_add_f32 v[44:45], v[44:45], v[48:49]\n"
"v_pk_add_f32 v[46:47], v[46:47], v[50:51]\n"
"v_pk_add_f32 v[52:53], v[52:53], v[56:57]\n"
"v_pk_add_f32 v[54:55], v[54:55], v[58:59]\n"
"v_pk_add_f32 v[44:45], v[44:45], v[52:53]\n"
"v_pk_add_f32 v[46:47], v[46:47], v[54:55]\n"
"s_nop 1\n"
"v_permlane32_swap_b32 v44, v46\n"
"v_permlane32_swap_b32 v45, v47\n"
"v_add_f32 v44, v44, v46\n"
"v_add_f32 v46, v45, v47\n"
"v_cvt_pk_bf16_f32 v44, v44, v46\n"
"v_lshlrev_b32 v46, 16, v44\n"
"v_and_b32 v47, 0xffff0000, v44\n"
"v_pk_add_f32 v[46:47], v[46:47], v[38:39]\n"
"v_cvt_pk_bf16_f32 v44, v46, v47\n"
"ds_write_b32 v37, v44\n"
"s_waitcnt lgkmcnt(0)\n"
"s_barrier\n"
"ds_read_b128 v[36:39], v41\n"
"s_waitcnt lgkmcnt(0)\n"
"ds_read_b128 v[224:227], v61 offset:8192\n"
"ds_read_b128 v[228:231], v61 offset:9216\n"
"ds_read_b128 v[232:235], v61 offset:10240\n"
"ds_read_b128 v[236:239], v61 offset:11264\n"
"ds_read_b128 v[240:243], v61 offset:12288\n"
"ds_read_b128 v[244:247], v61 offset:13312\n"
"ds_read_b128 v[248:251], v61 offset:14336\n"
"ds_read_b128 v[252:255], v61 offset:15360\n"
"s_waitcnt lgkmcnt(0)\n"
"s_mov_b32 s51, 1\n"
"s_cmp_eq_u32 s78, -1.0\n"
"s_cbranch_scc1 LSLOWZ\n"
"v_lshrrev_b32 v56, 4, v0\n"
"v_lshlrev_b32 v56, 2, v56\n"
"v_and_b32 v57, 2, v0\n"
"v_add_u32 v56, v57, v56\n"
"v_mul_u32_u24 v56, 72, v56\n"
"v_add_u32 v56, 162560, v56\n"
"v_mov_b32 v57, 163712\n"
"s_or_b64 vcc, s[62:63], s[66:67]\n"
"v_cndmask_b32 v56, v56, v57, vcc\n"
"v_mov_b32 v57, 0x3b000000\n"
"s_mov_b32 s79, 0x3b003b00\n"
"v_mov_b32 v58, 0x38a7c5ac\n"
"v_bfe_u32 v59, v0, 1, 1\n"
"v_mul_u32_u24 v59, 0xaaaa, v59\n"
"v_add_u32 v59, 0x4444, v59\n"
"v_mov_b32 v48, 0\n"
"v_mov_b32 v49, 0\n"
"v_mov_b32 v50, 0\n"
"v_mov_b32 v51, 0\n"
"v_mov_b32 v52, 0\n"
"v_mov_b32 v53, 0\n"
"v_mov_b32 v54, 0\n"
"v_mov_b32 v55, 0\n"
"s_nop 1\n"
"s_branch LFBRZ\n"
"LSLOWZ:\n"
"s_nop 7\n"
"v_lshlrev_b32 v40, 16, v36\n"
"v_and_b32 v41, 0xffff0000, v36\n"
"v_lshlrev_b32 v42, 16, v37\n"
"v_and_b32 v43, 0xffff0000, v37\n"
"v_readfirstlane_b32 s72, v40\n"
"v_lshlrev_b32 v44, 16, v38\n"
"v_and_b32 v45, 0xffff0000, v38\n"
"v_lshlrev_b32 v46, 16, v39\n"
"v_and_b32 v47, 0xffff0000, v39\n"
"v_pk_add_f32 v[48:49], v[40:41], s[72:73] op_sel_hi:[1,0] neg_lo:[0,1] neg_hi:[0,1]\n"
"v_pk_add_f32 v[50:51], v[42:43], s[72:73] op_sel_hi:[1,0] neg_lo:[0,1] neg_hi:[0,1]\n"
"v_pk_add_f32 v[52:53], v[44:45], s[72:73] op_sel_hi:[1,0] neg_lo:[0,1] neg_hi:[0,1]\n"
"v_pk_add_f32 v[54:55], v[46:47], s[72:73] op_sel_hi:[1,0] neg_lo:[0,1] neg_hi:[0,1]\n"
"v_pk_mul_f32 v[36:37], v[48:49], v[48:49]\n"
"v_pk_add_f32 v[38:39], v[48:49], v[50:51]\n"
"v_pk_fma_f32 v[36:37], v[50:51], v[50:51], v[36:37]\n"
"v_pk_add_f32 v[38:39], v[38:39], v[52:53]\n"
"v_pk_fma_f32 v[36:37], v[52:53], v[52:53], v[36:37]\n"
"v_pk_add_f32 v[38:39], v[38:39], v[54:55]\n"
"v_pk_fma_f32 v[36:37], v[54:55], v[54:55], v[36:37]\n"
"v_add_f32 v38, v38, v39\n"
"v_add_f32 v36, v36, v37\n"
"v_mov_b32 v49, s72\n"
"s_nop 0\n"
"v_permlane32_swap_b32 v38, v36\n"
"v_add_f32 v48, v38, v36\n"
"s_nop 1\n"
"v_add_f32_dpp v48, v48, v48 quad_perm:[1,0,3,2] row_mask:0xf bank_mask:0xf\n"
"s_nop 1\n"
"v_add_f32_dpp v48, v48, v48 quad_perm:[2,3,0,1] row_mask:0xf bank_mask:0xf\n"
"s_nop 1\n"
"v_add_f32_dpp v48, v48, v48 row_half_mirror row_mask:0xf bank_mask:0xf\n"
"s_nop 1\n"
"v_add_f32_dpp v48, v48, v48 row_mirror row_mask:0xf bank_mask:0xf\n"
"s_nop 1\n"
"v_add_f32_dpp v48, v48, v48 row_bcast:15 row_mask:0xa bank_mask:0xf\n"
"v_mul_f32 v48, 0x3b000000, v48\n"
"s_nop 0\n"
"v_readlane_b32 s74, v48, 31\n"
"v_readlane_b32 s75, v48, 63\n"
"s_nop 0\n"
"v_add_f32 v49, s74, v49\n"
"v_mov_b32 v50, s75\n"
"v_fma_f32 v50, -s74, s74, v50\n"
"v_max_f32 v50, 0, v50\n"
"v_cvt_pk_bf16_f32 v51, v49, v50\n"
"v_lshlrev_b32 v52, 16, v51\n"
"v_and_b32 v50, 0xffff0000, v51\n"
"v_add_f32 v50, 0x3727c5ac, v50\n"
"v_pk_add_f32 v[40:41], v[40:41], v[52:53] op_sel_hi:[1,0] neg_lo:[0,1] neg_hi:[0,1]\n"
"v_cvt_pk_bf16_f32 v50, v50, v50\n"
"v_pk_add_f32 v[42:43], v[42:43], v[52:53] op_sel_hi:[1,0] neg_lo:[0,1] neg_hi:[0,1]\n"
"v_lshlrev_b32 v50, 16, v50\n"
"v_pk_add_f32 v[44:45], v[44:45], v[52:53] op_sel_hi:[1,0] neg_lo:[0,1] neg_hi:[0,1]\n"
"v_sqrt_f32 v50, v50\n"
"v_pk_add_f32 v[46:47], v[46:47], v[52:53] op_sel_hi:[1,0] neg_lo:[0,1] neg_hi:[0,1]\n"
"v_cvt_pk_bf16_f32 v36, v40, v41\n"
"v_cvt_pk_bf16_f32 v50, v50, v50\n"
"v_cvt_pk_bf16_f32 v37, v42, v43\n"
"v_lshlrev_b32 v50, 16, v50\n"
"v_cvt_pk_bf16_f32 v38, v44, v45\n"
"v_rcp_f32 v50, v50\n"
"v_cvt_pk_bf16_f32 v39, v46, v47\n"
"v_lshlrev_b32 v40, 16, v36\n"
"v_and_b32 v41, 0xffff0000, v36\n"
"v_lshlrev_b32 v42, 16, v37\n"
"v_and_b32 v43, 0xffff0000, v37\n"
"v_lshlrev_b32 v44, 16, v38\n"
"v_and_b32 v45, 0xffff0000, v38\n"
"v_lshlrev_b32 v46, 16, v39\n"
"v_and_b32 v47, 0xffff0000, v39\n"
"v_pk_mul_f32 v[40:41], v[40:41], v[50:51] op_sel_hi:[1,0]\n"
"v_pk_mul_f32 v[42:43], v[42:43], v[50:51] op_sel_hi:[1,0]\n"
"v_pk_mul_f32 v[44:45], v[44:45], v[50:51] op_sel_hi:[1,0]\n"
"v_pk_mul_f32 v[46:47], v[46:47], v[50:51] op_sel_hi:[1,0]\n"
"v_cvt_pk_bf16_f32 v36, v40, v41\n"
"v_cvt_pk_bf16_f32 v37, v42, v43\n"
"v_cvt_pk_bf16_f32 v38, v44, v45\n"
"v_cvt_pk_bf16_f32 v39, v46, v47\n"
"v_lshrrev_b32 v56, 3, v0\n"
"v_lshl_add_u32 v56, v56, 4, v2\n"
"v_add_u32 v56, 161024, v56\n"
"ds_write_b128 v56, v[36:39]\n"
"v_lshrrev_b32 v56, 4, v0\n"
"v_lshlrev_b32 v56, 2, v56\n"
"v_and_b32 v57, 2, v0\n"
"v_add_u32 v56, v57, v56\n"
"v_mul_u32_u24 v56, 72, v56\n"
"v_add_u32 v56, 161024, v56\n"
"v_bfe_u32 v59, v0, 1, 1\n"
"v_mul_u32_u24 v59, 0xaaaa, v59\n"
"v_add_u32 v59, 0x4444, v59\n"
"v_mov_b32 v48, 0\n"
"v_mov_b32 v49, 0\n"
"v_mov_b32 v50, 0\n"
"v_mov_b32 v51, 0\n"
"v_mov_b32 v52, 0\n"
"v_mov_b32 v53, 0\n"
"v_mov_b32 v54, 0\n"
"v_mov_b32 v55, 0\n"
"s_waitcnt lgkmcnt(0)\n"
"ds_read_b128 v[192:195], v56 offset:0\n"
"ds_read_b128 v[196:199], v56 offset:16\n"
"ds_read_b128 v[200:203], v56 offset:32\n"
"ds_read_b128 v[204:207], v56 offset:48\n"
"ds_read_b128 v[208:211], v56 offset:64\n"
"ds_read_b128 v[212:215], v56 offset:80\n"
"ds_read_b128 v[216:219], v56 offset:96\n"
"ds_read_b128 v[220:223], v56 offset:112\n"
"s_waitcnt lgkmcnt(0)\n"
"v_smfmac_f32_16x16x64_bf16 v[48:51], v[192:195], v[128:135], v59\n"
"v_smfmac_f32_16x16x64_bf16 v[52:55], v[192:195], v[136:143], v59\n"
"v_smfmac_f32_16x16x64_bf16 v[48:51], v[196:199], v[144:151], v59\n"
"v_smfmac_f32_16x16x64_bf16 v[52:55], v[196:199], v[152:159], v59\n"
"v_smfmac_f32_16x16x64_bf16 v[48:51], v[200:203], v[160:167], v59\n"
"v_smfmac_f32_16x16x64_bf16 v[52:55], v[200:203], v[168:175], v59\n"
"v_smfmac_f32_16x16x64_bf16 v[48:51], v[204:207], v[176:183], v59\n"
"v_smfmac_f32_16x16x64_bf16 v[52:55], v[204:207], v[184:191], v59\n"
"v_smfmac_f32_16x16x64_bf16 v[48:51], v[208:211], v[4:11], v59\n"
"v_smfmac_f32_16x16x64_bf16 v[52:55], v[208:211], v[12:19], v59\n"
"v_smfmac_f32_16x16x64_bf16 v[48:51], v[212:215], v[20:27], v59\n"
"v_smfmac_f32_16x16x64_bf16 v[52:55], v[212:215], v[28:35], v59\n"
"v_smfmac_f32_16x16x64_bf16 v[48:51], v[216:219], v[224:231], v59\n"
"v_smfmac_f32_16x16x64_bf16 v[52:55], v[216:219], v[232:239], v59\n"
"v_smfmac_f32_16x16x64_bf16 v[48:51], v[220:223], v[240:247], v59\n"
"v_smfmac_f32_16x16x64_bf16 v[52:55], v[220:223], v[248:255], v59\n"
"s_nop 6\n"
"v_add_f32 v44, v48, v50\n"
"v_add_f32 v45, v52, v54\n"
"s_branch LPOSTZ\n"
"LSLOW:\n"
"s_nop 7\n"
"s_waitcnt vmcnt(0) lgkmcnt(0)\n"
"s_mov_b32 s21, 1\n"
"v_bfe_u32 v56, v0, 2, 2\n"
"v_lshlrev_b32 v1, 8, v56\n"
"v_lshrrev_b32 v56, 4, v0\n"
"v_lshl_or_b32 v1, v56, 6, v1\n"
"v_and_b32 v56, 3, v0\n"
"v_lshl_or_b32 v1, v56, 4, v1\n"
"v_and_b32 v58, 1, v0\n"
"v_lshlrev_b32 v58, 4, v58\n"
"v_mov_b32 v59, 0xffff\n"
"v_lshlrev_b32 v58, v58, v59\n"
"v_bfe_u32 v59, v0, 1, 3\n"
"v_cmp_eq_u32_e64 s[72:73], 0, v59\n"
"v_cmp_eq_u32_e64 s[74:75], 1, v59\n"
"v_cmp_eq_u32_e64 s[76:77], 2, v59\n"
"v_cmp_eq_u32_e64 s[78:79], 3, v59\n"
"v_cndmask_b32_e64 v20, 0, v58, s[72:73]\n"
"v_cndmask_b32_e64 v21, 0, v58, s[74:75]\n"
"v_cndmask_b32_e64 v22, 0, v58, s[76:77]\n"
"v_cndmask_b32_e64 v23, 0, v58, s[78:79]\n"
"v_mov_b32 v44, 0xff800000\n"
"v_mov_b32 v45, 0xff800000\n"
"v_mov_b32 v46, 0xff800000\n"
"v_mov_b32 v47, 0xff800000\n"
"v_mov_b32 v48, 0xff800000\n"
"v_mov_b32 v49, 0xff800000\n"
"v_mov_b32 v50, 0xff800000\n"
"v_mov_b32 v51, 0xff800000\n"
"v_mov_b32 v52, 0xff800000\n"
"v_mov_b32_dpp v4, v40 quad_perm:[0,1,2,3] row_mask:0xf bank_mask:0x1\n"
"v_mov_b32_dpp v5, v41 quad_perm:[0,1,2,3] row_mask:0xf bank_mask:0x1\n"
"v_mov_b32_dpp v6, v42 quad_perm:[0,1,2,3] row_mask:0xf bank_mask:0x1\n"
"v_mov_b32_dpp v7, v43 quad_perm:[0,1,2,3] row_mask:0xf bank_mask:0x1\n"
"v_mov_b32_dpp v8, v40 quad_perm:[0,1,2,3] row_mask:0xf bank_mask:0x2\n"
"v_mov_b32_dpp v9, v41 quad_perm:[0,1,2,3] row_mask:0xf bank_mask:0x2\n"
"v_mov_b32_dpp v10, v42 quad_perm:[0,1,2,3] row_mask:0xf bank_mask:0x2\n"
"v_mov_b32_dpp v11, v43 quad_perm:[0,1,2,3] row_mask:0xf bank_mask:0x2\n"
"v_mov_b32_dpp v12, v40 quad_perm:[0,1,2,3] row_mask:0xf bank_mask:0x4\n"
"v_mov_b32_dpp v13, v41 quad_perm:[0,1,2,3] row_mask:0xf bank_mask:0x4\n"
"v_mov_b32_dpp v14, v42 quad_perm:[0,1,2,3] row_mask:0xf bank_mask:0x4\n"
"v_mov_b32_dpp v15, v43 quad_perm:[0,1,2,3] row_mask:0xf bank_mask:0x4\n"
"v_mov_b32_dpp v16, v40 quad_perm:[0,1,2,3] row_mask:0xf bank_mask:0x8\n"
"v_mov_b32_dpp v17, v41 quad_perm:[0,1,2,3] row_mask:0xf bank_mask:0x8\n"
"v_mov_b32_dpp v18, v42 quad_perm:[0,1,2,3] row_mask:0xf bank_mask:0x8\n"
"v_mov_b32_dpp v19, v43 quad_perm:[0,1,2,3] row_mask:0xf bank_mask:0x8\n"
"s_add_u32 s52, s53, 0x0\n"
"s_and_b32 s52, s52, s54\n"
"buffer_load_dwordx4 v[64:67], v1, s[28:31], s52 offen offset:0 nt\n"
"s_add_u32 s52, s53, 0x7000\n"
"s_and_b32 s52, s52, s54\n"
"buffer_load_dwordx4 v[68:71], v1, s[28:31], s52 offen offset:0 nt\n"
"s_add_u32 s52, s53, 0xe000\n"
"s_and_b32 s52, s52, s54\n"
"buffer_load_dwordx4 v[72:75], v1, s[28:31], s52 offen offset:0 nt\n"
"s_add_u32 s52, s53, 0x15000\n"
"s_and_b32 s52, s52, s54\n"
"buffer_load_dwordx4 v[76:79], v1, s[28:31], s52 offen offset:0 nt\n"
"s_add_u32 s52, s53, 0x1c000\n"
"s_and_b32 s52, s52, s54\n"
"buffer_load_dwordx4 v[80:83], v1, s[28:31], s52 offen offset:0 nt\n"
"s_add_u32 s52, s53, 0x23000\n"
"s_and_b32 s52, s52, s54\n"
"buffer_load_dwordx4 v[84:87], v1, s[28:31], s52 offen offset:0 nt\n"
"s_add_u32 s52, s53, 0x2a000\n"
"s_and_b32 s52, s52, s54\n"
"buffer_load_dwordx4 v[88:91], v1, s[28:31], s52 offen offset:0 nt\n"
"s_add_u32 s52, s53, 0x31000\n"
"s_and_b32 s52, s52, s54\n"
"buffer_load_dwordx4 v[92:95], v1, s[28:31], s52 offen offset:0 nt\n"
"s_add_u32 s52, s53, 0x38000\n"
"s_and_b32 s52, s52, s54\n"
"buffer_load_dwordx4 v[96:99], v1, s[28:31], s52 offen offset:0 nt\n"
"s_add_u32 s52, s53, 0x3f000\n"
"s_and_b32 s52, s52, s54\n"
"buffer_load_dwordx4 v[100:103], v1, s[28:31], s52 offen offset:0 nt\n"
"s_add_u32 s52, s53, 0x46000\n"
"s_and_b32 s52, s52, s54\n"
"buffer_load_dwordx4 v[104:107], v1, s[28:31], s52 offen offset:0 nt\n"
"s_add_u32 s52, s53, 0x4d000\n"
"s_and_b32 s52, s52, s54\n"
"buffer_load_dwordx4 v[108:111], v1, s[28:31], s52 offen offset:0 nt\n"
"s_add_u32 s52, s53, 0x54000\n"
"s_and_b32 s52, s52, s54\n"
"buffer_load_dwordx4 v[112:115], v1, s[28:31], s52 offen offset:0 nt\n"
"s_add_u32 s52, s53, 0x5b000\n"
"s_and_b32 s52, s52, s54\n"
"buffer_load_dwordx4 v[116:119], v1, s[28:31], s52 offen offset:0 nt\n"
"s_add_u32 s52, s53, 0x62000\n"
"s_and_b32 s52, s52, s54\n"
"buffer_load_dwordx4 v[120:123], v1, s[28:31], s52 offen offset:0 nt\n"
"s_add_u32 s52, s53, 0x69000\n"
"s_and_b32 s52, s52, s54\n"
"buffer_load_dwordx4 v[124:127], v1, s[28:31], s52 offen offset:0 nt\n"
"s_add_u32 s52, s53, 0x70000\n"
"s_and_b32 s52, s52, s54\n"
"buffer_load_dwordx4 v[128:131], v1, s[28:31], s52 offen offset:0 nt\n"
"s_add_u32 s52, s53, 0x77000\n"
"s_and_b32 s52, s52, s54\n"
"buffer_load_dwordx4 v[132:135], v1, s[28:31], s52 offen offset:0 nt\n"
"s_add_u32 s52, s53, 0x7e000\n"
"s_and_b32 s52, s52, s54\n"
"buffer_load_dwordx4 v[136:139], v1, s[28:31], s52 offen offset:0 nt\n"
"s_add_u32 s52, s53, 0x85000\n"
"s_and_b32 s52, s52, s54\n"
"buffer_load_dwordx4 v[140:143], v1, s[28:31], s52 offen offset:0 nt\n"
"s_add_u32 s52, s53, 0x8c000\n"
"s_and_b32 s52, s52, s54\n"
"buffer_load_dwordx4 v[144:147], v1, s[28:31], s52 offen offset:0 nt\n"
"s_add_u32 s52, s53, 0x93000\n"
"s_and_b32 s52, s52, s54\n"
"buffer_load_dwordx4 v[148:151], v1, s[28:31], s52 offen offset:0 nt\n"
"s_add_u32 s52, s53, 0x9a000\n"
"s_and_b32 s52, s52, s54\n"
"buffer_load_dwordx4 v[152:155], v1, s[28:31], s52 offen offset:0 nt\n"
"s_add_u32 s52, s53, 0xa1000\n"
"s_and_b32 s52, s52, s54\n"
"buffer_load_dwordx4 v[156:159], v1, s[28:31], s52 offen offset:0 nt\n"
"s_add_u32 s52, s53, 0xa8000\n"
"s_and_b32 s52, s52, s54\n"
"buffer_load_dwordx4 v[160:163], v1, s[28:31], s52 offen offset:0 nt\n"
"s_add_u32 s52, s53, 0xaf000\n"
"s_and_b32 s52, s52, s54\n"
"buffer_load_dwordx4 v[164:167], v1, s[28:31], s52 offen offset:0 nt\n"
"s_add_u32 s52, s53, 0xb6000\n"
"s_and_b32 s52, s52, s54\n"
"buffer_load_dwordx4 v[168:171], v1, s[28:31], s52 offen offset:0 nt\n"
"s_add_u32 s52, s53, 0xbd000\n"
"s_and_b32 s52, s52, s54\n"
"buffer_load_dwordx4 v[172:175], v1, s[28:31], s52 offen offset:0 nt\n"
"s_add_u32 s52, s53, 0xc4000\n"
"s_and_b32 s52, s52, s54\n"
"buffer_load_dwordx4 v[176:179], v1, s[28:31], s52 offen offset:0 nt\n"
"s_add_u32 s52, s53, 0xcb000\n"
"s_and_b32 s52, s52, s54\n"
"buffer_load_dwordx4 v[180:183], v1, s[28:31], s52 offen offset:0 nt\n"
"s_add_u32 s52, s53, 0xd2000\n"
"s_and_b32 s52, s52, s54\n"
"buffer_load_dwordx4 v[184:187], v1, s[28:31], s52 offen offset:0 nt\n"
"s_add_u32 s52, s53, 0xd9000\n"
"s_and_b32 s52, s52, s54\n"
"buffer_load_dwordx4 v[188:191], v1, s[28:31], s52 offen offset:0 nt\n"
"s_waitcnt vmcnt(31)\n"
"v_mfma_f32_16x16x32_bf16 v[32:35], v[64:67], v[4:7], 0\n"
"s_add_u32 s52, s53, 0xe0000\n"
"s_and_b32 s52, s52, s54\n"
"buffer_load_dwordx4 v[192:195], v1, s[28:31], s52 offen offset:0 nt\n"
"s_waitcnt vmcnt(31)\n"
"v_mfma_f32_16x16x32_bf16 v[32:35], v[68:71], v[8:11], v[32:35]\n"
"s_add_u32 s52, s53, 0xe7000\n"
"s_and_b32 s52, s52, s54\n"
"buffer_load_dwordx4 v[196:199], v1, s[28:31], s52 offen offset:0 nt\n"
"s_waitcnt vmcnt(31)\n"
"v_mfma_f32_16x16x32_bf16 v[32:35], v[72:75], v[12:15], v[32:35]\n"
"s_add_u32 s52, s53, 0xee000\n"
"s_and_b32 s52, s52, s54\n"
"buffer_load_dwordx4 v[200:203], v1, s[28:31], s52 offen offset:0 nt\n"
"s_waitcnt vmcnt(31)\n"
"v_mfma_f32_16x16x32_bf16 v[32:35], v[76:79], v[16:19], v[32:35]\n"
"s_add_u32 s52, s53, 0xf5000\n"
"s_and_b32 s52, s52, s54\n"
"buffer_load_dwordx4 v[204:207], v1, s[28:31], s52 offen offset:0 nt\n"
"s_waitcnt vmcnt(31)\n"
"v_mfma_f32_16x16x32_bf16 v[36:39], v[80:83], v[4:7], 0\n"
"s_add_u32 s52, s53, 0xfc000\n"
"s_and_b32 s52, s52, s54\n"
"buffer_load_dwordx4 v[208:211], v1, s[28:31], s52 offen offset:0 nt\n"
"s_waitcnt vmcnt(31)\n"
"v_mfma_f32_16x16x32_bf16 v[36:39], v[84:87], v[8:11], v[36:39]\n"
"s_add_u32 s52, s53, 0x103000\n"
"s_and_b32 s52, s52, s54\n"
"buffer_load_dwordx4 v[212:215], v1, s[28:31], s52 offen offset:0 nt\n"
"s_waitcnt vmcnt(31)\n"
"v_mfma_f32_16x16x32_bf16 v[36:39], v[88:91], v[12:15], v[36:39]\n"
"s_add_u32 s52, s53, 0x10a000\n"
"s_and_b32 s52, s52, s54\n"
"buffer_load_dwordx4 v[216:219], v1, s[28:31], s52 offen offset:0 nt\n"
"v_mov_b32_dpp v63, v32 quad_perm:[0,0,0,0] row_mask:0xf bank_mask:0xf\n"
"v_add_f32_dpp v63, v33, v63 quad_perm:[1,1,1,1] row_mask:0xf bank_mask:0xf\n"
"v_add_f32_dpp v63, v34, v63 quad_perm:[2,2,2,2] row_mask:0xf bank_mask:0xf\n"
"v_add_f32_dpp v63, v35, v63 quad_perm:[3,3,3,3] row_mask:0xf bank_mask:0xf\n"
"v_cndmask_b32_e64 v44, v44, v63, s[60:61]\n"
"s_waitcnt vmcnt(31)\n"
"v_mfma_f32_16x16x32_bf16 v[36:39], v[92:95], v[16:19], v[36:39]\n"
"s_add_u32 s52, s53, 0x111000\n"
"s_and_b32 s52, s52, s54\n"
"buffer_load_dwordx4 v[220:223], v1, s[28:31], s52 offen offset:0 nt\n"
"s_waitcnt vmcnt(31)\n"
"v_mfma_f32_16x16x32_bf16 v[32:35], v[96:99], v[4:7], 0\n"
"s_add_u32 s52, s53, 0x118000\n"
"s_and_b32 s52, s52, s54\n"
"buffer_load_dwordx4 v[64:67], v1, s[28:31], s52 offen offset:0 nt\n"
"s_waitcnt vmcnt(31)\n"
"v_mfma_f32_16x16x32_bf16 v[32:35], v[100:103], v[8:11], v[32:35]\n"
"s_add_u32 s52, s53, 0x11f000\n"
"s_and_b32 s52, s52, s54\n"
"buffer_load_dwordx4 v[68:71], v1, s[28:31], s52 offen offset:0 nt\n"
"s_waitcnt vmcnt(31)\n"
"v_mfma_f32_16x16x32_bf16 v[32:35], v[104:107], v[12:15], v[32:35]\n"
"s_add_u32 s52, s53, 0x126000\n"
"s_and_b32 s52, s52, s54\n"
"buffer_load_dwordx4 v[72:75], v1, s[28:31], s52 offen offset:0 nt\n"
"v_mov_b32_dpp v63, v36 quad_perm:[0,0,0,0] row_mask:0xf bank_mask:0xf\n"
"v_add_f32_dpp v63, v37, v63 quad_perm:[1,1,1,1] row_mask:0xf bank_mask:0xf\n"
"v_add_f32_dpp v63, v38, v63 quad_perm:[2,2,2,2] row_mask:0xf bank_mask:0xf\n"
"v_add_f32_dpp v63, v39, v63 quad_perm:[3,3,3,3] row_mask:0xf bank_mask:0xf\n"
"v_cndmask_b32_e64 v44, v44, v63, s[62:63]\n"
"s_waitcnt vmcnt(31)\n"
"v_mfma_f32_16x16x32_bf16 v[32:35], v[108:111], v[16:19], v[32:35]\n"
"s_add_u32 s52, s53, 0x12d000\n"
"s_and_b32 s52, s52, s54\n"
"buffer_load_dwordx4 v[76:79], v1, s[28:31], s52 offen offset:0 nt\n"
"s_waitcnt vmcnt(31)\n"
"v_mfma_f32_16x16x32_bf16 v[36:39], v[112:115], v[4:7], 0\n"
"s_add_u32 s52, s53, 0x134000\n"
"s_and_b32 s52, s52, s54\n"
"buffer_load_dwordx4 v[80:83], v1, s[28:31], s52 offen offset:0 nt\n"
"s_waitcnt vmcnt(31)\n"
"v_mfma_f32_16x16x32_bf16 v[36:39], v[116:119], v[8:11], v[36:39]\n"
"s_add_u32 s52, s53, 0x13b000\n"
"s_and_b32 s52, s52, s54\n"
"buffer_load_dwordx4 v[84:87], v1, s[28:31], s52 offen offset:0 nt\n"
"s_waitcnt vmcnt(31)\n"
"v_mfma_f32_16x16x32_bf16 v[36:39], v[120:123], v[12:15], v[36:39]\n"
"s_add_u32 s52, s53, 0x142000\n"
"s_and_b32 s52, s52, s54\n"
"buffer_load_dwordx4 v[88:91], v1, s[28:31], s52 offen offset:0 nt\n"
"v_mov_b32_dpp v63, v32 quad_perm:[0,0,0,0] row_mask:0xf bank_mask:0xf\n"
"v_add_f32_dpp v63, v33, v63 quad_perm:[1,1,1,1] row_mask:0xf bank_mask:0xf\n"
"v_add_f32_dpp v63, v34, v63 quad_perm:[2,2,2,2] row_mask:0xf bank_mask:0xf\n"
"v_add_f32_dpp v63, v35, v63 quad_perm:[3,3,3,3] row_mask:0xf bank_mask:0xf\n"
"v_cndmask_b32_e64 v44, v44, v63, s[64:65]\n"
"s_waitcnt vmcnt(31)\n"
"v_mfma_f32_16x16x32_bf16 v[36:39], v[124:127], v[16:19], v[36:39]\n"
"s_add_u32 s52, s53, 0x149000\n"
"s_and_b32 s52, s52, s54\n"
"buffer_load_dwordx4 v[92:95], v1, s[28:31], s52 offen offset:0 nt\n"
"s_waitcnt vmcnt(31)\n"
"v_mfma_f32_16x16x32_bf16 v[32:35], v[128:131], v[4:7], 0\n"
"s_add_u32 s52, s53, 0x150000\n"
"s_and_b32 s52, s52, s54\n"
"buffer_load_dwordx4 v[96:99], v1, s[28:31], s52 offen offset:0 nt\n"
"s_waitcnt vmcnt(31)\n"
"v_mfma_f32_16x16x32_bf16 v[32:35], v[132:135], v[8:11], v[32:35]\n"
"s_add_u32 s52, s53, 0x157000\n"
"s_and_b32 s52, s52, s54\n"
"buffer_load_dwordx4 v[100:103], v1, s[28:31], s52 offen offset:0 nt\n"
"s_waitcnt vmcnt(31)\n"
"v_mfma_f32_16x16x32_bf16 v[32:35], v[136:139], v[12:15], v[32:35]\n"
"s_add_u32 s52, s53, 0x15e000\n"
"s_and_b32 s52, s52, s54\n"
"buffer_load_dwordx4 v[104:107], v1, s[28:31], s52 offen offset:0 nt\n"
"v_mov_b32_dpp v63, v36 quad_perm:[0,0,0,0] row_mask:0xf bank_mask:0xf\n"
"v_add_f32_dpp v63, v37, v63 quad_perm:[1,1,1,1] row_mask:0xf bank_mask:0xf\n"
"v_add_f32_dpp v63, v38, v63 quad_perm:[2,2,2,2] row_mask:0xf bank_mask:0xf\n"
"v_add_f32_dpp v63, v39, v63 quad_perm:[3,3,3,3] row_mask:0xf bank_mask:0xf\n"
"v_cndmask_b32_e64 v44, v44, v63, s[66:67]\n"
"s_waitcnt vmcnt(31)\n"
"v_mfma_f32_16x16x32_bf16 v[32:35], v[140:143], v[16:19], v[32:35]\n"
"s_add_u32 s52, s53, 0x165000\n"
"s_and_b32 s52, s52, s54\n"
"buffer_load_dwordx4 v[108:111], v1, s[28:31], s52 offen offset:0 nt\n"
"s_waitcnt vmcnt(31)\n"
"v_mfma_f32_16x16x32_bf16 v[36:39], v[144:147], v[4:7], 0\n"
"s_add_u32 s52, s53, 0x16c000\n"
"s_and_b32 s52, s52, s54\n"
"buffer_load_dwordx4 v[112:115], v1, s[28:31], s52 offen offset:0 nt\n"
"s_waitcnt vmcnt(31)\n"
"v_mfma_f32_16x16x32_bf16 v[36:39], v[148:151], v[8:11], v[36:39]\n"
"s_add_u32 s52, s53, 0x173000\n"
"s_and_b32 s52, s52, s54\n"
"buffer_load_dwordx4 v[116:119], v1, s[28:31], s52 offen offset:0 nt\n"
"s_waitcnt vmcnt(31)\n"
"v_mfma_f32_16x16x32_bf16 v[36:39], v[152:155], v[12:15], v[36:39]\n"
"s_add_u32 s52, s53, 0x17a000\n"
"s_and_b32 s52, s52, s54\n"
"buffer_load_dwordx4 v[120:123], v1, s[28:31], s52 offen offset:0 nt\n"
"v_mov_b32_dpp v63, v32 quad_perm:[0,0,0,0] row_mask:0xf bank_mask:0xf\n"
"v_add_f32_dpp v63, v33, v63 quad_perm:[1,1,1,1] row_mask:0xf bank_mask:0xf\n"
"v_add_f32_dpp v63, v34, v63 quad_perm:[2,2,2,2] row_mask:0xf bank_mask:0xf\n"
"v_add_f32_dpp v63, v35, v63 quad_perm:[3,3,3,3] row_mask:0xf bank_mask:0xf\n"
"v_cndmask_b32_e64 v45, v45, v63, s[60:61]\n"
"s_waitcnt vmcnt(31)\n"
"v_mfma_f32_16x16x32_bf16 v[36:39], v[156:159], v[16:19], v[36:39]\n"
"s_add_u32 s52, s53, 0x181000\n"
"s_and_b32 s52, s52, s54\n"
"buffer_load_dwordx4 v[124:127], v1, s[28:31], s52 offen offset:0 nt\n"
"s_waitcnt vmcnt(31)\n"
"v_mfma_f32_16x16x32_bf16 v[32:35], v[160:163], v[4:7], 0\n"
"s_add_u32 s52, s53, 0x188000\n"
"s_and_b32 s52, s52, s54\n"
"buffer_load_dwordx4 v[128:131], v1, s[28:31], s52 offen offset:0 nt\n"
"s_waitcnt vmcnt(31)\n"
"v_mfma_f32_16x16x32_bf16 v[32:35], v[164:167], v[8:11], v[32:35]\n"
"s_add_u32 s52, s53, 0x18f000\n"
"s_and_b32 s52, s52, s54\n"
"buffer_load_dwordx4 v[132:135], v1, s[28:31], s52 offen offset:0 nt\n"
"s_waitcnt vmcnt(31)\n"
"v_mfma_f32_16x16x32_bf16 v[32:35], v[168:171], v[12:15], v[32:35]\n"
"s_add_u32 s52, s53, 0x196000\n"
"s_and_b32 s52, s52, s54\n"
"buffer_load_dwordx4 v[136:139], v1, s[28:31], s52 offen offset:0 nt\n"
"v_mov_b32_dpp v63, v36 quad_perm:[0,0,0,0] row_mask:0xf bank_mask:0xf\n"
"v_add_f32_dpp v63, v37, v63 quad_perm:[1,1,1,1] row_mask:0xf bank_mask:0xf\n"
"v_add_f32_dpp v63, v38, v63 quad_perm:[2,2,2,2] row_mask:0xf bank_mask:0xf\n"
"v_add_f32_dpp v63, v39, v63 quad_perm:[3,3,3,3] row_mask:0xf bank_mask:0xf\n"
"v_cndmask_b32_e64 v45, v45, v63, s[62:63]\n"
"s_waitcnt vmcnt(31)\n"
"v_mfma_f32_16x16x32_bf16 v[32:35], v[172:175], v[16:19], v[32:35]\n"
"s_add_u32 s52, s53, 0x19d000\n"
"s_and_b32 s52, s52, s54\n"
"buffer_load_dwordx4 v[140:143], v1, s[28:31], s52 offen offset:0 nt\n"
"s_waitcnt vmcnt(31)\n"
"v_mfma_f32_16x16x32_bf16 v[36:39], v[176:179], v[4:7], 0\n"
"s_add_u32 s52, s53, 0x1a4000\n"
"s_and_b32 s52, s52, s54\n"
"buffer_load_dwordx4 v[144:147], v1, s[28:31], s52 offen offset:0 nt\n"
"s_waitcnt vmcnt(31)\n"
"v_mfma_f32_16x16x32_bf16 v[36:39], v[180:183], v[8:11], v[36:39]\n"
"s_add_u32 s52, s53, 0x1ab000\n"
"s_and_b32 s52, s52, s54\n"
"buffer_load_dwordx4 v[148:151], v1, s[28:31], s52 offen offset:0 nt\n"
"s_waitcnt vmcnt(31)\n"
"v_mfma_f32_16x16x32_bf16 v[36:39], v[184:187], v[12:15], v[36:39]\n"
"s_add_u32 s52, s53, 0x1b2000\n"
"s_and_b32 s52, s52, s54\n"
"buffer_load_dwordx4 v[152:155], v1, s[28:31], s52 offen offset:0 nt\n"
"v_mov_b32_dpp v63, v32 quad_perm:[0,0,0,0] row_mask:0xf bank_mask:0xf\n"
"v_add_f32_dpp v63, v33, v63 quad_perm:[1,1,1,1] row_mask:0xf bank_mask:0xf\n"
"v_add_f32_dpp v63, v34, v63 quad_perm:[2,2,2,2] row_mask:0xf bank_mask:0xf\n"
"v_add_f32_dpp v63, v35, v63 quad_perm:[3,3,3,3] row_mask:0xf bank_mask:0xf\n"
"v_cndmask_b32_e64 v45, v45, v63, s[64:65]\n"
"s_waitcnt vmcnt(31)\n"
"v_mfma_f32_16x16x32_bf16 v[36:39], v[188:191], v[16:19], v[36:39]\n"
"s_add_u32 s52, s53, 0x1b9000\n"
"s_and_b32 s52, s52, s54\n"
"buffer_load_dwordx4 v[156:159], v1, s[28:31], s52 offen offset:0 nt\n"
"s_waitcnt vmcnt(31)\n"
"v_mfma_f32_16x16x32_bf16 v[32:35], v[192:195], v[4:7], 0\n"
"s_add_u32 s52, s53, 0x1c0000\n"
"s_and_b32 s52, s52, s54\n"
"buffer_load_dwordx4 v[160:163], v1, s[28:31], s52 offen offset:0 nt\n"
"s_waitcnt vmcnt(31)\n"
"v_mfma_f32_16x16x32_bf16 v[32:35], v[196:199], v[8:11], v[32:35]\n"
"s_add_u32 s52, s53, 0x1c7000\n"
"s_and_b32 s52, s52, s54\n"
"buffer_load_dwordx4 v[164:167], v1, s[28:31], s52 offen offset:0 nt\n"
"s_waitcnt vmcnt(31)\n"
"v_mfma_f32_16x16x32_bf16 v[32:35], v[200:203], v[12:15], v[32:35]\n"
"s_add_u32 s52, s53, 0x1ce000\n"
"s_and_b32 s52, s52, s54\n"
"buffer_load_dwordx4 v[168:171], v1, s[28:31], s52 offen offset:0 nt\n"
"v_mov_b32_dpp v63, v36 quad_perm:[0,0,0,0] row_mask:0xf bank_mask:0xf\n"
"v_add_f32_dpp v63, v37, v63 quad_perm:[1,1,1,1] row_mask:0xf bank_mask:0xf\n"
"v_add_f32_dpp v63, v38, v63 quad_perm:[2,2,2,2] row_mask:0xf bank_mask:0xf\n"
"v_add_f32_dpp v63, v39, v63 quad_perm:[3,3,3,3] row_mask:0xf bank_mask:0xf\n"
"v_cndmask_b32_e64 v45, v45, v63, s[66:67]\n"
"s_waitcnt vmcnt(31)\n"
"v_mfma_f32_16x16x32_bf16 v[32:35], v[204:207], v[16:19], v[32:35]\n"
"s_add_u32 s52, s53, 0x1d5000\n"
"s_and_b32 s52, s52, s54\n"
"buffer_load_dwordx4 v[172:175], v1, s[28:31], s52 offen offset:0 nt\n"
"s_waitcnt vmcnt(31)\n"
"v_mfma_f32_16x16x32_bf16 v[36:39], v[208:211], v[4:7], 0\n"
"s_add_u32 s52, s53, 0x1dc000\n"
"s_and_b32 s52, s52, s54\n"
"buffer_load_dwordx4 v[176:179], v1, s[28:31], s52 offen offset:0 nt\n"
"s_waitcnt vmcnt(31)\n"
"v_mfma_f32_16x16x32_bf16 v[36:39], v[212:215], v[8:11], v[36:39]\n"
"s_add_u32 s52, s53, 0x1e3000\n"
"s_and_b32 s52, s52, s54\n"
"buffer_load_dwordx4 v[180:183], v1, s[28:31], s52 offen offset:0 nt\n"
"s_waitcnt vmcnt(31)\n"
"v_mfma_f32_16x16x32_bf16 v[36:39], v[216:219], v[12:15], v[36:39]\n"
"s_add_u32 s52, s53, 0x1ea000\n"
"s_and_b32 s52, s52, s54\n"
"buffer_load_dwordx4 v[184:187], v1, s[28:31], s52 offen offset:0 nt\n"
"v_mov_b32_dpp v63, v32 quad_perm:[0,0,0,0] row_mask:0xf bank_mask:0xf\n"
"v_add_f32_dpp v63, v33, v63 quad_perm:[1,1,1,1] row_mask:0xf bank_mask:0xf\n"
"v_add_f32_dpp v63, v34, v63 quad_perm:[2,2,2,2] row_mask:0xf bank_mask:0xf\n"
"v_add_f32_dpp v63, v35, v63 quad_perm:[3,3,3,3] row_mask:0xf bank_mask:0xf\n"
"v_cndmask_b32_e64 v46, v46, v63, s[60:61]\n"
"s_waitcnt vmcnt(31)\n"
"v_mfma_f32_16x16x32_bf16 v[36:39], v[220:223], v[16:19], v[36:39]\n"
"s_add_u32 s52, s53, 0x1f1000\n"
"s_and_b32 s52, s52, s54\n"
"buffer_load_dwordx4 v[188:191], v1, s[28:31], s52 offen offset:0 nt\n"
"s_waitcnt vmcnt(31)\n"
"v_mfma_f32_16x16x32_bf16 v[32:35], v[64:67], v[4:7], 0\n"
"s_add_u32 s52, s53, 0x1f8000\n"
"s_and_b32 s52, s52, s54\n"
"buffer_load_dwordx4 v[192:195], v1, s[28:31], s52 offen offset:0 nt\n"
"s_waitcnt vmcnt(31)\n"
"v_mfma_f32_16x16x32_bf16 v[32:35], v[68:71], v[8:11], v[32:35]\n"
"s_add_u32 s52, s53, 0x1ff000\n"
"s_and_b32 s52, s52, s54\n"
"buffer_load_dwordx4 v[196:199], v1, s[28:31], s52 offen offset:0 nt\n"
"s_waitcnt vmcnt(31)\n"
"v_mfma_f32_16x16x32_bf16 v[32:35], v[72:75], v[12:15], v[32:35]\n"
"s_add_u32 s52, s53, 0x206000\n"
"s_and_b32 s52, s52, s54\n"
"buffer_load_dwordx4 v[200:203], v1, s[28:31], s52 offen offset:0 nt\n"
"v_mov_b32_dpp v63, v36 quad_perm:[0,0,0,0] row_mask:0xf bank_mask:0xf\n"
"v_add_f32_dpp v63, v37, v63 quad_perm:[1,1,1,1] row_mask:0xf bank_mask:0xf\n"
"v_add_f32_dpp v63, v38, v63 quad_perm:[2,2,2,2] row_mask:0xf bank_mask:0xf\n"
"v_add_f32_dpp v63, v39, v63 quad_perm:[3,3,3,3] row_mask:0xf bank_mask:0xf\n"
"v_cndmask_b32_e64 v46, v46, v63, s[62:63]\n"
"s_waitcnt vmcnt(31)\n"
"v_mfma_f32_16x16x32_bf16 v[32:35], v[76:79], v[16:19], v[32:35]\n"
"s_add_u32 s52, s53, 0x20d000\n"
"s_and_b32 s52, s52, s54\n"
"buffer_load_dwordx4 v[204:207], v1, s[28:31], s52 offen offset:0 nt\n"
"s_waitcnt vmcnt(31)\n"
"v_mfma_f32_16x16x32_bf16 v[36:39], v[80:83], v[4:7], 0\n"
"s_add_u32 s52, s53, 0x214000\n"
"s_and_b32 s52, s52, s54\n"
"buffer_load_dwordx4 v[208:211], v1, s[28:31], s52 offen offset:0 nt\n"
"s_waitcnt vmcnt(31)\n"
"v_mfma_f32_16x16x32_bf16 v[36:39], v[84:87], v[8:11], v[36:39]\n"
"s_add_u32 s52, s53, 0x21b000\n"
"s_and_b32 s52, s52, s54\n"
"buffer_load_dwordx4 v[212:215], v1, s[28:31], s52 offen offset:0 nt\n"
"s_waitcnt vmcnt(31)\n"
"v_mfma_f32_16x16x32_bf16 v[36:39], v[88:91], v[12:15], v[36:39]\n"
"s_add_u32 s52, s53, 0x222000\n"
"s_and_b32 s52, s52, s54\n"
"buffer_load_dwordx4 v[216:219], v1, s[28:31], s52 offen offset:0 nt\n"
"v_mov_b32_dpp v63, v32 quad_perm:[0,0,0,0] row_mask:0xf bank_mask:0xf\n"
"v_add_f32_dpp v63, v33, v63 quad_perm:[1,1,1,1] row_mask:0xf bank_mask:0xf\n"
"v_add_f32_dpp v63, v34, v63 quad_perm:[2,2,2,2] row_mask:0xf bank_mask:0xf\n"
"v_add_f32_dpp v63, v35, v63 quad_perm:[3,3,3,3] row_mask:0xf bank_mask:0xf\n"
"v_cndmask_b32_e64 v46, v46, v63, s[64:65]\n"
"s_waitcnt vmcnt(31)\n"
"v_mfma_f32_16x16x32_bf16 v[36:39], v[92:95], v[16:19], v[36:39]\n"
"s_add_u32 s52, s53, 0x229000\n"
"s_and_b32 s52, s52, s54\n"
"buffer_load_dwordx4 v[220:223], v1, s[28:31], s52 offen offset:0 nt\n"
"s_waitcnt vmcnt(31)\n"
"v_mfma_f32_16x16x32_bf16 v[32:35], v[96:99], v[4:7], 0\n"
"s_add_u32 s52, s53, 0x230000\n"
"s_and_b32 s52, s52, s54\n"
"buffer_load_dwordx4 v[64:67], v1, s[28:31], s52 offen offset:0 nt\n"
"s_waitcnt vmcnt(31)\n"
"v_mfma_f32_16x16x32_bf16 v[32:35], v[100:103], v[8:11], v[32:35]\n"
"s_add_u32 s52, s53, 0x237000\n"
"s_and_b32 s52, s52, s54\n"
"buffer_load_dwordx4 v[68:71], v1, s[28:31], s52 offen offset:0 nt\n"
"s_waitcnt vmcnt(31)\n"
"v_mfma_f32_16x16x32_bf16 v[32:35], v[104:107], v[12:15], v[32:35]\n"
"s_add_u32 s52, s53, 0x23e000\n"
"s_and_b32 s52, s52, s54\n"
"buffer_load_dwordx4 v[72:75], v1, s[28:31], s52 offen offset:0 nt\n"
"v_mov_b32_dpp v63, v36 quad_perm:[0,0,0,0] row_mask:0xf bank_mask:0xf\n"
"v_add_f32_dpp v63, v37, v63 quad_perm:[1,1,1,1] row_mask:0xf bank_mask:0xf\n"
"v_add_f32_dpp v63, v38, v63 quad_perm:[2,2,2,2] row_mask:0xf bank_mask:0xf\n"
"v_add_f32_dpp v63, v39, v63 quad_perm:[3,3,3,3] row_mask:0xf bank_mask:0xf\n"
"v_cndmask_b32_e64 v46, v46, v63, s[66:67]\n"
"s_waitcnt vmcnt(31)\n"
"v_mfma_f32_16x16x32_bf16 v[32:35], v[108:111], v[16:19], v[32:35]\n"
"s_add_u32 s52, s53, 0x245000\n"
"s_and_b32 s52, s52, s54\n"
"buffer_load_dwordx4 v[76:79], v1, s[28:31], s52 offen offset:0 nt\n"
"s_waitcnt vmcnt(31)\n"
"v_mfma_f32_16x16x32_bf16 v[36:39], v[112:115], v[4:7], 0\n"
"s_add_u32 s52, s53, 0x24c000\n"
"s_and_b32 s52, s52, s54\n"
"buffer_load_dwordx4 v[80:83], v1, s[28:31], s52 offen offset:0 nt\n"
"s_waitcnt vmcnt(31)\n"
"v_mfma_f32_16x16x32_bf16 v[36:39], v[116:119], v[8:11], v[36:39]\n"
"s_add_u32 s52, s53, 0x253000\n"
"s_and_b32 s52, s52, s54\n"
"buffer_load_dwordx4 v[84:87], v1, s[28:31], s52 offen offset:0 nt\n"
"s_waitcnt vmcnt(31)\n"
"v_mfma_f32_16x16x32_bf16 v[36:39], v[120:123], v[12:15], v[36:39]\n"
"s_add_u32 s52, s53, 0x25a000\n"
"s_and_b32 s52, s52, s54\n"
"buffer_load_dwordx4 v[88:91], v1, s[28:31], s52 offen offset:0 nt\n"
"v_mov_b32_dpp v63, v32 quad_perm:[0,0,0,0] row_mask:0xf bank_mask:0xf\n"
"v_add_f32_dpp v63, v33, v63 quad_perm:[1,1,1,1] row_mask:0xf bank_mask:0xf\n"
"v_add_f32_dpp v63, v34, v63 quad_perm:[2,2,2,2] row_mask:0xf bank_mask:0xf\n"
"v_add_f32_dpp v63, v35, v63 quad_perm:[3,3,3,3] row_mask:0xf bank_mask:0xf\n"
"v_cndmask_b32_e64 v47, v47, v63, s[60:61]\n"
"s_waitcnt vmcnt(31)\n"
"v_mfma_f32_16x16x32_bf16 v[36:39], v[124:127], v[16:19], v[36:39]\n"
"s_add_u32 s52, s53, 0x261000\n"
"s_and_b32 s52, s52, s54\n"
"buffer_load_dwordx4 v[92:95], v1, s[28:31], s52 offen offset:0 nt\n"
"s_waitcnt vmcnt(31)\n"
"v_mfma_f32_16x16x32_bf16 v[32:35], v[128:131], v[4:7], 0\n"
"s_add_u32 s52, s53, 0x268000\n"
"s_and_b32 s52, s52, s54\n"
"buffer_load_dwordx4 v[96:99], v1, s[28:31], s52 offen offset:0 nt\n"
"s_waitcnt vmcnt(31)\n"
"v_mfma_f32_16x16x32_bf16 v[32:35], v[132:135], v[8:11], v[32:35]\n"
"s_add_u32 s52, s53, 0x26f000\n"
"s_and_b32 s52, s52, s54\n"
"buffer_load_dwordx4 v[100:103], v1, s[28:31], s52 offen offset:0 nt\n"
"s_waitcnt vmcnt(31)\n"
"v_mfma_f32_16x16x32_bf16 v[32:35], v[136:139], v[12:15], v[32:35]\n"
"s_add_u32 s52, s53, 0x276000\n"
"s_and_b32 s52, s52, s54\n"
"buffer_load_dwordx4 v[104:107], v1, s[28:31], s52 offen offset:0 nt\n"
"v_mov_b32_dpp v63, v36 quad_perm:[0,0,0,0] row_mask:0xf bank_mask:0xf\n"
"v_add_f32_dpp v63, v37, v63 quad_perm:[1,1,1,1] row_mask:0xf bank_mask:0xf\n"
"v_add_f32_dpp v63, v38, v63 quad_perm:[2,2,2,2] row_mask:0xf bank_mask:0xf\n"
"v_add_f32_dpp v63, v39, v63 quad_perm:[3,3,3,3] row_mask:0xf bank_mask:0xf\n"
"v_cndmask_b32_e64 v47, v47, v63, s[62:63]\n"
"s_waitcnt vmcnt(31)\n"
"v_mfma_f32_16x16x32_bf16 v[32:35], v[140:143], v[16:19], v[32:35]\n"
"s_add_u32 s52, s53, 0x27d000\n"
"s_and_b32 s52, s52, s54\n"
"buffer_load_dwordx4 v[108:111], v1, s[28:31], s52 offen offset:0 nt\n"
"s_waitcnt vmcnt(31)\n"
"v_mfma_f32_16x16x32_bf16 v[36:39], v[144:147], v[4:7], 0\n"
"s_add_u32 s52, s53, 0x284000\n"
"s_and_b32 s52, s52, s54\n"
"buffer_load_dwordx4 v[112:115], v1, s[28:31], s52 offen offset:0 nt\n"
"s_waitcnt vmcnt(31)\n"
"v_mfma_f32_16x16x32_bf16 v[36:39], v[148:151], v[8:11], v[36:39]\n"
"s_add_u32 s52, s53, 0x28b000\n"
"s_and_b32 s52, s52, s54\n"
"buffer_load_dwordx4 v[116:119], v1, s[28:31], s52 offen offset:0 nt\n"
"s_waitcnt vmcnt(31)\n"
"v_mfma_f32_16x16x32_bf16 v[36:39], v[152:155], v[12:15], v[36:39]\n"
"s_add_u32 s52, s53, 0x292000\n"
"s_and_b32 s52, s52, s54\n"
"buffer_load_dwordx4 v[120:123], v1, s[28:31], s52 offen offset:0 nt\n"
"v_mov_b32_dpp v63, v32 quad_perm:[0,0,0,0] row_mask:0xf bank_mask:0xf\n"
"v_add_f32_dpp v63, v33, v63 quad_perm:[1,1,1,1] row_mask:0xf bank_mask:0xf\n"
"v_add_f32_dpp v63, v34, v63 quad_perm:[2,2,2,2] row_mask:0xf bank_mask:0xf\n"
"v_add_f32_dpp v63, v35, v63 quad_perm:[3,3,3,3] row_mask:0xf bank_mask:0xf\n"
"v_cndmask_b32_e64 v47, v47, v63, s[64:65]\n"
"s_waitcnt vmcnt(31)\n"
"v_mfma_f32_16x16x32_bf16 v[36:39], v[156:159], v[16:19], v[36:39]\n"
"s_add_u32 s52, s53, 0x299000\n"
"s_and_b32 s52, s52, s54\n"
"buffer_load_dwordx4 v[124:127], v1, s[28:31], s52 offen offset:0 nt\n"
"s_waitcnt vmcnt(31)\n"
"v_mfma_f32_16x16x32_bf16 v[32:35], v[160:163], v[4:7], 0\n"
"s_add_u32 s52, s53, 0x2a0000\n"
"s_and_b32 s52, s52, s54\n"
"buffer_load_dwordx4 v[128:131], v1, s[28:31], s52 offen offset:0 nt\n"
"s_waitcnt vmcnt(31)\n"
"v_mfma_f32_16x16x32_bf16 v[32:35], v[164:167], v[8:11], v[32:35]\n"
"s_add_u32 s52, s53, 0x2a7000\n"
"s_and_b32 s52, s52, s54\n"
"buffer_load_dwordx4 v[132:135], v1, s[28:31], s52 offen offset:0 nt\n"
"s_waitcnt vmcnt(31)\n"
"v_mfma_f32_16x16x32_bf16 v[32:35], v[168:171], v[12:15], v[32:35]\n"
"s_add_u32 s52, s53, 0x2ae000\n"
"s_and_b32 s52, s52, s54\n"
"buffer_load_dwordx4 v[136:139], v1, s[28:31], s52 offen offset:0 nt\n"
"v_mov_b32_dpp v63, v36 quad_perm:[0,0,0,0] row_mask:0xf bank_mask:0xf\n"
"v_add_f32_dpp v63, v37, v63 quad_perm:[1,1,1,1] row_mask:0xf bank_mask:0xf\n"
"v_add_f32_dpp v63, v38, v63 quad_perm:[2,2,2,2] row_mask:0xf bank_mask:0xf\n"
"v_add_f32_dpp v63, v39, v63 quad_perm:[3,3,3,3] row_mask:0xf bank_mask:0xf\n"
"v_cndmask_b32_e64 v47, v47, v63, s[66:67]\n"
"s_waitcnt vmcnt(31)\n"
"v_mfma_f32_16x16x32_bf16 v[32:35], v[172:175], v[16:19], v[32:35]\n"
"s_add_u32 s52, s53, 0x2b5000\n"
"s_and_b32 s52, s52, s54\n"
"buffer_load_dwordx4 v[140:143], v1, s[28:31], s52 offen offset:0 nt\n"
"s_waitcnt vmcnt(31)\n"
"v_mfma_f32_16x16x32_bf16 v[36:39], v[176:179], v[4:7], 0\n"
"s_add_u32 s52, s53, 0x2bc000\n"
"s_and_b32 s52, s52, s54\n"
"buffer_load_dwordx4 v[144:147], v1, s[28:31], s52 offen offset:0 nt\n"
"s_waitcnt vmcnt(31)\n"
"v_mfma_f32_16x16x32_bf16 v[36:39], v[180:183], v[8:11], v[36:39]\n"
"s_add_u32 s52, s53, 0x2c3000\n"
"s_and_b32 s52, s52, s54\n"
"buffer_load_dwordx4 v[148:151], v1, s[28:31], s52 offen offset:0 nt\n"
"s_waitcnt vmcnt(31)\n"
"v_mfma_f32_16x16x32_bf16 v[36:39], v[184:187], v[12:15], v[36:39]\n"
"s_add_u32 s52, s53, 0x2ca000\n"
"s_and_b32 s52, s52, s54\n"
"buffer_load_dwordx4 v[152:155], v1, s[28:31], s52 offen offset:0 nt\n"
"v_mov_b32_dpp v63, v32 quad_perm:[0,0,0,0] row_mask:0xf bank_mask:0xf\n"
"v_add_f32_dpp v63, v33, v63 quad_perm:[1,1,1,1] row_mask:0xf bank_mask:0xf\n"
"v_add_f32_dpp v63, v34, v63 quad_perm:[2,2,2,2] row_mask:0xf bank_mask:0xf\n"
"v_add_f32_dpp v63, v35, v63 quad_perm:[3,3,3,3] row_mask:0xf bank_mask:0xf\n"
"v_cndmask_b32_e64 v48, v48, v63, s[60:61]\n"
"s_waitcnt vmcnt(31)\n"
"v_mfma_f32_16x16x32_bf16 v[36:39], v[188:191], v[16:19], v[36:39]\n"
"s_add_u32 s52, s53, 0x2d1000\n"
"s_and_b32 s52, s52, s54\n"
"buffer_load_dwordx4 v[156:159], v1, s[28:31], s52 offen offset:0 nt\n"
"s_waitcnt vmcnt(31)\n"
"v_mfma_f32_16x16x32_bf16 v[32:35], v[192:195], v[4:7], 0\n"
"s_add_u32 s52, s53, 0x2d8000\n"
"s_and_b32 s52, s52, s54\n"
"buffer_load_dwordx4 v[160:163], v1, s[28:31], s52 offen offset:0 nt\n"
"s_waitcnt vmcnt(31)\n"
"v_mfma_f32_16x16x32_bf16 v[32:35], v[196:199], v[8:11], v[32:35]\n"
"s_add_u32 s52, s53, 0x2df000\n"
"s_and_b32 s52, s52, s54\n"
"buffer_load_dwordx4 v[164:167], v1, s[28:31], s52 offen offset:0 nt\n"
"s_waitcnt vmcnt(31)\n"
"v_mfma_f32_16x16x32_bf16 v[32:35], v[200:203], v[12:15], v[32:35]\n"
"s_add_u32 s52, s53, 0x2e6000\n"
"s_and_b32 s52, s52, s54\n"
"buffer_load_dwordx4 v[168:171], v1, s[28:31], s52 offen offset:0 nt\n"
"v_mov_b32_dpp v63, v36 quad_perm:[0,0,0,0] row_mask:0xf bank_mask:0xf\n"
"v_add_f32_dpp v63, v37, v63 quad_perm:[1,1,1,1] row_mask:0xf bank_mask:0xf\n"
"v_add_f32_dpp v63, v38, v63 quad_perm:[2,2,2,2] row_mask:0xf bank_mask:0xf\n"
"v_add_f32_dpp v63, v39, v63 quad_perm:[3,3,3,3] row_mask:0xf bank_mask:0xf\n"
"v_cndmask_b32_e64 v48, v48, v63, s[62:63]\n"
"s_waitcnt vmcnt(31)\n"
"v_mfma_f32_16x16x32_bf16 v[32:35], v[204:207], v[16:19], v[32:35]\n"
"s_add_u32 s52, s53, 0x2ed000\n"
"s_and_b32 s52, s52, s54\n"
"buffer_load_dwordx4 v[172:175], v1, s[28:31], s52 offen offset:0 nt\n"
"s_waitcnt vmcnt(31)\n"
"v_mfma_f32_16x16x32_bf16 v[36:39], v[208:211], v[4:7], 0\n"
"s_add_u32 s52, s53, 0x2f4000\n"
"s_and_b32 s52, s52, s54\n"
"buffer_load_dwordx4 v[176:179], v1, s[28:31], s52 offen offset:0 nt\n"
"s_waitcnt vmcnt(31)\n"
"v_mfma_f32_16x16x32_bf16 v[36:39], v[212:215], v[8:11], v[36:39]\n"
"s_add_u32 s52, s53, 0x2fb000\n"
"s_and_b32 s52, s52, s54\n"
"buffer_load_dwordx4 v[180:183], v1, s[28:31], s52 offen offset:0 nt\n"
"s_waitcnt vmcnt(31)\n"
"v_mfma_f32_16x16x32_bf16 v[36:39], v[216:219], v[12:15], v[36:39]\n"
"s_add_u32 s52, s53, 0x302000\n"
"s_and_b32 s52, s52, s54\n"
"buffer_load_dwordx4 v[184:187], v1, s[28:31], s52 offen offset:0 nt\n"
"v_mov_b32_dpp v63, v32 quad_perm:[0,0,0,0] row_mask:0xf bank_mask:0xf\n"
"v_add_f32_dpp v63, v33, v63 quad_perm:[1,1,1,1] row_mask:0xf bank_mask:0xf\n"
"v_add_f32_dpp v63, v34, v63 quad_perm:[2,2,2,2] row_mask:0xf bank_mask:0xf\n"
"v_add_f32_dpp v63, v35, v63 quad_perm:[3,3,3,3] row_mask:0xf bank_mask:0xf\n"
"v_cndmask_b32_e64 v48, v48, v63, s[64:65]\n"
"s_waitcnt vmcnt(31)\n"
"v_mfma_f32_16x16x32_bf16 v[36:39], v[220:223], v[16:19], v[36:39]\n"
"s_add_u32 s52, s53, 0x309000\n"
"s_and_b32 s52, s52, s54\n"
"buffer_load_dwordx4 v[188:191], v1, s[28:31], s52 offen offset:0 nt\n"
"s_waitcnt vmcnt(31)\n"
"v_mfma_f32_16x16x32_bf16 v[32:35], v[64:67], v[4:7], 0\n"
"s_add_u32 s52, s53, 0x310000\n"
"s_and_b32 s52, s52, s54\n"
"buffer_load_dwordx4 v[192:195], v1, s[28:31], s52 offen offset:0 nt\n"
"s_waitcnt vmcnt(31)\n"
"v_mfma_f32_16x16x32_bf16 v[32:35], v[68:71], v[8:11], v[32:35]\n"
"s_add_u32 s52, s53, 0x317000\n"
"s_and_b32 s52, s52, s54\n"
"buffer_load_dwordx4 v[196:199], v1, s[28:31], s52 offen offset:0 nt\n"
"s_waitcnt vmcnt(31)\n"
"v_mfma_f32_16x16x32_bf16 v[32:35], v[72:75], v[12:15], v[32:35]\n"
"s_add_u32 s52, s53, 0x31e000\n"
"s_and_b32 s52, s52, s54\n"
"buffer_load_dwordx4 v[200:203], v1, s[28:31], s52 offen offset:0 nt\n"
"v_mov_b32_dpp v63, v36 quad_perm:[0,0,0,0] row_mask:0xf bank_mask:0xf\n"
"v_add_f32_dpp v63, v37, v63 quad_perm:[1,1,1,1] row_mask:0xf bank_mask:0xf\n"
"v_add_f32_dpp v63, v38, v63 quad_perm:[2,2,2,2] row_mask:0xf bank_mask:0xf\n"
"v_add_f32_dpp v63, v39, v63 quad_perm:[3,3,3,3] row_mask:0xf bank_mask:0xf\n"
"v_cndmask_b32_e64 v48, v48, v63, s[66:67]\n"
"s_waitcnt vmcnt(31)\n"
"v_mfma_f32_16x16x32_bf16 v[32:35], v[76:79], v[16:19], v[32:35]\n"
"s_add_u32 s52, s53, 0x325000\n"
"s_and_b32 s52, s52, s54\n"
"buffer_load_dwordx4 v[204:207], v1, s[28:31], s52 offen offset:0 nt\n"
"s_waitcnt vmcnt(31)\n"
"v_mfma_f32_16x16x32_bf16 v[36:39], v[80:83], v[4:7], 0\n"
"s_add_u32 s52, s53, 0x32c000\n"
"s_and_b32 s52, s52, s54\n"
"buffer_load_dwordx4 v[208:211], v1, s[28:31], s52 offen offset:0 nt\n"
"s_waitcnt vmcnt(31)\n"
"v_mfma_f32_16x16x32_bf16 v[36:39], v[84:87], v[8:11], v[36:39]\n"
"s_add_u32 s52, s53, 0x333000\n"
"s_and_b32 s52, s52, s54\n"
"buffer_load_dwordx4 v[212:215], v1, s[28:31], s52 offen offset:0 nt\n"
"s_waitcnt vmcnt(31)\n"
"v_mfma_f32_16x16x32_bf16 v[36:39], v[88:91], v[12:15], v[36:39]\n"
"s_add_u32 s52, s53, 0x33a000\n"
"s_and_b32 s52, s52, s54\n"
"buffer_load_dwordx4 v[216:219], v1, s[28:31], s52 offen offset:0 nt\n"
"v_mov_b32_dpp v63, v32 quad_perm:[0,0,0,0] row_mask:0xf bank_mask:0xf\n"
"v_add_f32_dpp v63, v33, v63 quad_perm:[1,1,1,1] row_mask:0xf bank_mask:0xf\n"
"v_add_f32_dpp v63, v34, v63 quad_perm:[2,2,2,2] row_mask:0xf bank_mask:0xf\n"
"v_add_f32_dpp v63, v35, v63 quad_perm:[3,3,3,3] row_mask:0xf bank_mask:0xf\n"
"v_cndmask_b32_e64 v49, v49, v63, s[60:61]\n"
"s_waitcnt vmcnt(31)\n"
"v_mfma_f32_16x16x32_bf16 v[36:39], v[92:95], v[16:19], v[36:39]\n"
"s_add_u32 s52, s53, 0x341000\n"
"s_and_b32 s52, s52, s54\n"
"buffer_load_dwordx4 v[220:223], v1, s[28:31], s52 offen offset:0 nt\n"
"s_waitcnt vmcnt(31)\n"
"v_mfma_f32_16x16x32_bf16 v[32:35], v[96:99], v[4:7], 0\n"
"s_add_u32 s52, s53, 0x348000\n"
"s_and_b32 s52, s52, s54\n"
"buffer_load_dwordx4 v[64:67], v1, s[28:31], s52 offen offset:0 nt\n"
"s_waitcnt vmcnt(31)\n"
"v_mfma_f32_16x16x32_bf16 v[32:35], v[100:103], v[8:11], v[32:35]\n"
"s_add_u32 s52, s53, 0x34f000\n"
"s_and_b32 s52, s52, s54\n"
"buffer_load_dwordx4 v[68:71], v1, s[28:31], s52 offen offset:0 nt\n"
"s_waitcnt vmcnt(31)\n"
"v_mfma_f32_16x16x32_bf16 v[32:35], v[104:107], v[12:15], v[32:35]\n"
"s_add_u32 s52, s53, 0x356000\n"
"s_and_b32 s52, s52, s54\n"
"buffer_load_dwordx4 v[72:75], v1, s[28:31], s52 offen offset:0 nt\n"
"v_mov_b32_dpp v63, v36 quad_perm:[0,0,0,0] row_mask:0xf bank_mask:0xf\n"
"v_add_f32_dpp v63, v37, v63 quad_perm:[1,1,1,1] row_mask:0xf bank_mask:0xf\n"
"v_add_f32_dpp v63, v38, v63 quad_perm:[2,2,2,2] row_mask:0xf bank_mask:0xf\n"
"v_add_f32_dpp v63, v39, v63 quad_perm:[3,3,3,3] row_mask:0xf bank_mask:0xf\n"
"v_cndmask_b32_e64 v49, v49, v63, s[62:63]\n"
"s_waitcnt vmcnt(31)\n"
"v_mfma_f32_16x16x32_bf16 v[32:35], v[108:111], v[16:19], v[32:35]\n"
"s_add_u32 s52, s53, 0x35d000\n"
"s_and_b32 s52, s52, s54\n"
"buffer_load_dwordx4 v[76:79], v1, s[28:31], s52 offen offset:0 nt\n"
"s_waitcnt vmcnt(31)\n"
"v_mfma_f32_16x16x32_bf16 v[36:39], v[112:115], v[4:7], 0\n"
"s_add_u32 s52, s53, 0x364000\n"
"s_and_b32 s52, s52, s54\n"
"buffer_load_dwordx4 v[80:83], v1, s[28:31], s52 offen offset:0 nt\n"
"s_waitcnt vmcnt(31)\n"
"v_mfma_f32_16x16x32_bf16 v[36:39], v[116:119], v[8:11], v[36:39]\n"
"s_add_u32 s52, s53, 0x36b000\n"
"s_and_b32 s52, s52, s54\n"
"buffer_load_dwordx4 v[84:87], v1, s[28:31], s52 offen offset:0 nt\n"
"s_waitcnt vmcnt(31)\n"
"v_mfma_f32_16x16x32_bf16 v[36:39], v[120:123], v[12:15], v[36:39]\n"
"s_add_u32 s52, s53, 0x372000\n"
"s_and_b32 s52, s52, s54\n"
"buffer_load_dwordx4 v[88:91], v1, s[28:31], s52 offen offset:0 nt\n"
"v_mov_b32_dpp v63, v32 quad_perm:[0,0,0,0] row_mask:0xf bank_mask:0xf\n"
"v_add_f32_dpp v63, v33, v63 quad_perm:[1,1,1,1] row_mask:0xf bank_mask:0xf\n"
"v_add_f32_dpp v63, v34, v63 quad_perm:[2,2,2,2] row_mask:0xf bank_mask:0xf\n"
"v_add_f32_dpp v63, v35, v63 quad_perm:[3,3,3,3] row_mask:0xf bank_mask:0xf\n"
"v_cndmask_b32_e64 v49, v49, v63, s[64:65]\n"
"s_waitcnt vmcnt(31)\n"
"v_mfma_f32_16x16x32_bf16 v[36:39], v[124:127], v[16:19], v[36:39]\n"
"s_add_u32 s52, s53, 0x379000\n"
"s_and_b32 s52, s52, s54\n"
"buffer_load_dwordx4 v[92:95], v1, s[28:31], s52 offen offset:0 nt\n"
"s_waitcnt vmcnt(31)\n"
"v_mfma_f32_16x16x32_bf16 v[32:35], v[128:131], v[4:7], 0\n"
"s_add_u32 s52, s53, 0x380000\n"
"s_and_b32 s52, s52, s54\n"
"buffer_load_dwordx4 v[96:99], v1, s[28:31], s52 offen offset:0 nt\n"
"s_waitcnt vmcnt(31)\n"
"v_mfma_f32_16x16x32_bf16 v[32:35], v[132:135], v[8:11], v[32:35]\n"
"s_add_u32 s52, s53, 0x387000\n"
"s_and_b32 s52, s52, s54\n"
"buffer_load_dwordx4 v[100:103], v1, s[28:31], s52 offen offset:0 nt\n"
"s_waitcnt vmcnt(31)\n"
"v_mfma_f32_16x16x32_bf16 v[32:35], v[136:139], v[12:15], v[32:35]\n"
"s_add_u32 s52, s53, 0x38e000\n"
"s_and_b32 s52, s52, s54\n"
"buffer_load_dwordx4 v[104:107], v1, s[28:31], s52 offen offset:0 nt\n"
"v_mov_b32_dpp v63, v36 quad_perm:[0,0,0,0] row_mask:0xf bank_mask:0xf\n"
"v_add_f32_dpp v63, v37, v63 quad_perm:[1,1,1,1] row_mask:0xf bank_mask:0xf\n"
"v_add_f32_dpp v63, v38, v63 quad_perm:[2,2,2,2] row_mask:0xf bank_mask:0xf\n"
"v_add_f32_dpp v63, v39, v63 quad_perm:[3,3,3,3] row_mask:0xf bank_mask:0xf\n"
"v_cndmask_b32_e64 v49, v49, v63, s[66:67]\n"
"s_waitcnt vmcnt(31)\n"
"v_mfma_f32_16x16x32_bf16 v[32:35], v[140:143], v[16:19], v[32:35]\n"
"s_add_u32 s52, s53, 0x395000\n"
"s_and_b32 s52, s52, s54\n"
"buffer_load_dwordx4 v[108:111], v1, s[28:31], s52 offen offset:0 nt\n"
"s_waitcnt vmcnt(31)\n"
"v_mfma_f32_16x16x32_bf16 v[36:39], v[144:147], v[4:7], 0\n"
"s_add_u32 s52, s53, 0x39c000\n"
"s_and_b32 s52, s52, s54\n"
"buffer_load_dwordx4 v[112:115], v1, s[28:31], s52 offen offset:0 nt\n"
"s_waitcnt vmcnt(31)\n"
"v_mfma_f32_16x16x32_bf16 v[36:39], v[148:151], v[8:11], v[36:39]\n"
"s_add_u32 s52, s53, 0x3a3000\n"
"s_and_b32 s52, s52, s54\n"
"buffer_load_dwordx4 v[116:119], v1, s[28:31], s52 offen offset:0 nt\n"
"s_waitcnt vmcnt(31)\n"
"v_mfma_f32_16x16x32_bf16 v[36:39], v[152:155], v[12:15], v[36:39]\n"
"s_add_u32 s52, s53, 0x3aa000\n"
"s_and_b32 s52, s52, s54\n"
"buffer_load_dwordx4 v[120:123], v1, s[28:31], s52 offen offset:0 nt\n"
"v_mov_b32_dpp v63, v32 quad_perm:[0,0,0,0] row_mask:0xf bank_mask:0xf\n"
"v_add_f32_dpp v63, v33, v63 quad_perm:[1,1,1,1] row_mask:0xf bank_mask:0xf\n"
"v_add_f32_dpp v63, v34, v63 quad_perm:[2,2,2,2] row_mask:0xf bank_mask:0xf\n"
"v_add_f32_dpp v63, v35, v63 quad_perm:[3,3,3,3] row_mask:0xf bank_mask:0xf\n"
"v_cndmask_b32_e64 v50, v50, v63, s[60:61]\n"
"s_waitcnt vmcnt(31)\n"
"v_mfma_f32_16x16x32_bf16 v[36:39], v[156:159], v[16:19], v[36:39]\n"
"s_add_u32 s52, s53, 0x3b1000\n"
"s_and_b32 s52, s52, s54\n"
"buffer_load_dwordx4 v[124:127], v1, s[28:31], s52 offen offset:0 nt\n"
"s_waitcnt vmcnt(31)\n"
"v_mfma_f32_16x16x32_bf16 v[32:35], v[160:163], v[4:7], 0\n"
"s_add_u32 s52, s53, 0x3b8000\n"
"s_and_b32 s52, s52, s54\n"
"buffer_load_dwordx4 v[128:131], v1, s[28:31], s52 offen offset:0 nt\n"
"s_waitcnt vmcnt(31)\n"
"v_mfma_f32_16x16x32_bf16 v[32:35], v[164:167], v[8:11], v[32:35]\n"
"s_add_u32 s52, s53, 0x3bf000\n"
"s_and_b32 s52, s52, s54\n"
"buffer_load_dwordx4 v[132:135], v1, s[28:31], s52 offen offset:0 nt\n"
"s_waitcnt vmcnt(31)\n"
"v_mfma_f32_16x16x32_bf16 v[32:35], v[168:171], v[12:15], v[32:35]\n"
"s_add_u32 s52, s53, 0x3c6000\n"
"s_and_b32 s52, s52, s54\n"
"buffer_load_dwordx4 v[136:139], v1, s[28:31], s52 offen offset:0 nt\n"
"v_mov_b32_dpp v63, v36 quad_perm:[0,0,0,0] row_mask:0xf bank_mask:0xf\n"
"v_add_f32_dpp v63, v37, v63 quad_perm:[1,1,1,1] row_mask:0xf bank_mask:0xf\n"
"v_add_f32_dpp v63, v38, v63 quad_perm:[2,2,2,2] row_mask:0xf bank_mask:0xf\n"
"v_add_f32_dpp v63, v39, v63 quad_perm:[3,3,3,3] row_mask:0xf bank_mask:0xf\n"
"v_cndmask_b32_e64 v50, v50, v63, s[62:63]\n"
"s_waitcnt vmcnt(31)\n"
"v_mfma_f32_16x16x32_bf16 v[32:35], v[172:175], v[16:19], v[32:35]\n"
"s_add_u32 s52, s53, 0x3cd000\n"
"s_and_b32 s52, s52, s54\n"
"buffer_load_dwordx4 v[140:143], v1, s[28:31], s52 offen offset:0 nt\n"
"s_waitcnt vmcnt(31)\n"
"v_mfma_f32_16x16x32_bf16 v[36:39], v[176:179], v[4:7], 0\n"
"s_add_u32 s52, s53, 0x0\n"
"s_and_b32 s52, s52, s54\n"
"buffer_load_dwordx4 v[144:147], v2, s[32:35], s52 offen offset:0 nt\n"
"s_waitcnt vmcnt(31)\n"
"v_mfma_f32_16x16x32_bf16 v[36:39], v[180:183], v[8:11], v[36:39]\n"
"s_add_u32 s52, s53, 0x7000\n"
"s_and_b32 s52, s52, s54\n"
"buffer_load_dwordx4 v[148:151], v2, s[32:35], s52 offen offset:0 nt\n"
"s_waitcnt vmcnt(31)\n"
"v_mfma_f32_16x16x32_bf16 v[36:39], v[184:187], v[12:15], v[36:39]\n"
"s_add_u32 s52, s53, 0xe000\n"
"s_and_b32 s52, s52, s54\n"
"buffer_load_dwordx4 v[152:155], v2, s[32:35], s52 offen offset:0 nt\n"
"v_mov_b32_dpp v63, v32 quad_perm:[0,0,0,0] row_mask:0xf bank_mask:0xf\n"
"v_add_f32_dpp v63, v33, v63 quad_perm:[1,1,1,1] row_mask:0xf bank_mask:0xf\n"
"v_add_f32_dpp v63, v34, v63 quad_perm:[2,2,2,2] row_mask:0xf bank_mask:0xf\n"
"v_add_f32_dpp v63, v35, v63 quad_perm:[3,3,3,3] row_mask:0xf bank_mask:0xf\n"
"v_cndmask_b32_e64 v50, v50, v63, s[64:65]\n"
"s_waitcnt vmcnt(31)\n"
"v_mfma_f32_16x16x32_bf16 v[36:39], v[188:191], v[16:19], v[36:39]\n"
"s_add_u32 s52, s53, 0x15000\n"
"s_and_b32 s52, s52, s54\n"
"buffer_load_dwordx4 v[156:159], v2, s[32:35], s52 offen offset:0 nt\n"
"s_waitcnt vmcnt(31)\n"
"v_mfma_f32_16x16x32_bf16 v[32:35], v[192:195], v[4:7], 0\n"
"s_add_u32 s52, s53, 0x1c000\n"
"s_and_b32 s52, s52, s54\n"
"buffer_load_dwordx4 v[160:163], v2, s[32:35], s52 offen offset:0 nt\n"
"s_waitcnt vmcnt(31)\n"
"v_mfma_f32_16x16x32_bf16 v[32:35], v[196:199], v[8:11], v[32:35]\n"
"s_add_u32 s52, s53, 0x23000\n"
"s_and_b32 s52, s52, s54\n"
"buffer_load_dwordx4 v[164:167], v2, s[32:35], s52 offen offset:0 nt\n"
"s_waitcnt vmcnt(31)\n"
"v_mfma_f32_16x16x32_bf16 v[32:35], v[200:203], v[12:15], v[32:35]\n"
"s_add_u32 s52, s53, 0x2a000\n"
"s_and_b32 s52, s52, s54\n"
"buffer_load_dwordx4 v[168:171], v2, s[32:35], s52 offen offset:0 nt\n"
"v_mov_b32_dpp v63, v36 quad_perm:[0,0,0,0] row_mask:0xf bank_mask:0xf\n"
"v_add_f32_dpp v63, v37, v63 quad_perm:[1,1,1,1] row_mask:0xf bank_mask:0xf\n"
"v_add_f32_dpp v63, v38, v63 quad_perm:[2,2,2,2] row_mask:0xf bank_mask:0xf\n"
"v_add_f32_dpp v63, v39, v63 quad_perm:[3,3,3,3] row_mask:0xf bank_mask:0xf\n"
"v_cndmask_b32_e64 v50, v50, v63, s[66:67]\n"
"s_waitcnt vmcnt(31)\n"
"v_mfma_f32_16x16x32_bf16 v[32:35], v[204:207], v[16:19], v[32:35]\n"
"s_add_u32 s52, s53, 0x31000\n"
"s_and_b32 s52, s52, s54\n"
"buffer_load_dwordx4 v[172:175], v2, s[32:35], s52 offen offset:0 nt\n"
"s_waitcnt vmcnt(31)\n"
"v_mfma_f32_16x16x32_bf16 v[36:39], v[208:211], v[4:7], 0\n"
"s_add_u32 s52, s53, 0x38000\n"
"s_and_b32 s52, s52, s54\n"
"buffer_load_dwordx4 v[176:179], v2, s[32:35], s52 offen offset:0 nt\n"
"s_waitcnt vmcnt(31)\n"
"v_mfma_f32_16x16x32_bf16 v[36:39], v[212:215], v[8:11], v[36:39]\n"
"s_add_u32 s52, s53, 0x3f000\n"
"s_and_b32 s52, s52, s54\n"
"buffer_load_dwordx4 v[180:183], v2, s[32:35], s52 offen offset:0 nt\n"
"s_waitcnt vmcnt(31)\n"
"v_mfma_f32_16x16x32_bf16 v[36:39], v[216:219], v[12:15], v[36:39]\n"
"s_add_u32 s52, s53, 0x46000\n"
"s_and_b32 s52, s52, s54\n"
"buffer_load_dwordx4 v[184:187], v2, s[32:35], s52 offen offset:0 nt\n"
"v_mov_b32_dpp v63, v32 quad_perm:[0,0,0,0] row_mask:0xf bank_mask:0xf\n"
"v_add_f32_dpp v63, v33, v63 quad_perm:[1,1,1,1] row_mask:0xf bank_mask:0xf\n"
"v_add_f32_dpp v63, v34, v63 quad_perm:[2,2,2,2] row_mask:0xf bank_mask:0xf\n"
"v_add_f32_dpp v63, v35, v63 quad_perm:[3,3,3,3] row_mask:0xf bank_mask:0xf\n"
"v_cndmask_b32_e64 v51, v51, v63, s[60:61]\n"
"s_waitcnt vmcnt(31)\n"
"v_mfma_f32_16x16x32_bf16 v[36:39], v[220:223], v[16:19], v[36:39]\n"
"s_add_u32 s52, s53, 0x4d000\n"
"s_and_b32 s52, s52, s54\n"
"buffer_load_dwordx4 v[188:191], v2, s[32:35], s52 offen offset:0 nt\n"
"s_waitcnt vmcnt(31)\n"
"v_mfma_f32_16x16x32_bf16 v[32:35], v[64:67], v[4:7], 0\n"
"s_add_u32 s52, s53, 0x54000\n"
"s_and_b32 s52, s52, s54\n"
"buffer_load_dwordx4 v[192:195], v2, s[32:35], s52 offen offset:0 nt\n"
"s_waitcnt vmcnt(31)\n"
"v_mfma_f32_16x16x32_bf16 v[32:35], v[68:71], v[8:11], v[32:35]\n"
"s_add_u32 s52, s53, 0x5b000\n"
"s_and_b32 s52, s52, s54\n"
"buffer_load_dwordx4 v[196:199], v2, s[32:35], s52 offen offset:0 nt\n"
"s_waitcnt vmcnt(31)\n"
"v_mfma_f32_16x16x32_bf16 v[32:35], v[72:75], v[12:15], v[32:35]\n"
"s_add_u32 s52, s53, 0x62000\n"
"s_and_b32 s52, s52, s54\n"
"buffer_load_dwordx4 v[200:203], v2, s[32:35], s52 offen offset:0 nt\n"
"v_mov_b32_dpp v63, v36 quad_perm:[0,0,0,0] row_mask:0xf bank_mask:0xf\n"
"v_add_f32_dpp v63, v37, v63 quad_perm:[1,1,1,1] row_mask:0xf bank_mask:0xf\n"
"v_add_f32_dpp v63, v38, v63 quad_perm:[2,2,2,2] row_mask:0xf bank_mask:0xf\n"
"v_add_f32_dpp v63, v39, v63 quad_perm:[3,3,3,3] row_mask:0xf bank_mask:0xf\n"
"v_cndmask_b32_e64 v51, v51, v63, s[62:63]\n"
"s_waitcnt vmcnt(31)\n"
"v_mfma_f32_16x16x32_bf16 v[32:35], v[76:79], v[16:19], v[32:35]\n"
"s_add_u32 s52, s53, 0x69000\n"
"s_and_b32 s52, s52, s54\n"
"buffer_load_dwordx4 v[204:207], v2, s[32:35], s52 offen offset:0 nt\n"
"s_waitcnt vmcnt(31)\n"
"v_mfma_f32_16x16x32_bf16 v[36:39], v[80:83], v[4:7], 0\n"
"s_add_u32 s52, s53, 0x70000\n"
"s_and_b32 s52, s52, s54\n"
"buffer_load_dwordx4 v[208:211], v2, s[32:35], s52 offen offset:0 nt\n"
"s_waitcnt vmcnt(31)\n"
"v_mfma_f32_16x16x32_bf16 v[36:39], v[84:87], v[8:11], v[36:39]\n"
"s_add_u32 s52, s53, 0x77000\n"
"s_and_b32 s52, s52, s54\n"
"buffer_load_dwordx4 v[212:215], v2, s[32:35], s52 offen offset:0 nt\n"
"s_waitcnt vmcnt(31)\n"
"v_mfma_f32_16x16x32_bf16 v[36:39], v[88:91], v[12:15], v[36:39]\n"
"s_add_u32 s52, s53, 0x7e000\n"
"s_and_b32 s52, s52, s54\n"
"buffer_load_dwordx4 v[216:219], v2, s[32:35], s52 offen offset:0 nt\n"
"v_mov_b32_dpp v63, v32 quad_perm:[0,0,0,0] row_mask:0xf bank_mask:0xf\n"
"v_add_f32_dpp v63, v33, v63 quad_perm:[1,1,1,1] row_mask:0xf bank_mask:0xf\n"
"v_add_f32_dpp v63, v34, v63 quad_perm:[2,2,2,2] row_mask:0xf bank_mask:0xf\n"
"v_add_f32_dpp v63, v35, v63 quad_perm:[3,3,3,3] row_mask:0xf bank_mask:0xf\n"
"v_cndmask_b32_e64 v51, v51, v63, s[64:65]\n"
"s_waitcnt vmcnt(31)\n"
"v_mfma_f32_16x16x32_bf16 v[36:39], v[92:95], v[16:19], v[36:39]\n"
"s_add_u32 s52, s53, 0x85000\n"
"s_and_b32 s52, s52, s54\n"
"buffer_load_dwordx4 v[220:223], v2, s[32:35], s52 offen offset:0 nt\n"
"s_waitcnt vmcnt(31)\n"
"v_mfma_f32_16x16x32_bf16 v[32:35], v[96:99], v[4:7], 0\n"
"s_add_u32 s52, s53, 0x8c000\n"
"s_and_b32 s52, s52, s54\n"
"buffer_load_dwordx4 v[64:67], v2, s[32:35], s52 offen offset:0 nt\n"
"s_waitcnt vmcnt(31)\n"
"v_mfma_f32_16x16x32_bf16 v[32:35], v[100:103], v[8:11], v[32:35]\n"
"s_add_u32 s52, s53, 0x93000\n"
"s_and_b32 s52, s52, s54\n"
"buffer_load_dwordx4 v[68:71], v2, s[32:35], s52 offen offset:0 nt\n"
"s_waitcnt vmcnt(31)\n"
"v_mfma_f32_16x16x32_bf16 v[32:35], v[104:107], v[12:15], v[32:35]\n"
"s_add_u32 s52, s53, 0x9a000\n"
"s_and_b32 s52, s52, s54\n"
"buffer_load_dwordx4 v[72:75], v2, s[32:35], s52 offen offset:0 nt\n"
"v_mov_b32_dpp v63, v36 quad_perm:[0,0,0,0] row_mask:0xf bank_mask:0xf\n"
"v_add_f32_dpp v63, v37, v63 quad_perm:[1,1,1,1] row_mask:0xf bank_mask:0xf\n"
"v_add_f32_dpp v63, v38, v63 quad_perm:[2,2,2,2] row_mask:0xf bank_mask:0xf\n"
"v_add_f32_dpp v63, v39, v63 quad_perm:[3,3,3,3] row_mask:0xf bank_mask:0xf\n"
"v_cndmask_b32_e64 v51, v51, v63, s[66:67]\n"
"s_waitcnt vmcnt(31)\n"
"v_mfma_f32_16x16x32_bf16 v[32:35], v[108:111], v[16:19], v[32:35]\n"
"s_add_u32 s52, s53, 0xa1000\n"
"s_and_b32 s52, s52, s54\n"
"buffer_load_dwordx4 v[76:79], v2, s[32:35], s52 offen offset:0 nt\n"
"s_waitcnt vmcnt(31)\n"
"v_mfma_f32_16x16x32_bf16 v[36:39], v[112:115], v[4:7], 0\n"
"s_add_u32 s52, s53, 0xa8000\n"
"s_and_b32 s52, s52, s54\n"
"buffer_load_dwordx4 v[80:83], v2, s[32:35], s52 offen offset:0 nt\n"
"s_waitcnt vmcnt(31)\n"
"v_mfma_f32_16x16x32_bf16 v[36:39], v[116:119], v[8:11], v[36:39]\n"
"s_add_u32 s52, s53, 0xaf000\n"
"s_and_b32 s52, s52, s54\n"
"buffer_load_dwordx4 v[84:87], v2, s[32:35], s52 offen offset:0 nt\n"
"s_waitcnt vmcnt(31)\n"
"v_mfma_f32_16x16x32_bf16 v[36:39], v[120:123], v[12:15], v[36:39]\n"
"s_add_u32 s52, s53, 0xb6000\n"
"s_and_b32 s52, s52, s54\n"
"buffer_load_dwordx4 v[88:91], v2, s[32:35], s52 offen offset:0 nt\n"
"v_mov_b32_dpp v63, v32 quad_perm:[0,0,0,0] row_mask:0xf bank_mask:0xf\n"
"v_add_f32_dpp v63, v33, v63 quad_perm:[1,1,1,1] row_mask:0xf bank_mask:0xf\n"
"v_add_f32_dpp v63, v34, v63 quad_perm:[2,2,2,2] row_mask:0xf bank_mask:0xf\n"
"v_add_f32_dpp v63, v35, v63 quad_perm:[3,3,3,3] row_mask:0xf bank_mask:0xf\n"
"v_cndmask_b32_e64 v52, v52, v63, s[60:61]\n"
"s_waitcnt vmcnt(31)\n"
"v_mfma_f32_16x16x32_bf16 v[36:39], v[124:127], v[16:19], v[36:39]\n"
"s_add_u32 s52, s53, 0xbd000\n"
"s_and_b32 s52, s52, s54\n"
"buffer_load_dwordx4 v[92:95], v2, s[32:35], s52 offen offset:0 nt\n"
"s_waitcnt vmcnt(31)\n"
"v_mfma_f32_16x16x32_bf16 v[32:35], v[128:131], v[4:7], 0\n"
"s_add_u32 s52, s53, 0xc4000\n"
"s_and_b32 s52, s52, s54\n"
"buffer_load_dwordx4 v[96:99], v2, s[32:35], s52 offen offset:0 nt\n"
"s_waitcnt vmcnt(31)\n"
"v_mfma_f32_16x16x32_bf16 v[32:35], v[132:135], v[8:11], v[32:35]\n"
"s_add_u32 s52, s53, 0xcb000\n"
"s_and_b32 s52, s52, s54\n"
"buffer_load_dwordx4 v[100:103], v2, s[32:35], s52 offen offset:0 nt\n"
"s_waitcnt vmcnt(31)\n"
"v_mfma_f32_16x16x32_bf16 v[32:35], v[136:139], v[12:15], v[32:35]\n"
"s_add_u32 s52, s53, 0xd2000\n"
"s_and_b32 s52, s52, s54\n"
"buffer_load_dwordx4 v[104:107], v2, s[32:35], s52 offen offset:0 nt\n"
"v_mov_b32_dpp v63, v36 quad_perm:[0,0,0,0] row_mask:0xf bank_mask:0xf\n"
"v_add_f32_dpp v63, v37, v63 quad_perm:[1,1,1,1] row_mask:0xf bank_mask:0xf\n"
"v_add_f32_dpp v63, v38, v63 quad_perm:[2,2,2,2] row_mask:0xf bank_mask:0xf\n"
"v_add_f32_dpp v63, v39, v63 quad_perm:[3,3,3,3] row_mask:0xf bank_mask:0xf\n"
"v_cndmask_b32_e64 v52, v52, v63, s[62:63]\n"
"s_waitcnt vmcnt(31)\n"
"v_mfma_f32_16x16x32_bf16 v[32:35], v[140:143], v[16:19], v[32:35]\n"
"s_add_u32 s52, s53, 0xd9000\n"
"s_and_b32 s52, s52, s54\n"
"buffer_load_dwordx4 v[108:111], v2, s[32:35], s52 offen offset:0 nt\n"
"s_nop 4\n"
"v_mov_b32_dpp v63, v32 quad_perm:[0,0,0,0] row_mask:0xf bank_mask:0xf\n"
"v_add_f32_dpp v63, v33, v63 quad_perm:[1,1,1,1] row_mask:0xf bank_mask:0xf\n"
"v_add_f32_dpp v63, v34, v63 quad_perm:[2,2,2,2] row_mask:0xf bank_mask:0xf\n"
"v_add_f32_dpp v63, v35, v63 quad_perm:[3,3,3,3] row_mask:0xf bank_mask:0xf\n"
"v_cndmask_b32_e64 v52, v52, v63, s[64:65]\n"
"v_cvt_pk_bf16_f32 v58, v44, v45\n"
"v_lshlrev_b32 v44, 16, v58\n"
"v_and_b32 v45, 0xffff0000, v58\n"
"v_mul_f32 v44, 0x3db504f3, v44\n"
"v_mul_f32 v45, 0x3db504f3, v45\n"
"v_cvt_pk_bf16_f32 v58, v44, v45\n"
"v_lshlrev_b32 v44, 16, v58\n"
"v_and_b32 v45, 0xffff0000, v58\n"
"v_cvt_pk_bf16_f32 v58, v46, v47\n"
"v_lshlrev_b32 v46, 16, v58\n"
"v_and_b32 v47, 0xffff0000, v58\n"
"v_mul_f32 v46, 0x3db504f3, v46\n"
"v_mul_f32 v47, 0x3db504f3, v47\n"
"v_cvt_pk_bf16_f32 v58, v46, v47\n"
"v_lshlrev_b32 v46, 16, v58\n"
"v_and_b32 v47, 0xffff0000, v58\n"
"v_cvt_pk_bf16_f32 v58, v48, v49\n"
"v_lshlrev_b32 v48, 16, v58\n"
"v_and_b32 v49, 0xffff0000, v58\n"
"v_mul_f32 v48, 0x3db504f3, v48\n"
"v_mul_f32 v49, 0x3db504f3, v49\n"
"v_cvt_pk_bf16_f32 v58, v48, v49\n"
"v_lshlrev_b32 v48, 16, v58\n"
"v_and_b32 v49, 0xffff0000, v58\n"
"v_cvt_pk_bf16_f32 v58, v50, v51\n"
"v_lshlrev_b32 v50, 16, v58\n"
"v_and_b32 v51, 0xffff0000, v58\n"
"v_mul_f32 v50, 0x3db504f3, v50\n"
"v_mul_f32 v51, 0x3db504f3, v51\n"
"v_cvt_pk_bf16_f32 v58, v50, v51\n"
"v_lshlrev_b32 v50, 16, v58\n"
"v_and_b32 v51, 0xffff0000, v58\n"
"v_cvt_pk_bf16_f32 v58, v52, v52\n"
"v_lshlrev_b32 v52, 16, v58\n"
"v_mul_f32 v52, 0x3db504f3, v52\n"
"v_cvt_pk_bf16_f32 v58, v52, v52\n"
"v_lshlrev_b32 v52, 16, v58\n"
"v_max_f32 v58, v44, v45\n"
"v_max3_f32 v58, v58, v46, v47\n"
"v_max3_f32 v58, v58, v48, v49\n"
"v_max3_f32 v58, v58, v50, v51\n"
"v_max_f32 v58, v58, v52\n"
"s_nop 1\n"
"v_max_f32_dpp v58, v58, v58 quad_perm:[1,0,3,2] row_mask:0xf bank_mask:0xf\n"
"s_nop 1\n"
"v_max_f32_dpp v58, v58, v58 quad_perm:[2,3,0,1] row_mask:0xf bank_mask:0xf\n"
"s_nop 1\n"
"v_max_f32_dpp v58, v58, v58 row_half_mirror row_mask:0xf bank_mask:0xf\n"
"s_nop 1\n"
"v_max_f32_dpp v58, v58, v58 row_mirror row_mask:0xf bank_mask:0xf\n"
"s_nop 1\n"
"v_max_f32_dpp v58, v58, v58 row_bcast:15 row_mask:0xa bank_mask:0xf\n"
"s_nop 1\n"
"v_max_f32_dpp v58, v58, v58 row_bcast:31 row_mask:0xc bank_mask:0xf\n"
"v_mul_f32 v58, 0x3fb8aa3b, v58\n"
"s_nop 0\n"
"v_readlane_b32 s58, v58, 63\n"
"v_mov_b32 v59, 0x3fb8aa3b\n"
"s_nop 0\n"
"v_fma_f32 v44, v44, v59, -s58\n"
"v_fma_f32 v45, v45, v59, -s58\n"
"v_fma_f32 v46, v46, v59, -s58\n"
"v_fma_f32 v47, v47, v59, -s58\n"
"v_fma_f32 v48, v48, v59, -s58\n"
"v_fma_f32 v49, v49, v59, -s58\n"
"v_fma_f32 v50, v50, v59, -s58\n"
"v_fma_f32 v51, v51, v59, -s58\n"
"v_fma_f32 v52, v52, v59, -s58\n"
"v_exp_f32 v44, v44\n"
"v_exp_f32 v45, v45\n"
"v_exp_f32 v46, v46\n"
"v_exp_f32 v47, v47\n"
"v_exp_f32 v48, v48\n"
"v_exp_f32 v49, v49\n"
"v_exp_f32 v50, v50\n"
"v_exp_f32 v51, v51\n"
"v_exp_f32 v52, v52\n"
"v_add_f32 v58, v44, v45\n"
"v_add_f32 v58, v58, v46\n"
"v_add_f32 v58, v58, v47\n"
"v_add_f32 v58, v58, v48\n"
"v_add_f32 v58, v58, v49\n"
"v_add_f32 v58, v58, v50\n"
"v_add_f32 v58, v58, v51\n"
"v_add_f32 v58, v58, v52\n"
"v_cvt_pk_bf16_f32 v44, v44, v44\n"
"v_cvt_pk_bf16_f32 v45, v45, v45\n"
"v_cvt_pk_bf16_f32 v46, v46, v46\n"
"v_cvt_pk_bf16_f32 v47, v47, v47\n"
"v_cvt_pk_bf16_f32 v48, v48, v48\n"
"v_cvt_pk_bf16_f32 v49, v49, v49\n"
"v_cvt_pk_bf16_f32 v50, v50, v50\n"
"v_cvt_pk_bf16_f32 v51, v51, v51\n"
"v_cvt_pk_bf16_f32 v52, v52, v52\n"
"v_add_f32_dpp v58, v58, v58 quad_perm:[1,0,3,2] row_mask:0xf bank_mask:0xf\n"
"s_nop 1\n"
"v_add_f32_dpp v58, v58, v58 quad_perm:[2,3,0,1] row_mask:0xf bank_mask:0xf\n"
"s_nop 1\n"
"v_add_f32_dpp v58, v58, v58 row_half_mirror row_mask:0xf bank_mask:0xf\n"
"s_nop 1\n"
"v_add_f32_dpp v58, v58, v58 row_mirror row_mask:0xf bank_mask:0xf\n"
"s_nop 1\n"
"v_add_f32_dpp v58, v58, v58 row_bcast:15 row_mask:0xa bank_mask:0xf\n"
"s_nop 1\n"
"v_add_f32_dpp v58, v58, v58 row_bcast:31 row_mask:0xc bank_mask:0xf\n"
"v_mov_b32 v61, v58\n"
"v_mov_b32 v60, s58\n"
"s_mul_i32 s56, s50, 576\n"
"s_add_u32 s56, s56, 160256\n"
"v_mov_b32 v59, s56\n"
"s_mov_b32 exec_lo, 0\n"
"s_and_b32 exec_hi, exec_hi, 0x80000000\n"
"ds_write_b64 v59, v[60:61]\n"
"s_mov_b64 exec, -1\n"
"v_and_b32_dpp v24, v44, v20 row_newbcast:0 row_mask:0xf bank_mask:0xf\n"
"v_and_b32_dpp v25, v44, v21 row_newbcast:0 row_mask:0xf bank_mask:0xf\n"
"v_and_b32_dpp v26, v44, v22 row_newbcast:0 row_mask:0xf bank_mask:0xf\n"
"v_and_b32_dpp v27, v44, v23 row_newbcast:0 row_mask:0xf bank_mask:0xf\n"
"s_waitcnt vmcnt(31)\n"
"s_nop 0\n"
"v_mfma_f32_16x16x32_bf16 v[40:43], v[24:27], v[144:147], 0\n"
"s_add_u32 s52, s53, 0xe0000\n"
"s_and_b32 s52, s52, s54\n"
"buffer_load_dwordx4 v[112:115], v2, s[32:35], s52 offen offset:0 nt\n"
"v_and_b32_dpp v24, v44, v20 row_newbcast:4 row_mask:0xf bank_mask:0xf\n"
"v_and_b32_dpp v25, v44, v21 row_newbcast:4 row_mask:0xf bank_mask:0xf\n"
"v_and_b32_dpp v26, v44, v22 row_newbcast:4 row_mask:0xf bank_mask:0xf\n"
"v_and_b32_dpp v27, v44, v23 row_newbcast:4 row_mask:0xf bank_mask:0xf\n"
"s_waitcnt vmcnt(31)\n"
"s_nop 0\n"
"v_mfma_f32_16x16x32_bf16 v[40:43], v[24:27], v[148:151], v[40:43]\n"
"s_add_u32 s52, s53, 0xe7000\n"
"s_and_b32 s52, s52, s54\n"
"buffer_load_dwordx4 v[116:119], v2, s[32:35], s52 offen offset:0 nt\n"
"v_and_b32_dpp v24, v44, v20 row_newbcast:8 row_mask:0xf bank_mask:0xf\n"
"v_and_b32_dpp v25, v44, v21 row_newbcast:8 row_mask:0xf bank_mask:0xf\n"
"v_and_b32_dpp v26, v44, v22 row_newbcast:8 row_mask:0xf bank_mask:0xf\n"
"v_and_b32_dpp v27, v44, v23 row_newbcast:8 row_mask:0xf bank_mask:0xf\n"
"s_waitcnt vmcnt(31)\n"
"s_nop 0\n"
"v_mfma_f32_16x16x32_bf16 v[40:43], v[24:27], v[152:155], v[40:43]\n"
"s_add_u32 s52, s53, 0xee000\n"
"s_and_b32 s52, s52, s54\n"
"buffer_load_dwordx4 v[120:123], v2, s[32:35], s52 offen offset:0 nt\n"
"v_and_b32_dpp v24, v44, v20 row_newbcast:12 row_mask:0xf bank_mask:0xf\n"
"v_and_b32_dpp v25, v44, v21 row_newbcast:12 row_mask:0xf bank_mask:0xf\n"
"v_and_b32_dpp v26, v44, v22 row_newbcast:12 row_mask:0xf bank_mask:0xf\n"
"v_and_b32_dpp v27, v44, v23 row_newbcast:12 row_mask:0xf bank_mask:0xf\n"
"s_waitcnt vmcnt(31)\n"
"s_nop 0\n"
"v_mfma_f32_16x16x32_bf16 v[40:43], v[24:27], v[156:159], v[40:43]\n"
"s_add_u32 s52, s53, 0xf5000\n"
"s_and_b32 s52, s52, s54\n"
"buffer_load_dwordx4 v[124:127], v2, s[32:35], s52 offen offset:0 nt\n"
"v_and_b32_dpp v24, v44, v20 row_newbcast:1 row_mask:0xf bank_mask:0xf\n"
"v_and_b32_dpp v25, v44, v21 row_newbcast:1 row_mask:0xf bank_mask:0xf\n"
"v_and_b32_dpp v26, v44, v22 row_newbcast:1 row_mask:0xf bank_mask:0xf\n"
"v_and_b32_dpp v27, v44, v23 row_newbcast:1 row_mask:0xf bank_mask:0xf\n"
"s_waitcnt vmcnt(31)\n"
"s_nop 0\n"
"v_mfma_f32_16x16x32_bf16 v[40:43], v[24:27], v[160:163], v[40:43]\n"
"s_add_u32 s52, s53, 0xfc000\n"
"s_and_b32 s52, s52, s54\n"
"buffer_load_dwordx4 v[128:131], v2, s[32:35], s52 offen offset:0 nt\n"
"v_and_b32_dpp v24, v44, v20 row_newbcast:5 row_mask:0xf bank_mask:0xf\n"
"v_and_b32_dpp v25, v44, v21 row_newbcast:5 row_mask:0xf bank_mask:0xf\n"
"v_and_b32_dpp v26, v44, v22 row_newbcast:5 row_mask:0xf bank_mask:0xf\n"
"v_and_b32_dpp v27, v44, v23 row_newbcast:5 row_mask:0xf bank_mask:0xf\n"
"s_waitcnt vmcnt(31)\n"
"s_nop 0\n"
"v_mfma_f32_16x16x32_bf16 v[40:43], v[24:27], v[164:167], v[40:43]\n"
"s_add_u32 s52, s53, 0x103000\n"
"s_and_b32 s52, s52, s54\n"
"buffer_load_dwordx4 v[132:135], v2, s[32:35], s52 offen offset:0 nt\n"
"v_and_b32_dpp v24, v44, v20 row_newbcast:9 row_mask:0xf bank_mask:0xf\n"
"v_and_b32_dpp v25, v44, v21 row_newbcast:9 row_mask:0xf bank_mask:0xf\n"
"v_and_b32_dpp v26, v44, v22 row_newbcast:9 row_mask:0xf bank_mask:0xf\n"
"v_and_b32_dpp v27, v44, v23 row_newbcast:9 row_mask:0xf bank_mask:0xf\n"
"s_waitcnt vmcnt(31)\n"
"s_nop 0\n"
"v_mfma_f32_16x16x32_bf16 v[40:43], v[24:27], v[168:171], v[40:43]\n"
"s_add_u32 s52, s53, 0x10a000\n"
"s_and_b32 s52, s52, s54\n"
"buffer_load_dwordx4 v[136:139], v2, s[32:35], s52 offen offset:0 nt\n"
"v_and_b32_dpp v24, v44, v20 row_newbcast:13 row_mask:0xf bank_mask:0xf\n"
"v_and_b32_dpp v25, v44, v21 row_newbcast:13 row_mask:0xf bank_mask:0xf\n"
"v_and_b32_dpp v26, v44, v22 row_newbcast:13 row_mask:0xf bank_mask:0xf\n"
"v_and_b32_dpp v27, v44, v23 row_newbcast:13 row_mask:0xf bank_mask:0xf\n"
"s_waitcnt vmcnt(31)\n"
"s_nop 0\n"
"v_mfma_f32_16x16x32_bf16 v[40:43], v[24:27], v[172:175], v[40:43]\n"
"s_add_u32 s52, s53, 0x111000\n"
"s_and_b32 s52, s52, s54\n"
"buffer_load_dwordx4 v[140:143], v2, s[32:35], s52 offen offset:0 nt\n"
"v_and_b32_dpp v24, v44, v20 row_newbcast:2 row_mask:0xf bank_mask:0xf\n"
"v_and_b32_dpp v25, v44, v21 row_newbcast:2 row_mask:0xf bank_mask:0xf\n"
"v_and_b32_dpp v26, v44, v22 row_newbcast:2 row_mask:0xf bank_mask:0xf\n"
"v_and_b32_dpp v27, v44, v23 row_newbcast:2 row_mask:0xf bank_mask:0xf\n"
"s_waitcnt vmcnt(31)\n"
"s_nop 0\n"
"v_mfma_f32_16x16x32_bf16 v[40:43], v[24:27], v[176:179], v[40:43]\n"
"s_add_u32 s52, s53, 0x118000\n"
"s_and_b32 s52, s52, s54\n"
"buffer_load_dwordx4 v[144:147], v2, s[32:35], s52 offen offset:0 nt\n"
"v_and_b32_dpp v24, v44, v20 row_newbcast:6 row_mask:0xf bank_mask:0xf\n"
"v_and_b32_dpp v25, v44, v21 row_newbcast:6 row_mask:0xf bank_mask:0xf\n"
"v_and_b32_dpp v26, v44, v22 row_newbcast:6 row_mask:0xf bank_mask:0xf\n"
"v_and_b32_dpp v27, v44, v23 row_newbcast:6 row_mask:0xf bank_mask:0xf\n"
"s_waitcnt vmcnt(31)\n"
"s_nop 0\n"
"v_mfma_f32_16x16x32_bf16 v[40:43], v[24:27], v[180:183], v[40:43]\n"
"s_add_u32 s52, s53, 0x11f000\n"
"s_and_b32 s52, s52, s54\n"
"buffer_load_dwordx4 v[148:151], v2, s[32:35], s52 offen offset:0 nt\n"
"v_and_b32_dpp v24, v44, v20 row_newbcast:10 row_mask:0xf bank_mask:0xf\n"
"v_and_b32_dpp v25, v44, v21 row_newbcast:10 row_mask:0xf bank_mask:0xf\n"
"v_and_b32_dpp v26, v44, v22 row_newbcast:10 row_mask:0xf bank_mask:0xf\n"
"v_and_b32_dpp v27, v44, v23 row_newbcast:10 row_mask:0xf bank_mask:0xf\n"
"s_waitcnt vmcnt(31)\n"
"s_nop 0\n"
"v_mfma_f32_16x16x32_bf16 v[40:43], v[24:27], v[184:187], v[40:43]\n"
"s_add_u32 s52, s53, 0x126000\n"
"s_and_b32 s52, s52, s54\n"
"buffer_load_dwordx4 v[152:155], v2, s[32:35], s52 offen offset:0 nt\n"
"v_and_b32_dpp v24, v44, v20 row_newbcast:14 row_mask:0xf bank_mask:0xf\n"
"v_and_b32_dpp v25, v44, v21 row_newbcast:14 row_mask:0xf bank_mask:0xf\n"
"v_and_b32_dpp v26, v44, v22 row_newbcast:14 row_mask:0xf bank_mask:0xf\n"
"v_and_b32_dpp v27, v44, v23 row_newbcast:14 row_mask:0xf bank_mask:0xf\n"
"s_waitcnt vmcnt(31)\n"
"s_nop 0\n"
"v_mfma_f32_16x16x32_bf16 v[40:43], v[24:27], v[188:191], v[40:43]\n"
"s_add_u32 s52, s53, 0x12d000\n"
"s_and_b32 s52, s52, s54\n"
"buffer_load_dwordx4 v[156:159], v2, s[32:35], s52 offen offset:0 nt\n"
"v_and_b32_dpp v24, v44, v20 row_newbcast:3 row_mask:0xf bank_mask:0xf\n"
"v_and_b32_dpp v25, v44, v21 row_newbcast:3 row_mask:0xf bank_mask:0xf\n"
"v_and_b32_dpp v26, v44, v22 row_newbcast:3 row_mask:0xf bank_mask:0xf\n"
"v_and_b32_dpp v27, v44, v23 row_newbcast:3 row_mask:0xf bank_mask:0xf\n"
"s_waitcnt vmcnt(31)\n"
"s_nop 0\n"
"v_mfma_f32_16x16x32_bf16 v[40:43], v[24:27], v[192:195], v[40:43]\n"
"s_add_u32 s52, s53, 0x134000\n"
"s_and_b32 s52, s52, s54\n"
"buffer_load_dwordx4 v[160:163], v2, s[32:35], s52 offen offset:0 nt\n"
"v_and_b32_dpp v24, v44, v20 row_newbcast:7 row_mask:0xf bank_mask:0xf\n"
"v_and_b32_dpp v25, v44, v21 row_newbcast:7 row_mask:0xf bank_mask:0xf\n"
"v_and_b32_dpp v26, v44, v22 row_newbcast:7 row_mask:0xf bank_mask:0xf\n"
"v_and_b32_dpp v27, v44, v23 row_newbcast:7 row_mask:0xf bank_mask:0xf\n"
"s_waitcnt vmcnt(31)\n"
"s_nop 0\n"
"v_mfma_f32_16x16x32_bf16 v[40:43], v[24:27], v[196:199], v[40:43]\n"
"s_add_u32 s52, s53, 0x13b000\n"
"s_and_b32 s52, s52, s54\n"
"buffer_load_dwordx4 v[164:167], v2, s[32:35], s52 offen offset:0 nt\n"
"v_and_b32_dpp v24, v44, v20 row_newbcast:11 row_mask:0xf bank_mask:0xf\n"
"v_and_b32_dpp v25, v44, v21 row_newbcast:11 row_mask:0xf bank_mask:0xf\n"
"v_and_b32_dpp v26, v44, v22 row_newbcast:11 row_mask:0xf bank_mask:0xf\n"
"v_and_b32_dpp v27, v44, v23 row_newbcast:11 row_mask:0xf bank_mask:0xf\n"
"s_waitcnt vmcnt(31)\n"
"s_nop 0\n"
"v_mfma_f32_16x16x32_bf16 v[40:43], v[24:27], v[200:203], v[40:43]\n"
"s_add_u32 s52, s53, 0x142000\n"
"s_and_b32 s52, s52, s54\n"
"buffer_load_dwordx4 v[168:171], v2, s[32:35], s52 offen offset:0 nt\n"
"v_and_b32_dpp v24, v44, v20 row_newbcast:15 row_mask:0xf bank_mask:0xf\n"
"v_and_b32_dpp v25, v44, v21 row_newbcast:15 row_mask:0xf bank_mask:0xf\n"
"v_and_b32_dpp v26, v44, v22 row_newbcast:15 row_mask:0xf bank_mask:0xf\n"
"v_and_b32_dpp v27, v44, v23 row_newbcast:15 row_mask:0xf bank_mask:0xf\n"
"s_waitcnt vmcnt(31)\n"
"s_nop 0\n"
"v_mfma_f32_16x16x32_bf16 v[40:43], v[24:27], v[204:207], v[40:43]\n"
"s_add_u32 s52, s53, 0x149000\n"
"s_and_b32 s52, s52, s54\n"
"buffer_load_dwordx4 v[172:175], v2, s[32:35], s52 offen offset:0 nt\n"
"v_and_b32_dpp v24, v45, v20 row_newbcast:0 row_mask:0xf bank_mask:0xf\n"
"v_and_b32_dpp v25, v45, v21 row_newbcast:0 row_mask:0xf bank_mask:0xf\n"
"v_and_b32_dpp v26, v45, v22 row_newbcast:0 row_mask:0xf bank_mask:0xf\n"
"v_and_b32_dpp v27, v45, v23 row_newbcast:0 row_mask:0xf bank_mask:0xf\n"
"s_waitcnt vmcnt(31)\n"
"s_nop 0\n"
"v_mfma_f32_16x16x32_bf16 v[40:43], v[24:27], v[208:211], v[40:43]\n"
"s_add_u32 s52, s53, 0x150000\n"
"s_and_b32 s52, s52, s54\n"
"buffer_load_dwordx4 v[176:179], v2, s[32:35], s52 offen offset:0 nt\n"
"v_and_b32_dpp v24, v45, v20 row_newbcast:4 row_mask:0xf bank_mask:0xf\n"
"v_and_b32_dpp v25, v45, v21 row_newbcast:4 row_mask:0xf bank_mask:0xf\n"
"v_and_b32_dpp v26, v45, v22 row_newbcast:4 row_mask:0xf bank_mask:0xf\n"
"v_and_b32_dpp v27, v45, v23 row_newbcast:4 row_mask:0xf bank_mask:0xf\n"
"s_waitcnt vmcnt(31)\n"
"s_nop 0\n"
"v_mfma_f32_16x16x32_bf16 v[40:43], v[24:27], v[212:215], v[40:43]\n"
"s_add_u32 s52, s53, 0x157000\n"
"s_and_b32 s52, s52, s54\n"
"buffer_load_dwordx4 v[180:183], v2, s[32:35], s52 offen offset:0 nt\n"
"v_and_b32_dpp v24, v45, v20 row_newbcast:8 row_mask:0xf bank_mask:0xf\n"
"v_and_b32_dpp v25, v45, v21 row_newbcast:8 row_mask:0xf bank_mask:0xf\n"
"v_and_b32_dpp v26, v45, v22 row_newbcast:8 row_mask:0xf bank_mask:0xf\n"
"v_and_b32_dpp v27, v45, v23 row_newbcast:8 row_mask:0xf bank_mask:0xf\n"
"s_waitcnt vmcnt(31)\n"
"s_nop 0\n"
"v_mfma_f32_16x16x32_bf16 v[40:43], v[24:27], v[216:219], v[40:43]\n"
"s_add_u32 s52, s53, 0x15e000\n"
"s_and_b32 s52, s52, s54\n"
"buffer_load_dwordx4 v[184:187], v2, s[32:35], s52 offen offset:0 nt\n"
"v_and_b32_dpp v24, v45, v20 row_newbcast:12 row_mask:0xf bank_mask:0xf\n"
"v_and_b32_dpp v25, v45, v21 row_newbcast:12 row_mask:0xf bank_mask:0xf\n"
"v_and_b32_dpp v26, v45, v22 row_newbcast:12 row_mask:0xf bank_mask:0xf\n"
"v_and_b32_dpp v27, v45, v23 row_newbcast:12 row_mask:0xf bank_mask:0xf\n"
"s_waitcnt vmcnt(31)\n"
"s_nop 0\n"
"v_mfma_f32_16x16x32_bf16 v[40:43], v[24:27], v[220:223], v[40:43]\n"
"s_add_u32 s52, s53, 0x165000\n"
"s_and_b32 s52, s52, s54\n"
"buffer_load_dwordx4 v[188:191], v2, s[32:35], s52 offen offset:0 nt\n"
"v_and_b32_dpp v24, v45, v20 row_newbcast:1 row_mask:0xf bank_mask:0xf\n"
"v_and_b32_dpp v25, v45, v21 row_newbcast:1 row_mask:0xf bank_mask:0xf\n"
"v_and_b32_dpp v26, v45, v22 row_newbcast:1 row_mask:0xf bank_mask:0xf\n"
"v_and_b32_dpp v27, v45, v23 row_newbcast:1 row_mask:0xf bank_mask:0xf\n"
"s_waitcnt vmcnt(31)\n"
"s_nop 0\n"
"v_mfma_f32_16x16x32_bf16 v[40:43], v[24:27], v[64:67], v[40:43]\n"
"s_add_u32 s52, s53, 0x16c000\n"
"s_and_b32 s52, s52, s54\n"
"buffer_load_dwordx4 v[192:195], v2, s[32:35], s52 offen offset:0 nt\n"
"v_and_b32_dpp v24, v45, v20 row_newbcast:5 row_mask:0xf bank_mask:0xf\n"
"v_and_b32_dpp v25, v45, v21 row_newbcast:5 row_mask:0xf bank_mask:0xf\n"
"v_and_b32_dpp v26, v45, v22 row_newbcast:5 row_mask:0xf bank_mask:0xf\n"
"v_and_b32_dpp v27, v45, v23 row_newbcast:5 row_mask:0xf bank_mask:0xf\n"
"s_waitcnt vmcnt(31)\n"
"s_nop 0\n"
"v_mfma_f32_16x16x32_bf16 v[40:43], v[24:27], v[68:71], v[40:43]\n"
"s_add_u32 s52, s53, 0x173000\n"
"s_and_b32 s52, s52, s54\n"
"buffer_load_dwordx4 v[196:199], v2, s[32:35], s52 offen offset:0 nt\n"
"v_and_b32_dpp v24, v45, v20 row_newbcast:9 row_mask:0xf bank_mask:0xf\n"
"v_and_b32_dpp v25, v45, v21 row_newbcast:9 row_mask:0xf bank_mask:0xf\n"
"v_and_b32_dpp v26, v45, v22 row_newbcast:9 row_mask:0xf bank_mask:0xf\n"
"v_and_b32_dpp v27, v45, v23 row_newbcast:9 row_mask:0xf bank_mask:0xf\n"
"s_waitcnt vmcnt(31)\n"
"s_nop 0\n"
"v_mfma_f32_16x16x32_bf16 v[40:43], v[24:27], v[72:75], v[40:43]\n"
"s_add_u32 s52, s53, 0x17a000\n"
"s_and_b32 s52, s52, s54\n"
"buffer_load_dwordx4 v[200:203], v2, s[32:35], s52 offen offset:0 nt\n"
"v_and_b32_dpp v24, v45, v20 row_newbcast:13 row_mask:0xf bank_mask:0xf\n"
"v_and_b32_dpp v25, v45, v21 row_newbcast:13 row_mask:0xf bank_mask:0xf\n"
"v_and_b32_dpp v26, v45, v22 row_newbcast:13 row_mask:0xf bank_mask:0xf\n"
"v_and_b32_dpp v27, v45, v23 row_newbcast:13 row_mask:0xf bank_mask:0xf\n"
"s_waitcnt vmcnt(31)\n"
"s_nop 0\n"
"v_mfma_f32_16x16x32_bf16 v[40:43], v[24:27], v[76:79], v[40:43]\n"
"s_add_u32 s52, s53, 0x181000\n"
"s_and_b32 s52, s52, s54\n"
"buffer_load_dwordx4 v[204:207], v2, s[32:35], s52 offen offset:0 nt\n"
"v_and_b32_dpp v24, v45, v20 row_newbcast:2 row_mask:0xf bank_mask:0xf\n"
"v_and_b32_dpp v25, v45, v21 row_newbcast:2 row_mask:0xf bank_mask:0xf\n"
"v_and_b32_dpp v26, v45, v22 row_newbcast:2 row_mask:0xf bank_mask:0xf\n"
"v_and_b32_dpp v27, v45, v23 row_newbcast:2 row_mask:0xf bank_mask:0xf\n"
"s_waitcnt vmcnt(31)\n"
"s_nop 0\n"
"v_mfma_f32_16x16x32_bf16 v[40:43], v[24:27], v[80:83], v[40:43]\n"
"s_add_u32 s52, s53, 0x188000\n"
"s_and_b32 s52, s52, s54\n"
"buffer_load_dwordx4 v[208:211], v2, s[32:35], s52 offen offset:0 nt\n"
"v_and_b32_dpp v24, v45, v20 row_newbcast:6 row_mask:0xf bank_mask:0xf\n"
"v_and_b32_dpp v25, v45, v21 row_newbcast:6 row_mask:0xf bank_mask:0xf\n"
"v_and_b32_dpp v26, v45, v22 row_newbcast:6 row_mask:0xf bank_mask:0xf\n"
"v_and_b32_dpp v27, v45, v23 row_newbcast:6 row_mask:0xf bank_mask:0xf\n"
"s_waitcnt vmcnt(31)\n"
"s_nop 0\n"
"v_mfma_f32_16x16x32_bf16 v[40:43], v[24:27], v[84:87], v[40:43]\n"
"s_add_u32 s52, s53, 0x18f000\n"
"s_and_b32 s52, s52, s54\n"
"buffer_load_dwordx4 v[212:215], v2, s[32:35], s52 offen offset:0 nt\n"
"v_and_b32_dpp v24, v45, v20 row_newbcast:10 row_mask:0xf bank_mask:0xf\n"
"v_and_b32_dpp v25, v45, v21 row_newbcast:10 row_mask:0xf bank_mask:0xf\n"
"v_and_b32_dpp v26, v45, v22 row_newbcast:10 row_mask:0xf bank_mask:0xf\n"
"v_and_b32_dpp v27, v45, v23 row_newbcast:10 row_mask:0xf bank_mask:0xf\n"
"s_waitcnt vmcnt(31)\n"
"s_nop 0\n"
"v_mfma_f32_16x16x32_bf16 v[40:43], v[24:27], v[88:91], v[40:43]\n"
"s_add_u32 s52, s53, 0x196000\n"
"s_and_b32 s52, s52, s54\n"
"buffer_load_dwordx4 v[216:219], v2, s[32:35], s52 offen offset:0 nt\n"
"v_and_b32_dpp v24, v45, v20 row_newbcast:14 row_mask:0xf bank_mask:0xf\n"
"v_and_b32_dpp v25, v45, v21 row_newbcast:14 row_mask:0xf bank_mask:0xf\n"
"v_and_b32_dpp v26, v45, v22 row_newbcast:14 row_mask:0xf bank_mask:0xf\n"
"v_and_b32_dpp v27, v45, v23 row_newbcast:14 row_mask:0xf bank_mask:0xf\n"
"s_waitcnt vmcnt(31)\n"
"s_nop 0\n"
"v_mfma_f32_16x16x32_bf16 v[40:43], v[24:27], v[92:95], v[40:43]\n"
"s_add_u32 s52, s53, 0x19d000\n"
"s_and_b32 s52, s52, s54\n"
"buffer_load_dwordx4 v[220:223], v2, s[32:35], s52 offen offset:0 nt\n"
"v_and_b32_dpp v24, v45, v20 row_newbcast:3 row_mask:0xf bank_mask:0xf\n"
"v_and_b32_dpp v25, v45, v21 row_newbcast:3 row_mask:0xf bank_mask:0xf\n"
"v_and_b32_dpp v26, v45, v22 row_newbcast:3 row_mask:0xf bank_mask:0xf\n"
"v_and_b32_dpp v27, v45, v23 row_newbcast:3 row_mask:0xf bank_mask:0xf\n"
"s_waitcnt vmcnt(31)\n"
"s_nop 0\n"
"v_mfma_f32_16x16x32_bf16 v[40:43], v[24:27], v[96:99], v[40:43]\n"
"s_add_u32 s52, s53, 0x1a4000\n"
"s_and_b32 s52, s52, s54\n"
"buffer_load_dwordx4 v[64:67], v2, s[32:35], s52 offen offset:0 nt\n"
"v_and_b32_dpp v24, v45, v20 row_newbcast:7 row_mask:0xf bank_mask:0xf\n"
"v_and_b32_dpp v25, v45, v21 row_newbcast:7 row_mask:0xf bank_mask:0xf\n"
"v_and_b32_dpp v26, v45, v22 row_newbcast:7 row_mask:0xf bank_mask:0xf\n"
"v_and_b32_dpp v27, v45, v23 row_newbcast:7 row_mask:0xf bank_mask:0xf\n"
"s_waitcnt vmcnt(31)\n"
"s_nop 0\n"
"v_mfma_f32_16x16x32_bf16 v[40:43], v[24:27], v[100:103], v[40:43]\n"
"s_add_u32 s52, s53, 0x1ab000\n"
"s_and_b32 s52, s52, s54\n"
"buffer_load_dwordx4 v[68:71], v2, s[32:35], s52 offen offset:0 nt\n"
"v_and_b32_dpp v24, v45, v20 row_newbcast:11 row_mask:0xf bank_mask:0xf\n"
"v_and_b32_dpp v25, v45, v21 row_newbcast:11 row_mask:0xf bank_mask:0xf\n"
"v_and_b32_dpp v26, v45, v22 row_newbcast:11 row_mask:0xf bank_mask:0xf\n"
"v_and_b32_dpp v27, v45, v23 row_newbcast:11 row_mask:0xf bank_mask:0xf\n"
"s_waitcnt vmcnt(31)\n"
"s_nop 0\n"
"v_mfma_f32_16x16x32_bf16 v[40:43], v[24:27], v[104:107], v[40:43]\n"
"s_add_u32 s52, s53, 0x1b2000\n"
"s_and_b32 s52, s52, s54\n"
"buffer_load_dwordx4 v[72:75], v2, s[32:35], s52 offen offset:0 nt\n"
"v_and_b32_dpp v24, v45, v20 row_newbcast:15 row_mask:0xf bank_mask:0xf\n"
"v_and_b32_dpp v25, v45, v21 row_newbcast:15 row_mask:0xf bank_mask:0xf\n"
"v_and_b32_dpp v26, v45, v22 row_newbcast:15 row_mask:0xf bank_mask:0xf\n"
"v_and_b32_dpp v27, v45, v23 row_newbcast:15 row_mask:0xf bank_mask:0xf\n"
"s_waitcnt vmcnt(31)\n"
"s_nop 0\n"
"v_mfma_f32_16x16x32_bf16 v[40:43], v[24:27], v[108:111], v[40:43]\n"
"s_add_u32 s52, s53, 0x1b9000\n"
"s_and_b32 s52, s52, s54\n"
"buffer_load_dwordx4 v[76:79], v2, s[32:35], s52 offen offset:0 nt\n"
"v_and_b32_dpp v24, v46, v20 row_newbcast:0 row_mask:0xf bank_mask:0xf\n"
"v_and_b32_dpp v25, v46, v21 row_newbcast:0 row_mask:0xf bank_mask:0xf\n"
"v_and_b32_dpp v26, v46, v22 row_newbcast:0 row_mask:0xf bank_mask:0xf\n"
"v_and_b32_dpp v27, v46, v23 row_newbcast:0 row_mask:0xf bank_mask:0xf\n"
"s_waitcnt vmcnt(31)\n"
"s_nop 0\n"
"v_mfma_f32_16x16x32_bf16 v[40:43], v[24:27], v[112:115], v[40:43]\n"
"s_add_u32 s52, s53, 0x1c0000\n"
"s_and_b32 s52, s52, s54\n"
"buffer_load_dwordx4 v[80:83], v2, s[32:35], s52 offen offset:0 nt\n"
"v_and_b32_dpp v24, v46, v20 row_newbcast:4 row_mask:0xf bank_mask:0xf\n"
"v_and_b32_dpp v25, v46, v21 row_newbcast:4 row_mask:0xf bank_mask:0xf\n"
"v_and_b32_dpp v26, v46, v22 row_newbcast:4 row_mask:0xf bank_mask:0xf\n"
"v_and_b32_dpp v27, v46, v23 row_newbcast:4 row_mask:0xf bank_mask:0xf\n"
"s_waitcnt vmcnt(31)\n"
"s_nop 0\n"
"v_mfma_f32_16x16x32_bf16 v[40:43], v[24:27], v[116:119], v[40:43]\n"
"s_add_u32 s52, s53, 0x1c7000\n"
"s_and_b32 s52, s52, s54\n"
"buffer_load_dwordx4 v[84:87], v2, s[32:35], s52 offen offset:0 nt\n"
"v_and_b32_dpp v24, v46, v20 row_newbcast:8 row_mask:0xf bank_mask:0xf\n"
"v_and_b32_dpp v25, v46, v21 row_newbcast:8 row_mask:0xf bank_mask:0xf\n"
"v_and_b32_dpp v26, v46, v22 row_newbcast:8 row_mask:0xf bank_mask:0xf\n"
"v_and_b32_dpp v27, v46, v23 row_newbcast:8 row_mask:0xf bank_mask:0xf\n"
"s_waitcnt vmcnt(31)\n"
"s_nop 0\n"
"v_mfma_f32_16x16x32_bf16 v[40:43], v[24:27], v[120:123], v[40:43]\n"
"s_add_u32 s52, s53, 0x1ce000\n"
"s_and_b32 s52, s52, s54\n"
"buffer_load_dwordx4 v[88:91], v2, s[32:35], s52 offen offset:0 nt\n"
"v_and_b32_dpp v24, v46, v20 row_newbcast:12 row_mask:0xf bank_mask:0xf\n"
"v_and_b32_dpp v25, v46, v21 row_newbcast:12 row_mask:0xf bank_mask:0xf\n"
"v_and_b32_dpp v26, v46, v22 row_newbcast:12 row_mask:0xf bank_mask:0xf\n"
"v_and_b32_dpp v27, v46, v23 row_newbcast:12 row_mask:0xf bank_mask:0xf\n"
"s_waitcnt vmcnt(31)\n"
"s_nop 0\n"
"v_mfma_f32_16x16x32_bf16 v[40:43], v[24:27], v[124:127], v[40:43]\n"
"s_add_u32 s52, s53, 0x1d5000\n"
"s_and_b32 s52, s52, s54\n"
"buffer_load_dwordx4 v[92:95], v2, s[32:35], s52 offen offset:0 nt\n"
"v_and_b32_dpp v24, v46, v20 row_newbcast:1 row_mask:0xf bank_mask:0xf\n"
"v_and_b32_dpp v25, v46, v21 row_newbcast:1 row_mask:0xf bank_mask:0xf\n"
"v_and_b32_dpp v26, v46, v22 row_newbcast:1 row_mask:0xf bank_mask:0xf\n"
"v_and_b32_dpp v27, v46, v23 row_newbcast:1 row_mask:0xf bank_mask:0xf\n"
"s_waitcnt vmcnt(31)\n"
"s_nop 0\n"
"v_mfma_f32_16x16x32_bf16 v[40:43], v[24:27], v[128:131], v[40:43]\n"
"s_add_u32 s52, s53, 0x1dc000\n"
"s_and_b32 s52, s52, s54\n"
"buffer_load_dwordx4 v[96:99], v2, s[32:35], s52 offen offset:0 nt\n"
"v_and_b32_dpp v24, v46, v20 row_newbcast:5 row_mask:0xf bank_mask:0xf\n"
"v_and_b32_dpp v25, v46, v21 row_newbcast:5 row_mask:0xf bank_mask:0xf\n"
"v_and_b32_dpp v26, v46, v22 row_newbcast:5 row_mask:0xf bank_mask:0xf\n"
"v_and_b32_dpp v27, v46, v23 row_newbcast:5 row_mask:0xf bank_mask:0xf\n"
"s_waitcnt vmcnt(31)\n"
"s_nop 0\n"
"v_mfma_f32_16x16x32_bf16 v[40:43], v[24:27], v[132:135], v[40:43]\n"
"s_add_u32 s52, s53, 0x1e3000\n"
"s_and_b32 s52, s52, s54\n"
"buffer_load_dwordx4 v[100:103], v2, s[32:35], s52 offen offset:0 nt\n"
"v_and_b32_dpp v24, v46, v20 row_newbcast:9 row_mask:0xf bank_mask:0xf\n"
"v_and_b32_dpp v25, v46, v21 row_newbcast:9 row_mask:0xf bank_mask:0xf\n"
"v_and_b32_dpp v26, v46, v22 row_newbcast:9 row_mask:0xf bank_mask:0xf\n"
"v_and_b32_dpp v27, v46, v23 row_newbcast:9 row_mask:0xf bank_mask:0xf\n"
"s_waitcnt vmcnt(31)\n"
"s_nop 0\n"
"v_mfma_f32_16x16x32_bf16 v[40:43], v[24:27], v[136:139], v[40:43]\n"
"s_add_u32 s52, s53, 0x1ea000\n"
"s_and_b32 s52, s52, s54\n"
"buffer_load_dwordx4 v[104:107], v2, s[32:35], s52 offen offset:0 nt\n"
"v_and_b32_dpp v24, v46, v20 row_newbcast:13 row_mask:0xf bank_mask:0xf\n"
"v_and_b32_dpp v25, v46, v21 row_newbcast:13 row_mask:0xf bank_mask:0xf\n"
"v_and_b32_dpp v26, v46, v22 row_newbcast:13 row_mask:0xf bank_mask:0xf\n"
"v_and_b32_dpp v27, v46, v23 row_newbcast:13 row_mask:0xf bank_mask:0xf\n"
"s_waitcnt vmcnt(31)\n"
"s_nop 0\n"
"v_mfma_f32_16x16x32_bf16 v[40:43], v[24:27], v[140:143], v[40:43]\n"
"s_add_u32 s52, s53, 0x1f1000\n"
"s_and_b32 s52, s52, s54\n"
"buffer_load_dwordx4 v[108:111], v2, s[32:35], s52 offen offset:0 nt\n"
"v_and_b32_dpp v24, v46, v20 row_newbcast:2 row_mask:0xf bank_mask:0xf\n"
"v_and_b32_dpp v25, v46, v21 row_newbcast:2 row_mask:0xf bank_mask:0xf\n"
"v_and_b32_dpp v26, v46, v22 row_newbcast:2 row_mask:0xf bank_mask:0xf\n"
"v_and_b32_dpp v27, v46, v23 row_newbcast:2 row_mask:0xf bank_mask:0xf\n"
"s_waitcnt vmcnt(31)\n"
"s_nop 0\n"
"v_mfma_f32_16x16x32_bf16 v[40:43], v[24:27], v[144:147], v[40:43]\n"
"s_add_u32 s52, s53, 0x1f8000\n"
"s_and_b32 s52, s52, s54\n"
"buffer_load_dwordx4 v[112:115], v2, s[32:35], s52 offen offset:0 nt\n"
"v_and_b32_dpp v24, v46, v20 row_newbcast:6 row_mask:0xf bank_mask:0xf\n"
"v_and_b32_dpp v25, v46, v21 row_newbcast:6 row_mask:0xf bank_mask:0xf\n"
"v_and_b32_dpp v26, v46, v22 row_newbcast:6 row_mask:0xf bank_mask:0xf\n"
"v_and_b32_dpp v27, v46, v23 row_newbcast:6 row_mask:0xf bank_mask:0xf\n"
"s_waitcnt vmcnt(31)\n"
"s_nop 0\n"
"v_mfma_f32_16x16x32_bf16 v[40:43], v[24:27], v[148:151], v[40:43]\n"
"s_add_u32 s52, s53, 0x1ff000\n"
"s_and_b32 s52, s52, s54\n"
"buffer_load_dwordx4 v[116:119], v2, s[32:35], s52 offen offset:0 nt\n"
"v_and_b32_dpp v24, v46, v20 row_newbcast:10 row_mask:0xf bank_mask:0xf\n"
"v_and_b32_dpp v25, v46, v21 row_newbcast:10 row_mask:0xf bank_mask:0xf\n"
"v_and_b32_dpp v26, v46, v22 row_newbcast:10 row_mask:0xf bank_mask:0xf\n"
"v_and_b32_dpp v27, v46, v23 row_newbcast:10 row_mask:0xf bank_mask:0xf\n"
"s_waitcnt vmcnt(31)\n"
"s_nop 0\n"
"v_mfma_f32_16x16x32_bf16 v[40:43], v[24:27], v[152:155], v[40:43]\n"
"s_add_u32 s52, s53, 0x206000\n"
"s_and_b32 s52, s52, s54\n"
"buffer_load_dwordx4 v[120:123], v2, s[32:35], s52 offen offset:0 nt\n"
"v_and_b32_dpp v24, v46, v20 row_newbcast:14 row_mask:0xf bank_mask:0xf\n"
"v_and_b32_dpp v25, v46, v21 row_newbcast:14 row_mask:0xf bank_mask:0xf\n"
"v_and_b32_dpp v26, v46, v22 row_newbcast:14 row_mask:0xf bank_mask:0xf\n"
"v_and_b32_dpp v27, v46, v23 row_newbcast:14 row_mask:0xf bank_mask:0xf\n"
"s_waitcnt vmcnt(31)\n"
"s_nop 0\n"
"v_mfma_f32_16x16x32_bf16 v[40:43], v[24:27], v[156:159], v[40:43]\n"
"s_add_u32 s52, s53, 0x20d000\n"
"s_and_b32 s52, s52, s54\n"
"buffer_load_dwordx4 v[124:127], v2, s[32:35], s52 offen offset:0 nt\n"
"v_and_b32_dpp v24, v46, v20 row_newbcast:3 row_mask:0xf bank_mask:0xf\n"
"v_and_b32_dpp v25, v46, v21 row_newbcast:3 row_mask:0xf bank_mask:0xf\n"
"v_and_b32_dpp v26, v46, v22 row_newbcast:3 row_mask:0xf bank_mask:0xf\n"
"v_and_b32_dpp v27, v46, v23 row_newbcast:3 row_mask:0xf bank_mask:0xf\n"
"s_waitcnt vmcnt(31)\n"
"s_nop 0\n"
"v_mfma_f32_16x16x32_bf16 v[40:43], v[24:27], v[160:163], v[40:43]\n"
"s_add_u32 s52, s53, 0x214000\n"
"s_and_b32 s52, s52, s54\n"
"buffer_load_dwordx4 v[128:131], v2, s[32:35], s52 offen offset:0 nt\n"
"v_and_b32_dpp v24, v46, v20 row_newbcast:7 row_mask:0xf bank_mask:0xf\n"
"v_and_b32_dpp v25, v46, v21 row_newbcast:7 row_mask:0xf bank_mask:0xf\n"
"v_and_b32_dpp v26, v46, v22 row_newbcast:7 row_mask:0xf bank_mask:0xf\n"
"v_and_b32_dpp v27, v46, v23 row_newbcast:7 row_mask:0xf bank_mask:0xf\n"
"s_waitcnt vmcnt(31)\n"
"s_nop 0\n"
"v_mfma_f32_16x16x32_bf16 v[40:43], v[24:27], v[164:167], v[40:43]\n"
"s_add_u32 s52, s53, 0x21b000\n"
"s_and_b32 s52, s52, s54\n"
"buffer_load_dwordx4 v[132:135], v2, s[32:35], s52 offen offset:0 nt\n"
"v_and_b32_dpp v24, v46, v20 row_newbcast:11 row_mask:0xf bank_mask:0xf\n"
"v_and_b32_dpp v25, v46, v21 row_newbcast:11 row_mask:0xf bank_mask:0xf\n"
"v_and_b32_dpp v26, v46, v22 row_newbcast:11 row_mask:0xf bank_mask:0xf\n"
"v_and_b32_dpp v27, v46, v23 row_newbcast:11 row_mask:0xf bank_mask:0xf\n"
"s_waitcnt vmcnt(31)\n"
"s_nop 0\n"
"v_mfma_f32_16x16x32_bf16 v[40:43], v[24:27], v[168:171], v[40:43]\n"
"s_add_u32 s52, s53, 0x222000\n"
"s_and_b32 s52, s52, s54\n"
"buffer_load_dwordx4 v[136:139], v2, s[32:35], s52 offen offset:0 nt\n"
"v_and_b32_dpp v24, v46, v20 row_newbcast:15 row_mask:0xf bank_mask:0xf\n"
"v_and_b32_dpp v25, v46, v21 row_newbcast:15 row_mask:0xf bank_mask:0xf\n"
"v_and_b32_dpp v26, v46, v22 row_newbcast:15 row_mask:0xf bank_mask:0xf\n"
"v_and_b32_dpp v27, v46, v23 row_newbcast:15 row_mask:0xf bank_mask:0xf\n"
"s_waitcnt vmcnt(31)\n"
"s_nop 0\n"
"v_mfma_f32_16x16x32_bf16 v[40:43], v[24:27], v[172:175], v[40:43]\n"
"s_add_u32 s52, s53, 0x229000\n"
"s_and_b32 s52, s52, s54\n"
"buffer_load_dwordx4 v[140:143], v2, s[32:35], s52 offen offset:0 nt\n"
"v_and_b32_dpp v24, v47, v20 row_newbcast:0 row_mask:0xf bank_mask:0xf\n"
"v_and_b32_dpp v25, v47, v21 row_newbcast:0 row_mask:0xf bank_mask:0xf\n"
"v_and_b32_dpp v26, v47, v22 row_newbcast:0 row_mask:0xf bank_mask:0xf\n"
"v_and_b32_dpp v27, v47, v23 row_newbcast:0 row_mask:0xf bank_mask:0xf\n"
"s_waitcnt vmcnt(31)\n"
"s_nop 0\n"
"v_mfma_f32_16x16x32_bf16 v[40:43], v[24:27], v[176:179], v[40:43]\n"
"s_add_u32 s52, s53, 0x230000\n"
"s_and_b32 s52, s52, s54\n"
"buffer_load_dwordx4 v[144:147], v2, s[32:35], s52 offen offset:0 nt\n"
"v_and_b32_dpp v24, v47, v20 row_newbcast:4 row_mask:0xf bank_mask:0xf\n"
"v_and_b32_dpp v25, v47, v21 row_newbcast:4 row_mask:0xf bank_mask:0xf\n"
"v_and_b32_dpp v26, v47, v22 row_newbcast:4 row_mask:0xf bank_mask:0xf\n"
"v_and_b32_dpp v27, v47, v23 row_newbcast:4 row_mask:0xf bank_mask:0xf\n"
"s_waitcnt vmcnt(31)\n"
"s_nop 0\n"
"v_mfma_f32_16x16x32_bf16 v[40:43], v[24:27], v[180:183], v[40:43]\n"
"s_add_u32 s52, s53, 0x237000\n"
"s_and_b32 s52, s52, s54\n"
"buffer_load_dwordx4 v[148:151], v2, s[32:35], s52 offen offset:0 nt\n"
"v_and_b32_dpp v24, v47, v20 row_newbcast:8 row_mask:0xf bank_mask:0xf\n"
"v_and_b32_dpp v25, v47, v21 row_newbcast:8 row_mask:0xf bank_mask:0xf\n"
"v_and_b32_dpp v26, v47, v22 row_newbcast:8 row_mask:0xf bank_mask:0xf\n"
"v_and_b32_dpp v27, v47, v23 row_newbcast:8 row_mask:0xf bank_mask:0xf\n"
"s_waitcnt vmcnt(31)\n"
"s_nop 0\n"
"v_mfma_f32_16x16x32_bf16 v[40:43], v[24:27], v[184:187], v[40:43]\n"
"s_add_u32 s52, s53, 0x23e000\n"
"s_and_b32 s52, s52, s54\n"
"buffer_load_dwordx4 v[152:155], v2, s[32:35], s52 offen offset:0 nt\n"
"v_and_b32_dpp v24, v47, v20 row_newbcast:12 row_mask:0xf bank_mask:0xf\n"
"v_and_b32_dpp v25, v47, v21 row_newbcast:12 row_mask:0xf bank_mask:0xf\n"
"v_and_b32_dpp v26, v47, v22 row_newbcast:12 row_mask:0xf bank_mask:0xf\n"
"v_and_b32_dpp v27, v47, v23 row_newbcast:12 row_mask:0xf bank_mask:0xf\n"
"s_waitcnt vmcnt(31)\n"
"s_nop 0\n"
"v_mfma_f32_16x16x32_bf16 v[40:43], v[24:27], v[188:191], v[40:43]\n"
"s_add_u32 s52, s53, 0x245000\n"
"s_and_b32 s52, s52, s54\n"
"buffer_load_dwordx4 v[156:159], v2, s[32:35], s52 offen offset:0 nt\n"
"v_and_b32_dpp v24, v47, v20 row_newbcast:1 row_mask:0xf bank_mask:0xf\n"
"v_and_b32_dpp v25, v47, v21 row_newbcast:1 row_mask:0xf bank_mask:0xf\n"
"v_and_b32_dpp v26, v47, v22 row_newbcast:1 row_mask:0xf bank_mask:0xf\n"
"v_and_b32_dpp v27, v47, v23 row_newbcast:1 row_mask:0xf bank_mask:0xf\n"
"s_waitcnt vmcnt(31)\n"
"s_nop 0\n"
"v_mfma_f32_16x16x32_bf16 v[40:43], v[24:27], v[192:195], v[40:43]\n"
"s_add_u32 s52, s53, 0x24c000\n"
"s_and_b32 s52, s52, s54\n"
"buffer_load_dwordx4 v[160:163], v2, s[32:35], s52 offen offset:0 nt\n"
"v_and_b32_dpp v24, v47, v20 row_newbcast:5 row_mask:0xf bank_mask:0xf\n"
"v_and_b32_dpp v25, v47, v21 row_newbcast:5 row_mask:0xf bank_mask:0xf\n"
"v_and_b32_dpp v26, v47, v22 row_newbcast:5 row_mask:0xf bank_mask:0xf\n"
"v_and_b32_dpp v27, v47, v23 row_newbcast:5 row_mask:0xf bank_mask:0xf\n"
"s_waitcnt vmcnt(31)\n"
"s_nop 0\n"
"v_mfma_f32_16x16x32_bf16 v[40:43], v[24:27], v[196:199], v[40:43]\n"
"s_add_u32 s52, s53, 0x253000\n"
"s_and_b32 s52, s52, s54\n"
"buffer_load_dwordx4 v[164:167], v2, s[32:35], s52 offen offset:0 nt\n"
"v_and_b32_dpp v24, v47, v20 row_newbcast:9 row_mask:0xf bank_mask:0xf\n"
"v_and_b32_dpp v25, v47, v21 row_newbcast:9 row_mask:0xf bank_mask:0xf\n"
"v_and_b32_dpp v26, v47, v22 row_newbcast:9 row_mask:0xf bank_mask:0xf\n"
"v_and_b32_dpp v27, v47, v23 row_newbcast:9 row_mask:0xf bank_mask:0xf\n"
"s_waitcnt vmcnt(31)\n"
"s_nop 0\n"
"v_mfma_f32_16x16x32_bf16 v[40:43], v[24:27], v[200:203], v[40:43]\n"
"s_add_u32 s52, s53, 0x25a000\n"
"s_and_b32 s52, s52, s54\n"
"buffer_load_dwordx4 v[168:171], v2, s[32:35], s52 offen offset:0 nt\n"
"v_and_b32_dpp v24, v47, v20 row_newbcast:13 row_mask:0xf bank_mask:0xf\n"
"v_and_b32_dpp v25, v47, v21 row_newbcast:13 row_mask:0xf bank_mask:0xf\n"
"v_and_b32_dpp v26, v47, v22 row_newbcast:13 row_mask:0xf bank_mask:0xf\n"
"v_and_b32_dpp v27, v47, v23 row_newbcast:13 row_mask:0xf bank_mask:0xf\n"
"s_waitcnt vmcnt(31)\n"
"s_nop 0\n"
"v_mfma_f32_16x16x32_bf16 v[40:43], v[24:27], v[204:207], v[40:43]\n"
"s_add_u32 s52, s53, 0x261000\n"
"s_and_b32 s52, s52, s54\n"
"buffer_load_dwordx4 v[172:175], v2, s[32:35], s52 offen offset:0 nt\n"
"v_and_b32_dpp v24, v47, v20 row_newbcast:2 row_mask:0xf bank_mask:0xf\n"
"v_and_b32_dpp v25, v47, v21 row_newbcast:2 row_mask:0xf bank_mask:0xf\n"
"v_and_b32_dpp v26, v47, v22 row_newbcast:2 row_mask:0xf bank_mask:0xf\n"
"v_and_b32_dpp v27, v47, v23 row_newbcast:2 row_mask:0xf bank_mask:0xf\n"
"s_waitcnt vmcnt(31)\n"
"s_nop 0\n"
"v_mfma_f32_16x16x32_bf16 v[40:43], v[24:27], v[208:211], v[40:43]\n"
"s_add_u32 s52, s53, 0x268000\n"
"s_and_b32 s52, s52, s54\n"
"buffer_load_dwordx4 v[176:179], v2, s[32:35], s52 offen offset:0 nt\n"
"v_and_b32_dpp v24, v47, v20 row_newbcast:6 row_mask:0xf bank_mask:0xf\n"
"v_and_b32_dpp v25, v47, v21 row_newbcast:6 row_mask:0xf bank_mask:0xf\n"
"v_and_b32_dpp v26, v47, v22 row_newbcast:6 row_mask:0xf bank_mask:0xf\n"
"v_and_b32_dpp v27, v47, v23 row_newbcast:6 row_mask:0xf bank_mask:0xf\n"
"s_waitcnt vmcnt(31)\n"
"s_nop 0\n"
"v_mfma_f32_16x16x32_bf16 v[40:43], v[24:27], v[212:215], v[40:43]\n"
"s_add_u32 s52, s53, 0x26f000\n"
"s_and_b32 s52, s52, s54\n"
"buffer_load_dwordx4 v[180:183], v2, s[32:35], s52 offen offset:0 nt\n"
"v_and_b32_dpp v24, v47, v20 row_newbcast:10 row_mask:0xf bank_mask:0xf\n"
"v_and_b32_dpp v25, v47, v21 row_newbcast:10 row_mask:0xf bank_mask:0xf\n"
"v_and_b32_dpp v26, v47, v22 row_newbcast:10 row_mask:0xf bank_mask:0xf\n"
"v_and_b32_dpp v27, v47, v23 row_newbcast:10 row_mask:0xf bank_mask:0xf\n"
"s_waitcnt vmcnt(31)\n"
"s_nop 0\n"
"v_mfma_f32_16x16x32_bf16 v[40:43], v[24:27], v[216:219], v[40:43]\n"
"s_add_u32 s52, s53, 0x276000\n"
"s_and_b32 s52, s52, s54\n"
"buffer_load_dwordx4 v[184:187], v2, s[32:35], s52 offen offset:0 nt\n"
"v_and_b32_dpp v24, v47, v20 row_newbcast:14 row_mask:0xf bank_mask:0xf\n"
"v_and_b32_dpp v25, v47, v21 row_newbcast:14 row_mask:0xf bank_mask:0xf\n"
"v_and_b32_dpp v26, v47, v22 row_newbcast:14 row_mask:0xf bank_mask:0xf\n"
"v_and_b32_dpp v27, v47, v23 row_newbcast:14 row_mask:0xf bank_mask:0xf\n"
"s_waitcnt vmcnt(31)\n"
"s_nop 0\n"
"v_mfma_f32_16x16x32_bf16 v[40:43], v[24:27], v[220:223], v[40:43]\n"
"s_add_u32 s52, s53, 0x27d000\n"
"s_and_b32 s52, s52, s54\n"
"buffer_load_dwordx4 v[188:191], v2, s[32:35], s52 offen offset:0 nt\n"
"v_and_b32_dpp v24, v47, v20 row_newbcast:3 row_mask:0xf bank_mask:0xf\n"
"v_and_b32_dpp v25, v47, v21 row_newbcast:3 row_mask:0xf bank_mask:0xf\n"
"v_and_b32_dpp v26, v47, v22 row_newbcast:3 row_mask:0xf bank_mask:0xf\n"
"v_and_b32_dpp v27, v47, v23 row_newbcast:3 row_mask:0xf bank_mask:0xf\n"
"s_waitcnt vmcnt(31)\n"
"s_nop 0\n"
"v_mfma_f32_16x16x32_bf16 v[40:43], v[24:27], v[64:67], v[40:43]\n"
"s_add_u32 s52, s53, 0x284000\n"
"s_and_b32 s52, s52, s54\n"
"buffer_load_dwordx4 v[192:195], v2, s[32:35], s52 offen offset:0 nt\n"
"v_and_b32_dpp v24, v47, v20 row_newbcast:7 row_mask:0xf bank_mask:0xf\n"
"v_and_b32_dpp v25, v47, v21 row_newbcast:7 row_mask:0xf bank_mask:0xf\n"
"v_and_b32_dpp v26, v47, v22 row_newbcast:7 row_mask:0xf bank_mask:0xf\n"
"v_and_b32_dpp v27, v47, v23 row_newbcast:7 row_mask:0xf bank_mask:0xf\n"
"s_waitcnt vmcnt(31)\n"
"s_nop 0\n"
"v_mfma_f32_16x16x32_bf16 v[40:43], v[24:27], v[68:71], v[40:43]\n"
"s_add_u32 s52, s53, 0x28b000\n"
"s_and_b32 s52, s52, s54\n"
"buffer_load_dwordx4 v[196:199], v2, s[32:35], s52 offen offset:0 nt\n"
"v_and_b32_dpp v24, v47, v20 row_newbcast:11 row_mask:0xf bank_mask:0xf\n"
"v_and_b32_dpp v25, v47, v21 row_newbcast:11 row_mask:0xf bank_mask:0xf\n"
"v_and_b32_dpp v26, v47, v22 row_newbcast:11 row_mask:0xf bank_mask:0xf\n"
"v_and_b32_dpp v27, v47, v23 row_newbcast:11 row_mask:0xf bank_mask:0xf\n"
"s_waitcnt vmcnt(31)\n"
"s_nop 0\n"
"v_mfma_f32_16x16x32_bf16 v[40:43], v[24:27], v[72:75], v[40:43]\n"
"s_add_u32 s52, s53, 0x292000\n"
"s_and_b32 s52, s52, s54\n"
"buffer_load_dwordx4 v[200:203], v2, s[32:35], s52 offen offset:0 nt\n"
"v_and_b32_dpp v24, v47, v20 row_newbcast:15 row_mask:0xf bank_mask:0xf\n"
"v_and_b32_dpp v25, v47, v21 row_newbcast:15 row_mask:0xf bank_mask:0xf\n"
"v_and_b32_dpp v26, v47, v22 row_newbcast:15 row_mask:0xf bank_mask:0xf\n"
"v_and_b32_dpp v27, v47, v23 row_newbcast:15 row_mask:0xf bank_mask:0xf\n"
"s_waitcnt vmcnt(31)\n"
"s_nop 0\n"
"v_mfma_f32_16x16x32_bf16 v[40:43], v[24:27], v[76:79], v[40:43]\n"
"s_add_u32 s52, s53, 0x299000\n"
"s_and_b32 s52, s52, s54\n"
"buffer_load_dwordx4 v[204:207], v2, s[32:35], s52 offen offset:0 nt\n"
"v_and_b32_dpp v24, v48, v20 row_newbcast:0 row_mask:0xf bank_mask:0xf\n"
"v_and_b32_dpp v25, v48, v21 row_newbcast:0 row_mask:0xf bank_mask:0xf\n"
"v_and_b32_dpp v26, v48, v22 row_newbcast:0 row_mask:0xf bank_mask:0xf\n"
"v_and_b32_dpp v27, v48, v23 row_newbcast:0 row_mask:0xf bank_mask:0xf\n"
"s_waitcnt vmcnt(31)\n"
"s_nop 0\n"
"v_mfma_f32_16x16x32_bf16 v[40:43], v[24:27], v[80:83], v[40:43]\n"
"s_add_u32 s52, s53, 0x2a0000\n"
"s_and_b32 s52, s52, s54\n"
"buffer_load_dwordx4 v[208:211], v2, s[32:35], s52 offen offset:0 nt\n"
"v_and_b32_dpp v24, v48, v20 row_newbcast:4 row_mask:0xf bank_mask:0xf\n"
"v_and_b32_dpp v25, v48, v21 row_newbcast:4 row_mask:0xf bank_mask:0xf\n"
"v_and_b32_dpp v26, v48, v22 row_newbcast:4 row_mask:0xf bank_mask:0xf\n"
"v_and_b32_dpp v27, v48, v23 row_newbcast:4 row_mask:0xf bank_mask:0xf\n"
"s_waitcnt vmcnt(31)\n"
"s_nop 0\n"
"v_mfma_f32_16x16x32_bf16 v[40:43], v[24:27], v[84:87], v[40:43]\n"
"s_add_u32 s52, s53, 0x2a7000\n"
"s_and_b32 s52, s52, s54\n"
"buffer_load_dwordx4 v[212:215], v2, s[32:35], s52 offen offset:0 nt\n"
"v_and_b32_dpp v24, v48, v20 row_newbcast:8 row_mask:0xf bank_mask:0xf\n"
"v_and_b32_dpp v25, v48, v21 row_newbcast:8 row_mask:0xf bank_mask:0xf\n"
"v_and_b32_dpp v26, v48, v22 row_newbcast:8 row_mask:0xf bank_mask:0xf\n"
"v_and_b32_dpp v27, v48, v23 row_newbcast:8 row_mask:0xf bank_mask:0xf\n"
"s_waitcnt vmcnt(31)\n"
"s_nop 0\n"
"v_mfma_f32_16x16x32_bf16 v[40:43], v[24:27], v[88:91], v[40:43]\n"
"s_add_u32 s52, s53, 0x2ae000\n"
"s_and_b32 s52, s52, s54\n"
"buffer_load_dwordx4 v[216:219], v2, s[32:35], s52 offen offset:0 nt\n"
"v_and_b32_dpp v24, v48, v20 row_newbcast:12 row_mask:0xf bank_mask:0xf\n"
"v_and_b32_dpp v25, v48, v21 row_newbcast:12 row_mask:0xf bank_mask:0xf\n"
"v_and_b32_dpp v26, v48, v22 row_newbcast:12 row_mask:0xf bank_mask:0xf\n"
"v_and_b32_dpp v27, v48, v23 row_newbcast:12 row_mask:0xf bank_mask:0xf\n"
"s_waitcnt vmcnt(31)\n"
"s_nop 0\n"
"v_mfma_f32_16x16x32_bf16 v[40:43], v[24:27], v[92:95], v[40:43]\n"
"s_add_u32 s52, s53, 0x2b5000\n"
"s_and_b32 s52, s52, s54\n"
"buffer_load_dwordx4 v[220:223], v2, s[32:35], s52 offen offset:0 nt\n"
"v_and_b32_dpp v24, v48, v20 row_newbcast:1 row_mask:0xf bank_mask:0xf\n"
"v_and_b32_dpp v25, v48, v21 row_newbcast:1 row_mask:0xf bank_mask:0xf\n"
"v_and_b32_dpp v26, v48, v22 row_newbcast:1 row_mask:0xf bank_mask:0xf\n"
"v_and_b32_dpp v27, v48, v23 row_newbcast:1 row_mask:0xf bank_mask:0xf\n"
"s_waitcnt vmcnt(31)\n"
"s_nop 0\n"
"v_mfma_f32_16x16x32_bf16 v[40:43], v[24:27], v[96:99], v[40:43]\n"
"s_add_u32 s52, s53, 0x2bc000\n"
"s_and_b32 s52, s52, s54\n"
"buffer_load_dwordx4 v[64:67], v2, s[32:35], s52 offen offset:0 nt\n"
"v_and_b32_dpp v24, v48, v20 row_newbcast:5 row_mask:0xf bank_mask:0xf\n"
"v_and_b32_dpp v25, v48, v21 row_newbcast:5 row_mask:0xf bank_mask:0xf\n"
"v_and_b32_dpp v26, v48, v22 row_newbcast:5 row_mask:0xf bank_mask:0xf\n"
"v_and_b32_dpp v27, v48, v23 row_newbcast:5 row_mask:0xf bank_mask:0xf\n"
"s_waitcnt vmcnt(31)\n"
"s_nop 0\n"
"v_mfma_f32_16x16x32_bf16 v[40:43], v[24:27], v[100:103], v[40:43]\n"
"s_add_u32 s52, s53, 0x2c3000\n"
"s_and_b32 s52, s52, s54\n"
"buffer_load_dwordx4 v[68:71], v2, s[32:35], s52 offen offset:0 nt\n"
"v_and_b32_dpp v24, v48, v20 row_newbcast:9 row_mask:0xf bank_mask:0xf\n"
"v_and_b32_dpp v25, v48, v21 row_newbcast:9 row_mask:0xf bank_mask:0xf\n"
"v_and_b32_dpp v26, v48, v22 row_newbcast:9 row_mask:0xf bank_mask:0xf\n"
"v_and_b32_dpp v27, v48, v23 row_newbcast:9 row_mask:0xf bank_mask:0xf\n"
"s_waitcnt vmcnt(31)\n"
"s_nop 0\n"
"v_mfma_f32_16x16x32_bf16 v[40:43], v[24:27], v[104:107], v[40:43]\n"
"s_add_u32 s52, s53, 0x2ca000\n"
"s_and_b32 s52, s52, s54\n"
"buffer_load_dwordx4 v[72:75], v2, s[32:35], s52 offen offset:0 nt\n"
"v_and_b32_dpp v24, v48, v20 row_newbcast:13 row_mask:0xf bank_mask:0xf\n"
"v_and_b32_dpp v25, v48, v21 row_newbcast:13 row_mask:0xf bank_mask:0xf\n"
"v_and_b32_dpp v26, v48, v22 row_newbcast:13 row_mask:0xf bank_mask:0xf\n"
"v_and_b32_dpp v27, v48, v23 row_newbcast:13 row_mask:0xf bank_mask:0xf\n"
"s_waitcnt vmcnt(31)\n"
"s_nop 0\n"
"v_mfma_f32_16x16x32_bf16 v[40:43], v[24:27], v[108:111], v[40:43]\n"
"s_add_u32 s52, s53, 0x2d1000\n"
"s_and_b32 s52, s52, s54\n"
"buffer_load_dwordx4 v[76:79], v2, s[32:35], s52 offen offset:0 nt\n"
"v_and_b32_dpp v24, v48, v20 row_newbcast:2 row_mask:0xf bank_mask:0xf\n"
"v_and_b32_dpp v25, v48, v21 row_newbcast:2 row_mask:0xf bank_mask:0xf\n"
"v_and_b32_dpp v26, v48, v22 row_newbcast:2 row_mask:0xf bank_mask:0xf\n"
"v_and_b32_dpp v27, v48, v23 row_newbcast:2 row_mask:0xf bank_mask:0xf\n"
"s_waitcnt vmcnt(31)\n"
"s_nop 0\n"
"v_mfma_f32_16x16x32_bf16 v[40:43], v[24:27], v[112:115], v[40:43]\n"
"s_add_u32 s52, s53, 0x2d8000\n"
"s_and_b32 s52, s52, s54\n"
"buffer_load_dwordx4 v[80:83], v2, s[32:35], s52 offen offset:0 nt\n"
"v_and_b32_dpp v24, v48, v20 row_newbcast:6 row_mask:0xf bank_mask:0xf\n"
"v_and_b32_dpp v25, v48, v21 row_newbcast:6 row_mask:0xf bank_mask:0xf\n"
"v_and_b32_dpp v26, v48, v22 row_newbcast:6 row_mask:0xf bank_mask:0xf\n"
"v_and_b32_dpp v27, v48, v23 row_newbcast:6 row_mask:0xf bank_mask:0xf\n"
"s_waitcnt vmcnt(31)\n"
"s_nop 0\n"
"v_mfma_f32_16x16x32_bf16 v[40:43], v[24:27], v[116:119], v[40:43]\n"
"s_add_u32 s52, s53, 0x2df000\n"
"s_and_b32 s52, s52, s54\n"
"buffer_load_dwordx4 v[84:87], v2, s[32:35], s52 offen offset:0 nt\n"
"v_and_b32_dpp v24, v48, v20 row_newbcast:10 row_mask:0xf bank_mask:0xf\n"
"v_and_b32_dpp v25, v48, v21 row_newbcast:10 row_mask:0xf bank_mask:0xf\n"
"v_and_b32_dpp v26, v48, v22 row_newbcast:10 row_mask:0xf bank_mask:0xf\n"
"v_and_b32_dpp v27, v48, v23 row_newbcast:10 row_mask:0xf bank_mask:0xf\n"
"s_waitcnt vmcnt(31)\n"
"s_nop 0\n"
"v_mfma_f32_16x16x32_bf16 v[40:43], v[24:27], v[120:123], v[40:43]\n"
"s_add_u32 s52, s53, 0x2e6000\n"
"s_and_b32 s52, s52, s54\n"
"buffer_load_dwordx4 v[88:91], v2, s[32:35], s52 offen offset:0 nt\n"
"v_and_b32_dpp v24, v48, v20 row_newbcast:14 row_mask:0xf bank_mask:0xf\n"
"v_and_b32_dpp v25, v48, v21 row_newbcast:14 row_mask:0xf bank_mask:0xf\n"
"v_and_b32_dpp v26, v48, v22 row_newbcast:14 row_mask:0xf bank_mask:0xf\n"
"v_and_b32_dpp v27, v48, v23 row_newbcast:14 row_mask:0xf bank_mask:0xf\n"
"s_waitcnt vmcnt(31)\n"
"s_nop 0\n"
"v_mfma_f32_16x16x32_bf16 v[40:43], v[24:27], v[124:127], v[40:43]\n"
"s_add_u32 s52, s53, 0x2ed000\n"
"s_and_b32 s52, s52, s54\n"
"buffer_load_dwordx4 v[92:95], v2, s[32:35], s52 offen offset:0 nt\n"
"v_and_b32_dpp v24, v48, v20 row_newbcast:3 row_mask:0xf bank_mask:0xf\n"
"v_and_b32_dpp v25, v48, v21 row_newbcast:3 row_mask:0xf bank_mask:0xf\n"
"v_and_b32_dpp v26, v48, v22 row_newbcast:3 row_mask:0xf bank_mask:0xf\n"
"v_and_b32_dpp v27, v48, v23 row_newbcast:3 row_mask:0xf bank_mask:0xf\n"
"s_waitcnt vmcnt(31)\n"
"s_nop 0\n"
"v_mfma_f32_16x16x32_bf16 v[40:43], v[24:27], v[128:131], v[40:43]\n"
"s_add_u32 s52, s53, 0x2f4000\n"
"s_and_b32 s52, s52, s54\n"
"buffer_load_dwordx4 v[96:99], v2, s[32:35], s52 offen offset:0 nt\n"
"v_and_b32_dpp v24, v48, v20 row_newbcast:7 row_mask:0xf bank_mask:0xf\n"
"v_and_b32_dpp v25, v48, v21 row_newbcast:7 row_mask:0xf bank_mask:0xf\n"
"v_and_b32_dpp v26, v48, v22 row_newbcast:7 row_mask:0xf bank_mask:0xf\n"
"v_and_b32_dpp v27, v48, v23 row_newbcast:7 row_mask:0xf bank_mask:0xf\n"
"s_waitcnt vmcnt(31)\n"
"s_nop 0\n"
"v_mfma_f32_16x16x32_bf16 v[40:43], v[24:27], v[132:135], v[40:43]\n"
"s_add_u32 s52, s53, 0x2fb000\n"
"s_and_b32 s52, s52, s54\n"
"buffer_load_dwordx4 v[100:103], v2, s[32:35], s52 offen offset:0 nt\n"
"v_and_b32_dpp v24, v48, v20 row_newbcast:11 row_mask:0xf bank_mask:0xf\n"
"v_and_b32_dpp v25, v48, v21 row_newbcast:11 row_mask:0xf bank_mask:0xf\n"
"v_and_b32_dpp v26, v48, v22 row_newbcast:11 row_mask:0xf bank_mask:0xf\n"
"v_and_b32_dpp v27, v48, v23 row_newbcast:11 row_mask:0xf bank_mask:0xf\n"
"s_waitcnt vmcnt(31)\n"
"s_nop 0\n"
"v_mfma_f32_16x16x32_bf16 v[40:43], v[24:27], v[136:139], v[40:43]\n"
"s_add_u32 s52, s53, 0x302000\n"
"s_and_b32 s52, s52, s54\n"
"buffer_load_dwordx4 v[104:107], v2, s[32:35], s52 offen offset:0 nt\n"
"v_and_b32_dpp v24, v48, v20 row_newbcast:15 row_mask:0xf bank_mask:0xf\n"
"v_and_b32_dpp v25, v48, v21 row_newbcast:15 row_mask:0xf bank_mask:0xf\n"
"v_and_b32_dpp v26, v48, v22 row_newbcast:15 row_mask:0xf bank_mask:0xf\n"
"v_and_b32_dpp v27, v48, v23 row_newbcast:15 row_mask:0xf bank_mask:0xf\n"
"s_waitcnt vmcnt(31)\n"
"s_nop 0\n"
"v_mfma_f32_16x16x32_bf16 v[40:43], v[24:27], v[140:143], v[40:43]\n"
"s_add_u32 s52, s53, 0x309000\n"
"s_and_b32 s52, s52, s54\n"
"buffer_load_dwordx4 v[108:111], v2, s[32:35], s52 offen offset:0 nt\n"
"v_and_b32_dpp v24, v49, v20 row_newbcast:0 row_mask:0xf bank_mask:0xf\n"
"v_and_b32_dpp v25, v49, v21 row_newbcast:0 row_mask:0xf bank_mask:0xf\n"
"v_and_b32_dpp v26, v49, v22 row_newbcast:0 row_mask:0xf bank_mask:0xf\n"
"v_and_b32_dpp v27, v49, v23 row_newbcast:0 row_mask:0xf bank_mask:0xf\n"
"s_waitcnt vmcnt(31)\n"
"s_nop 0\n"
"v_mfma_f32_16x16x32_bf16 v[40:43], v[24:27], v[144:147], v[40:43]\n"
"s_add_u32 s52, s53, 0x310000\n"
"s_and_b32 s52, s52, s54\n"
"buffer_load_dwordx4 v[112:115], v2, s[32:35], s52 offen offset:0 nt\n"
"v_and_b32_dpp v24, v49, v20 row_newbcast:4 row_mask:0xf bank_mask:0xf\n"
"v_and_b32_dpp v25, v49, v21 row_newbcast:4 row_mask:0xf bank_mask:0xf\n"
"v_and_b32_dpp v26, v49, v22 row_newbcast:4 row_mask:0xf bank_mask:0xf\n"
"v_and_b32_dpp v27, v49, v23 row_newbcast:4 row_mask:0xf bank_mask:0xf\n"
"s_waitcnt vmcnt(31)\n"
"s_nop 0\n"
"v_mfma_f32_16x16x32_bf16 v[40:43], v[24:27], v[148:151], v[40:43]\n"
"s_add_u32 s52, s53, 0x317000\n"
"s_and_b32 s52, s52, s54\n"
"buffer_load_dwordx4 v[116:119], v2, s[32:35], s52 offen offset:0 nt\n"
"v_and_b32_dpp v24, v49, v20 row_newbcast:8 row_mask:0xf bank_mask:0xf\n"
"v_and_b32_dpp v25, v49, v21 row_newbcast:8 row_mask:0xf bank_mask:0xf\n"
"v_and_b32_dpp v26, v49, v22 row_newbcast:8 row_mask:0xf bank_mask:0xf\n"
"v_and_b32_dpp v27, v49, v23 row_newbcast:8 row_mask:0xf bank_mask:0xf\n"
"s_waitcnt vmcnt(31)\n"
"s_nop 0\n"
"v_mfma_f32_16x16x32_bf16 v[40:43], v[24:27], v[152:155], v[40:43]\n"
"s_add_u32 s52, s53, 0x31e000\n"
"s_and_b32 s52, s52, s54\n"
"buffer_load_dwordx4 v[120:123], v2, s[32:35], s52 offen offset:0 nt\n"
"v_and_b32_dpp v24, v49, v20 row_newbcast:12 row_mask:0xf bank_mask:0xf\n"
"v_and_b32_dpp v25, v49, v21 row_newbcast:12 row_mask:0xf bank_mask:0xf\n"
"v_and_b32_dpp v26, v49, v22 row_newbcast:12 row_mask:0xf bank_mask:0xf\n"
"v_and_b32_dpp v27, v49, v23 row_newbcast:12 row_mask:0xf bank_mask:0xf\n"
"s_waitcnt vmcnt(31)\n"
"s_nop 0\n"
"v_mfma_f32_16x16x32_bf16 v[40:43], v[24:27], v[156:159], v[40:43]\n"
"s_add_u32 s52, s53, 0x325000\n"
"s_and_b32 s52, s52, s54\n"
"buffer_load_dwordx4 v[124:127], v2, s[32:35], s52 offen offset:0 nt\n"
"v_and_b32_dpp v24, v49, v20 row_newbcast:1 row_mask:0xf bank_mask:0xf\n"
"v_and_b32_dpp v25, v49, v21 row_newbcast:1 row_mask:0xf bank_mask:0xf\n"
"v_and_b32_dpp v26, v49, v22 row_newbcast:1 row_mask:0xf bank_mask:0xf\n"
"v_and_b32_dpp v27, v49, v23 row_newbcast:1 row_mask:0xf bank_mask:0xf\n"
"s_waitcnt vmcnt(31)\n"
"s_nop 0\n"
"v_mfma_f32_16x16x32_bf16 v[40:43], v[24:27], v[160:163], v[40:43]\n"
"s_add_u32 s52, s53, 0x32c000\n"
"s_and_b32 s52, s52, s54\n"
"buffer_load_dwordx4 v[128:131], v2, s[32:35], s52 offen offset:0 nt\n"
"v_and_b32_dpp v24, v49, v20 row_newbcast:5 row_mask:0xf bank_mask:0xf\n"
"v_and_b32_dpp v25, v49, v21 row_newbcast:5 row_mask:0xf bank_mask:0xf\n"
"v_and_b32_dpp v26, v49, v22 row_newbcast:5 row_mask:0xf bank_mask:0xf\n"
"v_and_b32_dpp v27, v49, v23 row_newbcast:5 row_mask:0xf bank_mask:0xf\n"
"s_waitcnt vmcnt(31)\n"
"s_nop 0\n"
"v_mfma_f32_16x16x32_bf16 v[40:43], v[24:27], v[164:167], v[40:43]\n"
"s_add_u32 s52, s53, 0x333000\n"
"s_and_b32 s52, s52, s54\n"
"buffer_load_dwordx4 v[132:135], v2, s[32:35], s52 offen offset:0 nt\n"
"v_and_b32_dpp v24, v49, v20 row_newbcast:9 row_mask:0xf bank_mask:0xf\n"
"v_and_b32_dpp v25, v49, v21 row_newbcast:9 row_mask:0xf bank_mask:0xf\n"
"v_and_b32_dpp v26, v49, v22 row_newbcast:9 row_mask:0xf bank_mask:0xf\n"
"v_and_b32_dpp v27, v49, v23 row_newbcast:9 row_mask:0xf bank_mask:0xf\n"
"s_waitcnt vmcnt(31)\n"
"s_nop 0\n"
"v_mfma_f32_16x16x32_bf16 v[40:43], v[24:27], v[168:171], v[40:43]\n"
"s_add_u32 s52, s53, 0x33a000\n"
"s_and_b32 s52, s52, s54\n"
"buffer_load_dwordx4 v[136:139], v2, s[32:35], s52 offen offset:0 nt\n"
"v_and_b32_dpp v24, v49, v20 row_newbcast:13 row_mask:0xf bank_mask:0xf\n"
"v_and_b32_dpp v25, v49, v21 row_newbcast:13 row_mask:0xf bank_mask:0xf\n"
"v_and_b32_dpp v26, v49, v22 row_newbcast:13 row_mask:0xf bank_mask:0xf\n"
"v_and_b32_dpp v27, v49, v23 row_newbcast:13 row_mask:0xf bank_mask:0xf\n"
"s_waitcnt vmcnt(31)\n"
"s_nop 0\n"
"v_mfma_f32_16x16x32_bf16 v[40:43], v[24:27], v[172:175], v[40:43]\n"
"s_add_u32 s52, s53, 0x341000\n"
"s_and_b32 s52, s52, s54\n"
"buffer_load_dwordx4 v[140:143], v2, s[32:35], s52 offen offset:0 nt\n"
"v_and_b32_dpp v24, v49, v20 row_newbcast:2 row_mask:0xf bank_mask:0xf\n"
"v_and_b32_dpp v25, v49, v21 row_newbcast:2 row_mask:0xf bank_mask:0xf\n"
"v_and_b32_dpp v26, v49, v22 row_newbcast:2 row_mask:0xf bank_mask:0xf\n"
"v_and_b32_dpp v27, v49, v23 row_newbcast:2 row_mask:0xf bank_mask:0xf\n"
"s_waitcnt vmcnt(31)\n"
"s_nop 0\n"
"v_mfma_f32_16x16x32_bf16 v[40:43], v[24:27], v[176:179], v[40:43]\n"
"s_add_u32 s52, s53, 0x348000\n"
"s_and_b32 s52, s52, s54\n"
"buffer_load_dwordx4 v[144:147], v2, s[32:35], s52 offen offset:0 nt\n"
"v_and_b32_dpp v24, v49, v20 row_newbcast:6 row_mask:0xf bank_mask:0xf\n"
"v_and_b32_dpp v25, v49, v21 row_newbcast:6 row_mask:0xf bank_mask:0xf\n"
"v_and_b32_dpp v26, v49, v22 row_newbcast:6 row_mask:0xf bank_mask:0xf\n"
"v_and_b32_dpp v27, v49, v23 row_newbcast:6 row_mask:0xf bank_mask:0xf\n"
"s_waitcnt vmcnt(31)\n"
"s_nop 0\n"
"v_mfma_f32_16x16x32_bf16 v[40:43], v[24:27], v[180:183], v[40:43]\n"
"s_add_u32 s52, s53, 0x34f000\n"
"s_and_b32 s52, s52, s54\n"
"buffer_load_dwordx4 v[148:151], v2, s[32:35], s52 offen offset:0 nt\n"
"v_and_b32_dpp v24, v49, v20 row_newbcast:10 row_mask:0xf bank_mask:0xf\n"
"v_and_b32_dpp v25, v49, v21 row_newbcast:10 row_mask:0xf bank_mask:0xf\n"
"v_and_b32_dpp v26, v49, v22 row_newbcast:10 row_mask:0xf bank_mask:0xf\n"
"v_and_b32_dpp v27, v49, v23 row_newbcast:10 row_mask:0xf bank_mask:0xf\n"
"s_waitcnt vmcnt(31)\n"
"s_nop 0\n"
"v_mfma_f32_16x16x32_bf16 v[40:43], v[24:27], v[184:187], v[40:43]\n"
"s_add_u32 s52, s53, 0x356000\n"
"s_and_b32 s52, s52, s54\n"
"buffer_load_dwordx4 v[152:155], v2, s[32:35], s52 offen offset:0 nt\n"
"v_and_b32_dpp v24, v49, v20 row_newbcast:14 row_mask:0xf bank_mask:0xf\n"
"v_and_b32_dpp v25, v49, v21 row_newbcast:14 row_mask:0xf bank_mask:0xf\n"
"v_and_b32_dpp v26, v49, v22 row_newbcast:14 row_mask:0xf bank_mask:0xf\n"
"v_and_b32_dpp v27, v49, v23 row_newbcast:14 row_mask:0xf bank_mask:0xf\n"
"s_waitcnt vmcnt(31)\n"
"s_nop 0\n"
"v_mfma_f32_16x16x32_bf16 v[40:43], v[24:27], v[188:191], v[40:43]\n"
"s_add_u32 s52, s53, 0x35d000\n"
"s_and_b32 s52, s52, s54\n"
"buffer_load_dwordx4 v[156:159], v2, s[32:35], s52 offen offset:0 nt\n"
"v_and_b32_dpp v24, v49, v20 row_newbcast:3 row_mask:0xf bank_mask:0xf\n"
"v_and_b32_dpp v25, v49, v21 row_newbcast:3 row_mask:0xf bank_mask:0xf\n"
"v_and_b32_dpp v26, v49, v22 row_newbcast:3 row_mask:0xf bank_mask:0xf\n"
"v_and_b32_dpp v27, v49, v23 row_newbcast:3 row_mask:0xf bank_mask:0xf\n"
"s_waitcnt vmcnt(31)\n"
"s_nop 0\n"
"v_mfma_f32_16x16x32_bf16 v[40:43], v[24:27], v[192:195], v[40:43]\n"
"s_add_u32 s52, s53, 0x364000\n"
"s_and_b32 s52, s52, s54\n"
"buffer_load_dwordx4 v[160:163], v2, s[32:35], s52 offen offset:0 nt\n"
"v_and_b32_dpp v24, v49, v20 row_newbcast:7 row_mask:0xf bank_mask:0xf\n"
"v_and_b32_dpp v25, v49, v21 row_newbcast:7 row_mask:0xf bank_mask:0xf\n"
"v_and_b32_dpp v26, v49, v22 row_newbcast:7 row_mask:0xf bank_mask:0xf\n"
"v_and_b32_dpp v27, v49, v23 row_newbcast:7 row_mask:0xf bank_mask:0xf\n"
"s_waitcnt vmcnt(31)\n"
"s_nop 0\n"
"v_mfma_f32_16x16x32_bf16 v[40:43], v[24:27], v[196:199], v[40:43]\n"
"s_add_u32 s52, s53, 0x36b000\n"
"s_and_b32 s52, s52, s54\n"
"buffer_load_dwordx4 v[164:167], v2, s[32:35], s52 offen offset:0 nt\n"
"v_and_b32_dpp v24, v49, v20 row_newbcast:11 row_mask:0xf bank_mask:0xf\n"
"v_and_b32_dpp v25, v49, v21 row_newbcast:11 row_mask:0xf bank_mask:0xf\n"
"v_and_b32_dpp v26, v49, v22 row_newbcast:11 row_mask:0xf bank_mask:0xf\n"
"v_and_b32_dpp v27, v49, v23 row_newbcast:11 row_mask:0xf bank_mask:0xf\n"
"s_waitcnt vmcnt(31)\n"
"s_nop 0\n"
"v_mfma_f32_16x16x32_bf16 v[40:43], v[24:27], v[200:203], v[40:43]\n"
"s_add_u32 s52, s53, 0x372000\n"
"s_and_b32 s52, s52, s54\n"
"buffer_load_dwordx4 v[168:171], v2, s[32:35], s52 offen offset:0 nt\n"
"v_and_b32_dpp v24, v49, v20 row_newbcast:15 row_mask:0xf bank_mask:0xf\n"
"v_and_b32_dpp v25, v49, v21 row_newbcast:15 row_mask:0xf bank_mask:0xf\n"
"v_and_b32_dpp v26, v49, v22 row_newbcast:15 row_mask:0xf bank_mask:0xf\n"
"v_and_b32_dpp v27, v49, v23 row_newbcast:15 row_mask:0xf bank_mask:0xf\n"
"s_waitcnt vmcnt(31)\n"
"s_nop 0\n"
"v_mfma_f32_16x16x32_bf16 v[40:43], v[24:27], v[204:207], v[40:43]\n"
"s_add_u32 s52, s53, 0x379000\n"
"s_and_b32 s52, s52, s54\n"
"buffer_load_dwordx4 v[172:175], v2, s[32:35], s52 offen offset:0 nt\n"
"v_and_b32_dpp v24, v50, v20 row_newbcast:0 row_mask:0xf bank_mask:0xf\n"
"v_and_b32_dpp v25, v50, v21 row_newbcast:0 row_mask:0xf bank_mask:0xf\n"
"v_and_b32_dpp v26, v50, v22 row_newbcast:0 row_mask:0xf bank_mask:0xf\n"
"v_and_b32_dpp v27, v50, v23 row_newbcast:0 row_mask:0xf bank_mask:0xf\n"
"s_waitcnt vmcnt(31)\n"
"s_nop 0\n"
"v_mfma_f32_16x16x32_bf16 v[40:43], v[24:27], v[208:211], v[40:43]\n"
"s_add_u32 s52, s53, 0x380000\n"
"s_and_b32 s52, s52, s54\n"
"buffer_load_dwordx4 v[176:179], v2, s[32:35], s52 offen offset:0 nt\n"
"v_and_b32_dpp v24, v50, v20 row_newbcast:4 row_mask:0xf bank_mask:0xf\n"
"v_and_b32_dpp v25, v50, v21 row_newbcast:4 row_mask:0xf bank_mask:0xf\n"
"v_and_b32_dpp v26, v50, v22 row_newbcast:4 row_mask:0xf bank_mask:0xf\n"
"v_and_b32_dpp v27, v50, v23 row_newbcast:4 row_mask:0xf bank_mask:0xf\n"
"s_waitcnt vmcnt(31)\n"
"s_nop 0\n"
"v_mfma_f32_16x16x32_bf16 v[40:43], v[24:27], v[212:215], v[40:43]\n"
"s_add_u32 s52, s53, 0x387000\n"
"s_and_b32 s52, s52, s54\n"
"buffer_load_dwordx4 v[180:183], v2, s[32:35], s52 offen offset:0 nt\n"
"v_and_b32_dpp v24, v50, v20 row_newbcast:8 row_mask:0xf bank_mask:0xf\n"
"v_and_b32_dpp v25, v50, v21 row_newbcast:8 row_mask:0xf bank_mask:0xf\n"
"v_and_b32_dpp v26, v50, v22 row_newbcast:8 row_mask:0xf bank_mask:0xf\n"
"v_and_b32_dpp v27, v50, v23 row_newbcast:8 row_mask:0xf bank_mask:0xf\n"
"s_waitcnt vmcnt(31)\n"
"s_nop 0\n"
"v_mfma_f32_16x16x32_bf16 v[40:43], v[24:27], v[216:219], v[40:43]\n"
"s_add_u32 s52, s53, 0x38e000\n"
"s_and_b32 s52, s52, s54\n"
"buffer_load_dwordx4 v[184:187], v2, s[32:35], s52 offen offset:0 nt\n"
"v_and_b32_dpp v24, v50, v20 row_newbcast:12 row_mask:0xf bank_mask:0xf\n"
"v_and_b32_dpp v25, v50, v21 row_newbcast:12 row_mask:0xf bank_mask:0xf\n"
"v_and_b32_dpp v26, v50, v22 row_newbcast:12 row_mask:0xf bank_mask:0xf\n"
"v_and_b32_dpp v27, v50, v23 row_newbcast:12 row_mask:0xf bank_mask:0xf\n"
"s_waitcnt vmcnt(31)\n"
"s_nop 0\n"
"v_mfma_f32_16x16x32_bf16 v[40:43], v[24:27], v[220:223], v[40:43]\n"
"s_add_u32 s52, s53, 0x395000\n"
"s_and_b32 s52, s52, s54\n"
"buffer_load_dwordx4 v[188:191], v2, s[32:35], s52 offen offset:0 nt\n"
"v_and_b32_dpp v24, v50, v20 row_newbcast:1 row_mask:0xf bank_mask:0xf\n"
"v_and_b32_dpp v25, v50, v21 row_newbcast:1 row_mask:0xf bank_mask:0xf\n"
"v_and_b32_dpp v26, v50, v22 row_newbcast:1 row_mask:0xf bank_mask:0xf\n"
"v_and_b32_dpp v27, v50, v23 row_newbcast:1 row_mask:0xf bank_mask:0xf\n"
"s_waitcnt vmcnt(31)\n"
"s_nop 0\n"
"v_mfma_f32_16x16x32_bf16 v[40:43], v[24:27], v[64:67], v[40:43]\n"
"s_add_u32 s52, s53, 0x39c000\n"
"s_and_b32 s52, s52, s54\n"
"buffer_load_dwordx4 v[192:195], v2, s[32:35], s52 offen offset:0 nt\n"
"v_and_b32_dpp v24, v50, v20 row_newbcast:5 row_mask:0xf bank_mask:0xf\n"
"v_and_b32_dpp v25, v50, v21 row_newbcast:5 row_mask:0xf bank_mask:0xf\n"
"v_and_b32_dpp v26, v50, v22 row_newbcast:5 row_mask:0xf bank_mask:0xf\n"
"v_and_b32_dpp v27, v50, v23 row_newbcast:5 row_mask:0xf bank_mask:0xf\n"
"s_waitcnt vmcnt(31)\n"
"s_nop 0\n"
"v_mfma_f32_16x16x32_bf16 v[40:43], v[24:27], v[68:71], v[40:43]\n"
"s_add_u32 s52, s53, 0x3a3000\n"
"s_and_b32 s52, s52, s54\n"
"buffer_load_dwordx4 v[196:199], v2, s[32:35], s52 offen offset:0 nt\n"
"v_and_b32_dpp v24, v50, v20 row_newbcast:9 row_mask:0xf bank_mask:0xf\n"
"v_and_b32_dpp v25, v50, v21 row_newbcast:9 row_mask:0xf bank_mask:0xf\n"
"v_and_b32_dpp v26, v50, v22 row_newbcast:9 row_mask:0xf bank_mask:0xf\n"
"v_and_b32_dpp v27, v50, v23 row_newbcast:9 row_mask:0xf bank_mask:0xf\n"
"s_waitcnt vmcnt(31)\n"
"s_nop 0\n"
"v_mfma_f32_16x16x32_bf16 v[40:43], v[24:27], v[72:75], v[40:43]\n"
"s_add_u32 s52, s53, 0x3aa000\n"
"s_and_b32 s52, s52, s54\n"
"buffer_load_dwordx4 v[200:203], v2, s[32:35], s52 offen offset:0 nt\n"
"v_and_b32_dpp v24, v50, v20 row_newbcast:13 row_mask:0xf bank_mask:0xf\n"
"v_and_b32_dpp v25, v50, v21 row_newbcast:13 row_mask:0xf bank_mask:0xf\n"
"v_and_b32_dpp v26, v50, v22 row_newbcast:13 row_mask:0xf bank_mask:0xf\n"
"v_and_b32_dpp v27, v50, v23 row_newbcast:13 row_mask:0xf bank_mask:0xf\n"
"s_waitcnt vmcnt(31)\n"
"s_nop 0\n"
"v_mfma_f32_16x16x32_bf16 v[40:43], v[24:27], v[76:79], v[40:43]\n"
"s_add_u32 s52, s53, 0x3b1000\n"
"s_and_b32 s52, s52, s54\n"
"buffer_load_dwordx4 v[204:207], v2, s[32:35], s52 offen offset:0 nt\n"
"v_and_b32_dpp v24, v50, v20 row_newbcast:2 row_mask:0xf bank_mask:0xf\n"
"v_and_b32_dpp v25, v50, v21 row_newbcast:2 row_mask:0xf bank_mask:0xf\n"
"v_and_b32_dpp v26, v50, v22 row_newbcast:2 row_mask:0xf bank_mask:0xf\n"
"v_and_b32_dpp v27, v50, v23 row_newbcast:2 row_mask:0xf bank_mask:0xf\n"
"s_waitcnt vmcnt(31)\n"
"s_nop 0\n"
"v_mfma_f32_16x16x32_bf16 v[40:43], v[24:27], v[80:83], v[40:43]\n"
"s_add_u32 s52, s53, 0x3b8000\n"
"s_and_b32 s52, s52, s54\n"
"buffer_load_dwordx4 v[208:211], v2, s[32:35], s52 offen offset:0 nt\n"
"v_and_b32_dpp v24, v50, v20 row_newbcast:6 row_mask:0xf bank_mask:0xf\n"
"v_and_b32_dpp v25, v50, v21 row_newbcast:6 row_mask:0xf bank_mask:0xf\n"
"v_and_b32_dpp v26, v50, v22 row_newbcast:6 row_mask:0xf bank_mask:0xf\n"
"v_and_b32_dpp v27, v50, v23 row_newbcast:6 row_mask:0xf bank_mask:0xf\n"
"s_waitcnt vmcnt(31)\n"
"s_nop 0\n"
"v_mfma_f32_16x16x32_bf16 v[40:43], v[24:27], v[84:87], v[40:43]\n"
"s_add_u32 s52, s53, 0x3bf000\n"
"s_and_b32 s52, s52, s54\n"
"buffer_load_dwordx4 v[212:215], v2, s[32:35], s52 offen offset:0 nt\n"
"v_and_b32_dpp v24, v50, v20 row_newbcast:10 row_mask:0xf bank_mask:0xf\n"
"v_and_b32_dpp v25, v50, v21 row_newbcast:10 row_mask:0xf bank_mask:0xf\n"
"v_and_b32_dpp v26, v50, v22 row_newbcast:10 row_mask:0xf bank_mask:0xf\n"
"v_and_b32_dpp v27, v50, v23 row_newbcast:10 row_mask:0xf bank_mask:0xf\n"
"s_waitcnt vmcnt(31)\n"
"s_nop 0\n"
"v_mfma_f32_16x16x32_bf16 v[40:43], v[24:27], v[88:91], v[40:43]\n"
"s_add_u32 s52, s53, 0x3c6000\n"
"s_and_b32 s52, s52, s54\n"
"buffer_load_dwordx4 v[216:219], v2, s[32:35], s52 offen offset:0 nt\n"
"v_and_b32_dpp v24, v50, v20 row_newbcast:14 row_mask:0xf bank_mask:0xf\n"
"v_and_b32_dpp v25, v50, v21 row_newbcast:14 row_mask:0xf bank_mask:0xf\n"
"v_and_b32_dpp v26, v50, v22 row_newbcast:14 row_mask:0xf bank_mask:0xf\n"
"v_and_b32_dpp v27, v50, v23 row_newbcast:14 row_mask:0xf bank_mask:0xf\n"
"s_waitcnt vmcnt(31)\n"
"s_nop 0\n"
"v_mfma_f32_16x16x32_bf16 v[40:43], v[24:27], v[92:95], v[40:43]\n"
"s_add_u32 s52, s53, 0x3cd000\n"
"s_and_b32 s52, s52, s54\n"
"buffer_load_dwordx4 v[220:223], v2, s[32:35], s52 offen offset:0 nt\n"
"v_and_b32_dpp v24, v50, v20 row_newbcast:3 row_mask:0xf bank_mask:0xf\n"
"v_and_b32_dpp v25, v50, v21 row_newbcast:3 row_mask:0xf bank_mask:0xf\n"
"v_and_b32_dpp v26, v50, v22 row_newbcast:3 row_mask:0xf bank_mask:0xf\n"
"v_and_b32_dpp v27, v50, v23 row_newbcast:3 row_mask:0xf bank_mask:0xf\n"
"s_waitcnt vmcnt(31)\n"
"s_nop 0\n"
"v_mfma_f32_16x16x32_bf16 v[40:43], v[24:27], v[96:99], v[40:43]\n"
"v_and_b32_dpp v24, v50, v20 row_newbcast:7 row_mask:0xf bank_mask:0xf\n"
"v_and_b32_dpp v25, v50, v21 row_newbcast:7 row_mask:0xf bank_mask:0xf\n"
"v_and_b32_dpp v26, v50, v22 row_newbcast:7 row_mask:0xf bank_mask:0xf\n"
"v_and_b32_dpp v27, v50, v23 row_newbcast:7 row_mask:0xf bank_mask:0xf\n"
"s_waitcnt vmcnt(30)\n"
"s_nop 0\n"
"v_mfma_f32_16x16x32_bf16 v[40:43], v[24:27], v[100:103], v[40:43]\n"
"v_and_b32_dpp v24, v50, v20 row_newbcast:11 row_mask:0xf bank_mask:0xf\n"
"v_and_b32_dpp v25, v50, v21 row_newbcast:11 row_mask:0xf bank_mask:0xf\n"
"v_and_b32_dpp v26, v50, v22 row_newbcast:11 row_mask:0xf bank_mask:0xf\n"
"v_and_b32_dpp v27, v50, v23 row_newbcast:11 row_mask:0xf bank_mask:0xf\n"
"s_waitcnt vmcnt(29)\n"
"s_nop 0\n"
"v_mfma_f32_16x16x32_bf16 v[40:43], v[24:27], v[104:107], v[40:43]\n"
"v_and_b32_dpp v24, v50, v20 row_newbcast:15 row_mask:0xf bank_mask:0xf\n"
"v_and_b32_dpp v25, v50, v21 row_newbcast:15 row_mask:0xf bank_mask:0xf\n"
"v_and_b32_dpp v26, v50, v22 row_newbcast:15 row_mask:0xf bank_mask:0xf\n"
"v_and_b32_dpp v27, v50, v23 row_newbcast:15 row_mask:0xf bank_mask:0xf\n"
"s_waitcnt vmcnt(28)\n"
"s_nop 0\n"
"v_mfma_f32_16x16x32_bf16 v[40:43], v[24:27], v[108:111], v[40:43]\n"
"v_and_b32_dpp v24, v51, v20 row_newbcast:0 row_mask:0xf bank_mask:0xf\n"
"v_and_b32_dpp v25, v51, v21 row_newbcast:0 row_mask:0xf bank_mask:0xf\n"
"v_and_b32_dpp v26, v51, v22 row_newbcast:0 row_mask:0xf bank_mask:0xf\n"
"v_and_b32_dpp v27, v51, v23 row_newbcast:0 row_mask:0xf bank_mask:0xf\n"
"s_waitcnt vmcnt(27)\n"
"s_nop 0\n"
"v_mfma_f32_16x16x32_bf16 v[40:43], v[24:27], v[112:115], v[40:43]\n"
"v_and_b32_dpp v24, v51, v20 row_newbcast:4 row_mask:0xf bank_mask:0xf\n"
"v_and_b32_dpp v25, v51, v21 row_newbcast:4 row_mask:0xf bank_mask:0xf\n"
"v_and_b32_dpp v26, v51, v22 row_newbcast:4 row_mask:0xf bank_mask:0xf\n"
"v_and_b32_dpp v27, v51, v23 row_newbcast:4 row_mask:0xf bank_mask:0xf\n"
"s_waitcnt vmcnt(26)\n"
"s_nop 0\n"
"v_mfma_f32_16x16x32_bf16 v[40:43], v[24:27], v[116:119], v[40:43]\n"
"v_and_b32_dpp v24, v51, v20 row_newbcast:8 row_mask:0xf bank_mask:0xf\n"
"v_and_b32_dpp v25, v51, v21 row_newbcast:8 row_mask:0xf bank_mask:0xf\n"
"v_and_b32_dpp v26, v51, v22 row_newbcast:8 row_mask:0xf bank_mask:0xf\n"
"v_and_b32_dpp v27, v51, v23 row_newbcast:8 row_mask:0xf bank_mask:0xf\n"
"s_waitcnt vmcnt(25)\n"
"s_nop 0\n"
"v_mfma_f32_16x16x32_bf16 v[40:43], v[24:27], v[120:123], v[40:43]\n"
"v_and_b32_dpp v24, v51, v20 row_newbcast:12 row_mask:0xf bank_mask:0xf\n"
"v_and_b32_dpp v25, v51, v21 row_newbcast:12 row_mask:0xf bank_mask:0xf\n"
"v_and_b32_dpp v26, v51, v22 row_newbcast:12 row_mask:0xf bank_mask:0xf\n"
"v_and_b32_dpp v27, v51, v23 row_newbcast:12 row_mask:0xf bank_mask:0xf\n"
"s_waitcnt vmcnt(24)\n"
"s_nop 0\n"
"v_mfma_f32_16x16x32_bf16 v[40:43], v[24:27], v[124:127], v[40:43]\n"
"v_and_b32_dpp v24, v51, v20 row_newbcast:1 row_mask:0xf bank_mask:0xf\n"
"v_and_b32_dpp v25, v51, v21 row_newbcast:1 row_mask:0xf bank_mask:0xf\n"
"v_and_b32_dpp v26, v51, v22 row_newbcast:1 row_mask:0xf bank_mask:0xf\n"
"v_and_b32_dpp v27, v51, v23 row_newbcast:1 row_mask:0xf bank_mask:0xf\n"
"s_waitcnt vmcnt(23)\n"
"s_nop 0\n"
"v_mfma_f32_16x16x32_bf16 v[40:43], v[24:27], v[128:131], v[40:43]\n"
"v_and_b32_dpp v24, v51, v20 row_newbcast:5 row_mask:0xf bank_mask:0xf\n"
"v_and_b32_dpp v25, v51, v21 row_newbcast:5 row_mask:0xf bank_mask:0xf\n"
"v_and_b32_dpp v26, v51, v22 row_newbcast:5 row_mask:0xf bank_mask:0xf\n"
"v_and_b32_dpp v27, v51, v23 row_newbcast:5 row_mask:0xf bank_mask:0xf\n"
"s_waitcnt vmcnt(22)\n"
"s_nop 0\n"
"v_mfma_f32_16x16x32_bf16 v[40:43], v[24:27], v[132:135], v[40:43]\n"
"v_and_b32_dpp v24, v51, v20 row_newbcast:9 row_mask:0xf bank_mask:0xf\n"
"v_and_b32_dpp v25, v51, v21 row_newbcast:9 row_mask:0xf bank_mask:0xf\n"
"v_and_b32_dpp v26, v51, v22 row_newbcast:9 row_mask:0xf bank_mask:0xf\n"
"v_and_b32_dpp v27, v51, v23 row_newbcast:9 row_mask:0xf bank_mask:0xf\n"
"s_waitcnt vmcnt(21)\n"
"s_nop 0\n"
"v_mfma_f32_16x16x32_bf16 v[40:43], v[24:27], v[136:139], v[40:43]\n"
"v_and_b32_dpp v24, v51, v20 row_newbcast:13 row_mask:0xf bank_mask:0xf\n"
"v_and_b32_dpp v25, v51, v21 row_newbcast:13 row_mask:0xf bank_mask:0xf\n"
"v_and_b32_dpp v26, v51, v22 row_newbcast:13 row_mask:0xf bank_mask:0xf\n"
"v_and_b32_dpp v27, v51, v23 row_newbcast:13 row_mask:0xf bank_mask:0xf\n"
"s_waitcnt vmcnt(20)\n"
"s_nop 0\n"
"v_mfma_f32_16x16x32_bf16 v[40:43], v[24:27], v[140:143], v[40:43]\n"
"v_and_b32_dpp v24, v51, v20 row_newbcast:2 row_mask:0xf bank_mask:0xf\n"
"v_and_b32_dpp v25, v51, v21 row_newbcast:2 row_mask:0xf bank_mask:0xf\n"
"v_and_b32_dpp v26, v51, v22 row_newbcast:2 row_mask:0xf bank_mask:0xf\n"
"v_and_b32_dpp v27, v51, v23 row_newbcast:2 row_mask:0xf bank_mask:0xf\n"
"s_waitcnt vmcnt(19)\n"
"s_nop 0\n"
"v_mfma_f32_16x16x32_bf16 v[40:43], v[24:27], v[144:147], v[40:43]\n"
"v_and_b32_dpp v24, v51, v20 row_newbcast:6 row_mask:0xf bank_mask:0xf\n"
"v_and_b32_dpp v25, v51, v21 row_newbcast:6 row_mask:0xf bank_mask:0xf\n"
"v_and_b32_dpp v26, v51, v22 row_newbcast:6 row_mask:0xf bank_mask:0xf\n"
"v_and_b32_dpp v27, v51, v23 row_newbcast:6 row_mask:0xf bank_mask:0xf\n"
"s_waitcnt vmcnt(18)\n"
"s_nop 0\n"
"v_mfma_f32_16x16x32_bf16 v[40:43], v[24:27], v[148:151], v[40:43]\n"
"v_and_b32_dpp v24, v51, v20 row_newbcast:10 row_mask:0xf bank_mask:0xf\n"
"v_and_b32_dpp v25, v51, v21 row_newbcast:10 row_mask:0xf bank_mask:0xf\n"
"v_and_b32_dpp v26, v51, v22 row_newbcast:10 row_mask:0xf bank_mask:0xf\n"
"v_and_b32_dpp v27, v51, v23 row_newbcast:10 row_mask:0xf bank_mask:0xf\n"
"s_waitcnt vmcnt(17)\n"
"s_nop 0\n"
"v_mfma_f32_16x16x32_bf16 v[40:43], v[24:27], v[152:155], v[40:43]\n"
"v_and_b32_dpp v24, v51, v20 row_newbcast:14 row_mask:0xf bank_mask:0xf\n"
"v_and_b32_dpp v25, v51, v21 row_newbcast:14 row_mask:0xf bank_mask:0xf\n"
"v_and_b32_dpp v26, v51, v22 row_newbcast:14 row_mask:0xf bank_mask:0xf\n"
"v_and_b32_dpp v27, v51, v23 row_newbcast:14 row_mask:0xf bank_mask:0xf\n"
"s_waitcnt vmcnt(16)\n"
"s_nop 0\n"
"v_mfma_f32_16x16x32_bf16 v[40:43], v[24:27], v[156:159], v[40:43]\n"
"v_and_b32_dpp v24, v51, v20 row_newbcast:3 row_mask:0xf bank_mask:0xf\n"
"v_and_b32_dpp v25, v51, v21 row_newbcast:3 row_mask:0xf bank_mask:0xf\n"
"v_and_b32_dpp v26, v51, v22 row_newbcast:3 row_mask:0xf bank_mask:0xf\n"
"v_and_b32_dpp v27, v51, v23 row_newbcast:3 row_mask:0xf bank_mask:0xf\n"
"s_waitcnt vmcnt(15)\n"
"s_nop 0\n"
"v_mfma_f32_16x16x32_bf16 v[40:43], v[24:27], v[160:163], v[40:43]\n"
"v_and_b32_dpp v24, v51, v20 row_newbcast:7 row_mask:0xf bank_mask:0xf\n"
"v_and_b32_dpp v25, v51, v21 row_newbcast:7 row_mask:0xf bank_mask:0xf\n"
"v_and_b32_dpp v26, v51, v22 row_newbcast:7 row_mask:0xf bank_mask:0xf\n"
"v_and_b32_dpp v27, v51, v23 row_newbcast:7 row_mask:0xf bank_mask:0xf\n"
"s_waitcnt vmcnt(14)\n"
"s_nop 0\n"
"v_mfma_f32_16x16x32_bf16 v[40:43], v[24:27], v[164:167], v[40:43]\n"
"v_and_b32_dpp v24, v51, v20 row_newbcast:11 row_mask:0xf bank_mask:0xf\n"
"v_and_b32_dpp v25, v51, v21 row_newbcast:11 row_mask:0xf bank_mask:0xf\n"
"v_and_b32_dpp v26, v51, v22 row_newbcast:11 row_mask:0xf bank_mask:0xf\n"
"v_and_b32_dpp v27, v51, v23 row_newbcast:11 row_mask:0xf bank_mask:0xf\n"
"s_waitcnt vmcnt(13)\n"
"s_nop 0\n"
"v_mfma_f32_16x16x32_bf16 v[40:43], v[24:27], v[168:171], v[40:43]\n"
"v_and_b32_dpp v24, v51, v20 row_newbcast:15 row_mask:0xf bank_mask:0xf\n"
"v_and_b32_dpp v25, v51, v21 row_newbcast:15 row_mask:0xf bank_mask:0xf\n"
"v_and_b32_dpp v26, v51, v22 row_newbcast:15 row_mask:0xf bank_mask:0xf\n"
"v_and_b32_dpp v27, v51, v23 row_newbcast:15 row_mask:0xf bank_mask:0xf\n"
"s_waitcnt vmcnt(12)\n"
"s_nop 0\n"
"v_mfma_f32_16x16x32_bf16 v[40:43], v[24:27], v[172:175], v[40:43]\n"
"v_and_b32_dpp v24, v52, v20 row_newbcast:0 row_mask:0xf bank_mask:0xf\n"
"v_and_b32_dpp v25, v52, v21 row_newbcast:0 row_mask:0xf bank_mask:0xf\n"
"v_and_b32_dpp v26, v52, v22 row_newbcast:0 row_mask:0xf bank_mask:0xf\n"
"v_and_b32_dpp v27, v52, v23 row_newbcast:0 row_mask:0xf bank_mask:0xf\n"
"s_waitcnt vmcnt(11)\n"
"s_nop 0\n"
"v_mfma_f32_16x16x32_bf16 v[40:43], v[24:27], v[176:179], v[40:43]\n"
"v_and_b32_dpp v24, v52, v20 row_newbcast:4 row_mask:0xf bank_mask:0xf\n"
"v_and_b32_dpp v25, v52, v21 row_newbcast:4 row_mask:0xf bank_mask:0xf\n"
"v_and_b32_dpp v26, v52, v22 row_newbcast:4 row_mask:0xf bank_mask:0xf\n"
"v_and_b32_dpp v27, v52, v23 row_newbcast:4 row_mask:0xf bank_mask:0xf\n"
"s_waitcnt vmcnt(10)\n"
"s_nop 0\n"
"v_mfma_f32_16x16x32_bf16 v[40:43], v[24:27], v[180:183], v[40:43]\n"
"v_and_b32_dpp v24, v52, v20 row_newbcast:8 row_mask:0xf bank_mask:0xf\n"
"v_and_b32_dpp v25, v52, v21 row_newbcast:8 row_mask:0xf bank_mask:0xf\n"
"v_and_b32_dpp v26, v52, v22 row_newbcast:8 row_mask:0xf bank_mask:0xf\n"
"v_and_b32_dpp v27, v52, v23 row_newbcast:8 row_mask:0xf bank_mask:0xf\n"
"s_waitcnt vmcnt(9)\n"
"s_nop 0\n"
"v_mfma_f32_16x16x32_bf16 v[40:43], v[24:27], v[184:187], v[40:43]\n"
"v_and_b32_dpp v24, v52, v20 row_newbcast:12 row_mask:0xf bank_mask:0xf\n"
"v_and_b32_dpp v25, v52, v21 row_newbcast:12 row_mask:0xf bank_mask:0xf\n"
"v_and_b32_dpp v26, v52, v22 row_newbcast:12 row_mask:0xf bank_mask:0xf\n"
"v_and_b32_dpp v27, v52, v23 row_newbcast:12 row_mask:0xf bank_mask:0xf\n"
"s_waitcnt vmcnt(8)\n"
"s_nop 0\n"
"v_mfma_f32_16x16x32_bf16 v[40:43], v[24:27], v[188:191], v[40:43]\n"
"v_and_b32_dpp v24, v52, v20 row_newbcast:1 row_mask:0xf bank_mask:0xf\n"
"v_and_b32_dpp v25, v52, v21 row_newbcast:1 row_mask:0xf bank_mask:0xf\n"
"v_and_b32_dpp v26, v52, v22 row_newbcast:1 row_mask:0xf bank_mask:0xf\n"
"v_and_b32_dpp v27, v52, v23 row_newbcast:1 row_mask:0xf bank_mask:0xf\n"
"s_waitcnt vmcnt(7)\n"
"s_nop 0\n"
"v_mfma_f32_16x16x32_bf16 v[40:43], v[24:27], v[192:195], v[40:43]\n"
"v_and_b32_dpp v24, v52, v20 row_newbcast:5 row_mask:0xf bank_mask:0xf\n"
"v_and_b32_dpp v25, v52, v21 row_newbcast:5 row_mask:0xf bank_mask:0xf\n"
"v_and_b32_dpp v26, v52, v22 row_newbcast:5 row_mask:0xf bank_mask:0xf\n"
"v_and_b32_dpp v27, v52, v23 row_newbcast:5 row_mask:0xf bank_mask:0xf\n"
"s_waitcnt vmcnt(6)\n"
"s_nop 0\n"
"v_mfma_f32_16x16x32_bf16 v[40:43], v[24:27], v[196:199], v[40:43]\n"
"v_min_u32 v62, 32, v0\n"
"v_lshlrev_b32 v62, 4, v62\n"
"v_mov_b32 v61, 0x7ff00000\n"
"v_cmp_gt_u32 vcc, 33, v0\n"
"s_nop 1\n"
"v_cndmask_b32 v61, v61, v62, vcc\n"
"v_add_u32 v62, 159744, v62\n"
"v_mov_b32 v63, 160256\n"
"s_mul_i32 s70, s48, 32768\n"
"s_mul_i32 s56, s49, 640\n"
"s_add_u32 s70, s70, s56\n"
"s_mul_i32 s56, s51, 16384\n"
"s_add_u32 s70, s70, s56\n"
"s_add_u32 s70, s70, 0x100000\n"
"s_add_u32 s70, s70, s99\n"
"v_and_b32 v58, 15, v0\n"
"v_lshlrev_b32 v58, 5, v58\n"
"v_lshrrev_b32 v59, 4, v0\n"
"v_lshl_add_u32 v58, v59, 4, v58\n"
"s_mul_i32 s56, s50, 576\n"
"s_add_u32 s56, s56, 159744\n"
"v_add_u32 v58, s56, v58\n"
"v_mov_b32 v60, v58\n"
"v_and_b32_dpp v24, v52, v20 row_newbcast:9 row_mask:0xf bank_mask:0xf\n"
"v_and_b32_dpp v25, v52, v21 row_newbcast:9 row_mask:0xf bank_mask:0xf\n"
"v_and_b32_dpp v26, v52, v22 row_newbcast:9 row_mask:0xf bank_mask:0xf\n"
"v_and_b32_dpp v27, v52, v23 row_newbcast:9 row_mask:0xf bank_mask:0xf\n"
"s_waitcnt vmcnt(5)\n"
"s_nop 0\n"
"v_mfma_f32_16x16x32_bf16 v[40:43], v[24:27], v[200:203], v[40:43]\n"
"v_and_b32_dpp v24, v52, v20 row_newbcast:13 row_mask:0xf bank_mask:0xf\n"
"v_and_b32_dpp v25, v52, v21 row_newbcast:13 row_mask:0xf bank_mask:0xf\n"
"v_and_b32_dpp v26, v52, v22 row_newbcast:13 row_mask:0xf bank_mask:0xf\n"
"v_and_b32_dpp v27, v52, v23 row_newbcast:13 row_mask:0xf bank_mask:0xf\n"
"s_waitcnt vmcnt(4)\n"
"s_nop 0\n"
"v_mfma_f32_16x16x32_bf16 v[40:43], v[24:27], v[204:207], v[40:43]\n"
"v_and_b32_dpp v24, v52, v20 row_newbcast:2 row_mask:0xf bank_mask:0xf\n"
"v_and_b32_dpp v25, v52, v21 row_newbcast:2 row_mask:0xf bank_mask:0xf\n"
"v_and_b32_dpp v26, v52, v22 row_newbcast:2 row_mask:0xf bank_mask:0xf\n"
"v_and_b32_dpp v27, v52, v23 row_newbcast:2 row_mask:0xf bank_mask:0xf\n"
"s_waitcnt vmcnt(3)\n"
"s_nop 0\n"
"v_mfma_f32_16x16x32_bf16 v[40:43], v[24:27], v[208:211], v[40:43]\n"
"v_and_b32_dpp v24, v52, v20 row_newbcast:6 row_mask:0xf bank_mask:0xf\n"
"v_and_b32_dpp v25, v52, v21 row_newbcast:6 row_mask:0xf bank_mask:0xf\n"
"v_and_b32_dpp v26, v52, v22 row_newbcast:6 row_mask:0xf bank_mask:0xf\n"
"v_and_b32_dpp v27, v52, v23 row_newbcast:6 row_mask:0xf bank_mask:0xf\n"
"s_waitcnt vmcnt(2)\n"
"s_nop 0\n"
"v_mfma_f32_16x16x32_bf16 v[40:43], v[24:27], v[212:215], v[40:43]\n"
"v_and_b32_dpp v24, v52, v20 row_newbcast:10 row_mask:0xf bank_mask:0xf\n"
"v_and_b32_dpp v25, v52, v21 row_newbcast:10 row_mask:0xf bank_mask:0xf\n"
"v_and_b32_dpp v26, v52, v22 row_newbcast:10 row_mask:0xf bank_mask:0xf\n"
"v_and_b32_dpp v27, v52, v23 row_newbcast:10 row_mask:0xf bank_mask:0xf\n"
"s_waitcnt vmcnt(1)\n"
"s_nop 0\n"
"v_mfma_f32_16x16x32_bf16 v[40:43], v[24:27], v[216:219], v[40:43]\n"
"v_and_b32_dpp v24, v52, v20 row_newbcast:14 row_mask:0xf bank_mask:0xf\n"
"v_and_b32_dpp v25, v52, v21 row_newbcast:14 row_mask:0xf bank_mask:0xf\n"
"v_and_b32_dpp v26, v52, v22 row_newbcast:14 row_mask:0xf bank_mask:0xf\n"
"v_and_b32_dpp v27, v52, v23 row_newbcast:14 row_mask:0xf bank_mask:0xf\n"
"s_waitcnt vmcnt(0)\n"
"s_nop 0\n"
"v_mfma_f32_16x16x32_bf16 v[40:43], v[24:27], v[220:223], v[40:43]\n"
"s_mov_b32 exec_hi, 0\n"
"s_nop 6\n"
"ds_write_b128 v60, v[40:43]\n"
"s_mov_b64 exec, -1\n"
"s_waitcnt lgkmcnt(0)\n"
"s_barrier\n"
"s_cmp_lg_u32 s50, 0\n"
"s_cbranch_scc1 Lnomerge_503\n"
"ds_read_b32 v24, v63 offset:0\n"
"ds_read_b32 v25, v63 offset:576\n"
"ds_read_b32 v26, v63 offset:1152\n"
"ds_read_b32 v27, v63 offset:1728\n"
"ds_read_b128 v[4:7], v62 offset:0\n"
"ds_read_b128 v[8:11], v62 offset:576\n"
"ds_read_b128 v[12:15], v62 offset:1152\n"
"ds_read_b128 v[16:19], v62 offset:1728\n"
"s_waitcnt lgkmcnt(4)\n"
"v_max3_f32 v28, v24, v25, v26\n"
"v_max_f32 v28, v28, v27\n"
"v_sub_f32 v24, v24, v28\n"
"v_sub_f32 v25, v25, v28\n"
"v_sub_f32 v26, v26, v28\n"
"v_sub_f32 v27, v27, v28\n"
"v_exp_f32 v24, v24\n"
"v_exp_f32 v25, v25\n"
"v_exp_f32 v26, v26\n"
"v_exp_f32 v27, v27\n"
"s_waitcnt lgkmcnt(0)\n"
"v_mul_f32 v4, v4, v24\n"
"v_mul_f32 v5, v5, v24\n"
"v_mul_f32 v6, v6, v24\n"
"v_mul_f32 v7, v7, v24\n"
"v_fmac_f32 v4, v8, v25\n"
"v_fmac_f32 v5, v9, v25\n"
"v_fmac_f32 v6, v10, v25\n"
"v_fmac_f32 v7, v11, v25\n"
"v_fmac_f32 v4, v12, v26\n"
"v_fmac_f32 v5, v13, v26\n"
"v_fmac_f32 v6, v14, v26\n"
"v_fmac_f32 v7, v15, v26\n"
"v_fmac_f32 v4, v16, v27\n"
"v_fmac_f32 v5, v17, v27\n"
"v_fmac_f32 v6, v18, v27\n"
"v_fmac_f32 v7, v19, v27\n"
"v_cmp_eq_u32 vcc, 32, v0\n"
"s_nop 1\n"
"v_cndmask_b32 v4, v4, v28, vcc\n"
"buffer_store_dwordx4 v[4:7], v61, s[36:39], s70 offen sc1\n"
"s_xor_b32 s70, s70, 8192\n"
"buffer_store_dwordx4 v[4:7], v61, s[36:39], s70 offen sc1\n"
"Lnomerge_503:\n"
"s_cmp_lg_u32 s51, 0\n"
"s_cbranch_scc1 LA_END\n"
"s_branch LA_P2B\n"
"LEND:\n"
      :
      : "s"(__builtin_amdgcn_kernarg_segment_ptr()), "s"(blockIdx.x), "s"(wid), "s"(ldsaddr)
      : "v0", "v1", "v2", "v3", "v4", "v5", "v6", "v7", "v8", "v9", "v10", "v11", "v12", "v13", "v14", "v15", "v16", "v17", "v18", "v19", "v20", "v21", "v22", "v23", "v24", "v25", "v26", "v27", "v28", "v29", "v30", "v31", "v32", "v33", "v34", "v35", "v36", "v37", "v38", "v39", "v40", "v41", "v42", "v43", "v44", "v45", "v46", "v47", "v48", "v49", "v50", "v51", "v52", "v53", "v54", "v55", "v56", "v57", "v58", "v59", "v60", "v61", "v62", "v63", "v64", "v65", "v66", "v67", "v68", "v69", "v70", "v71", "v72", "v73", "v74", "v75", "v76", "v77", "v78", "v79", "v80", "v81", "v82", "v83", "v84", "v85", "v86", "v87", "v88", "v89", "v90", "v91", "v92", "v93", "v94", "v95", "v96", "v97", "v98", "v99", "v100", "v101", "v102", "v103", "v104", "v105", "v106", "v107", "v108", "v109", "v110", "v111", "v112", "v113", "v114", "v115", "v116", "v117", "v118", "v119", "v120", "v121", "v122", "v123", "v124", "v125", "v126", "v127", "v128", "v129", "v130", "v131", "v132", "v133", "v134", "v135", "v136", "v137", "v138", "v139", "v140", "v141", "v142", "v143", "v144", "v145", "v146", "v147", "v148", "v149", "v150", "v151", "v152", "v153", "v154", "v155", "v156", "v157", "v158", "v159", "v160", "v161", "v162", "v163", "v164", "v165", "v166", "v167", "v168", "v169", "v170", "v171", "v172", "v173", "v174", "v175", "v176", "v177", "v178", "v179", "v180", "v181", "v182", "v183", "v184", "v185", "v186", "v187", "v188", "v189", "v190", "v191", "v192", "v193", "v194", "v195", "v196", "v197", "v198", "v199", "v200", "v201", "v202", "v203", "v204", "v205", "v206", "v207", "v208", "v209", "v210", "v211", "v212", "v213", "v214", "v215", "v216", "v217", "v218", "v219", "v220", "v221", "v222", "v223", "v224", "v225", "v226", "v227", "v228", "v229", "v230", "v231", "v232", "v233", "v234", "v235", "v236", "v237", "v238", "v239", "v240", "v241", "v242", "v243", "v244", "v245", "v246", "v247", "v248", "v249", "v250", "v251", "v252", "v253", "v254", "v255", "a0", "a1", "a2", "a3", "a4", "a5", "a6", "a7", "a8", "a9", "a10", "a11", "a12", "a13", "a14", "a15", "a16", "a17", "a18", "a19", "a20", "a21", "a22", "a23", "a24", "a25", "a26", "a27", "a28", "a29", "a30", "a31", "a32", "a33", "a34", "a35", "a36", "a37", "a38", "a39", "a40", "a41", "a42", "a43", "a44", "a45", "a46", "a47", "a48", "a49", "a50", "a51", "a52", "a53", "a54", "a55", "a56", "a57", "a58", "a59", "a60", "a61", "a62", "a63", "a64", "a65", "a66", "a67", "a68", "a69", "a70", "a71", "a72", "a73", "a74", "a75", "a76", "a77", "a78", "a79", "a80", "a81", "a82", "a83", "a84", "a85", "a86", "a87", "a88", "a89", "a90", "a91", "a92", "a93", "a94", "a95", "a96", "a97", "a98", "a99", "a100", "a101", "a102", "a103", "a104", "a105", "a106", "a107", "a108", "a109", "a110", "a111", "a112", "a113", "a114", "a115", "a116", "a117", "a118", "a119", "a120", "a121", "a122", "a123", "a124", "a125", "a126", "a127", "a128", "a129", "a130", "a131", "a132", "a133", "a134", "a135", "a136", "a137", "a138", "a139", "a140", "a141", "a142", "a143", "a144", "a145", "a146", "a147", "a148", "a149", "a150", "a151", "a152", "a153", "a154", "a155", "a156", "a157", "a158", "a159", "a160", "a161", "a162", "a163", "a164", "a165", "a166", "a167", "a168", "a169", "a170", "a171", "a172", "a173", "a174", "a175", "a176", "a177", "a178", "a179", "a180", "a181", "a182", "a183", "a184", "a185", "a186", "a187", "a188", "a189", "a190", "a191", "a192", "a193", "a194", "a195", "a196", "a197", "a198", "a199", "a200", "a201", "a202", "a203", "a204", "a205", "a206", "a207", "a208", "a209", "a210", "a211", "a212", "a213", "a214", "a215", "a216", "a217", "a218", "a219", "a220", "a221", "a222", "a223", "a224", "a225", "a226", "a227", "a228", "a229", "a230", "a231", "a232", "a233", "a234", "a235", "a236", "a237", "a238", "a239", "a240", "a241", "a242", "a243", "a244", "a245", "a246", "a247", "a248", "a249", "a250", "a251", "a252", "a253", "a254", "a255", "s8", "s9", "s10", "s11", "s12", "s13", "s14", "s15", "s16", "s17", "s18", "s19", "s20", "s21", "s22", "s23", "s24", "s25", "s26", "s27", "s28", "s29", "s30", "s31", "s33", "s34", "s35", "s36", "s37", "s38", "s39", "s40", "s41", "s42", "s43", "s44", "s45", "s46", "s47", "s48", "s49", "s50", "s51", "s52", "s53", "s54", "s55", "s56", "s57", "s58", "s59", "s60", "s61", "s62", "s63", "s64", "s65", "s66", "s67", "s68", "s69", "s70", "s71", "s72", "s73", "s74", "s75", "s76", "s77", "s78", "s79", "s80", "s81", "s82", "s83", "s84", "s85", "s86", "s87", "s88", "s89", "s90", "s91", "s92", "s93", "s94", "s95", "s96", "s97", "s98", "s99", "vcc", "scc", "memory");
}

extern "C" void kernel_launch(void* const* d_in, const int* in_sizes, int n_in, void* d_out, int out_size, void* d_ws, size_t ws_size,
                              hipStream_t stream) {
  fused_block_kernel<<<256, 256, 0, stream>>>(d_in[0], d_in[1], d_in[2], d_in[3], d_in[4], d_in[6], d_in[7], d_in[8], d_out, d_ws);
}
```
